```python
import torch

def _softmax(x, axis=-1):
    return torch.softmax(x, dim=axis)
def _gelu(x):
    return torch.nn.functional.gelu(x, approximate="tanh")
def _silu(x):
    return torch.nn.functional.silu(x)
def _sigmoid(x):
    return torch.sigmoid(x)
def _layernorm(x, axis=-1, eps=1e-5):
    m = x.mean(dim=axis, keepdim=True)
    v = x.var(dim=axis, keepdim=True, unbiased=False)
    return (x - m) / torch.sqrt(v + eps)
def _scatter_add(tgt, idx, src, axis=0):
    return tgt.clone().index_add_(axis, idx, src)
def _segment_sum(src, seg, n):
    out = torch.zeros((n,) + src.shape[1:], dtype=src.dtype)
    return out.index_add_(0, seg, src)
def _pad(x, pw):
    flat = tuple(v for pair in reversed(pw) for v in pair)
    return torch.nn.functional.pad(x, flat)

kernel_name = 'concat_heads__mha_block_cached__gqa_block_cached__near_ridge'

def setup_inputs():

    _w = (lambda a: (lambda t: t.to(torch.bfloat16) if t.is_floating_point() else t)(torch.as_tensor(a)))
    def _A_setup():
        B, Sq, D = 8, 224, 512
        H, Sk, Dh = 32, 512, 128
        return {
            "x": _w((lambda *s: torch.randn(*s, dtype=torch.bfloat16) * 0.8)(B, Sq, D)),
            "k_cache": _w((lambda *s: torch.randn(*s, dtype=torch.bfloat16) * 0.8)(B, H, Sk, Dh)),
            "v_cache": _w((lambda *s: torch.randn(*s, dtype=torch.bfloat16) * 0.05)(B, H, Sk, Dh)),
            "wq": _w((lambda *s: torch.randn(*s, dtype=torch.bfloat16) * 0.8)(D, H * Dh) * (D ** -0.5)),
            "wo": _w((lambda *s: torch.randn(*s, dtype=torch.bfloat16) * 0.05)(H * Dh, D) * ((H * Dh) ** -0.5)),
        }
    _A_in = _A_setup()

    def _B_setup():
        B, Sq, D = 8, 224, 512
        Hq, Hkv, Sk, Dh = 32, 32, 512, 128
        assert Hq % Hkv == 0, (Hq, Hkv)
        return {
            "x": _w((lambda *s: torch.randn(*s, dtype=torch.bfloat16) * 0.8)(B, Sq, D)),
            "k_cache": _w((lambda *s: torch.randn(*s, dtype=torch.bfloat16) * 0.8)(B, Hkv, Sk, Dh)),
            "v_cache": _w((lambda *s: torch.randn(*s, dtype=torch.bfloat16) * 0.05)(B, Hkv, Sk, Dh)),
            "wq": _w((lambda *s: torch.randn(*s, dtype=torch.bfloat16) * 0.8)(D, Hq * Dh) * (D ** -0.5)),
            "wo": _w((lambda *s: torch.randn(*s, dtype=torch.bfloat16) * 0.05)(Hq * Dh, D) * ((Hq * Dh) ** -0.5)),
        }
    _B_in = _B_setup()

    return {
        "A_x": _A_in["x"],
        "A_k_cache": _A_in["k_cache"],
        "A_v_cache": _A_in["v_cache"],
        "A_wq": _A_in["wq"],
        "A_wo": _A_in["wo"],
        "B_x": _B_in["x"],
        "B_k_cache": _B_in["k_cache"],
        "B_v_cache": _B_in["v_cache"],
        "B_wq": _B_in["wq"],
        "B_wo": _B_in["wo"],
    }

def reference(A_x, A_k_cache, A_v_cache, A_wq, A_wo, B_x, B_k_cache, B_v_cache, B_wq, B_wo):
    def _A_ref(x, k_cache, v_cache, wq, wo):
        B, Sq, D = x.shape
        H, Dh = 32, 128
        q = (lambda x, *d: x.permute(*d))((x @ wq).reshape(B, Sq, H, Dh), 0, 2, 1, 3)
        scores = torch.einsum("bhqd,bhkd->bhqk", q, k_cache) * (Dh ** -0.5)
        p = _softmax(scores, axis=-1)
        o = torch.einsum("bhqk,bhkd->bhqd", p, v_cache)
        return (lambda x, *d: x.permute(*d))(o, 0, 2, 1, 3).reshape(B, Sq, H * Dh) @ wo

    def _B_ref(x, k_cache, v_cache, wq, wo):
        B, Sq, D = x.shape
        Hq, Dh = 32, 128
        Hkv = k_cache.shape[1]
        rep = Hq // Hkv
        q = (lambda x, *d: x.permute(*d))((x @ wq).reshape(B, Sq, Hq, Dh), 0, 2, 1, 3)
        k = (lambda x, n, axis: torch.repeat_interleave(x, n, dim=axis))(k_cache, rep, axis=1)
        v = (lambda x, n, axis: torch.repeat_interleave(x, n, dim=axis))(v_cache, rep, axis=1)
        scores = torch.einsum("bhqd,bhkd->bhqk", q, k) * (Dh ** -0.5)
        p = _softmax(scores, axis=-1)
        o = torch.einsum("bhqk,bhkd->bhqd", p, v)
        return (lambda x, *d: x.permute(*d))(o, 0, 2, 1, 3).reshape(B, Sq, Hq * Dh) @ wo

    a = _A_ref(A_x, A_k_cache, A_v_cache, A_wq, A_wo)
    b = _B_ref(B_x, B_k_cache, B_v_cache, B_wq, B_wo)
    return torch.concatenate([a, b], axis=-1)
```

```cpp
#include <hip/hip_runtime.h>
#include <hip/hip_bf16.h>

__global__ __launch_bounds__(256, 1) void fused_kernel(const char* xA, const char* kA, const char* vA, const char* wqA, const char* woA,
                                                       const char* xB, const char* kB, const char* vB, const char* wqB, const char* woB,
                                                       char* ws, char* out) {
  __shared__ __attribute__((aligned(16))) char lds[163840];
  const unsigned w = __builtin_amdgcn_readfirstlane(threadIdx.x >> 6);
  asm volatile(
    "s_bfe_u32 s64, %1, 0x10002\n"
    "s_mul_i32 s64, s64, 40\n"
    "s_load_dwordx8 s[8:15], %0, s64\n"
    "s_add_u32 s64, s64, 32\n"
    "s_load_dwordx2 s[16:17], %0, s64\n"
    "s_load_dwordx4 s[20:23], %0, 0x50\n"
    "s_memrealtime s[26:27]\n"
    "s_getpc_b64 s[28:29]\n"
    "L_pc_%=:\n"
    "s_mov_b32 s30, L_slow_%=-L_pc_%=\n"
    "s_mov_b32 s31, 0x20000\n"
    "s_lshr_b32 s66, %1, 3\n"
    "s_lshl2_add_u32 s66, s66, %2\n"
    "s_lshl_b32 s66, s66, 10\n"
    "v_mbcnt_lo_u32_b32 v0, -1, 0\n"
    "v_mbcnt_hi_u32_b32 v0, -1, v0\n"
    "v_lshlrev_b32 v1, 4, v0\n"
    "buffer_load_dword a255, v1, s[28:31], s66 offen\n"
    "s_lshr_b32 s6, %1, 5\n"
    "s_and_b32 s7, %1, 3\n"
    "s_bfe_u32 s34, %1, 0x20003\n"
    "s_lshl2_add_u32 s7, s7, s34\n"
    "s_lshl_b32 s34, s34, 1\n"
    "s_mov_b32 s58, s6\n"
    "s_mul_i32 s34, s34, 3\n"
    "s_mov_b32 s52, 0xd11ac688\n"
    "s_mov_b32 s53, 0x6b1a2358\n"
    "s_lshr_b64 s[52:53], s[52:53], s34\n"
    "s_mov_b32 s18, s52\n"
    "s_mul_i32 s58, s58, 3\n"
    "s_mov_b32 s52, 0x88fac688\n"
    "s_mov_b32 s53, 0xfac6\n"
    "s_lshr_b64 s[52:53], s[52:53], s58\n"
    "s_mov_b32 s19, s52\n"
    "s_mov_b32 s38, 229376\n"
    "s_mov_b32 s39, 0x20000\n"
    "s_mov_b32 s42, 4186624\n"
    "s_mov_b32 s43, 0x20000\n"
    "s_mov_b32 s60, %2\n"
    "s_lshl_b32 s56, s60, 10\n"
    "s_and_b32 s61, s60, 1\n"
    "s_lshr_b32 s62, s60, 1\n"
    "s_lshl_b32 s57, s62, 14\n"
    "s_lshl_b32 s58, s61, 10\n"
    "s_add_u32 s57, s57, s58\n"
    "v_and_b32 v33, 3, v0\n"
    "v_bfe_u32 v38, v0, 2, 2\n"
    "v_lshrrev_b32 v39, 4, v0\n"
    "v_lshl_add_u32 v40, v39, 2, v33\n"
    "v_lshlrev_b32 v7, 13, v38\n"
    "v_lshl_add_u32 v7, v40, 4, v7\n"
    "s_lshl_b32 s58, s62, 8\n"
    "s_lshl_b32 s64, s61, 15\n"
    "s_add_u32 s58, s58, s64\n"
    "v_add_u32 v7, s58, v7\n"
    "v_bfe_u32 v32, v0, 2, 3\n"
    "v_lshrrev_b32 v34, 5, v0\n"
    "v_lshrrev_b32 v35, 2, v32\n"
    "s_lshl_b32 s58, s61, 1\n"
    "v_add_u32 v35, s58, v35\n"
    "v_lshrrev_b32 v37, 1, v35\n"
    "v_xor_b32 v36, v37, v35\n"
    "v_and_b32 v36, 1, v36\n"
    "v_lshl_add_u32 v36, v36, 1, v37\n"
    "v_xor_b32 v36, v36, v33\n"
    "v_lshlrev_b32 v6, 10, v32\n"
    "v_lshl_add_u32 v6, v34, 6, v6\n"
    "v_lshl_add_u32 v6, v36, 4, v6\n"
    "s_lshl_b32 s58, s60, 13\n"
    "v_add_u32 v6, s58, v6\n"
    "s_mul_i32 s58, s6, 229376\n"
    "s_lshl_b32 s64, s7, 9\n"
    "s_waitcnt lgkmcnt(0)\n"
    "s_add_u32 s40, s14, s64\n"
    "s_addc_u32 s41, s15, 0\n"
    "s_add_u32 s36, s8, s58\n"
    "s_addc_u32 s37, s9, 0\n"
    "s_bfe_u32 s67, s19, 0x30000\n"
    "s_lshl_b32 s67, s67, 12\n"
    "s_lshl4_add_u32 s58, s67, 0\n"
    "s_add_u32 s67, s67, s56\n"
    "s_add_u32 m0, s67, 32768\n"
    "s_nop 0\n"
    "buffer_load_dwordx4 v7, s[40:43], s58 offen lds\n"
    "s_bfe_u32 s67, s18, 0x30000\n"
    "s_lshl_b32 s67, s67, 12\n"
    "s_lshl3_add_u32 s58, s67, 0\n"
    "s_add_u32 s67, s67, s56\n"
    "s_add_u32 m0, s67, 0\n"
    "s_nop 0\n"
    "buffer_load_dwordx4 v6, s[36:39], s58 offen lds\n"
    "s_bfe_u32 s67, s18, 0x30003\n"
    "s_lshl_b32 s67, s67, 12\n"
    "s_lshl3_add_u32 s58, s67, 0\n"
    "s_add_u32 s67, s67, s56\n"
    "s_add_u32 m0, s67, 0\n"
    "s_nop 0\n"
    "buffer_load_dwordx4 v6, s[36:39], s58 offen lds\n"
    "s_bfe_u32 s67, s19, 0x30003\n"
    "s_lshl_b32 s67, s67, 12\n"
    "s_lshl4_add_u32 s58, s67, 0\n"
    "s_add_u32 s67, s67, s56\n"
    "s_add_u32 m0, s67, 32768\n"
    "s_nop 0\n"
    "buffer_load_dwordx4 v7, s[40:43], s58 offen lds\n"
    "s_bfe_u32 s67, s19, 0x30006\n"
    "s_lshl_b32 s67, s67, 12\n"
    "s_lshl4_add_u32 s58, s67, 0\n"
    "s_add_u32 s67, s67, s56\n"
    "s_add_u32 m0, s67, 32768\n"
    "s_nop 0\n"
    "buffer_load_dwordx4 v7, s[40:43], s58 offen lds\n"
    "s_bfe_u32 s67, s18, 0x30006\n"
    "s_lshl_b32 s67, s67, 12\n"
    "s_lshl3_add_u32 s58, s67, 0\n"
    "s_add_u32 s67, s67, s56\n"
    "s_add_u32 m0, s67, 0\n"
    "s_nop 0\n"
    "buffer_load_dwordx4 v6, s[36:39], s58 offen lds\n"
    "s_bfe_u32 s67, s19, 0x30009\n"
    "s_lshl_b32 s67, s67, 12\n"
    "s_lshl4_add_u32 s58, s67, 0\n"
    "s_add_u32 s67, s67, s56\n"
    "s_add_u32 m0, s67, 32768\n"
    "s_nop 0\n"
    "buffer_load_dwordx4 v7, s[40:43], s58 offen lds\n"
    "s_bfe_u32 s67, s18, 0x30009\n"
    "s_lshl_b32 s67, s67, 12\n"
    "s_lshl3_add_u32 s58, s67, 0\n"
    "s_add_u32 s67, s67, s56\n"
    "s_add_u32 m0, s67, 0\n"
    "s_nop 0\n"
    "buffer_load_dwordx4 v6, s[36:39], s58 offen lds\n"
    "s_bfe_u32 s67, s19, 0x3000c\n"
    "s_lshl_b32 s67, s67, 12\n"
    "s_lshl4_add_u32 s58, s67, 0\n"
    "s_add_u32 s67, s67, s56\n"
    "s_add_u32 m0, s67, 32768\n"
    "s_nop 0\n"
    "buffer_load_dwordx4 v7, s[40:43], s58 offen lds\n"
    "s_bfe_u32 s67, s18, 0x3000c\n"
    "s_lshl_b32 s67, s67, 12\n"
    "s_lshl3_add_u32 s58, s67, 0\n"
    "s_add_u32 s67, s67, s56\n"
    "s_add_u32 m0, s67, 0\n"
    "s_nop 0\n"
    "buffer_load_dwordx4 v6, s[36:39], s58 offen lds\n"
    "s_bfe_u32 s67, s19, 0x3000f\n"
    "s_lshl_b32 s67, s67, 12\n"
    "s_lshl4_add_u32 s58, s67, 0\n"
    "s_add_u32 s67, s67, s56\n"
    "s_add_u32 m0, s67, 32768\n"
    "s_nop 0\n"
    "buffer_load_dwordx4 v7, s[40:43], s58 offen lds\n"
    "s_bfe_u32 s67, s18, 0x3000f\n"
    "s_lshl_b32 s67, s67, 12\n"
    "s_lshl3_add_u32 s58, s67, 0\n"
    "s_add_u32 s67, s67, s56\n"
    "s_add_u32 m0, s67, 0\n"
    "s_nop 0\n"
    "buffer_load_dwordx4 v6, s[36:39], s58 offen lds\n"
    "s_bfe_u32 s67, s19, 0x30012\n"
    "s_lshl_b32 s67, s67, 12\n"
    "s_lshl4_add_u32 s58, s67, 0\n"
    "s_add_u32 s67, s67, s56\n"
    "s_add_u32 m0, s67, 32768\n"
    "s_nop 0\n"
    "buffer_load_dwordx4 v7, s[40:43], s58 offen lds\n"
    "s_bfe_u32 s67, s18, 0x30012\n"
    "s_lshl_b32 s67, s67, 12\n"
    "s_lshl3_add_u32 s58, s67, 0\n"
    "s_add_u32 s67, s67, s56\n"
    "s_add_u32 m0, s67, 0\n"
    "s_nop 0\n"
    "buffer_load_dwordx4 v6, s[36:39], s58 offen lds\n"
    "s_bfe_u32 s67, s19, 0x30015\n"
    "s_lshl_b32 s67, s67, 12\n"
    "s_lshl4_add_u32 s58, s67, 0\n"
    "s_add_u32 s67, s67, s56\n"
    "s_add_u32 m0, s67, 32768\n"
    "s_nop 0\n"
    "buffer_load_dwordx4 v7, s[40:43], s58 offen lds\n"
    "s_lshl1_add_u32 s34, s7, s62\n"
    "s_lshl_b32 s64, s6, 5\n"
    "s_add_u32 s64, s64, s34\n"
    "s_lshl_b32 s64, s64, 17\n"
    "s_add_u32 s44, s10, s64\n"
    "s_addc_u32 s45, s11, 0\n"
    "s_mov_b32 s46, 131072\n"
    "s_bfe_u32 s67, s18, 0x30000\n"
    "s_lshl_b32 s67, s67, 12\n"
    "s_lshl3_add_u32 s58, s67, 128\n"
    "s_add_u32 s67, s67, s56\n"
    "s_add_u32 m0, s67, 65536\n"
    "s_nop 0\n"
    "buffer_load_dwordx4 v6, s[36:39], s58 offen lds\n"
    "s_add_u32 s48, s12, s64\n"
    "s_addc_u32 s49, s13, 0\n"
    "s_mov_b32 s50, 131072\n"
    "s_bfe_u32 s64, %1, 0x10002\n"
    "s_lshl3_add_u32 s6, s64, s6\n"
    "s_lshl1_add_u32 s64, s6, s61\n"
    "s_mul_i32 s64, s64, 1053696\n"
    "s_bfe_u32 s67, s18, 0x30003\n"
    "s_lshl_b32 s67, s67, 12\n"
    "s_lshl3_add_u32 s58, s67, 128\n"
    "s_add_u32 s67, s67, s56\n"
    "s_add_u32 m0, s67, 65536\n"
    "s_nop 0\n"
    "buffer_load_dwordx4 v6, s[36:39], s58 offen lds\n"
    "s_mul_i32 s34, s34, 29696\n"
    "s_add_u32 s64, s64, s34\n"
    "s_add_u32 s52, s20, s64\n"
    "s_addc_u32 s53, s21, 0\n"
    "s_mov_b32 s54, 28672\n"
    "s_lshl_b32 s64, s6, 16\n"
    "s_add_u32 s64, s64, 37748736\n"
    "s_bfe_u32 s67, s19, 0x30000\n"
    "s_lshl_b32 s67, s67, 12\n"
    "s_lshl4_add_u32 s58, s67, 524288\n"
    "s_add_u32 s67, s67, s56\n"
    "s_add_u32 m0, s67, 98304\n"
    "s_nop 0\n"
    "buffer_load_dwordx4 v7, s[40:43], s58 offen lds\n"
    "s_add_u32 s68, s20, s64\n"
    "s_addc_u32 s69, s21, 0\n"
    "s_mov_b32 s70, 65536\n"
    "s_mov_b32 s47, 0x20000\n"
    "s_mov_b32 s51, 0x20000\n"
    "s_mov_b32 s55, 0x20000\n"
    "s_mov_b32 s71, 0x20000\n"
    "s_bfe_u32 s67, s19, 0x30000\n"
    "s_lshl_b32 s67, s67, 12\n"
    "s_lshl4_add_u32 s58, s67, 1048576\n"
    "s_add_u32 s67, s67, s56\n"
    "s_add_u32 m0, s67, 131072\n"
    "s_nop 0\n"
    "buffer_load_dwordx4 v7, s[40:43], s58 offen lds\n"
    "s_mov_b32 s75, 0x20000\n"
    "s_mov_b64 s[72:73], s[68:69]\n"
    "s_cmp_eq_u32 s60, 0\n"
    "s_cselect_b32 s74, 65536, 0\n"
    "s_lshr_b32 s66, %1, 3\n"
    "s_lshl1_add_u32 s66, s66, s62\n"
    "s_lshl_b32 s66, s66, 11\n"
    "s_bfe_u32 s67, s18, 0x30006\n"
    "s_lshl_b32 s67, s67, 12\n"
    "s_lshl3_add_u32 s58, s67, 128\n"
    "s_add_u32 s67, s67, s56\n"
    "s_add_u32 m0, s67, 65536\n"
    "s_nop 0\n"
    "buffer_load_dwordx4 v6, s[36:39], s58 offen lds\n"
    "s_lshl1_add_u32 s65, s7, s62\n"
    "s_mul_i32 s65, s65, 1\n"
    "s_mul_i32 s64, s6, 0\n"
    "s_add_u32 s65, s65, s64\n"
    "s_and_b32 s65, s65, 15\n"
    "s_lshl_b32 s65, s65, 13\n"
    "v_lshlrev_b32 v1, 7, v0\n"
    "s_bfe_u32 s67, s18, 0x30009\n"
    "s_lshl_b32 s67, s67, 12\n"
    "s_lshl3_add_u32 s58, s67, 128\n"
    "s_add_u32 s67, s67, s56\n"
    "s_add_u32 m0, s67, 65536\n"
    "s_nop 0\n"
    "buffer_load_dwordx4 v6, s[36:39], s58 offen lds\n"
    "v_lshlrev_b32 v41, 8, v38\n"
    "v_lshl_add_u32 v41, v39, 6, v41\n"
    "s_lshl_b32 s64, s61, 10\n"
    "v_add_u32 v41, s64, v41\n"
    "s_add_u32 s64, s61, 0\n"
    "v_mov_b32 v42, s64\n"
    "v_lshrrev_b32 v44, 1, v42\n"
    "s_bfe_u32 s67, s18, 0x3000c\n"
    "s_lshl_b32 s67, s67, 12\n"
    "s_lshl3_add_u32 s58, s67, 128\n"
    "s_add_u32 s67, s67, s56\n"
    "s_add_u32 m0, s67, 65536\n"
    "s_nop 0\n"
    "buffer_load_dwordx4 v6, s[36:39], s58 offen lds\n"
    "v_xor_b32 v43, v44, v42\n"
    "v_and_b32 v43, 1, v43\n"
    "v_lshl_add_u32 v43, v43, 1, v44\n"
    "v_xor_b32 v43, v43, v33\n"
    "v_lshl_add_u32 v8, v43, 4, v41\n"
    "s_add_u32 s64, s61, 2\n"
    "v_mov_b32 v42, s64\n"
    "s_bfe_u32 s67, s18, 0x3000f\n"
    "s_lshl_b32 s67, s67, 12\n"
    "s_lshl3_add_u32 s58, s67, 128\n"
    "s_add_u32 s67, s67, s56\n"
    "s_add_u32 m0, s67, 65536\n"
    "s_nop 0\n"
    "buffer_load_dwordx4 v6, s[36:39], s58 offen lds\n"
    "v_lshrrev_b32 v44, 1, v42\n"
    "v_xor_b32 v43, v44, v42\n"
    "v_and_b32 v43, 1, v43\n"
    "v_lshl_add_u32 v43, v43, 1, v44\n"
    "v_xor_b32 v43, v43, v33\n"
    "v_lshl_add_u32 v9, v43, 4, v41\n"
    "s_lshl_b32 s64, s61, 1\n"
    "s_bfe_u32 s67, s18, 0x30012\n"
    "s_lshl_b32 s67, s67, 12\n"
    "s_lshl3_add_u32 s58, s67, 128\n"
    "s_add_u32 s67, s67, s56\n"
    "s_add_u32 m0, s67, 65536\n"
    "s_nop 0\n"
    "buffer_load_dwordx4 v6, s[36:39], s58 offen lds\n"
    "v_xor_b32 v43, s64, v33\n"
    "v_lshl_add_u32 v10, v43, 4, v41\n"
    "v_and_b32 v32, 15, v0\n"
    "v_lshrrev_b32 v34, 2, v32\n"
    "v_lshrrev_b32 v36, 1, v34\n"
    "v_xor_b32 v35, v36, v34\n"
    "v_and_b32 v35, 1, v35\n"
    "s_bfe_u32 s67, s19, 0x30003\n"
    "s_lshl_b32 s67, s67, 12\n"
    "s_lshl4_add_u32 s58, s67, 524288\n"
    "s_add_u32 s67, s67, s56\n"
    "s_add_u32 m0, s67, 98304\n"
    "s_nop 0\n"
    "buffer_load_dwordx4 v7, s[40:43], s58 offen lds\n"
    "v_lshl_add_u32 v35, v35, 1, v36\n"
    "v_xor_b32 v35, v35, v39\n"
    "v_lshrrev_b32 v36, 3, v32\n"
    "v_and_b32 v37, 7, v32\n"
    "v_lshlrev_b32 v2, 10, v36\n"
    "v_lshl_add_u32 v2, v37, 6, v2\n"
    "v_lshl_add_u32 v2, v35, 4, v2\n"
    "s_bfe_u32 s67, s19, 0x30006\n"
    "s_lshl_b32 s67, s67, 12\n"
    "s_lshl4_add_u32 s58, s67, 524288\n"
    "s_add_u32 s67, s67, s56\n"
    "s_add_u32 m0, s67, 98304\n"
    "s_nop 0\n"
    "buffer_load_dwordx4 v7, s[40:43], s58 offen lds\n"
    "s_mul_i32 s64, s61, 14336\n"
    "v_add_u32 v2, s64, v2\n"
    "v_add_u32 v3, 65536, v2\n"
    "s_lshl_b32 s64, s62, 11\n"
    "v_lshlrev_b32 v4, 12, v39\n"
    "v_lshl_add_u32 v4, v32, 4, v4\n"
    "v_add_u32 v4, s64, v4\n"
    "s_bfe_u32 s67, s19, 0x30009\n"
    "s_lshl_b32 s67, s67, 12\n"
    "s_lshl4_add_u32 s58, s67, 524288\n"
    "s_add_u32 s67, s67, s56\n"
    "s_add_u32 m0, s67, 98304\n"
    "s_nop 0\n"
    "buffer_load_dwordx4 v7, s[40:43], s58 offen lds\n"
    "v_add_u32 v5, 98304, v4\n"
    "v_add_u32 v4, 32768, v4\n"
    "s_lshl_b32 s64, s62, 14\n"
    "v_lshlrev_b32 v11, 10, v34\n"
    "v_lshl_add_u32 v11, v33, 6, v11\n"
    "v_lshl_add_u32 v11, v35, 4, v11\n"
    "v_add_u32 v11, s64, v11\n"
    "s_bfe_u32 s67, s19, 0x3000c\n"
    "s_lshl_b32 s67, s67, 12\n"
    "s_lshl4_add_u32 s58, s67, 524288\n"
    "s_add_u32 s67, s67, s56\n"
    "s_add_u32 m0, s67, 98304\n"
    "s_nop 0\n"
    "buffer_load_dwordx4 v7, s[40:43], s58 offen lds\n"
    "v_add_u32 v12, 65536, v11\n"
    "v_add_u32 v13, 131072, v11\n"
    "v_lshlrev_b32 v40, 10, v39\n"
    "v_lshl_add_u32 v40, v38, 6, v40\n"
    "v_lshl_add_u32 v40, v33, 3, v40\n"
    "v_add_u32 v40, s64, v40\n"
    "v_add_u32 v40, 8192, v40\n"
    "s_bfe_u32 s67, s19, 0x3000f\n"
    "s_lshl_b32 s67, s67, 12\n"
    "s_lshl4_add_u32 s58, s67, 524288\n"
    "s_add_u32 s67, s67, s56\n"
    "s_add_u32 m0, s67, 98304\n"
    "s_nop 0\n"
    "buffer_load_dwordx4 v7, s[40:43], s58 offen lds\n"
    "v_and_b32 v42, 1, v39\n"
    "v_lshl_add_u32 v14, v42, 5, v40\n"
    "v_xor_b32 v17, 32, v14\n"
    "v_add_u32 v15, 65536, v14\n"
    "v_add_u32 v16, 131072, v14\n"
    "v_add_u32 v18, 65536, v17\n"
    "v_add_u32 v19, 131072, v17\n"
    "s_bfe_u32 s67, s19, 0x30012\n"
    "s_lshl_b32 s67, s67, 12\n"
    "s_lshl4_add_u32 s58, s67, 524288\n"
    "s_add_u32 s67, s67, s56\n"
    "s_add_u32 m0, s67, 98304\n"
    "s_nop 0\n"
    "buffer_load_dwordx4 v7, s[40:43], s58 offen lds\n"
    "v_lshlrev_b32 v27, 4, v39\n"
    "v_add_u32 v27, s64, v27\n"
    "v_add_u32 v27, 131072, v27\n"
    "s_mov_b32 s27, 0x20000\n"
    "s_add_u32 s26, s26, 1470\n"
    "s_mov_b32 s59, 0x3e0293ee\n"
    "s_mov_b32 s63, 0x57800000\n"
    "v_mov_b32 v20, 0\n"
    "v_mov_b32 v21, 0\n"
    "v_mov_b32 v22, 0\n"
    "v_mov_b32 v23, 0\n"
    "s_lshr_b32 s34, s7, 2\n"
    "s_lshl3_add_u32 s34, s34, s62\n"
    "s_lshl1_add_u32 s64, s6, s61\n"
    "s_mul_i32 s64, s64, 1053696\n"
    "s_mul_i32 s63, s34, 29696\n"
    "s_add_u32 s64, s64, s63\n"
    "s_add_u32 s10, s20, s64\n"
    "s_addc_u32 s11, s21, 0\n"
    "s_and_b32 s35, s7, 3\n"
    "s_xor_b32 s63, s7, s6\n"
    "s_and_b32 s63, s63, 3\n"
    "s_lshl_b32 s63, s63, 8\n"
    "s_lshl_b32 s64, s34, 17\n"
    "s_add_u32 s64, s64, s63\n"
    "s_add_u32 s12, s16, s64\n"
    "s_addc_u32 s13, s17, 0\n"
    "s_and_b32 s64, s6, 7\n"
    "s_lshl1_add_u32 s64, s64, s61\n"
    "s_mul_i32 s64, s64, 229376\n"
    "s_add_u32 s64, s64, s63\n"
    "s_lshr_b32 s63, s6, 3\n"
    "s_lshl_b32 s63, s63, 10\n"
    "s_add_u32 s64, s64, s63\n"
    "s_lshr_b32 s63, s7, 2\n"
    "s_lshl1_add_u32 s63, s63, s62\n"
    "s_lshl_b32 s63, s63, 5\n"
    "s_add_u32 s64, s64, s63\n"
    "s_add_u32 s22, s22, s64\n"
    "s_addc_u32 s23, s23, 0\n"
    "s_cmp_ge_u32 0, s35\n"
    "s_cselect_b32 s8, 1, 0\n"
    "s_cmp_ge_u32 1, s35\n"
    "s_cselect_b32 s9, 2, 1\n"
    "s_cmp_ge_u32 2, s35\n"
    "s_cselect_b32 s14, 3, 2\n"
    "s_lshl_b32 s35, s35, 18\n"
    "s_mov_b32 s63, 0x57800000\n"
    "v_bfe_u32 v30, v1, 7, 2\n"
    "v_bfe_u32 v29, v1, 9, 2\n"
    "v_lshlrev_b32 v29, 10, v29\n"
    "v_lshl_add_u32 v29, v30, 2, v29\n"
    "s_lshl_b32 s34, s7, 12\n"
    "v_add_u32 v29, s34, v29\n"
    "v_bfe_u32 v28, v1, 10, 1\n"
    "v_lshl_add_u32 v28, v28, 1, 12\n"
    "v_lshlrev_b32 v28, v28, v30\n"
    "v_lshrrev_b32 v30, 7, v1\n"
    "v_min_u32 v30, 16, v30\n"
    "s_mov_b32 s34, 0xfffff111\n"
    "v_lshrrev_b32 v30, v30, s34\n"
    "v_and_b32 v30, 1, v30\n"
    "v_lshl_add_u32 v29, v30, 22, v29\n"
    "v_xor_b32 v28, v28, v29\n"
    "s_waitcnt vmcnt(15)\n"
    "s_barrier\n"
    "ds_read_b128 a[0:3], v2 offset:0\n"
    "ds_read_b64_tr_b16 a[112:113], v4 offset:0\n"
    "ds_read_b64_tr_b16 a[114:115], v4 offset:1024\n"
    "ds_read_b128 a[4:7], v2 offset:512\n"
    "ds_read_b64_tr_b16 a[116:117], v4 offset:16384\n"
    "ds_read_b64_tr_b16 a[118:119], v4 offset:17408\n"
    "ds_read_b128 a[8:11], v2 offset:2048\n"
    "ds_read_b128 a[12:15], v2 offset:2560\n"
    "ds_read_b128 a[16:19], v2 offset:4096\n"
    "ds_read_b128 a[20:23], v2 offset:4608\n"
    "ds_read_b128 a[24:27], v2 offset:6144\n"
    "ds_read_b128 a[28:31], v2 offset:6656\n"
    "s_waitcnt lgkmcnt(9)\n"
    "v_mfma_f32_16x16x32_bf16 v[32:35], a[112:115], a[0:3], 0\n"
    "s_waitcnt lgkmcnt(6)\n"
    "ds_read_b128 a[32:35], v2 offset:8192\n"
    "s_bfe_u32 s67, s19, 0x30015\n"
    "ds_read_b128 a[36:39], v2 offset:8704\n"
    "v_mfma_f32_16x16x32_bf16 v[32:35], a[116:119], a[4:7], v[32:35]\n"
    "s_waitcnt lgkmcnt(7)\n"
    "ds_read_b128 a[40:43], v2 offset:10240\n"
    "s_lshl_b32 s67, s67, 12\n"
    "ds_read_b128 a[44:47], v2 offset:10752\n"
    "v_mfma_f32_16x16x32_bf16 v[64:67], a[112:115], a[8:11], 0\n"
    "s_waitcnt lgkmcnt(8)\n"
    "ds_read_b128 a[48:51], v2 offset:12288\n"
    "s_lshl4_add_u32 s58, s67, 524288\n"
    "ds_read_b128 a[52:55], v2 offset:12800\n"
    "v_mfma_f32_16x16x32_bf16 v[64:67], a[116:119], a[12:15], v[64:67]\n"
    "s_waitcnt lgkmcnt(9)\n"
    "ds_read_b64_tr_b16 a[120:121], v4 offset:8\n"
    "s_add_u32 s67, s67, s56\n"
    "ds_read_b64_tr_b16 a[122:123], v4 offset:1032\n"
    "v_mfma_f32_16x16x32_bf16 v[96:99], a[112:115], a[16:19], 0\n"
    "s_waitcnt lgkmcnt(10)\n"
    "ds_read_b64_tr_b16 a[124:125], v4 offset:16392\n"
    "s_add_u32 m0, s67, 98304\n"
    "ds_read_b64_tr_b16 a[126:127], v4 offset:17416\n"
    "v_mfma_f32_16x16x32_bf16 v[96:99], a[116:119], a[20:23], v[96:99]\n"
    "s_waitcnt lgkmcnt(11)\n"
    "ds_read_b64_tr_b16 a[128:129], v4 offset:256\n"
    "s_bfe_u32 s67, s19, 0x30003\n"
    "ds_read_b64_tr_b16 a[130:131], v4 offset:1280\n"
    "v_mfma_f32_16x16x32_bf16 v[128:131], a[112:115], a[24:27], 0\n"
    "s_waitcnt lgkmcnt(12)\n"
    "ds_read_b64_tr_b16 a[132:133], v4 offset:16640\n"
    "s_lshl_b32 s67, s67, 12\n"
    "ds_read_b64_tr_b16 a[134:135], v4 offset:17664\n"
    "v_mfma_f32_16x16x32_bf16 v[128:131], a[116:119], a[28:31], v[128:131]\n"
    "s_waitcnt lgkmcnt(13)\n"
    "ds_read_b64_tr_b16 a[136:137], v4 offset:264\n"
    "ds_read_b64_tr_b16 a[138:139], v4 offset:1288\n"
    "v_mfma_f32_16x16x32_bf16 v[160:163], a[112:115], a[32:35], 0\n"
    "s_waitcnt lgkmcnt(14)\n"
    "ds_read_b64_tr_b16 a[140:141], v4 offset:16648\n"
    "ds_read_b64_tr_b16 a[142:143], v4 offset:17672\n"
    "v_mfma_f32_16x16x32_bf16 v[160:163], a[116:119], a[36:39], v[160:163]\n"
    "s_waitcnt lgkmcnt(15)\n"
    "ds_read_b64_tr_b16 a[144:145], v4 offset:512\n"
    "ds_read_b64_tr_b16 a[146:147], v4 offset:1536\n"
    "v_mfma_f32_16x16x32_bf16 v[192:195], a[112:115], a[40:43], 0\n"
    "s_waitcnt lgkmcnt(15)\n"
    "ds_read_b64_tr_b16 a[148:149], v4 offset:16896\n"
    "ds_read_b64_tr_b16 a[150:151], v4 offset:17920\n"
    "v_mfma_f32_16x16x32_bf16 v[192:195], a[116:119], a[44:47], v[192:195]\n"
    "buffer_load_dwordx4 v7, s[40:43], s58 offen lds\n"
    "ds_read_b64_tr_b16 a[152:153], v4 offset:520\n"
    "v_mfma_f32_16x16x32_bf16 v[224:227], a[112:115], a[48:51], 0\n"
    "s_waitcnt lgkmcnt(15)\n"
    "ds_read_b64_tr_b16 a[154:155], v4 offset:1544\n"
    "s_lshl4_add_u32 s58, s67, 1048576\n"
    "ds_read_b64_tr_b16 a[156:157], v4 offset:16904\n"
    "v_mfma_f32_16x16x32_bf16 v[224:227], a[116:119], a[52:55], v[224:227]\n"
    "s_add_u32 s67, s67, s56\n"
    "ds_read_b64_tr_b16 a[158:159], v4 offset:17928\n"
    "s_add_u32 m0, s67, 131072\n"
    "ds_read_b64_tr_b16 a[160:161], v4 offset:768\n"
    "v_mfma_f32_16x16x32_bf16 v[36:39], a[120:123], a[0:3], 0\n"
    "s_waitcnt lgkmcnt(15)\n"
    "ds_read_b64_tr_b16 a[162:163], v4 offset:1792\n"
    "s_bfe_u32 s67, s19, 0x30006\n"
    "ds_read_b64_tr_b16 a[164:165], v4 offset:17152\n"
    "v_mfma_f32_16x16x32_bf16 v[36:39], a[124:127], a[4:7], v[36:39]\n"
    "s_lshl_b32 s67, s67, 12\n"
    "ds_read_b64_tr_b16 a[166:167], v4 offset:18176\n"
    "ds_read_b64_tr_b16 a[168:169], v4 offset:776\n"
    "v_mfma_f32_16x16x32_bf16 v[68:71], a[120:123], a[8:11], 0\n"
    "buffer_load_dwordx4 v7, s[40:43], s58 offen lds\n"
    "ds_read_b64_tr_b16 a[170:171], v4 offset:1800\n"
    "v_mfma_f32_16x16x32_bf16 v[68:71], a[124:127], a[12:15], v[68:71]\n"
    "s_lshl4_add_u32 s58, s67, 1048576\n"
    "ds_read_b64_tr_b16 a[172:173], v4 offset:17160\n"
    "s_add_u32 s67, s67, s56\n"
    "ds_read_b64_tr_b16 a[174:175], v4 offset:18184\n"
    "v_mfma_f32_16x16x32_bf16 v[100:103], a[120:123], a[16:19], 0\n"
    "s_add_u32 m0, s67, 131072\n"
    "s_bfe_u32 s67, s19, 0x30009\n"
    "s_lshl_b32 s67, s67, 12\n"
    "v_mfma_f32_16x16x32_bf16 v[100:103], a[124:127], a[20:23], v[100:103]\n"
    "v_mfma_f32_16x16x32_bf16 v[132:135], a[120:123], a[24:27], 0\n"
    "v_mfma_f32_16x16x32_bf16 v[132:135], a[124:127], a[28:31], v[132:135]\n"
    "buffer_load_dwordx4 v7, s[40:43], s58 offen lds\n"
    "v_mfma_f32_16x16x32_bf16 v[164:167], a[120:123], a[32:35], 0\n"
    "s_lshl4_add_u32 s58, s67, 1048576\n"
    "s_add_u32 s67, s67, s56\n"
    "s_add_u32 m0, s67, 131072\n"
    "v_mfma_f32_16x16x32_bf16 v[164:167], a[124:127], a[36:39], v[164:167]\n"
    "s_bfe_u32 s67, s19, 0x3000c\n"
    "s_lshl_b32 s67, s67, 12\n"
    "v_mfma_f32_16x16x32_bf16 v[196:199], a[120:123], a[40:43], 0\n"
    "s_waitcnt vmcnt(9) lgkmcnt(0)\n"
    "s_barrier\n"
    "ds_read_b128 a[56:59], v3 offset:0\n"
    "v_mfma_f32_16x16x32_bf16 v[196:199], a[124:127], a[44:47], v[196:199]\n"
    "ds_read_b128 a[60:63], v3 offset:512\n"
    "v_mfma_f32_16x16x32_bf16 v[228:231], a[120:123], a[48:51], 0\n"
    "buffer_load_dwordx4 v7, s[40:43], s58 offen lds\n"
    "v_mfma_f32_16x16x32_bf16 v[228:231], a[124:127], a[52:55], v[228:231]\n"
    "s_lshl4_add_u32 s58, s67, 1048576\n"
    "ds_read_b128 a[64:67], v3 offset:2048\n"
    "s_add_u32 s67, s67, s56\n"
    "v_mfma_f32_16x16x32_bf16 v[40:43], a[128:131], a[0:3], 0\n"
    "s_add_u32 m0, s67, 131072\n"
    "ds_read_b128 a[68:71], v3 offset:2560\n"
    "s_bfe_u32 s67, s18, 0x30000\n"
    "v_mfma_f32_16x16x32_bf16 v[40:43], a[132:135], a[4:7], v[40:43]\n"
    "s_lshl_b32 s67, s67, 12\n"
    "ds_read_b128 a[72:75], v3 offset:4096\n"
    "v_mfma_f32_16x16x32_bf16 v[72:75], a[128:131], a[8:11], 0\n"
    "ds_read_b128 a[76:79], v3 offset:4608\n"
    "v_mfma_f32_16x16x32_bf16 v[72:75], a[132:135], a[12:15], v[72:75]\n"
    "buffer_load_dwordx4 v7, s[40:43], s58 offen lds\n"
    "ds_read_b128 a[80:83], v3 offset:6144\n"
    "v_mfma_f32_16x16x32_bf16 v[104:107], a[128:131], a[16:19], 0\n"
    "s_lshl3_add_u32 s58, s67, 256\n"
    "ds_read_b128 a[84:87], v3 offset:6656\n"
    "s_add_u32 s67, s67, s56\n"
    "v_mfma_f32_16x16x32_bf16 v[104:107], a[132:135], a[20:23], v[104:107]\n"
    "s_add_u32 m0, s67, 0\n"
    "ds_read_b128 a[88:91], v3 offset:8192\n"
    "s_bfe_u32 s67, s18, 0x30003\n"
    "v_mfma_f32_16x16x32_bf16 v[136:139], a[128:131], a[24:27], 0\n"
    "s_lshl_b32 s67, s67, 12\n"
    "ds_read_b128 a[92:95], v3 offset:8704\n"
    "v_mfma_f32_16x16x32_bf16 v[136:139], a[132:135], a[28:31], v[136:139]\n"
    "ds_read_b128 a[96:99], v3 offset:10240\n"
    "v_mfma_f32_16x16x32_bf16 v[168:171], a[128:131], a[32:35], 0\n"
    "buffer_load_dwordx4 v6, s[36:39], s58 offen lds\n"
    "ds_read_b128 a[100:103], v3 offset:10752\n"
    "v_mfma_f32_16x16x32_bf16 v[168:171], a[132:135], a[36:39], v[168:171]\n"
    "s_lshl3_add_u32 s58, s67, 256\n"
    "ds_read_b128 a[104:107], v3 offset:12288\n"
    "s_add_u32 s67, s67, s56\n"
    "v_mfma_f32_16x16x32_bf16 v[200:203], a[128:131], a[40:43], 0\n"
    "s_add_u32 m0, s67, 0\n"
    "ds_read_b128 a[108:111], v3 offset:12800\n"
    "s_bfe_u32 s67, s19, 0x30000\n"
    "v_mfma_f32_16x16x32_bf16 v[200:203], a[132:135], a[44:47], v[200:203]\n"
    "s_lshl_b32 s67, s67, 12\n"
    "v_mfma_f32_16x16x32_bf16 v[232:235], a[128:131], a[48:51], 0\n"
    "v_mfma_f32_16x16x32_bf16 v[232:235], a[132:135], a[52:55], v[232:235]\n"
    "buffer_load_dwordx4 v6, s[36:39], s58 offen lds\n"
    "v_mfma_f32_16x16x32_bf16 v[44:47], a[136:139], a[0:3], 0\n"
    "s_lshl4_add_u32 s58, s67, 1572864\n"
    "s_add_u32 s67, s67, s56\n"
    "s_add_u32 m0, s67, 32768\n"
    "v_mfma_f32_16x16x32_bf16 v[44:47], a[140:143], a[4:7], v[44:47]\n"
    "s_bfe_u32 s67, s18, 0x30006\n"
    "s_lshl_b32 s67, s67, 12\n"
    "v_mfma_f32_16x16x32_bf16 v[76:79], a[136:139], a[8:11], 0\n"
    "v_mfma_f32_16x16x32_bf16 v[76:79], a[140:143], a[12:15], v[76:79]\n"
    "s_waitcnt vmcnt(6) lgkmcnt(0)\n"
    "s_barrier\n"
    "ds_read_b64_tr_b16 a[176:177], v5 offset:0\n"
    "v_mfma_f32_16x16x32_bf16 v[108:111], a[136:139], a[16:19], 0\n"
    "buffer_load_dwordx4 v7, s[40:43], s58 offen lds\n"
    "v_mfma_f32_16x16x32_bf16 v[108:111], a[140:143], a[20:23], v[108:111]\n"
    "s_lshl3_add_u32 s58, s67, 256\n"
    "ds_read_b64_tr_b16 a[178:179], v5 offset:1024\n"
    "s_add_u32 s67, s67, s56\n"
    "v_mfma_f32_16x16x32_bf16 v[140:143], a[136:139], a[24:27], 0\n"
    "s_add_u32 m0, s67, 0\n"
    "ds_read_b64_tr_b16 a[180:181], v5 offset:16384\n"
    "s_bfe_u32 s67, s18, 0x30000\n"
    "v_mfma_f32_16x16x32_bf16 v[140:143], a[140:143], a[28:31], v[140:143]\n"
    "s_lshl_b32 s67, s67, 12\n"
    "ds_read_b64_tr_b16 a[182:183], v5 offset:17408\n"
    "v_mfma_f32_16x16x32_bf16 v[172:175], a[136:139], a[32:35], 0\n"
    "ds_read_b64_tr_b16 a[184:185], v5 offset:8\n"
    "v_mfma_f32_16x16x32_bf16 v[172:175], a[140:143], a[36:39], v[172:175]\n"
    "buffer_load_dwordx4 v6, s[36:39], s58 offen lds\n"
    "v_mfma_f32_16x16x32_bf16 v[204:207], a[136:139], a[40:43], 0\n"
    "s_lshl3_add_u32 s58, s67, 384\n"
    "ds_read_b64_tr_b16 a[186:187], v5 offset:1032\n"
    "s_add_u32 s67, s67, s56\n"
    "v_mfma_f32_16x16x32_bf16 v[204:207], a[140:143], a[44:47], v[204:207]\n"
    "s_add_u32 m0, s67, 65536\n"
    "ds_read_b64_tr_b16 a[188:189], v5 offset:16392\n"
    "s_bfe_u32 s67, s18, 0x30003\n"
    "v_mfma_f32_16x16x32_bf16 v[236:239], a[136:139], a[48:51], 0\n"
    "s_lshl_b32 s67, s67, 12\n"
    "ds_read_b64_tr_b16 a[190:191], v5 offset:17416\n"
    "v_mfma_f32_16x16x32_bf16 v[236:239], a[140:143], a[52:55], v[236:239]\n"
    "ds_read_b64_tr_b16 a[192:193], v5 offset:256\n"
    "v_mfma_f32_16x16x32_bf16 v[48:51], a[144:147], a[0:3], 0\n"
    "buffer_load_dwordx4 v6, s[36:39], s58 offen lds\n"
    "v_mfma_f32_16x16x32_bf16 v[48:51], a[148:151], a[4:7], v[48:51]\n"
    "s_lshl3_add_u32 s58, s67, 384\n"
    "ds_read_b64_tr_b16 a[194:195], v5 offset:1280\n"
    "s_add_u32 s67, s67, s56\n"
    "v_mfma_f32_16x16x32_bf16 v[80:83], a[144:147], a[8:11], 0\n"
    "s_add_u32 m0, s67, 65536\n"
    "ds_read_b64_tr_b16 a[196:197], v5 offset:16640\n"
    "s_bfe_u32 s67, s18, 0x30009\n"
    "v_mfma_f32_16x16x32_bf16 v[80:83], a[148:151], a[12:15], v[80:83]\n"
    "s_lshl_b32 s67, s67, 12\n"
    "ds_read_b64_tr_b16 a[198:199], v5 offset:17664\n"
    "v_mfma_f32_16x16x32_bf16 v[112:115], a[144:147], a[16:19], 0\n"
    "ds_read_b64_tr_b16 a[200:201], v5 offset:264\n"
    "v_mfma_f32_16x16x32_bf16 v[112:115], a[148:151], a[20:23], v[112:115]\n"
    "buffer_load_dwordx4 v6, s[36:39], s58 offen lds\n"
    "v_mfma_f32_16x16x32_bf16 v[144:147], a[144:147], a[24:27], 0\n"
    "s_lshl3_add_u32 s58, s67, 256\n"
    "ds_read_b64_tr_b16 a[202:203], v5 offset:1288\n"
    "s_add_u32 s67, s67, s56\n"
    "v_mfma_f32_16x16x32_bf16 v[144:147], a[148:151], a[28:31], v[144:147]\n"
    "s_add_u32 m0, s67, 0\n"
    "ds_read_b64_tr_b16 a[204:205], v5 offset:16648\n"
    "s_bfe_u32 s67, s18, 0x3000c\n"
    "v_mfma_f32_16x16x32_bf16 v[176:179], a[144:147], a[32:35], 0\n"
    "s_lshl_b32 s67, s67, 12\n"
    "ds_read_b64_tr_b16 a[206:207], v5 offset:17672\n"
    "v_mfma_f32_16x16x32_bf16 v[176:179], a[148:151], a[36:39], v[176:179]\n"
    "ds_read_b64_tr_b16 a[208:209], v5 offset:512\n"
    "v_mfma_f32_16x16x32_bf16 v[208:211], a[144:147], a[40:43], 0\n"
    "buffer_load_dwordx4 v6, s[36:39], s58 offen lds\n"
    "v_mfma_f32_16x16x32_bf16 v[208:211], a[148:151], a[44:47], v[208:211]\n"
    "s_lshl3_add_u32 s58, s67, 256\n"
    "ds_read_b64_tr_b16 a[210:211], v5 offset:1536\n"
    "s_add_u32 s67, s67, s56\n"
    "v_mfma_f32_16x16x32_bf16 v[240:243], a[144:147], a[48:51], 0\n"
    "s_add_u32 m0, s67, 0\n"
    "ds_read_b64_tr_b16 a[212:213], v5 offset:16896\n"
    "s_bfe_u32 s67, s18, 0x3000f\n"
    "v_mfma_f32_16x16x32_bf16 v[240:243], a[148:151], a[52:55], v[240:243]\n"
    "s_lshl_b32 s67, s67, 12\n"
    "ds_read_b64_tr_b16 a[214:215], v5 offset:17920\n"
    "v_mfma_f32_16x16x32_bf16 v[52:55], a[152:155], a[0:3], 0\n"
    "ds_read_b64_tr_b16 a[216:217], v5 offset:520\n"
    "v_mfma_f32_16x16x32_bf16 v[52:55], a[156:159], a[4:7], v[52:55]\n"
    "buffer_load_dwordx4 v6, s[36:39], s58 offen lds\n"
    "v_mfma_f32_16x16x32_bf16 v[84:87], a[152:155], a[8:11], 0\n"
    "s_lshl3_add_u32 s58, s67, 256\n"
    "ds_read_b64_tr_b16 a[218:219], v5 offset:1544\n"
    "s_add_u32 s67, s67, s56\n"
    "v_mfma_f32_16x16x32_bf16 v[84:87], a[156:159], a[12:15], v[84:87]\n"
    "s_add_u32 m0, s67, 0\n"
    "ds_read_b64_tr_b16 a[220:221], v5 offset:16904\n"
    "s_bfe_u32 s67, s18, 0x30012\n"
    "v_mfma_f32_16x16x32_bf16 v[116:119], a[152:155], a[16:19], 0\n"
    "s_lshl_b32 s67, s67, 12\n"
    "ds_read_b64_tr_b16 a[222:223], v5 offset:17928\n"
    "v_mfma_f32_16x16x32_bf16 v[116:119], a[156:159], a[20:23], v[116:119]\n"
    "ds_read_b64_tr_b16 a[224:225], v5 offset:768\n"
    "v_mfma_f32_16x16x32_bf16 v[148:151], a[152:155], a[24:27], 0\n"
    "buffer_load_dwordx4 v6, s[36:39], s58 offen lds\n"
    "v_mfma_f32_16x16x32_bf16 v[148:151], a[156:159], a[28:31], v[148:151]\n"
    "s_lshl3_add_u32 s58, s67, 256\n"
    "ds_read_b64_tr_b16 a[226:227], v5 offset:1792\n"
    "s_add_u32 s67, s67, s56\n"
    "v_mfma_f32_16x16x32_bf16 v[180:183], a[152:155], a[32:35], 0\n"
    "s_add_u32 m0, s67, 0\n"
    "ds_read_b64_tr_b16 a[228:229], v5 offset:17152\n"
    "s_bfe_u32 s67, s19, 0x3000f\n"
    "v_mfma_f32_16x16x32_bf16 v[180:183], a[156:159], a[36:39], v[180:183]\n"
    "s_lshl_b32 s67, s67, 12\n"
    "ds_read_b64_tr_b16 a[230:231], v5 offset:18176\n"
    "v_mfma_f32_16x16x32_bf16 v[212:215], a[152:155], a[40:43], 0\n"
    "ds_read_b64_tr_b16 a[232:233], v5 offset:776\n"
    "v_mfma_f32_16x16x32_bf16 v[212:215], a[156:159], a[44:47], v[212:215]\n"
    "buffer_load_dwordx4 v6, s[36:39], s58 offen lds\n"
    "v_mfma_f32_16x16x32_bf16 v[244:247], a[152:155], a[48:51], 0\n"
    "s_lshl4_add_u32 s58, s67, 1048576\n"
    "ds_read_b64_tr_b16 a[234:235], v5 offset:1800\n"
    "s_add_u32 s67, s67, s56\n"
    "v_mfma_f32_16x16x32_bf16 v[244:247], a[156:159], a[52:55], v[244:247]\n"
    "s_add_u32 m0, s67, 131072\n"
    "ds_read_b64_tr_b16 a[236:237], v5 offset:17160\n"
    "s_bfe_u32 s67, s19, 0x30012\n"
    "v_mfma_f32_16x16x32_bf16 v[56:59], a[160:163], a[0:3], 0\n"
    "s_lshl_b32 s67, s67, 12\n"
    "ds_read_b64_tr_b16 a[238:239], v5 offset:18184\n"
    "v_mfma_f32_16x16x32_bf16 v[56:59], a[164:167], a[4:7], v[56:59]\n"
    "v_mfma_f32_16x16x32_bf16 v[88:91], a[160:163], a[8:11], 0\n"
    "buffer_load_dwordx4 v7, s[40:43], s58 offen lds\n"
    "v_mfma_f32_16x16x32_bf16 v[88:91], a[164:167], a[12:15], v[88:91]\n"
    "s_lshl4_add_u32 s58, s67, 1048576\n"
    "s_add_u32 s67, s67, s56\n"
    "s_add_u32 m0, s67, 131072\n"
    "v_mfma_f32_16x16x32_bf16 v[120:123], a[160:163], a[16:19], 0\n"
    "s_bfe_u32 s67, s19, 0x30015\n"
    "s_lshl_b32 s67, s67, 12\n"
    "v_mfma_f32_16x16x32_bf16 v[120:123], a[164:167], a[20:23], v[120:123]\n"
    "v_mfma_f32_16x16x32_bf16 v[152:155], a[160:163], a[24:27], 0\n"
    "v_mfma_f32_16x16x32_bf16 v[152:155], a[164:167], a[28:31], v[152:155]\n"
    "buffer_load_dwordx4 v7, s[40:43], s58 offen lds\n"
    "v_mfma_f32_16x16x32_bf16 v[184:187], a[160:163], a[32:35], 0\n"
    "s_lshl4_add_u32 s58, s67, 1048576\n"
    "s_add_u32 s67, s67, s56\n"
    "s_add_u32 m0, s67, 131072\n"
    "v_mfma_f32_16x16x32_bf16 v[184:187], a[164:167], a[36:39], v[184:187]\n"
    "s_bfe_u32 s67, s18, 0x30006\n"
    "s_lshl_b32 s67, s67, 12\n"
    "v_mfma_f32_16x16x32_bf16 v[216:219], a[160:163], a[40:43], 0\n"
    "v_mfma_f32_16x16x32_bf16 v[216:219], a[164:167], a[44:47], v[216:219]\n"
    "v_mfma_f32_16x16x32_bf16 v[248:251], a[160:163], a[48:51], 0\n"
    "buffer_load_dwordx4 v7, s[40:43], s58 offen lds\n"
    "v_mfma_f32_16x16x32_bf16 v[248:251], a[164:167], a[52:55], v[248:251]\n"
    "s_lshl3_add_u32 s58, s67, 384\n"
    "s_add_u32 s67, s67, s56\n"
    "s_add_u32 m0, s67, 65536\n"
    "v_mfma_f32_16x16x32_bf16 v[60:63], a[168:171], a[0:3], 0\n"
    "s_bfe_u32 s67, s18, 0x30009\n"
    "s_lshl_b32 s67, s67, 12\n"
    "v_mfma_f32_16x16x32_bf16 v[60:63], a[172:175], a[4:7], v[60:63]\n"
    "v_mfma_f32_16x16x32_bf16 v[92:95], a[168:171], a[8:11], 0\n"
    "v_mfma_f32_16x16x32_bf16 v[92:95], a[172:175], a[12:15], v[92:95]\n"
    "buffer_load_dwordx4 v6, s[36:39], s58 offen lds\n"
    "v_mfma_f32_16x16x32_bf16 v[124:127], a[168:171], a[16:19], 0\n"
    "s_lshl3_add_u32 s58, s67, 384\n"
    "s_add_u32 s67, s67, s56\n"
    "s_add_u32 m0, s67, 65536\n"
    "v_mfma_f32_16x16x32_bf16 v[124:127], a[172:175], a[20:23], v[124:127]\n"
    "s_bfe_u32 s67, s18, 0x3000c\n"
    "s_lshl_b32 s67, s67, 12\n"
    "v_mfma_f32_16x16x32_bf16 v[156:159], a[168:171], a[24:27], 0\n"
    "v_mfma_f32_16x16x32_bf16 v[156:159], a[172:175], a[28:31], v[156:159]\n"
    "v_mfma_f32_16x16x32_bf16 v[188:191], a[168:171], a[32:35], 0\n"
    "buffer_load_dwordx4 v6, s[36:39], s58 offen lds\n"
    "v_mfma_f32_16x16x32_bf16 v[188:191], a[172:175], a[36:39], v[188:191]\n"
    "s_lshl3_add_u32 s58, s67, 384\n"
    "s_add_u32 s67, s67, s56\n"
    "s_add_u32 m0, s67, 65536\n"
    "v_mfma_f32_16x16x32_bf16 v[220:223], a[168:171], a[40:43], 0\n"
    "s_bfe_u32 s67, s18, 0x3000f\n"
    "s_lshl_b32 s67, s67, 12\n"
    "v_mfma_f32_16x16x32_bf16 v[220:223], a[172:175], a[44:47], v[220:223]\n"
    "v_mfma_f32_16x16x32_bf16 v[252:255], a[168:171], a[48:51], 0\n"
    "v_mfma_f32_16x16x32_bf16 v[252:255], a[172:175], a[52:55], v[252:255]\n"
    "s_waitcnt lgkmcnt(0)\n"
    "v_mfma_f32_16x16x32_bf16 v[32:35], a[180:183], a[60:63], v[32:35]\n"
    "buffer_load_dwordx4 v6, s[36:39], s58 offen lds\n"
    "v_mfma_f32_16x16x32_bf16 v[32:35], a[176:179], a[56:59], v[32:35]\n"
    "s_lshl3_add_u32 s58, s67, 384\n"
    "s_add_u32 s67, s67, s56\n"
    "s_add_u32 m0, s67, 65536\n"
    "v_mfma_f32_16x16x32_bf16 v[36:39], a[184:187], a[56:59], v[36:39]\n"
    "s_bfe_u32 s67, s18, 0x30012\n"
    "s_lshl_b32 s67, s67, 12\n"
    "v_mfma_f32_16x16x32_bf16 v[36:39], a[188:191], a[60:63], v[36:39]\n"
    "v_mfma_f32_16x16x32_bf16 v[40:43], a[196:199], a[60:63], v[40:43]\n"
    "v_mfma_f32_16x16x32_bf16 v[40:43], a[192:195], a[56:59], v[40:43]\n"
    "buffer_load_dwordx4 v6, s[36:39], s58 offen lds\n"
    "v_mfma_f32_16x16x32_bf16 v[44:47], a[200:203], a[56:59], v[44:47]\n"
    "s_lshl3_add_u32 s58, s67, 384\n"
    "s_add_u32 s67, s67, s56\n"
    "s_add_u32 m0, s67, 65536\n"
    "v_mfma_f32_16x16x32_bf16 v[44:47], a[204:207], a[60:63], v[44:47]\n"
    "s_bfe_u32 s67, s19, 0x30003\n"
    "s_lshl_b32 s67, s67, 12\n"
    "v_mfma_f32_16x16x32_bf16 v[48:51], a[212:215], a[60:63], v[48:51]\n"
    "v_mfma_f32_16x16x32_bf16 v[48:51], a[208:211], a[56:59], v[48:51]\n"
    "v_mfma_f32_16x16x32_bf16 v[52:55], a[216:219], a[56:59], v[52:55]\n"
    "buffer_load_dwordx4 v6, s[36:39], s58 offen lds\n"
    "v_mfma_f32_16x16x32_bf16 v[52:55], a[220:223], a[60:63], v[52:55]\n"
    "s_lshl4_add_u32 s58, s67, 1572864\n"
    "s_add_u32 s67, s67, s56\n"
    "s_add_u32 m0, s67, 32768\n"
    "v_mfma_f32_16x16x32_bf16 v[56:59], a[228:231], a[60:63], v[56:59]\n"
    "s_bfe_u32 s67, s19, 0x30006\n"
    "s_lshl_b32 s67, s67, 12\n"
    "v_mfma_f32_16x16x32_bf16 v[56:59], a[224:227], a[56:59], v[56:59]\n"
    "v_mfma_f32_16x16x32_bf16 v[60:63], a[232:235], a[56:59], v[60:63]\n"
    "v_mfma_f32_16x16x32_bf16 v[60:63], a[236:239], a[60:63], v[60:63]\n"
    "buffer_load_dwordx4 v7, s[40:43], s58 offen lds\n"
    "v_mfma_f32_16x16x32_bf16 v[64:67], a[180:183], a[68:71], v[64:67]\n"
    "s_lshl4_add_u32 s58, s67, 1572864\n"
    "s_add_u32 s67, s67, s56\n"
    "s_add_u32 m0, s67, 32768\n"
    "v_mfma_f32_16x16x32_bf16 v[64:67], a[176:179], a[64:67], v[64:67]\n"
    "s_bfe_u32 s67, s19, 0x30009\n"
    "s_lshl_b32 s67, s67, 12\n"
    "v_mfma_f32_16x16x32_bf16 v[68:71], a[184:187], a[64:67], v[68:71]\n"
    "v_mfma_f32_16x16x32_bf16 v[68:71], a[188:191], a[68:71], v[68:71]\n"
    "v_mfma_f32_16x16x32_bf16 v[72:75], a[196:199], a[68:71], v[72:75]\n"
    "buffer_load_dwordx4 v7, s[40:43], s58 offen lds\n"
    "v_mfma_f32_16x16x32_bf16 v[72:75], a[192:195], a[64:67], v[72:75]\n"
    "s_lshl4_add_u32 s58, s67, 1572864\n"
    "s_add_u32 s67, s67, s56\n"
    "s_add_u32 m0, s67, 32768\n"
    "v_mfma_f32_16x16x32_bf16 v[76:79], a[200:203], a[64:67], v[76:79]\n"
    "s_bfe_u32 s67, s19, 0x3000c\n"
    "s_lshl_b32 s67, s67, 12\n"
    "v_mfma_f32_16x16x32_bf16 v[76:79], a[204:207], a[68:71], v[76:79]\n"
    "v_mfma_f32_16x16x32_bf16 v[80:83], a[212:215], a[68:71], v[80:83]\n"
    "v_mfma_f32_16x16x32_bf16 v[80:83], a[208:211], a[64:67], v[80:83]\n"
    "buffer_load_dwordx4 v7, s[40:43], s58 offen lds\n"
    "v_mfma_f32_16x16x32_bf16 v[84:87], a[216:219], a[64:67], v[84:87]\n"
    "s_lshl4_add_u32 s58, s67, 1572864\n"
    "s_add_u32 s67, s67, s56\n"
    "s_add_u32 m0, s67, 32768\n"
    "v_mfma_f32_16x16x32_bf16 v[84:87], a[220:223], a[68:71], v[84:87]\n"
    "s_bfe_u32 s67, s19, 0x3000f\n"
    "s_lshl_b32 s67, s67, 12\n"
    "v_mfma_f32_16x16x32_bf16 v[88:91], a[228:231], a[68:71], v[88:91]\n"
    "v_mfma_f32_16x16x32_bf16 v[88:91], a[224:227], a[64:67], v[88:91]\n"
    "v_mfma_f32_16x16x32_bf16 v[92:95], a[232:235], a[64:67], v[92:95]\n"
    "buffer_load_dwordx4 v7, s[40:43], s58 offen lds\n"
    "v_mfma_f32_16x16x32_bf16 v[92:95], a[236:239], a[68:71], v[92:95]\n"
    "s_lshl4_add_u32 s58, s67, 1572864\n"
    "s_add_u32 s67, s67, s56\n"
    "s_add_u32 m0, s67, 32768\n"
    "v_mfma_f32_16x16x32_bf16 v[96:99], a[180:183], a[76:79], v[96:99]\n"
    "s_bfe_u32 s67, s19, 0x30012\n"
    "s_lshl_b32 s67, s67, 12\n"
    "v_mfma_f32_16x16x32_bf16 v[96:99], a[176:179], a[72:75], v[96:99]\n"
    "v_mfma_f32_16x16x32_bf16 v[100:103], a[184:187], a[72:75], v[100:103]\n"
    "v_mfma_f32_16x16x32_bf16 v[100:103], a[188:191], a[76:79], v[100:103]\n"
    "buffer_load_dwordx4 v7, s[40:43], s58 offen lds\n"
    "v_mfma_f32_16x16x32_bf16 v[104:107], a[196:199], a[76:79], v[104:107]\n"
    "s_lshl4_add_u32 s58, s67, 1572864\n"
    "s_add_u32 s67, s67, s56\n"
    "s_add_u32 m0, s67, 32768\n"
    "v_mfma_f32_16x16x32_bf16 v[104:107], a[192:195], a[72:75], v[104:107]\n"
    "s_bfe_u32 s67, s19, 0x30015\n"
    "s_lshl_b32 s67, s67, 12\n"
    "v_mfma_f32_16x16x32_bf16 v[108:111], a[200:203], a[72:75], v[108:111]\n"
    "v_mfma_f32_16x16x32_bf16 v[108:111], a[204:207], a[76:79], v[108:111]\n"
    "v_mfma_f32_16x16x32_bf16 v[112:115], a[212:215], a[76:79], v[112:115]\n"
    "buffer_load_dwordx4 v7, s[40:43], s58 offen lds\n"
    "v_mfma_f32_16x16x32_bf16 v[112:115], a[208:211], a[72:75], v[112:115]\n"
    "s_lshl4_add_u32 s58, s67, 1572864\n"
    "s_add_u32 s67, s67, s56\n"
    "s_add_u32 m0, s67, 32768\n"
    "v_mfma_f32_16x16x32_bf16 v[116:119], a[216:219], a[72:75], v[116:119]\n"
    "s_bfe_u32 s67, s19, 0x30000\n"
    "s_lshl_b32 s67, s67, 12\n"
    "v_mfma_f32_16x16x32_bf16 v[116:119], a[220:223], a[76:79], v[116:119]\n"
    "v_mfma_f32_16x16x32_bf16 v[120:123], a[228:231], a[76:79], v[120:123]\n"
    "v_mfma_f32_16x16x32_bf16 v[120:123], a[224:227], a[72:75], v[120:123]\n"
    "buffer_load_dwordx4 v7, s[40:43], s58 offen lds\n"
    "v_mfma_f32_16x16x32_bf16 v[124:127], a[232:235], a[72:75], v[124:127]\n"
    "s_lshl4_add_u32 s58, s67, 2097152\n"
    "s_add_u32 s67, s67, s56\n"
    "s_add_u32 m0, s67, 98304\n"
    "v_mfma_f32_16x16x32_bf16 v[124:127], a[236:239], a[76:79], v[124:127]\n"
    "s_bfe_u32 s67, s19, 0x30003\n"
    "s_lshl_b32 s67, s67, 12\n"
    "v_mfma_f32_16x16x32_bf16 v[128:131], a[180:183], a[84:87], v[128:131]\n"
    "s_waitcnt vmcnt(15)\n"
    "s_barrier\n"
    "ds_read_b128 a[0:3], v2 offset:0\n"
    "v_mfma_f32_16x16x32_bf16 v[128:131], a[176:179], a[80:83], v[128:131]\n"
    "ds_read_b128 a[4:7], v2 offset:512\n"
    "v_mfma_f32_16x16x32_bf16 v[132:135], a[184:187], a[80:83], v[132:135]\n"
    "buffer_load_dwordx4 v7, s[40:43], s58 offen lds\n"
    "v_mfma_f32_16x16x32_bf16 v[132:135], a[188:191], a[84:87], v[132:135]\n"
    "s_lshl4_add_u32 s58, s67, 2097152\n"
    "ds_read_b128 a[8:11], v2 offset:2048\n"
    "s_add_u32 s67, s67, s56\n"
    "v_mfma_f32_16x16x32_bf16 v[136:139], a[196:199], a[84:87], v[136:139]\n"
    "s_add_u32 m0, s67, 98304\n"
    "ds_read_b128 a[12:15], v2 offset:2560\n"
    "s_bfe_u32 s67, s19, 0x30006\n"
    "v_mfma_f32_16x16x32_bf16 v[136:139], a[192:195], a[80:83], v[136:139]\n"
    "s_lshl_b32 s67, s67, 12\n"
    "ds_read_b128 a[16:19], v2 offset:4096\n"
    "v_mfma_f32_16x16x32_bf16 v[140:143], a[200:203], a[80:83], v[140:143]\n"
    "ds_read_b128 a[20:23], v2 offset:4608\n"
    "v_mfma_f32_16x16x32_bf16 v[140:143], a[204:207], a[84:87], v[140:143]\n"
    "buffer_load_dwordx4 v7, s[40:43], s58 offen lds\n"
    "ds_read_b128 a[24:27], v2 offset:6144\n"
    "v_mfma_f32_16x16x32_bf16 v[144:147], a[212:215], a[84:87], v[144:147]\n"
    "s_lshl4_add_u32 s58, s67, 2097152\n"
    "ds_read_b128 a[28:31], v2 offset:6656\n"
    "s_add_u32 s67, s67, s56\n"
    "v_mfma_f32_16x16x32_bf16 v[144:147], a[208:211], a[80:83], v[144:147]\n"
    "s_add_u32 m0, s67, 98304\n"
    "ds_read_b128 a[32:35], v2 offset:8192\n"
    "s_bfe_u32 s67, s19, 0x30009\n"
    "v_mfma_f32_16x16x32_bf16 v[148:151], a[216:219], a[80:83], v[148:151]\n"
    "s_lshl_b32 s67, s67, 12\n"
    "ds_read_b128 a[36:39], v2 offset:8704\n"
    "v_mfma_f32_16x16x32_bf16 v[148:151], a[220:223], a[84:87], v[148:151]\n"
    "ds_read_b128 a[40:43], v2 offset:10240\n"
    "v_mfma_f32_16x16x32_bf16 v[152:155], a[228:231], a[84:87], v[152:155]\n"
    "buffer_load_dwordx4 v7, s[40:43], s58 offen lds\n"
    "ds_read_b128 a[44:47], v2 offset:10752\n"
    "v_mfma_f32_16x16x32_bf16 v[152:155], a[224:227], a[80:83], v[152:155]\n"
    "s_lshl4_add_u32 s58, s67, 2097152\n"
    "ds_read_b128 a[48:51], v2 offset:12288\n"
    "s_add_u32 s67, s67, s56\n"
    "v_mfma_f32_16x16x32_bf16 v[156:159], a[232:235], a[80:83], v[156:159]\n"
    "s_add_u32 m0, s67, 98304\n"
    "ds_read_b128 a[52:55], v2 offset:12800\n"
    "s_bfe_u32 s67, s19, 0x3000c\n"
    "v_mfma_f32_16x16x32_bf16 v[156:159], a[236:239], a[84:87], v[156:159]\n"
    "s_lshl_b32 s67, s67, 12\n"
    "v_mfma_f32_16x16x32_bf16 v[160:163], a[180:183], a[92:95], v[160:163]\n"
    "v_mfma_f32_16x16x32_bf16 v[160:163], a[176:179], a[88:91], v[160:163]\n"
    "buffer_load_dwordx4 v7, s[40:43], s58 offen lds\n"
    "v_mfma_f32_16x16x32_bf16 v[164:167], a[184:187], a[88:91], v[164:167]\n"
    "s_lshl4_add_u32 s58, s67, 2097152\n"
    "s_add_u32 s67, s67, s56\n"
    "s_add_u32 m0, s67, 98304\n"
    "v_mfma_f32_16x16x32_bf16 v[164:167], a[188:191], a[92:95], v[164:167]\n"
    "s_bfe_u32 s67, s18, 0x30000\n"
    "s_lshl_b32 s67, s67, 12\n"
    "v_mfma_f32_16x16x32_bf16 v[168:171], a[196:199], a[92:95], v[168:171]\n"
    "v_mfma_f32_16x16x32_bf16 v[168:171], a[192:195], a[88:91], v[168:171]\n"
    "s_waitcnt vmcnt(16) lgkmcnt(0)\n"
    "s_barrier\n"
    "ds_read_b64_tr_b16 a[112:113], v5 offset:32768\n"
    "v_mfma_f32_16x16x32_bf16 v[172:175], a[200:203], a[88:91], v[172:175]\n"
    "buffer_load_dwordx4 v7, s[40:43], s58 offen lds\n"
    "v_mfma_f32_16x16x32_bf16 v[172:175], a[204:207], a[92:95], v[172:175]\n"
    "s_lshl3_add_u32 s58, s67, 512\n"
    "ds_read_b64_tr_b16 a[114:115], v5 offset:33792\n"
    "s_add_u32 s67, s67, s56\n"
    "v_mfma_f32_16x16x32_bf16 v[176:179], a[212:215], a[92:95], v[176:179]\n"
    "s_add_u32 m0, s67, 0\n"
    "ds_read_b64_tr_b16 a[116:117], v5 offset:49152\n"
    "s_bfe_u32 s67, s18, 0x30003\n"
    "v_mfma_f32_16x16x32_bf16 v[176:179], a[208:211], a[88:91], v[176:179]\n"
    "s_lshl_b32 s67, s67, 12\n"
    "ds_read_b64_tr_b16 a[118:119], v5 offset:50176\n"
    "v_mfma_f32_16x16x32_bf16 v[180:183], a[216:219], a[88:91], v[180:183]\n"
    "ds_read_b64_tr_b16 a[120:121], v5 offset:32776\n"
    "v_mfma_f32_16x16x32_bf16 v[180:183], a[220:223], a[92:95], v[180:183]\n"
    "buffer_load_dwordx4 v6, s[36:39], s58 offen lds\n"
    "v_mfma_f32_16x16x32_bf16 v[184:187], a[228:231], a[92:95], v[184:187]\n"
    "s_lshl3_add_u32 s58, s67, 512\n"
    "ds_read_b64_tr_b16 a[122:123], v5 offset:33800\n"
    "s_add_u32 s67, s67, s56\n"
    "v_mfma_f32_16x16x32_bf16 v[184:187], a[224:227], a[88:91], v[184:187]\n"
    "s_add_u32 m0, s67, 0\n"
    "ds_read_b64_tr_b16 a[124:125], v5 offset:49160\n"
    "s_bfe_u32 s67, s18, 0x30006\n"
    "v_mfma_f32_16x16x32_bf16 v[188:191], a[232:235], a[88:91], v[188:191]\n"
    "s_lshl_b32 s67, s67, 12\n"
    "ds_read_b64_tr_b16 a[126:127], v5 offset:50184\n"
    "v_mfma_f32_16x16x32_bf16 v[188:191], a[236:239], a[92:95], v[188:191]\n"
    "ds_read_b64_tr_b16 a[128:129], v5 offset:33024\n"
    "v_mfma_f32_16x16x32_bf16 v[192:195], a[180:183], a[100:103], v[192:195]\n"
    "buffer_load_dwordx4 v6, s[36:39], s58 offen lds\n"
    "v_mfma_f32_16x16x32_bf16 v[192:195], a[176:179], a[96:99], v[192:195]\n"
    "s_lshl3_add_u32 s58, s67, 512\n"
    "ds_read_b64_tr_b16 a[130:131], v5 offset:34048\n"
    "s_add_u32 s67, s67, s56\n"
    "v_mfma_f32_16x16x32_bf16 v[196:199], a[184:187], a[96:99], v[196:199]\n"
    "s_add_u32 m0, s67, 0\n"
    "ds_read_b64_tr_b16 a[132:133], v5 offset:49408\n"
    "s_bfe_u32 s67, s18, 0x30009\n"
    "v_mfma_f32_16x16x32_bf16 v[196:199], a[188:191], a[100:103], v[196:199]\n"
    "s_lshl_b32 s67, s67, 12\n"
    "ds_read_b64_tr_b16 a[134:135], v5 offset:50432\n"
    "v_mfma_f32_16x16x32_bf16 v[200:203], a[196:199], a[100:103], v[200:203]\n"
    "ds_read_b64_tr_b16 a[136:137], v5 offset:33032\n"
    "v_mfma_f32_16x16x32_bf16 v[200:203], a[192:195], a[96:99], v[200:203]\n"
    "buffer_load_dwordx4 v6, s[36:39], s58 offen lds\n"
    "v_mfma_f32_16x16x32_bf16 v[204:207], a[200:203], a[96:99], v[204:207]\n"
    "s_lshl3_add_u32 s58, s67, 512\n"
    "ds_read_b64_tr_b16 a[138:139], v5 offset:34056\n"
    "s_add_u32 s67, s67, s56\n"
    "v_mfma_f32_16x16x32_bf16 v[204:207], a[204:207], a[100:103], v[204:207]\n"
    "s_add_u32 m0, s67, 0\n"
    "ds_read_b64_tr_b16 a[140:141], v5 offset:49416\n"
    "s_bfe_u32 s67, s18, 0x3000c\n"
    "v_mfma_f32_16x16x32_bf16 v[208:211], a[212:215], a[100:103], v[208:211]\n"
    "s_lshl_b32 s67, s67, 12\n"
    "ds_read_b64_tr_b16 a[142:143], v5 offset:50440\n"
    "v_mfma_f32_16x16x32_bf16 v[208:211], a[208:211], a[96:99], v[208:211]\n"
    "ds_read_b64_tr_b16 a[144:145], v5 offset:33280\n"
    "v_mfma_f32_16x16x32_bf16 v[212:215], a[216:219], a[96:99], v[212:215]\n"
    "buffer_load_dwordx4 v6, s[36:39], s58 offen lds\n"
    "v_mfma_f32_16x16x32_bf16 v[212:215], a[220:223], a[100:103], v[212:215]\n"
    "s_lshl3_add_u32 s58, s67, 512\n"
    "ds_read_b64_tr_b16 a[146:147], v5 offset:34304\n"
    "s_add_u32 s67, s67, s56\n"
    "v_mfma_f32_16x16x32_bf16 v[216:219], a[228:231], a[100:103], v[216:219]\n"
    "s_add_u32 m0, s67, 0\n"
    "ds_read_b64_tr_b16 a[148:149], v5 offset:49664\n"
    "s_bfe_u32 s67, s18, 0x3000f\n"
    "v_mfma_f32_16x16x32_bf16 v[216:219], a[224:227], a[96:99], v[216:219]\n"
    "s_lshl_b32 s67, s67, 12\n"
    "ds_read_b64_tr_b16 a[150:151], v5 offset:50688\n"
    "v_mfma_f32_16x16x32_bf16 v[220:223], a[232:235], a[96:99], v[220:223]\n"
    "ds_read_b64_tr_b16 a[152:153], v5 offset:33288\n"
    "v_mfma_f32_16x16x32_bf16 v[220:223], a[236:239], a[100:103], v[220:223]\n"
    "buffer_load_dwordx4 v6, s[36:39], s58 offen lds\n"
    "v_mfma_f32_16x16x32_bf16 v[224:227], a[180:183], a[108:111], v[224:227]\n"
    "s_lshl3_add_u32 s58, s67, 512\n"
    "ds_read_b64_tr_b16 a[154:155], v5 offset:34312\n"
    "s_add_u32 s67, s67, s56\n"
    "v_mfma_f32_16x16x32_bf16 v[224:227], a[176:179], a[104:107], v[224:227]\n"
    "s_add_u32 m0, s67, 0\n"
    "ds_read_b64_tr_b16 a[156:157], v5 offset:49672\n"
    "s_bfe_u32 s67, s18, 0x30012\n"
    "v_mfma_f32_16x16x32_bf16 v[228:231], a[184:187], a[104:107], v[228:231]\n"
    "s_lshl_b32 s67, s67, 12\n"
    "ds_read_b64_tr_b16 a[158:159], v5 offset:50696\n"
    "v_mfma_f32_16x16x32_bf16 v[228:231], a[188:191], a[108:111], v[228:231]\n"
    "ds_read_b64_tr_b16 a[160:161], v5 offset:33536\n"
    "v_mfma_f32_16x16x32_bf16 v[232:235], a[196:199], a[108:111], v[232:235]\n"
    "buffer_load_dwordx4 v6, s[36:39], s58 offen lds\n"
    "v_mfma_f32_16x16x32_bf16 v[232:235], a[192:195], a[104:107], v[232:235]\n"
    "s_lshl3_add_u32 s58, s67, 512\n"
    "ds_read_b64_tr_b16 a[162:163], v5 offset:34560\n"
    "s_add_u32 s67, s67, s56\n"
    "v_mfma_f32_16x16x32_bf16 v[236:239], a[200:203], a[104:107], v[236:239]\n"
    "s_add_u32 m0, s67, 0\n"
    "ds_read_b64_tr_b16 a[164:165], v5 offset:49920\n"
    "s_bfe_u32 s67, s19, 0x3000f\n"
    "v_mfma_f32_16x16x32_bf16 v[236:239], a[204:207], a[108:111], v[236:239]\n"
    "s_lshl_b32 s67, s67, 12\n"
    "ds_read_b64_tr_b16 a[166:167], v5 offset:50944\n"
    "v_mfma_f32_16x16x32_bf16 v[240:243], a[212:215], a[108:111], v[240:243]\n"
    "ds_read_b64_tr_b16 a[168:169], v5 offset:33544\n"
    "v_mfma_f32_16x16x32_bf16 v[240:243], a[208:211], a[104:107], v[240:243]\n"
    "buffer_load_dwordx4 v6, s[36:39], s58 offen lds\n"
    "v_mfma_f32_16x16x32_bf16 v[244:247], a[216:219], a[104:107], v[244:247]\n"
    "s_lshl4_add_u32 s58, s67, 2097152\n"
    "ds_read_b64_tr_b16 a[170:171], v5 offset:34568\n"
    "s_add_u32 s67, s67, s56\n"
    "v_mfma_f32_16x16x32_bf16 v[244:247], a[220:223], a[108:111], v[244:247]\n"
    "s_add_u32 m0, s67, 98304\n"
    "ds_read_b64_tr_b16 a[172:173], v5 offset:49928\n"
    "s_bfe_u32 s67, s19, 0x30012\n"
    "v_mfma_f32_16x16x32_bf16 v[248:251], a[228:231], a[108:111], v[248:251]\n"
    "s_lshl_b32 s67, s67, 12\n"
    "ds_read_b64_tr_b16 a[174:175], v5 offset:50952\n"
    "v_mfma_f32_16x16x32_bf16 v[248:251], a[224:227], a[104:107], v[248:251]\n"
    "v_mfma_f32_16x16x32_bf16 v[252:255], a[232:235], a[104:107], v[252:255]\n"
    "buffer_load_dwordx4 v7, s[40:43], s58 offen lds\n"
    "v_mfma_f32_16x16x32_bf16 v[252:255], a[236:239], a[108:111], v[252:255]\n"
    "s_waitcnt lgkmcnt(7)\n"
    "s_lshl4_add_u32 s58, s67, 2097152\n"
    "s_add_u32 s67, s67, s56\n"
    "v_mfma_f32_16x16x32_bf16 v[32:35], a[116:119], a[4:7], v[32:35]\n"
    "s_add_u32 m0, s67, 98304\n"
    "s_bfe_u32 s67, s19, 0x30015\n"
    "s_lshl_b32 s67, s67, 12\n"
    "v_mfma_f32_16x16x32_bf16 v[32:35], a[112:115], a[0:3], v[32:35]\n"
    "v_mfma_f32_16x16x32_bf16 v[36:39], a[120:123], a[0:3], v[36:39]\n"
    "v_mfma_f32_16x16x32_bf16 v[36:39], a[124:127], a[4:7], v[36:39]\n"
    "buffer_load_dwordx4 v7, s[40:43], s58 offen lds\n"
    "v_mfma_f32_16x16x32_bf16 v[40:43], a[132:135], a[4:7], v[40:43]\n"
    "s_lshl4_add_u32 s58, s67, 2097152\n"
    "s_add_u32 s67, s67, s56\n"
    "s_add_u32 m0, s67, 98304\n"
    "v_mfma_f32_16x16x32_bf16 v[40:43], a[128:131], a[0:3], v[40:43]\n"
    "s_bfe_u32 s67, s19, 0x30000\n"
    "s_lshl_b32 s67, s67, 12\n"
    "v_mfma_f32_16x16x32_bf16 v[44:47], a[136:139], a[0:3], v[44:47]\n"
    "v_mfma_f32_16x16x32_bf16 v[44:47], a[140:143], a[4:7], v[44:47]\n"
    "v_mfma_f32_16x16x32_bf16 v[48:51], a[148:151], a[4:7], v[48:51]\n"
    "buffer_load_dwordx4 v7, s[40:43], s58 offen lds\n"
    "v_mfma_f32_16x16x32_bf16 v[48:51], a[144:147], a[0:3], v[48:51]\n"
    "s_lshl4_add_u32 s58, s67, 2621440\n"
    "s_add_u32 s67, s67, s56\n"
    "s_add_u32 m0, s67, 131072\n"
    "v_mfma_f32_16x16x32_bf16 v[52:55], a[152:155], a[0:3], v[52:55]\n"
    "s_bfe_u32 s67, s19, 0x30003\n"
    "s_lshl_b32 s67, s67, 12\n"
    "v_mfma_f32_16x16x32_bf16 v[52:55], a[156:159], a[4:7], v[52:55]\n"
    "s_waitcnt lgkmcnt(0)\n"
    "v_mfma_f32_16x16x32_bf16 v[56:59], a[164:167], a[4:7], v[56:59]\n"
    "v_mfma_f32_16x16x32_bf16 v[56:59], a[160:163], a[0:3], v[56:59]\n"
    "buffer_load_dword v30, v28, s[68:71], 0 offen sc1\n"
    "v_mfma_f32_16x16x32_bf16 v[60:63], a[168:171], a[0:3], v[60:63]\n"
    "v_mfma_f32_16x16x32_bf16 v[60:63], a[172:175], a[4:7], v[60:63]\n"
    "v_mfma_f32_16x16x32_bf16 v[64:67], a[116:119], a[12:15], v[64:67]\n"
    "v_mfma_f32_16x16x32_bf16 v[64:67], a[112:115], a[8:11], v[64:67]\n"
    "v_mfma_f32_16x16x32_bf16 v[68:71], a[120:123], a[8:11], v[68:71]\n"
    "v_mfma_f32_16x16x32_bf16 v[68:71], a[124:127], a[12:15], v[68:71]\n"
    "v_mfma_f32_16x16x32_bf16 v[72:75], a[132:135], a[12:15], v[72:75]\n"
    "v_mfma_f32_16x16x32_bf16 v[72:75], a[128:131], a[8:11], v[72:75]\n"
    "v_mfma_f32_16x16x32_bf16 v[76:79], a[136:139], a[8:11], v[76:79]\n"
    "v_mfma_f32_16x16x32_bf16 v[76:79], a[140:143], a[12:15], v[76:79]\n"
    "v_mfma_f32_16x16x32_bf16 v[80:83], a[148:151], a[12:15], v[80:83]\n"
    "v_mfma_f32_16x16x32_bf16 v[80:83], a[144:147], a[8:11], v[80:83]\n"
    "v_mfma_f32_16x16x32_bf16 v[84:87], a[152:155], a[8:11], v[84:87]\n"
    "v_mfma_f32_16x16x32_bf16 v[84:87], a[156:159], a[12:15], v[84:87]\n"
    "v_mfma_f32_16x16x32_bf16 v[88:91], a[164:167], a[12:15], v[88:91]\n"
    "v_mfma_f32_16x16x32_bf16 v[88:91], a[160:163], a[8:11], v[88:91]\n"
    "v_mfma_f32_16x16x32_bf16 v[92:95], a[168:171], a[8:11], v[92:95]\n"
    "v_mfma_f32_16x16x32_bf16 v[92:95], a[172:175], a[12:15], v[92:95]\n"
    "v_mfma_f32_16x16x32_bf16 v[96:99], a[116:119], a[20:23], v[96:99]\n"
    "v_mfma_f32_16x16x32_bf16 v[96:99], a[112:115], a[16:19], v[96:99]\n"
    "v_mfma_f32_16x16x32_bf16 v[100:103], a[120:123], a[16:19], v[100:103]\n"
    "v_mfma_f32_16x16x32_bf16 v[100:103], a[124:127], a[20:23], v[100:103]\n"
    "v_mfma_f32_16x16x32_bf16 v[104:107], a[132:135], a[20:23], v[104:107]\n"
    "s_waitcnt vmcnt(23)\n"
    "s_barrier\n"
    "ds_read_b128 a[56:59], v3 offset:0\n"
    "v_mfma_f32_16x16x32_bf16 v[104:107], a[128:131], a[16:19], v[104:107]\n"
    "buffer_load_dwordx4 v7, s[40:43], s58 offen lds\n"
    "v_mfma_f32_16x16x32_bf16 v[108:111], a[136:139], a[16:19], v[108:111]\n"
    "s_lshl4_add_u32 s58, s67, 2621440\n"
    "ds_read_b128 a[60:63], v3 offset:512\n"
    "s_add_u32 s67, s67, s56\n"
    "v_mfma_f32_16x16x32_bf16 v[108:111], a[140:143], a[20:23], v[108:111]\n"
    "s_add_u32 m0, s67, 131072\n"
    "ds_read_b128 a[64:67], v3 offset:2048\n"
    "s_bfe_u32 s67, s19, 0x30006\n"
    "v_mfma_f32_16x16x32_bf16 v[112:115], a[148:151], a[20:23], v[112:115]\n"
    "s_lshl_b32 s67, s67, 12\n"
    "ds_read_b128 a[68:71], v3 offset:2560\n"
    "v_mfma_f32_16x16x32_bf16 v[112:115], a[144:147], a[16:19], v[112:115]\n"
    "ds_read_b128 a[72:75], v3 offset:4096\n"
    "v_mfma_f32_16x16x32_bf16 v[116:119], a[152:155], a[16:19], v[116:119]\n"
    "buffer_load_dwordx4 v7, s[40:43], s58 offen lds\n"
    "ds_read_b128 a[76:79], v3 offset:4608\n"
    "v_mfma_f32_16x16x32_bf16 v[116:119], a[156:159], a[20:23], v[116:119]\n"
    "s_lshl4_add_u32 s58, s67, 2621440\n"
    "ds_read_b128 a[80:83], v3 offset:6144\n"
    "s_add_u32 s67, s67, s56\n"
    "v_mfma_f32_16x16x32_bf16 v[120:123], a[164:167], a[20:23], v[120:123]\n"
    "s_add_u32 m0, s67, 131072\n"
    "ds_read_b128 a[84:87], v3 offset:6656\n"
    "s_bfe_u32 s67, s19, 0x30009\n"
    "v_mfma_f32_16x16x32_bf16 v[120:123], a[160:163], a[16:19], v[120:123]\n"
    "s_lshl_b32 s67, s67, 12\n"
    "ds_read_b128 a[88:91], v3 offset:8192\n"
    "v_mfma_f32_16x16x32_bf16 v[124:127], a[168:171], a[16:19], v[124:127]\n"
    "ds_read_b128 a[92:95], v3 offset:8704\n"
    "v_mfma_f32_16x16x32_bf16 v[124:127], a[172:175], a[20:23], v[124:127]\n"
    "buffer_load_dwordx4 v7, s[40:43], s58 offen lds\n"
    "ds_read_b128 a[96:99], v3 offset:10240\n"
    "v_mfma_f32_16x16x32_bf16 v[128:131], a[116:119], a[28:31], v[128:131]\n"
    "s_lshl4_add_u32 s58, s67, 2621440\n"
    "ds_read_b128 a[100:103], v3 offset:10752\n"
    "s_add_u32 s67, s67, s56\n"
    "v_mfma_f32_16x16x32_bf16 v[128:131], a[112:115], a[24:27], v[128:131]\n"
    "s_add_u32 m0, s67, 131072\n"
    "ds_read_b128 a[104:107], v3 offset:12288\n"
    "s_bfe_u32 s67, s19, 0x3000c\n"
    "v_mfma_f32_16x16x32_bf16 v[132:135], a[120:123], a[24:27], v[132:135]\n"
    "s_lshl_b32 s67, s67, 12\n"
    "ds_read_b128 a[108:111], v3 offset:12800\n"
    "v_mfma_f32_16x16x32_bf16 v[132:135], a[124:127], a[28:31], v[132:135]\n"
    "v_mfma_f32_16x16x32_bf16 v[136:139], a[132:135], a[28:31], v[136:139]\n"
    "buffer_load_dwordx4 v7, s[40:43], s58 offen lds\n"
    "v_mfma_f32_16x16x32_bf16 v[136:139], a[128:131], a[24:27], v[136:139]\n"
    "s_lshl4_add_u32 s58, s67, 2621440\n"
    "s_add_u32 s67, s67, s56\n"
    "s_add_u32 m0, s67, 131072\n"
    "v_mfma_f32_16x16x32_bf16 v[140:143], a[136:139], a[24:27], v[140:143]\n"
    "s_bfe_u32 s67, s18, 0x30000\n"
    "s_lshl_b32 s67, s67, 12\n"
    "v_mfma_f32_16x16x32_bf16 v[140:143], a[140:143], a[28:31], v[140:143]\n"
    "v_mfma_f32_16x16x32_bf16 v[144:147], a[148:151], a[28:31], v[144:147]\n"
    "v_mfma_f32_16x16x32_bf16 v[144:147], a[144:147], a[24:27], v[144:147]\n"
    "s_waitcnt vmcnt(20) lgkmcnt(0)\n"
    "s_barrier\n"
    "ds_read_b64_tr_b16 a[176:177], v4 offset:0\n"
    "v_mfma_f32_16x16x32_bf16 v[148:151], a[152:155], a[24:27], v[148:151]\n"
    "buffer_load_dwordx4 v7, s[40:43], s58 offen lds\n"
    "v_mfma_f32_16x16x32_bf16 v[148:151], a[156:159], a[28:31], v[148:151]\n"
    "s_lshl3_add_u32 s58, s67, 640\n"
    "ds_read_b64_tr_b16 a[178:179], v4 offset:1024\n"
    "s_add_u32 s67, s67, s56\n"
    "v_mfma_f32_16x16x32_bf16 v[152:155], a[164:167], a[28:31], v[152:155]\n"
    "s_add_u32 m0, s67, 65536\n"
    "ds_read_b64_tr_b16 a[180:181], v4 offset:16384\n"
    "s_bfe_u32 s67, s18, 0x30003\n"
    "v_mfma_f32_16x16x32_bf16 v[152:155], a[160:163], a[24:27], v[152:155]\n"
    "s_lshl_b32 s67, s67, 12\n"
    "ds_read_b64_tr_b16 a[182:183], v4 offset:17408\n"
    "v_mfma_f32_16x16x32_bf16 v[156:159], a[168:171], a[24:27], v[156:159]\n"
    "ds_read_b64_tr_b16 a[184:185], v4 offset:8\n"
    "v_mfma_f32_16x16x32_bf16 v[156:159], a[172:175], a[28:31], v[156:159]\n"
    "buffer_load_dwordx4 v6, s[36:39], s58 offen lds\n"
    "v_mfma_f32_16x16x32_bf16 v[160:163], a[116:119], a[36:39], v[160:163]\n"
    "s_lshl3_add_u32 s58, s67, 640\n"
    "ds_read_b64_tr_b16 a[186:187], v4 offset:1032\n"
    "s_add_u32 s67, s67, s56\n"
    "v_mfma_f32_16x16x32_bf16 v[160:163], a[112:115], a[32:35], v[160:163]\n"
    "s_add_u32 m0, s67, 65536\n"
    "ds_read_b64_tr_b16 a[188:189], v4 offset:16392\n"
    "s_bfe_u32 s67, s18, 0x30006\n"
    "v_mfma_f32_16x16x32_bf16 v[164:167], a[120:123], a[32:35], v[164:167]\n"
    "s_lshl_b32 s67, s67, 12\n"
    "ds_read_b64_tr_b16 a[190:191], v4 offset:17416\n"
    "v_mfma_f32_16x16x32_bf16 v[164:167], a[124:127], a[36:39], v[164:167]\n"
    "ds_read_b64_tr_b16 a[192:193], v4 offset:256\n"
    "v_mfma_f32_16x16x32_bf16 v[168:171], a[132:135], a[36:39], v[168:171]\n"
    "buffer_load_dwordx4 v6, s[36:39], s58 offen lds\n"
    "v_mfma_f32_16x16x32_bf16 v[168:171], a[128:131], a[32:35], v[168:171]\n"
    "s_lshl3_add_u32 s58, s67, 640\n"
    "ds_read_b64_tr_b16 a[194:195], v4 offset:1280\n"
    "s_add_u32 s67, s67, s56\n"
    "v_mfma_f32_16x16x32_bf16 v[172:175], a[136:139], a[32:35], v[172:175]\n"
    "s_add_u32 m0, s67, 65536\n"
    "ds_read_b64_tr_b16 a[196:197], v4 offset:16640\n"
    "s_bfe_u32 s67, s18, 0x30009\n"
    "v_mfma_f32_16x16x32_bf16 v[172:175], a[140:143], a[36:39], v[172:175]\n"
    "s_lshl_b32 s67, s67, 12\n"
    "ds_read_b64_tr_b16 a[198:199], v4 offset:17664\n"
    "v_mfma_f32_16x16x32_bf16 v[176:179], a[148:151], a[36:39], v[176:179]\n"
    "ds_read_b64_tr_b16 a[200:201], v4 offset:264\n"
    "v_mfma_f32_16x16x32_bf16 v[176:179], a[144:147], a[32:35], v[176:179]\n"
    "buffer_load_dwordx4 v6, s[36:39], s58 offen lds\n"
    "v_mfma_f32_16x16x32_bf16 v[180:183], a[152:155], a[32:35], v[180:183]\n"
    "s_lshl3_add_u32 s58, s67, 640\n"
    "ds_read_b64_tr_b16 a[202:203], v4 offset:1288\n"
    "s_add_u32 s67, s67, s56\n"
    "v_mfma_f32_16x16x32_bf16 v[180:183], a[156:159], a[36:39], v[180:183]\n"
    "s_add_u32 m0, s67, 65536\n"
    "ds_read_b64_tr_b16 a[204:205], v4 offset:16648\n"
    "s_bfe_u32 s67, s18, 0x3000c\n"
    "v_mfma_f32_16x16x32_bf16 v[184:187], a[164:167], a[36:39], v[184:187]\n"
    "s_lshl_b32 s67, s67, 12\n"
    "ds_read_b64_tr_b16 a[206:207], v4 offset:17672\n"
    "v_mfma_f32_16x16x32_bf16 v[184:187], a[160:163], a[32:35], v[184:187]\n"
    "ds_read_b64_tr_b16 a[208:209], v4 offset:512\n"
    "v_mfma_f32_16x16x32_bf16 v[188:191], a[168:171], a[32:35], v[188:191]\n"
    "buffer_load_dwordx4 v6, s[36:39], s58 offen lds\n"
    "v_mfma_f32_16x16x32_bf16 v[188:191], a[172:175], a[36:39], v[188:191]\n"
    "s_lshl3_add_u32 s58, s67, 640\n"
    "ds_read_b64_tr_b16 a[210:211], v4 offset:1536\n"
    "s_add_u32 s67, s67, s56\n"
    "v_mfma_f32_16x16x32_bf16 v[192:195], a[116:119], a[44:47], v[192:195]\n"
    "s_add_u32 m0, s67, 65536\n"
    "ds_read_b64_tr_b16 a[212:213], v4 offset:16896\n"
    "s_bfe_u32 s67, s18, 0x3000f\n"
    "v_mfma_f32_16x16x32_bf16 v[192:195], a[112:115], a[40:43], v[192:195]\n"
    "s_lshl_b32 s67, s67, 12\n"
    "ds_read_b64_tr_b16 a[214:215], v4 offset:17920\n"
    "v_mfma_f32_16x16x32_bf16 v[196:199], a[120:123], a[40:43], v[196:199]\n"
    "ds_read_b64_tr_b16 a[216:217], v4 offset:520\n"
    "v_mfma_f32_16x16x32_bf16 v[196:199], a[124:127], a[44:47], v[196:199]\n"
    "buffer_load_dwordx4 v6, s[36:39], s58 offen lds\n"
    "v_mfma_f32_16x16x32_bf16 v[200:203], a[132:135], a[44:47], v[200:203]\n"
    "s_lshl3_add_u32 s58, s67, 640\n"
    "ds_read_b64_tr_b16 a[218:219], v4 offset:1544\n"
    "s_add_u32 s67, s67, s56\n"
    "v_mfma_f32_16x16x32_bf16 v[200:203], a[128:131], a[40:43], v[200:203]\n"
    "s_add_u32 m0, s67, 65536\n"
    "ds_read_b64_tr_b16 a[220:221], v4 offset:16904\n"
    "s_bfe_u32 s67, s18, 0x30012\n"
    "v_mfma_f32_16x16x32_bf16 v[204:207], a[136:139], a[40:43], v[204:207]\n"
    "s_lshl_b32 s67, s67, 12\n"
    "ds_read_b64_tr_b16 a[222:223], v4 offset:17928\n"
    "v_mfma_f32_16x16x32_bf16 v[204:207], a[140:143], a[44:47], v[204:207]\n"
    "ds_read_b64_tr_b16 a[224:225], v4 offset:768\n"
    "v_mfma_f32_16x16x32_bf16 v[208:211], a[148:151], a[44:47], v[208:211]\n"
    "buffer_load_dwordx4 v6, s[36:39], s58 offen lds\n"
    "v_mfma_f32_16x16x32_bf16 v[208:211], a[144:147], a[40:43], v[208:211]\n"
    "s_lshl3_add_u32 s58, s67, 640\n"
    "ds_read_b64_tr_b16 a[226:227], v4 offset:1792\n"
    "s_add_u32 s67, s67, s56\n"
    "v_mfma_f32_16x16x32_bf16 v[212:215], a[152:155], a[40:43], v[212:215]\n"
    "s_add_u32 m0, s67, 65536\n"
    "ds_read_b64_tr_b16 a[228:229], v4 offset:17152\n"
    "s_bfe_u32 s67, s19, 0x3000f\n"
    "v_mfma_f32_16x16x32_bf16 v[212:215], a[156:159], a[44:47], v[212:215]\n"
    "s_lshl_b32 s67, s67, 12\n"
    "ds_read_b64_tr_b16 a[230:231], v4 offset:18176\n"
    "v_mfma_f32_16x16x32_bf16 v[216:219], a[164:167], a[44:47], v[216:219]\n"
    "ds_read_b64_tr_b16 a[232:233], v4 offset:776\n"
    "v_mfma_f32_16x16x32_bf16 v[216:219], a[160:163], a[40:43], v[216:219]\n"
    "buffer_load_dwordx4 v6, s[36:39], s58 offen lds\n"
    "v_mfma_f32_16x16x32_bf16 v[220:223], a[168:171], a[40:43], v[220:223]\n"
    "s_lshl4_add_u32 s58, s67, 2621440\n"
    "ds_read_b64_tr_b16 a[234:235], v4 offset:1800\n"
    "s_add_u32 s67, s67, s56\n"
    "v_mfma_f32_16x16x32_bf16 v[220:223], a[172:175], a[44:47], v[220:223]\n"
    "s_add_u32 m0, s67, 131072\n"
    "ds_read_b64_tr_b16 a[236:237], v4 offset:17160\n"
    "s_bfe_u32 s67, s19, 0x30012\n"
    "v_mfma_f32_16x16x32_bf16 v[224:227], a[116:119], a[52:55], v[224:227]\n"
    "s_lshl_b32 s67, s67, 12\n"
    "ds_read_b64_tr_b16 a[238:239], v4 offset:18184\n"
    "v_mfma_f32_16x16x32_bf16 v[224:227], a[112:115], a[48:51], v[224:227]\n"
    "v_mfma_f32_16x16x32_bf16 v[228:231], a[120:123], a[48:51], v[228:231]\n"
    "buffer_load_dwordx4 v7, s[40:43], s58 offen lds\n"
    "v_mfma_f32_16x16x32_bf16 v[228:231], a[124:127], a[52:55], v[228:231]\n"
    "s_lshl4_add_u32 s58, s67, 2621440\n"
    "s_add_u32 s67, s67, s56\n"
    "s_add_u32 m0, s67, 131072\n"
    "v_mfma_f32_16x16x32_bf16 v[232:235], a[132:135], a[52:55], v[232:235]\n"
    "s_bfe_u32 s67, s19, 0x30015\n"
    "s_lshl_b32 s67, s67, 12\n"
    "v_mfma_f32_16x16x32_bf16 v[232:235], a[128:131], a[48:51], v[232:235]\n"
    "v_mfma_f32_16x16x32_bf16 v[236:239], a[136:139], a[48:51], v[236:239]\n"
    "v_mfma_f32_16x16x32_bf16 v[236:239], a[140:143], a[52:55], v[236:239]\n"
    "buffer_load_dwordx4 v7, s[40:43], s58 offen lds\n"
    "v_mfma_f32_16x16x32_bf16 v[240:243], a[148:151], a[52:55], v[240:243]\n"
    "s_lshl4_add_u32 s58, s67, 2621440\n"
    "s_add_u32 s67, s67, s56\n"
    "s_add_u32 m0, s67, 131072\n"
    "v_mfma_f32_16x16x32_bf16 v[240:243], a[144:147], a[48:51], v[240:243]\n"
    "s_bfe_u32 s67, s19, 0x30000\n"
    "s_lshl_b32 s67, s67, 12\n"
    "v_mfma_f32_16x16x32_bf16 v[244:247], a[152:155], a[48:51], v[244:247]\n"
    "v_mfma_f32_16x16x32_bf16 v[244:247], a[156:159], a[52:55], v[244:247]\n"
    "v_mfma_f32_16x16x32_bf16 v[248:251], a[164:167], a[52:55], v[248:251]\n"
    "buffer_load_dwordx4 v7, s[40:43], s58 offen lds\n"
    "v_mfma_f32_16x16x32_bf16 v[248:251], a[160:163], a[48:51], v[248:251]\n"
    "s_lshl4_add_u32 s58, s67, 3145728\n"
    "s_add_u32 s67, s67, s56\n"
    "s_add_u32 m0, s67, 32768\n"
    "v_mfma_f32_16x16x32_bf16 v[252:255], a[168:171], a[48:51], v[252:255]\n"
    "s_bfe_u32 s67, s19, 0x30003\n"
    "s_lshl_b32 s67, s67, 12\n"
    "v_mfma_f32_16x16x32_bf16 v[252:255], a[172:175], a[52:55], v[252:255]\n"
    "s_waitcnt lgkmcnt(0)\n"
    "v_mfma_f32_16x16x32_bf16 v[32:35], a[180:183], a[60:63], v[32:35]\n"
    "v_mfma_f32_16x16x32_bf16 v[32:35], a[176:179], a[56:59], v[32:35]\n"
    "v_mfma_f32_16x16x32_bf16 v[36:39], a[184:187], a[56:59], v[36:39]\n"
    "v_mfma_f32_16x16x32_bf16 v[36:39], a[188:191], a[60:63], v[36:39]\n"
    "v_mfma_f32_16x16x32_bf16 v[40:43], a[196:199], a[60:63], v[40:43]\n"
    "v_mfma_f32_16x16x32_bf16 v[40:43], a[192:195], a[56:59], v[40:43]\n"
    "v_mfma_f32_16x16x32_bf16 v[44:47], a[200:203], a[56:59], v[44:47]\n"
    "v_mfma_f32_16x16x32_bf16 v[44:47], a[204:207], a[60:63], v[44:47]\n"
    "v_mfma_f32_16x16x32_bf16 v[48:51], a[212:215], a[60:63], v[48:51]\n"
    "v_mfma_f32_16x16x32_bf16 v[48:51], a[208:211], a[56:59], v[48:51]\n"
    "v_mfma_f32_16x16x32_bf16 v[52:55], a[216:219], a[56:59], v[52:55]\n"
    "v_mfma_f32_16x16x32_bf16 v[52:55], a[220:223], a[60:63], v[52:55]\n"
    "v_mfma_f32_16x16x32_bf16 v[56:59], a[228:231], a[60:63], v[56:59]\n"
    "v_mfma_f32_16x16x32_bf16 v[56:59], a[224:227], a[56:59], v[56:59]\n"
    "v_mfma_f32_16x16x32_bf16 v[60:63], a[232:235], a[56:59], v[60:63]\n"
    "v_mfma_f32_16x16x32_bf16 v[60:63], a[236:239], a[60:63], v[60:63]\n"
    "v_mfma_f32_16x16x32_bf16 v[64:67], a[180:183], a[68:71], v[64:67]\n"
    "v_mfma_f32_16x16x32_bf16 v[64:67], a[176:179], a[64:67], v[64:67]\n"
    "v_mfma_f32_16x16x32_bf16 v[68:71], a[184:187], a[64:67], v[68:71]\n"
    "v_mfma_f32_16x16x32_bf16 v[68:71], a[188:191], a[68:71], v[68:71]\n"
    "v_mfma_f32_16x16x32_bf16 v[72:75], a[196:199], a[68:71], v[72:75]\n"
    "s_waitcnt vmcnt(19)\n"
    "s_barrier\n"
    "ds_read_b128 a[0:3], v2 offset:0\n"
    "v_mfma_f32_16x16x32_bf16 v[72:75], a[192:195], a[64:67], v[72:75]\n"
    "buffer_load_dwordx4 v7, s[40:43], s58 offen lds\n"
    "v_mfma_f32_16x16x32_bf16 v[76:79], a[200:203], a[64:67], v[76:79]\n"
    "s_lshl4_add_u32 s58, s67, 3145728\n"
    "ds_read_b128 a[4:7], v2 offset:512\n"
    "s_add_u32 s67, s67, s56\n"
    "v_mfma_f32_16x16x32_bf16 v[76:79], a[204:207], a[68:71], v[76:79]\n"
    "s_add_u32 m0, s67, 32768\n"
    "ds_read_b128 a[8:11], v2 offset:2048\n"
    "s_bfe_u32 s67, s19, 0x30006\n"
    "v_mfma_f32_16x16x32_bf16 v[80:83], a[212:215], a[68:71], v[80:83]\n"
    "s_lshl_b32 s67, s67, 12\n"
    "ds_read_b128 a[12:15], v2 offset:2560\n"
    "v_mfma_f32_16x16x32_bf16 v[80:83], a[208:211], a[64:67], v[80:83]\n"
    "ds_read_b128 a[16:19], v2 offset:4096\n"
    "v_mfma_f32_16x16x32_bf16 v[84:87], a[216:219], a[64:67], v[84:87]\n"
    "buffer_load_dwordx4 v7, s[40:43], s58 offen lds\n"
    "ds_read_b128 a[20:23], v2 offset:4608\n"
    "v_mfma_f32_16x16x32_bf16 v[84:87], a[220:223], a[68:71], v[84:87]\n"
    "s_lshl4_add_u32 s58, s67, 3145728\n"
    "ds_read_b128 a[24:27], v2 offset:6144\n"
    "s_add_u32 s67, s67, s56\n"
    "v_mfma_f32_16x16x32_bf16 v[88:91], a[228:231], a[68:71], v[88:91]\n"
    "s_add_u32 m0, s67, 32768\n"
    "ds_read_b128 a[28:31], v2 offset:6656\n"
    "s_bfe_u32 s67, s19, 0x30009\n"
    "v_mfma_f32_16x16x32_bf16 v[88:91], a[224:227], a[64:67], v[88:91]\n"
    "s_lshl_b32 s67, s67, 12\n"
    "ds_read_b128 a[32:35], v2 offset:8192\n"
    "v_mfma_f32_16x16x32_bf16 v[92:95], a[232:235], a[64:67], v[92:95]\n"
    "ds_read_b128 a[36:39], v2 offset:8704\n"
    "v_mfma_f32_16x16x32_bf16 v[92:95], a[236:239], a[68:71], v[92:95]\n"
    "buffer_load_dwordx4 v7, s[40:43], s58 offen lds\n"
    "ds_read_b128 a[40:43], v2 offset:10240\n"
    "v_mfma_f32_16x16x32_bf16 v[96:99], a[180:183], a[76:79], v[96:99]\n"
    "s_lshl4_add_u32 s58, s67, 3145728\n"
    "ds_read_b128 a[44:47], v2 offset:10752\n"
    "s_add_u32 s67, s67, s56\n"
    "v_mfma_f32_16x16x32_bf16 v[96:99], a[176:179], a[72:75], v[96:99]\n"
    "s_add_u32 m0, s67, 32768\n"
    "ds_read_b128 a[48:51], v2 offset:12288\n"
    "s_bfe_u32 s67, s19, 0x3000c\n"
    "v_mfma_f32_16x16x32_bf16 v[100:103], a[184:187], a[72:75], v[100:103]\n"
    "s_lshl_b32 s67, s67, 12\n"
    "ds_read_b128 a[52:55], v2 offset:12800\n"
    "v_mfma_f32_16x16x32_bf16 v[100:103], a[188:191], a[76:79], v[100:103]\n"
    "v_mfma_f32_16x16x32_bf16 v[104:107], a[196:199], a[76:79], v[104:107]\n"
    "buffer_load_dwordx4 v7, s[40:43], s58 offen lds\n"
    "v_mfma_f32_16x16x32_bf16 v[104:107], a[192:195], a[72:75], v[104:107]\n"
    "s_lshl4_add_u32 s58, s67, 3145728\n"
    "s_add_u32 s67, s67, s56\n"
    "s_add_u32 m0, s67, 32768\n"
    "v_mfma_f32_16x16x32_bf16 v[108:111], a[200:203], a[72:75], v[108:111]\n"
    "s_bfe_u32 s67, s18, 0x30000\n"
    "s_lshl_b32 s67, s67, 12\n"
    "s_lshl_b32 s34, s7, 12\n"
    "v_mfma_f32_16x16x32_bf16 v[108:111], a[204:207], a[76:79], v[108:111]\n"
    "v_mfma_f32_16x16x32_bf16 v[112:115], a[212:215], a[76:79], v[112:115]\n"
    "v_mfma_f32_16x16x32_bf16 v[112:115], a[208:211], a[72:75], v[112:115]\n"
    "s_waitcnt vmcnt(20) lgkmcnt(0)\n"
    "s_barrier\n"
    "ds_read_b64_tr_b16 a[112:113], v5 offset:0\n"
    "v_mfma_f32_16x16x32_bf16 v[116:119], a[216:219], a[72:75], v[116:119]\n"
    "buffer_load_dwordx4 v7, s[40:43], s58 offen lds\n"
    "v_mfma_f32_16x16x32_bf16 v[116:119], a[220:223], a[76:79], v[116:119]\n"
    "s_lshl3_add_u32 s58, s67, 768\n"
    "ds_read_b64_tr_b16 a[114:115], v5 offset:1024\n"
    "s_add_u32 s67, s67, s56\n"
    "v_mfma_f32_16x16x32_bf16 v[120:123], a[228:231], a[76:79], v[120:123]\n"
    "s_add_u32 m0, s67, 0\n"
    "ds_read_b64_tr_b16 a[116:117], v5 offset:16384\n"
    "s_bfe_u32 s67, s18, 0x30003\n"
    "v_mfma_f32_16x16x32_bf16 v[120:123], a[224:227], a[72:75], v[120:123]\n"
    "s_lshl_b32 s67, s67, 12\n"
    "ds_read_b64_tr_b16 a[118:119], v5 offset:17408\n"
    "v_mfma_f32_16x16x32_bf16 v[124:127], a[232:235], a[72:75], v[124:127]\n"
    "ds_read_b64_tr_b16 a[120:121], v5 offset:8\n"
    "v_mfma_f32_16x16x32_bf16 v[124:127], a[236:239], a[76:79], v[124:127]\n"
    "buffer_load_dwordx4 v6, s[36:39], s58 offen lds\n"
    "v_mfma_f32_16x16x32_bf16 v[128:131], a[180:183], a[84:87], v[128:131]\n"
    "s_lshl3_add_u32 s58, s67, 768\n"
    "ds_read_b64_tr_b16 a[122:123], v5 offset:1032\n"
    "s_add_u32 s67, s67, s56\n"
    "v_mfma_f32_16x16x32_bf16 v[128:131], a[176:179], a[80:83], v[128:131]\n"
    "s_add_u32 m0, s67, 0\n"
    "ds_read_b64_tr_b16 a[124:125], v5 offset:16392\n"
    "s_bfe_u32 s67, s18, 0x30006\n"
    "v_mfma_f32_16x16x32_bf16 v[132:135], a[184:187], a[80:83], v[132:135]\n"
    "s_lshl_b32 s67, s67, 12\n"
    "ds_read_b64_tr_b16 a[126:127], v5 offset:17416\n"
    "v_mfma_f32_16x16x32_bf16 v[132:135], a[188:191], a[84:87], v[132:135]\n"
    "ds_read_b64_tr_b16 a[128:129], v5 offset:256\n"
    "v_mfma_f32_16x16x32_bf16 v[136:139], a[196:199], a[84:87], v[136:139]\n"
    "buffer_load_dwordx4 v6, s[36:39], s58 offen lds\n"
    "v_mfma_f32_16x16x32_bf16 v[136:139], a[192:195], a[80:83], v[136:139]\n"
    "s_lshl3_add_u32 s58, s67, 768\n"
    "ds_read_b64_tr_b16 a[130:131], v5 offset:1280\n"
    "s_add_u32 s67, s67, s56\n"
    "v_mfma_f32_16x16x32_bf16 v[140:143], a[200:203], a[80:83], v[140:143]\n"
    "s_add_u32 m0, s67, 0\n"
    "ds_read_b64_tr_b16 a[132:133], v5 offset:16640\n"
    "s_bfe_u32 s67, s18, 0x30009\n"
    "v_mfma_f32_16x16x32_bf16 v[140:143], a[204:207], a[84:87], v[140:143]\n"
    "s_lshl_b32 s67, s67, 12\n"
    "ds_read_b64_tr_b16 a[134:135], v5 offset:17664\n"
    "v_mfma_f32_16x16x32_bf16 v[144:147], a[212:215], a[84:87], v[144:147]\n"
    "ds_read_b64_tr_b16 a[136:137], v5 offset:264\n"
    "v_mfma_f32_16x16x32_bf16 v[144:147], a[208:211], a[80:83], v[144:147]\n"
    "buffer_load_dwordx4 v6, s[36:39], s58 offen lds\n"
    "v_mfma_f32_16x16x32_bf16 v[148:151], a[216:219], a[80:83], v[148:151]\n"
    "s_lshl3_add_u32 s58, s67, 768\n"
    "ds_read_b64_tr_b16 a[138:139], v5 offset:1288\n"
    "s_add_u32 s67, s67, s56\n"
    "v_mfma_f32_16x16x32_bf16 v[148:151], a[220:223], a[84:87], v[148:151]\n"
    "s_add_u32 m0, s67, 0\n"
    "ds_read_b64_tr_b16 a[140:141], v5 offset:16648\n"
    "s_bfe_u32 s67, s18, 0x3000c\n"
    "v_mfma_f32_16x16x32_bf16 v[152:155], a[228:231], a[84:87], v[152:155]\n"
    "s_lshl_b32 s67, s67, 12\n"
    "ds_read_b64_tr_b16 a[142:143], v5 offset:17672\n"
    "v_mfma_f32_16x16x32_bf16 v[152:155], a[224:227], a[80:83], v[152:155]\n"
    "ds_read_b64_tr_b16 a[144:145], v5 offset:512\n"
    "v_mfma_f32_16x16x32_bf16 v[156:159], a[232:235], a[80:83], v[156:159]\n"
    "buffer_load_dwordx4 v6, s[36:39], s58 offen lds\n"
    "v_mfma_f32_16x16x32_bf16 v[156:159], a[236:239], a[84:87], v[156:159]\n"
    "s_lshl3_add_u32 s58, s67, 768\n"
    "ds_read_b64_tr_b16 a[146:147], v5 offset:1536\n"
    "s_add_u32 s67, s67, s56\n"
    "v_mfma_f32_16x16x32_bf16 v[160:163], a[180:183], a[92:95], v[160:163]\n"
    "s_add_u32 m0, s67, 0\n"
    "ds_read_b64_tr_b16 a[148:149], v5 offset:16896\n"
    "s_bfe_u32 s67, s18, 0x3000f\n"
    "v_mfma_f32_16x16x32_bf16 v[160:163], a[176:179], a[88:91], v[160:163]\n"
    "s_lshl_b32 s67, s67, 12\n"
    "ds_read_b64_tr_b16 a[150:151], v5 offset:17920\n"
    "v_mfma_f32_16x16x32_bf16 v[164:167], a[184:187], a[88:91], v[164:167]\n"
    "ds_read_b64_tr_b16 a[152:153], v5 offset:520\n"
    "v_mfma_f32_16x16x32_bf16 v[164:167], a[188:191], a[92:95], v[164:167]\n"
    "buffer_load_dwordx4 v6, s[36:39], s58 offen lds\n"
    "v_mfma_f32_16x16x32_bf16 v[168:171], a[196:199], a[92:95], v[168:171]\n"
    "s_lshl3_add_u32 s58, s67, 768\n"
    "ds_read_b64_tr_b16 a[154:155], v5 offset:1544\n"
    "s_add_u32 s67, s67, s56\n"
    "v_mfma_f32_16x16x32_bf16 v[168:171], a[192:195], a[88:91], v[168:171]\n"
    "s_add_u32 m0, s67, 0\n"
    "ds_read_b64_tr_b16 a[156:157], v5 offset:16904\n"
    "s_bfe_u32 s67, s18, 0x30012\n"
    "v_mfma_f32_16x16x32_bf16 v[172:175], a[200:203], a[88:91], v[172:175]\n"
    "s_lshl_b32 s67, s67, 12\n"
    "ds_read_b64_tr_b16 a[158:159], v5 offset:17928\n"
    "v_mfma_f32_16x16x32_bf16 v[172:175], a[204:207], a[92:95], v[172:175]\n"
    "ds_read_b64_tr_b16 a[160:161], v5 offset:768\n"
    "v_mfma_f32_16x16x32_bf16 v[176:179], a[212:215], a[92:95], v[176:179]\n"
    "buffer_load_dwordx4 v6, s[36:39], s58 offen lds\n"
    "v_mfma_f32_16x16x32_bf16 v[176:179], a[208:211], a[88:91], v[176:179]\n"
    "s_lshl3_add_u32 s58, s67, 768\n"
    "ds_read_b64_tr_b16 a[162:163], v5 offset:1792\n"
    "s_add_u32 s67, s67, s56\n"
    "v_mfma_f32_16x16x32_bf16 v[180:183], a[216:219], a[88:91], v[180:183]\n"
    "s_add_u32 m0, s67, 0\n"
    "ds_read_b64_tr_b16 a[164:165], v5 offset:17152\n"
    "s_bfe_u32 s67, s19, 0x3000f\n"
    "v_mfma_f32_16x16x32_bf16 v[180:183], a[220:223], a[92:95], v[180:183]\n"
    "s_lshl_b32 s67, s67, 12\n"
    "ds_read_b64_tr_b16 a[166:167], v5 offset:18176\n"
    "v_mfma_f32_16x16x32_bf16 v[184:187], a[228:231], a[92:95], v[184:187]\n"
    "ds_read_b64_tr_b16 a[168:169], v5 offset:776\n"
    "v_mfma_f32_16x16x32_bf16 v[184:187], a[224:227], a[88:91], v[184:187]\n"
    "buffer_load_dwordx4 v6, s[36:39], s58 offen lds\n"
    "v_mfma_f32_16x16x32_bf16 v[188:191], a[232:235], a[88:91], v[188:191]\n"
    "s_lshl4_add_u32 s58, s67, 3145728\n"
    "ds_read_b64_tr_b16 a[170:171], v5 offset:1800\n"
    "s_add_u32 s67, s67, s56\n"
    "v_mfma_f32_16x16x32_bf16 v[188:191], a[236:239], a[92:95], v[188:191]\n"
    "s_add_u32 m0, s67, 32768\n"
    "ds_read_b64_tr_b16 a[172:173], v5 offset:17160\n"
    "s_bfe_u32 s67, s19, 0x30012\n"
    "v_mfma_f32_16x16x32_bf16 v[192:195], a[180:183], a[100:103], v[192:195]\n"
    "s_lshl_b32 s67, s67, 12\n"
    "ds_read_b64_tr_b16 a[174:175], v5 offset:18184\n"
    "v_mfma_f32_16x16x32_bf16 v[192:195], a[176:179], a[96:99], v[192:195]\n"
    "v_mfma_f32_16x16x32_bf16 v[196:199], a[184:187], a[96:99], v[196:199]\n"
    "buffer_load_dwordx4 v7, s[40:43], s58 offen lds\n"
    "v_mfma_f32_16x16x32_bf16 v[196:199], a[188:191], a[100:103], v[196:199]\n"
    "s_lshl4_add_u32 s58, s67, 3145728\n"
    "s_add_u32 s67, s67, s56\n"
    "s_add_u32 m0, s67, 32768\n"
    "v_mfma_f32_16x16x32_bf16 v[200:203], a[196:199], a[100:103], v[200:203]\n"
    "s_bfe_u32 s67, s19, 0x30015\n"
    "s_lshl_b32 s67, s67, 12\n"
    "v_mfma_f32_16x16x32_bf16 v[200:203], a[192:195], a[96:99], v[200:203]\n"
    "v_mfma_f32_16x16x32_bf16 v[204:207], a[200:203], a[96:99], v[204:207]\n"
    "v_mfma_f32_16x16x32_bf16 v[204:207], a[204:207], a[100:103], v[204:207]\n"
    "buffer_load_dwordx4 v7, s[40:43], s58 offen lds\n"
    "v_mfma_f32_16x16x32_bf16 v[208:211], a[212:215], a[100:103], v[208:211]\n"
    "s_lshl4_add_u32 s58, s67, 3145728\n"
    "s_add_u32 s67, s67, s56\n"
    "s_add_u32 m0, s67, 32768\n"
    "v_mfma_f32_16x16x32_bf16 v[208:211], a[208:211], a[96:99], v[208:211]\n"
    "s_bfe_u32 s67, s19, 0x30000\n"
    "s_lshl_b32 s67, s67, 12\n"
    "v_mfma_f32_16x16x32_bf16 v[212:215], a[216:219], a[96:99], v[212:215]\n"
    "v_mfma_f32_16x16x32_bf16 v[212:215], a[220:223], a[100:103], v[212:215]\n"
    "v_mfma_f32_16x16x32_bf16 v[216:219], a[228:231], a[100:103], v[216:219]\n"
    "buffer_load_dwordx4 v7, s[40:43], s58 offen lds\n"
    "v_mfma_f32_16x16x32_bf16 v[216:219], a[224:227], a[96:99], v[216:219]\n"
    "s_lshl4_add_u32 s58, s67, 3670016\n"
    "s_add_u32 s67, s67, s56\n"
    "s_add_u32 m0, s67, 98304\n"
    "v_mfma_f32_16x16x32_bf16 v[220:223], a[232:235], a[96:99], v[220:223]\n"
    "s_bfe_u32 s67, s19, 0x30003\n"
    "s_lshl_b32 s67, s67, 12\n"
    "v_mfma_f32_16x16x32_bf16 v[220:223], a[236:239], a[100:103], v[220:223]\n"
    "v_mfma_f32_16x16x32_bf16 v[224:227], a[180:183], a[108:111], v[224:227]\n"
    "v_mfma_f32_16x16x32_bf16 v[224:227], a[176:179], a[104:107], v[224:227]\n"
    "v_mfma_f32_16x16x32_bf16 v[228:231], a[184:187], a[104:107], v[228:231]\n"
    "v_mfma_f32_16x16x32_bf16 v[228:231], a[188:191], a[108:111], v[228:231]\n"
    "v_mfma_f32_16x16x32_bf16 v[232:235], a[196:199], a[108:111], v[232:235]\n"
    "v_mfma_f32_16x16x32_bf16 v[232:235], a[192:195], a[104:107], v[232:235]\n"
    "v_mfma_f32_16x16x32_bf16 v[236:239], a[200:203], a[104:107], v[236:239]\n"
    "v_mfma_f32_16x16x32_bf16 v[236:239], a[204:207], a[108:111], v[236:239]\n"
    "v_mfma_f32_16x16x32_bf16 v[240:243], a[212:215], a[108:111], v[240:243]\n"
    "v_mfma_f32_16x16x32_bf16 v[240:243], a[208:211], a[104:107], v[240:243]\n"
    "v_mfma_f32_16x16x32_bf16 v[244:247], a[216:219], a[104:107], v[244:247]\n"
    "v_mfma_f32_16x16x32_bf16 v[244:247], a[220:223], a[108:111], v[244:247]\n"
    "v_mfma_f32_16x16x32_bf16 v[248:251], a[228:231], a[108:111], v[248:251]\n"
    "v_mfma_f32_16x16x32_bf16 v[248:251], a[224:227], a[104:107], v[248:251]\n"
    "v_mfma_f32_16x16x32_bf16 v[252:255], a[232:235], a[104:107], v[252:255]\n"
    "v_mfma_f32_16x16x32_bf16 v[252:255], a[236:239], a[108:111], v[252:255]\n"
    "s_waitcnt lgkmcnt(0)\n"
    "v_mfma_f32_16x16x32_bf16 v[32:35], a[116:119], a[4:7], v[32:35]\n"
    "s_waitcnt vmcnt(18)\n"
    "s_barrier\n"
    "ds_read_b128 a[56:59], v3 offset:0\n"
    "v_mfma_f32_16x16x32_bf16 v[32:35], a[112:115], a[0:3], v[32:35]\n"
    "buffer_load_dwordx4 v7, s[40:43], s58 offen lds\n"
    "v_mfma_f32_16x16x32_bf16 v[36:39], a[120:123], a[0:3], v[36:39]\n"
    "s_lshl4_add_u32 s58, s67, 3670016\n"
    "ds_read_b128 a[60:63], v3 offset:512\n"
    "v_add_u32 v30, 1, v30\n"
    "v_mfma_f32_16x16x32_bf16 v[36:39], a[124:127], a[4:7], v[36:39]\n"
    "s_add_u32 s67, s67, s56\n"
    "ds_read_b128 a[64:67], v3 offset:2048\n"
    "v_mov_b32 v24, 0\n"
    "v_mfma_f32_16x16x32_bf16 v[40:43], a[132:135], a[4:7], v[40:43]\n"
    "s_add_u32 m0, s67, 98304\n"
    "ds_read_b128 a[68:71], v3 offset:2560\n"
    "v_mov_b32 v25, 0\n"
    "v_mfma_f32_16x16x32_bf16 v[40:43], a[128:131], a[0:3], v[40:43]\n"
    "s_bfe_u32 s67, s19, 0x30006\n"
    "ds_read_b128 a[72:75], v3 offset:4096\n"
    "v_mov_b32 v26, 0\n"
    "v_mfma_f32_16x16x32_bf16 v[44:47], a[136:139], a[0:3], v[44:47]\n"
    "buffer_load_dwordx4 v7, s[40:43], s58 offen lds\n"
    "ds_read_b128 a[76:79], v3 offset:4608\n"
    "v_mfma_f32_16x16x32_bf16 v[44:47], a[140:143], a[4:7], v[44:47]\n"
    "s_lshl_b32 s67, s67, 12\n"
    "ds_read_b128 a[80:83], v3 offset:6144\n"
    "s_lshl4_add_u32 s58, s67, 3670016\n"
    "v_mfma_f32_16x16x32_bf16 v[48:51], a[148:151], a[4:7], v[48:51]\n"
    "s_add_u32 s67, s67, s56\n"
    "ds_read_b128 a[84:87], v3 offset:6656\n"
    "s_add_u32 m0, s67, 98304\n"
    "v_mfma_f32_16x16x32_bf16 v[48:51], a[144:147], a[0:3], v[48:51]\n"
    "s_bfe_u32 s67, s19, 0x30009\n"
    "ds_read_b128 a[88:91], v3 offset:8192\n"
    "s_lshl_b32 s67, s67, 12\n"
    "v_mfma_f32_16x16x32_bf16 v[52:55], a[152:155], a[0:3], v[52:55]\n"
    "ds_read_b128 a[92:95], v3 offset:8704\n"
    "v_mfma_f32_16x16x32_bf16 v[52:55], a[156:159], a[4:7], v[52:55]\n"
    "buffer_load_dwordx4 v7, s[40:43], s58 offen lds\n"
    "ds_read_b128 a[96:99], v3 offset:10240\n"
    "v_mfma_f32_16x16x32_bf16 v[56:59], a[164:167], a[4:7], v[56:59]\n"
    "s_lshl4_add_u32 s58, s67, 3670016\n"
    "ds_read_b128 a[100:103], v3 offset:10752\n"
    "s_add_u32 s67, s67, s56\n"
    "v_mfma_f32_16x16x32_bf16 v[56:59], a[160:163], a[0:3], v[56:59]\n"
    "s_add_u32 m0, s67, 98304\n"
    "ds_read_b128 a[104:107], v3 offset:12288\n"
    "s_bfe_u32 s67, s19, 0x3000c\n"
    "v_mfma_f32_16x16x32_bf16 v[60:63], a[168:171], a[0:3], v[60:63]\n"
    "s_lshl_b32 s67, s67, 12\n"
    "ds_read_b128 a[108:111], v3 offset:12800\n"
    "v_mfma_f32_16x16x32_bf16 v[60:63], a[172:175], a[4:7], v[60:63]\n"
    "v_mfma_f32_16x16x32_bf16 v[64:67], a[116:119], a[12:15], v[64:67]\n"
    "buffer_load_dwordx4 v7, s[40:43], s58 offen lds\n"
    "v_mfma_f32_16x16x32_bf16 v[64:67], a[112:115], a[8:11], v[64:67]\n"
    "s_lshl4_add_u32 s58, s67, 3670016\n"
    "s_add_u32 s67, s67, s56\n"
    "s_add_u32 m0, s67, 98304\n"
    "v_mfma_f32_16x16x32_bf16 v[68:71], a[120:123], a[8:11], v[68:71]\n"
    "s_bfe_u32 s67, s18, 0x30000\n"
    "s_lshl_b32 s67, s67, 12\n"
    "v_mfma_f32_16x16x32_bf16 v[68:71], a[124:127], a[12:15], v[68:71]\n"
    "v_mfma_f32_16x16x32_bf16 v[72:75], a[132:135], a[12:15], v[72:75]\n"
    "v_mfma_f32_16x16x32_bf16 v[72:75], a[128:131], a[8:11], v[72:75]\n"
    "s_waitcnt vmcnt(19) lgkmcnt(0)\n"
    "s_barrier\n"
    "ds_read_b64_tr_b16 a[176:177], v5 offset:32768\n"
    "v_mfma_f32_16x16x32_bf16 v[76:79], a[136:139], a[8:11], v[76:79]\n"
    "buffer_load_dwordx4 v7, s[40:43], s58 offen lds\n"
    "v_mfma_f32_16x16x32_bf16 v[76:79], a[140:143], a[12:15], v[76:79]\n"
    "s_lshl3_add_u32 s58, s67, 896\n"
    "ds_read_b64_tr_b16 a[178:179], v5 offset:33792\n"
    "s_add_u32 s67, s67, s56\n"
    "v_mfma_f32_16x16x32_bf16 v[80:83], a[148:151], a[12:15], v[80:83]\n"
    "s_add_u32 m0, s67, 65536\n"
    "ds_read_b64_tr_b16 a[180:181], v5 offset:49152\n"
    "s_bfe_u32 s67, s18, 0x30003\n"
    "v_mfma_f32_16x16x32_bf16 v[80:83], a[144:147], a[8:11], v[80:83]\n"
    "s_lshl_b32 s67, s67, 12\n"
    "ds_read_b64_tr_b16 a[182:183], v5 offset:50176\n"
    "v_mfma_f32_16x16x32_bf16 v[84:87], a[152:155], a[8:11], v[84:87]\n"
    "ds_read_b64_tr_b16 a[184:185], v5 offset:32776\n"
    "v_mfma_f32_16x16x32_bf16 v[84:87], a[156:159], a[12:15], v[84:87]\n"
    "buffer_load_dwordx4 v6, s[36:39], s58 offen lds\n"
    "v_mfma_f32_16x16x32_bf16 v[88:91], a[164:167], a[12:15], v[88:91]\n"
    "s_lshl3_add_u32 s58, s67, 896\n"
    "ds_read_b64_tr_b16 a[186:187], v5 offset:33800\n"
    "s_add_u32 s67, s67, s56\n"
    "v_mfma_f32_16x16x32_bf16 v[88:91], a[160:163], a[8:11], v[88:91]\n"
    "s_add_u32 m0, s67, 65536\n"
    "ds_read_b64_tr_b16 a[188:189], v5 offset:49160\n"
    "s_bfe_u32 s67, s18, 0x30006\n"
    "v_mfma_f32_16x16x32_bf16 v[92:95], a[168:171], a[8:11], v[92:95]\n"
    "s_lshl_b32 s67, s67, 12\n"
    "ds_read_b64_tr_b16 a[190:191], v5 offset:50184\n"
    "v_mfma_f32_16x16x32_bf16 v[92:95], a[172:175], a[12:15], v[92:95]\n"
    "ds_read_b64_tr_b16 a[192:193], v5 offset:33024\n"
    "v_mfma_f32_16x16x32_bf16 v[96:99], a[116:119], a[20:23], v[96:99]\n"
    "buffer_load_dwordx4 v6, s[36:39], s58 offen lds\n"
    "v_mfma_f32_16x16x32_bf16 v[96:99], a[112:115], a[16:19], v[96:99]\n"
    "s_lshl3_add_u32 s58, s67, 896\n"
    "ds_read_b64_tr_b16 a[194:195], v5 offset:34048\n"
    "s_add_u32 s67, s67, s56\n"
    "v_mfma_f32_16x16x32_bf16 v[100:103], a[120:123], a[16:19], v[100:103]\n"
    "s_add_u32 m0, s67, 65536\n"
    "ds_read_b64_tr_b16 a[196:197], v5 offset:49408\n"
    "s_bfe_u32 s67, s18, 0x30009\n"
    "v_mfma_f32_16x16x32_bf16 v[100:103], a[124:127], a[20:23], v[100:103]\n"
    "s_lshl_b32 s67, s67, 12\n"
    "ds_read_b64_tr_b16 a[198:199], v5 offset:50432\n"
    "v_mfma_f32_16x16x32_bf16 v[104:107], a[132:135], a[20:23], v[104:107]\n"
    "ds_read_b64_tr_b16 a[200:201], v5 offset:33032\n"
    "v_mfma_f32_16x16x32_bf16 v[104:107], a[128:131], a[16:19], v[104:107]\n"
    "buffer_load_dwordx4 v6, s[36:39], s58 offen lds\n"
    "v_mfma_f32_16x16x32_bf16 v[108:111], a[136:139], a[16:19], v[108:111]\n"
    "s_lshl3_add_u32 s58, s67, 896\n"
    "ds_read_b64_tr_b16 a[202:203], v5 offset:34056\n"
    "s_add_u32 s67, s67, s56\n"
    "v_mfma_f32_16x16x32_bf16 v[108:111], a[140:143], a[20:23], v[108:111]\n"
    "s_add_u32 m0, s67, 65536\n"
    "ds_read_b64_tr_b16 a[204:205], v5 offset:49416\n"
    "s_bfe_u32 s67, s18, 0x3000c\n"
    "v_mfma_f32_16x16x32_bf16 v[112:115], a[148:151], a[20:23], v[112:115]\n"
    "s_lshl_b32 s67, s67, 12\n"
    "ds_read_b64_tr_b16 a[206:207], v5 offset:50440\n"
    "v_mfma_f32_16x16x32_bf16 v[112:115], a[144:147], a[16:19], v[112:115]\n"
    "ds_read_b64_tr_b16 a[208:209], v5 offset:33280\n"
    "v_mfma_f32_16x16x32_bf16 v[116:119], a[152:155], a[16:19], v[116:119]\n"
    "buffer_load_dwordx4 v6, s[36:39], s58 offen lds\n"
    "v_mfma_f32_16x16x32_bf16 v[116:119], a[156:159], a[20:23], v[116:119]\n"
    "s_lshl3_add_u32 s58, s67, 896\n"
    "ds_read_b64_tr_b16 a[210:211], v5 offset:34304\n"
    "s_add_u32 s67, s67, s56\n"
    "v_mfma_f32_16x16x32_bf16 v[120:123], a[164:167], a[20:23], v[120:123]\n"
    "s_add_u32 m0, s67, 65536\n"
    "ds_read_b64_tr_b16 a[212:213], v5 offset:49664\n"
    "s_bfe_u32 s67, s18, 0x3000f\n"
    "v_mfma_f32_16x16x32_bf16 v[120:123], a[160:163], a[16:19], v[120:123]\n"
    "s_lshl_b32 s67, s67, 12\n"
    "ds_read_b64_tr_b16 a[214:215], v5 offset:50688\n"
    "v_mfma_f32_16x16x32_bf16 v[124:127], a[168:171], a[16:19], v[124:127]\n"
    "ds_read_b64_tr_b16 a[216:217], v5 offset:33288\n"
    "v_mfma_f32_16x16x32_bf16 v[124:127], a[172:175], a[20:23], v[124:127]\n"
    "buffer_load_dwordx4 v6, s[36:39], s58 offen lds\n"
    "v_mfma_f32_16x16x32_bf16 v[128:131], a[116:119], a[28:31], v[128:131]\n"
    "s_lshl3_add_u32 s58, s67, 896\n"
    "ds_read_b64_tr_b16 a[218:219], v5 offset:34312\n"
    "s_add_u32 s67, s67, s56\n"
    "v_mfma_f32_16x16x32_bf16 v[128:131], a[112:115], a[24:27], v[128:131]\n"
    "s_add_u32 m0, s67, 65536\n"
    "ds_read_b64_tr_b16 a[220:221], v5 offset:49672\n"
    "s_bfe_u32 s67, s18, 0x30012\n"
    "v_mfma_f32_16x16x32_bf16 v[132:135], a[120:123], a[24:27], v[132:135]\n"
    "s_lshl_b32 s67, s67, 12\n"
    "ds_read_b64_tr_b16 a[222:223], v5 offset:50696\n"
    "v_mfma_f32_16x16x32_bf16 v[132:135], a[124:127], a[28:31], v[132:135]\n"
    "ds_read_b64_tr_b16 a[224:225], v5 offset:33536\n"
    "v_mfma_f32_16x16x32_bf16 v[136:139], a[132:135], a[28:31], v[136:139]\n"
    "buffer_load_dwordx4 v6, s[36:39], s58 offen lds\n"
    "v_mfma_f32_16x16x32_bf16 v[136:139], a[128:131], a[24:27], v[136:139]\n"
    "s_lshl3_add_u32 s58, s67, 896\n"
    "ds_read_b64_tr_b16 a[226:227], v5 offset:34560\n"
    "s_add_u32 s67, s67, s56\n"
    "v_mfma_f32_16x16x32_bf16 v[140:143], a[136:139], a[24:27], v[140:143]\n"
    "s_add_u32 m0, s67, 65536\n"
    "ds_read_b64_tr_b16 a[228:229], v5 offset:49920\n"
    "s_bfe_u32 s67, s19, 0x3000f\n"
    "v_mfma_f32_16x16x32_bf16 v[140:143], a[140:143], a[28:31], v[140:143]\n"
    "s_lshl_b32 s67, s67, 12\n"
    "ds_read_b64_tr_b16 a[230:231], v5 offset:50944\n"
    "v_mfma_f32_16x16x32_bf16 v[144:147], a[148:151], a[28:31], v[144:147]\n"
    "ds_read_b64_tr_b16 a[232:233], v5 offset:33544\n"
    "v_mfma_f32_16x16x32_bf16 v[144:147], a[144:147], a[24:27], v[144:147]\n"
    "buffer_load_dwordx4 v6, s[36:39], s58 offen lds\n"
    "v_mfma_f32_16x16x32_bf16 v[148:151], a[152:155], a[24:27], v[148:151]\n"
    "s_lshl4_add_u32 s58, s67, 3670016\n"
    "ds_read_b64_tr_b16 a[234:235], v5 offset:34568\n"
    "v_bfe_u32 v6, v1, 7, 2\n"
    "v_mfma_f32_16x16x32_bf16 v[148:151], a[156:159], a[28:31], v[148:151]\n"
    "s_add_u32 s67, s67, s56\n"
    "ds_read_b64_tr_b16 a[236:237], v5 offset:49928\n"
    "s_add_u32 m0, s67, 98304\n"
    "v_mfma_f32_16x16x32_bf16 v[152:155], a[164:167], a[28:31], v[152:155]\n"
    "s_bfe_u32 s67, s19, 0x30012\n"
    "ds_read_b64_tr_b16 a[238:239], v5 offset:50952\n"
    "s_lshl_b32 s67, s67, 12\n"
    "v_mfma_f32_16x16x32_bf16 v[152:155], a[160:163], a[24:27], v[152:155]\n"
    "s_mov_b64 s[36:37], s[10:11]\n"
    "s_mov_b32 s38, 206848\n"
    "s_lshl_b32 s10, s8, 18\n"
    "v_mfma_f32_16x16x32_bf16 v[156:159], a[168:171], a[24:27], v[156:159]\n"
    "buffer_load_dwordx4 v7, s[40:43], s58 offen lds\n"
    "v_mfma_f32_16x16x32_bf16 v[156:159], a[172:175], a[28:31], v[156:159]\n"
    "s_lshl4_add_u32 s58, s67, 3670016\n"
    "s_add_u32 s67, s67, s56\n"
    "s_add_u32 m0, s67, 98304\n"
    "v_mfma_f32_16x16x32_bf16 v[160:163], a[116:119], a[36:39], v[160:163]\n"
    "s_bfe_u32 s67, s19, 0x30015\n"
    "s_lshl_b32 s67, s67, 12\n"
    "s_lshr_b32 s19, s7, 2\n"
    "v_mfma_f32_16x16x32_bf16 v[160:163], a[112:115], a[32:35], v[160:163]\n"
    "s_mul_i32 s8, s8, 59392\n"
    "s_lshl_b32 s11, s9, 18\n"
    "s_mul_i32 s9, s9, 59392\n"
    "v_mfma_f32_16x16x32_bf16 v[164:167], a[120:123], a[32:35], v[164:167]\n"
    "v_mfma_f32_16x16x32_bf16 v[164:167], a[124:127], a[36:39], v[164:167]\n"
    "buffer_load_dwordx4 v7, s[40:43], s58 offen lds\n"
    "v_mfma_f32_16x16x32_bf16 v[168:171], a[132:135], a[36:39], v[168:171]\n"
    "s_waitcnt lgkmcnt(0)\n"
    "s_barrier\n"
    "s_add_u32 m0, s57, 131072\n"
    "v_mfma_f32_16x16x32_bf16 v[168:171], a[128:131], a[32:35], v[168:171]\n"
    "s_add_u32 s58, s65, 0\n"
    "s_and_b32 s58, s58, 0x1ffff\n"
    "v_mfma_f32_16x16x32_bf16 v[172:175], a[136:139], a[32:35], v[172:175]\n"
    "v_mfma_f32_16x16x32_bf16 v[172:175], a[140:143], a[36:39], v[172:175]\n"
    "v_mfma_f32_16x16x32_bf16 v[176:179], a[148:151], a[36:39], v[176:179]\n"
    "buffer_load_dwordx4 v8, s[44:47], s58 offen nt lds\n"
    "v_mfma_f32_16x16x32_bf16 v[176:179], a[144:147], a[32:35], v[176:179]\n"
    "s_lshl4_add_u32 s58, s67, 3670016\n"
    "s_add_u32 s67, s67, s56\n"
    "s_add_u32 m0, s67, 98304\n"
    "v_mfma_f32_16x16x32_bf16 v[180:183], a[152:155], a[32:35], v[180:183]\n"
    "s_lshl1_add_u32 s67, s19, s62\n"
    "s_xor_b32 s67, s67, s61\n"
    "s_lshl_b32 s67, s67, 1\n"
    "v_mfma_f32_16x16x32_bf16 v[180:183], a[156:159], a[36:39], v[180:183]\n"
    "v_mfma_f32_16x16x32_bf16 v[184:187], a[164:167], a[36:39], v[184:187]\n"
    "v_mfma_f32_16x16x32_bf16 v[184:187], a[160:163], a[32:35], v[184:187]\n"
    "buffer_load_dwordx4 v7, s[40:43], s58 offen lds\n"
    "v_mfma_f32_16x16x32_bf16 v[188:191], a[168:171], a[32:35], v[188:191]\n"
    "s_add_u32 m0, s57, 131072\n"
    "s_add_u32 s58, s65, 0\n"
    "s_and_b32 s58, s58, 0x1ffff\n"
    "v_mfma_f32_16x16x32_bf16 v[188:191], a[172:175], a[36:39], v[188:191]\n"
    "s_mov_b64 s[40:41], s[12:13]\n"
    "s_mov_b32 s42, 916736\n"
    "s_lshl_b32 s12, s14, 18\n"
    "v_mfma_f32_16x16x32_bf16 v[192:195], a[116:119], a[44:47], v[192:195]\n"
    "s_mul_i32 s14, s14, 59392\n"
    "s_and_b32 s13, s6, 4\n"
    "s_lshl_b32 s13, s13, 10\n"
    "v_mfma_f32_16x16x32_bf16 v[192:195], a[112:115], a[40:43], v[192:195]\n"
    "v_mfma_f32_16x16x32_bf16 v[196:199], a[120:123], a[40:43], v[196:199]\n"
    "v_mfma_f32_16x16x32_bf16 v[196:199], a[124:127], a[44:47], v[196:199]\n"
    "v_mfma_f32_16x16x32_bf16 v[200:203], a[132:135], a[44:47], v[200:203]\n"
    "v_mfma_f32_16x16x32_bf16 v[200:203], a[128:131], a[40:43], v[200:203]\n"
    "buffer_load_dwordx4 v9, s[44:47], s58 offen offset:2048 nt lds\n"
    "v_mfma_f32_16x16x32_bf16 v[204:207], a[136:139], a[40:43], v[204:207]\n"
    "s_add_u32 m0, s57, 135168\n"
    "s_add_u32 s58, s58, 4096\n"
    "v_mfma_f32_16x16x32_bf16 v[204:207], a[140:143], a[44:47], v[204:207]\n"
    "v_mfma_f32_16x16x32_bf16 v[208:211], a[148:151], a[44:47], v[208:211]\n"
    "v_mfma_f32_16x16x32_bf16 v[208:211], a[144:147], a[40:43], v[208:211]\n"
    "v_mfma_f32_16x16x32_bf16 v[212:215], a[152:155], a[40:43], v[212:215]\n"
    "v_mfma_f32_16x16x32_bf16 v[212:215], a[156:159], a[44:47], v[212:215]\n"
    "v_mfma_f32_16x16x32_bf16 v[216:219], a[164:167], a[44:47], v[216:219]\n"
    "v_mfma_f32_16x16x32_bf16 v[216:219], a[160:163], a[40:43], v[216:219]\n"
    "v_mfma_f32_16x16x32_bf16 v[220:223], a[168:171], a[40:43], v[220:223]\n"
    "v_mfma_f32_16x16x32_bf16 v[220:223], a[172:175], a[44:47], v[220:223]\n"
    "v_mfma_f32_16x16x32_bf16 v[224:227], a[116:119], a[52:55], v[224:227]\n"
    "v_mfma_f32_16x16x32_bf16 v[224:227], a[112:115], a[48:51], v[224:227]\n"
    "v_mfma_f32_16x16x32_bf16 v[228:231], a[120:123], a[48:51], v[228:231]\n"
    "v_mfma_f32_16x16x32_bf16 v[228:231], a[124:127], a[52:55], v[228:231]\n"
    "v_mfma_f32_16x16x32_bf16 v[232:235], a[132:135], a[52:55], v[232:235]\n"
    "v_mfma_f32_16x16x32_bf16 v[232:235], a[128:131], a[48:51], v[232:235]\n"
    "v_mfma_f32_16x16x32_bf16 v[236:239], a[136:139], a[48:51], v[236:239]\n"
    "buffer_load_dwordx4 v8, s[44:47], s58 offen nt lds\n"
    "v_mfma_f32_16x16x32_bf16 v[236:239], a[140:143], a[52:55], v[236:239]\n"
    "v_mfma_f32_16x16x32_bf16 v[240:243], a[148:151], a[52:55], v[240:243]\n"
    "v_mfma_f32_16x16x32_bf16 v[240:243], a[144:147], a[48:51], v[240:243]\n"
    "v_mfma_f32_16x16x32_bf16 v[244:247], a[152:155], a[48:51], v[244:247]\n"
    "v_mfma_f32_16x16x32_bf16 v[244:247], a[156:159], a[52:55], v[244:247]\n"
    "v_mfma_f32_16x16x32_bf16 v[248:251], a[164:167], a[52:55], v[248:251]\n"
    "v_mfma_f32_16x16x32_bf16 v[248:251], a[160:163], a[48:51], v[248:251]\n"
    "v_mfma_f32_16x16x32_bf16 v[252:255], a[168:171], a[48:51], v[252:255]\n"
    "v_mfma_f32_16x16x32_bf16 v[252:255], a[172:175], a[52:55], v[252:255]\n"
    "v_mfma_f32_16x16x32_bf16 v[32:35], a[180:183], a[60:63], v[32:35]\n"
    "s_waitcnt vmcnt(21)\n"
    "s_barrier\n"
    "ds_read_b128 a[0:3], v2 offset:0\n"
    "v_mfma_f32_16x16x32_bf16 v[32:35], a[176:179], a[56:59], v[32:35]\n"
    "ds_read_b128 a[4:7], v2 offset:512\n"
    "v_mfma_f32_16x16x32_bf16 v[36:39], a[184:187], a[56:59], v[36:39]\n"
    "ds_read_b128 a[8:11], v2 offset:2048\n"
    "v_mfma_f32_16x16x32_bf16 v[36:39], a[188:191], a[60:63], v[36:39]\n"
    "ds_read_b128 a[12:15], v2 offset:2560\n"
    "v_mfma_f32_16x16x32_bf16 v[40:43], a[196:199], a[60:63], v[40:43]\n"
    "ds_read_b128 a[16:19], v2 offset:4096\n"
    "v_mfma_f32_16x16x32_bf16 v[40:43], a[192:195], a[56:59], v[40:43]\n"
    "ds_read_b128 a[20:23], v2 offset:4608\n"
    "v_mfma_f32_16x16x32_bf16 v[44:47], a[200:203], a[56:59], v[44:47]\n"
    "ds_read_b128 a[24:27], v2 offset:6144\n"
    "v_mfma_f32_16x16x32_bf16 v[44:47], a[204:207], a[60:63], v[44:47]\n"
    "buffer_load_dwordx4 v9, s[44:47], s58 offen offset:2048 nt lds\n"
    "v_mfma_f32_16x16x32_bf16 v[48:51], a[212:215], a[60:63], v[48:51]\n"
    "s_add_u32 m0, s57, 139264\n"
    "ds_read_b128 a[28:31], v2 offset:6656\n"
    "s_add_u32 s58, s65, 0\n"
    "v_mfma_f32_16x16x32_bf16 v[48:51], a[208:211], a[56:59], v[48:51]\n"
    "s_and_b32 s58, s58, 0x1ffff\n"
    "ds_read_b128 a[32:35], v2 offset:8192\n"
    "v_mfma_f32_16x16x32_bf16 v[52:55], a[216:219], a[56:59], v[52:55]\n"
    "ds_read_b128 a[36:39], v2 offset:8704\n"
    "v_mfma_f32_16x16x32_bf16 v[52:55], a[220:223], a[60:63], v[52:55]\n"
    "ds_read_b128 a[40:43], v2 offset:10240\n"
    "v_mfma_f32_16x16x32_bf16 v[56:59], a[228:231], a[60:63], v[56:59]\n"
    "ds_read_b128 a[44:47], v2 offset:10752\n"
    "v_mfma_f32_16x16x32_bf16 v[56:59], a[224:227], a[56:59], v[56:59]\n"
    "ds_read_b128 a[48:51], v2 offset:12288\n"
    "v_mfma_f32_16x16x32_bf16 v[60:63], a[232:235], a[56:59], v[60:63]\n"
    "ds_read_b128 a[52:55], v2 offset:12800\n"
    "v_bfe_u32 v2, v1, 10, 1\n"
    "v_mfma_f32_16x16x32_bf16 v[60:63], a[236:239], a[60:63], v[60:63]\n"
    "v_lshl_add_u32 v2, v2, 1, 12\n"
    "v_lshlrev_b32 v2, v2, v6\n"
    "v_mfma_f32_16x16x32_bf16 v[64:67], a[180:183], a[68:71], v[64:67]\n"
    "v_mfma_f32_16x16x32_bf16 v[64:67], a[176:179], a[64:67], v[64:67]\n"
    "v_mfma_f32_16x16x32_bf16 v[68:71], a[184:187], a[64:67], v[68:71]\n"
    "v_mfma_f32_16x16x32_bf16 v[68:71], a[188:191], a[68:71], v[68:71]\n"
    "v_mfma_f32_16x16x32_bf16 v[72:75], a[196:199], a[68:71], v[72:75]\n"
    "v_mfma_f32_16x16x32_bf16 v[72:75], a[192:195], a[64:67], v[72:75]\n"
    "s_waitcnt vmcnt(19) lgkmcnt(0)\n"
    "s_barrier\n"
    "ds_read_b64_tr_b16 a[112:113], v4 offset:0\n"
    "v_mfma_f32_16x16x32_bf16 v[76:79], a[200:203], a[64:67], v[76:79]\n"
    "ds_read_b64_tr_b16 a[114:115], v4 offset:1024\n"
    "v_mfma_f32_16x16x32_bf16 v[76:79], a[204:207], a[68:71], v[76:79]\n"
    "buffer_load_dwordx4 v10, s[48:51], s58 offen nt lds\n"
    "v_mfma_f32_16x16x32_bf16 v[80:83], a[212:215], a[68:71], v[80:83]\n"
    "ds_read_b64_tr_b16 a[116:117], v4 offset:16384\n"
    "v_mfma_f32_16x16x32_bf16 v[80:83], a[208:211], a[64:67], v[80:83]\n"
    "ds_read_b64_tr_b16 a[118:119], v4 offset:17408\n"
    "v_mfma_f32_16x16x32_bf16 v[84:87], a[216:219], a[64:67], v[84:87]\n"
    "ds_read_b64_tr_b16 a[120:121], v4 offset:8\n"
    "v_mfma_f32_16x16x32_bf16 v[84:87], a[220:223], a[68:71], v[84:87]\n"
    "ds_read_b64_tr_b16 a[122:123], v4 offset:1032\n"
    "v_mfma_f32_16x16x32_bf16 v[88:91], a[228:231], a[68:71], v[88:91]\n"
    "ds_read_b64_tr_b16 a[124:125], v4 offset:16392\n"
    "v_mfma_f32_16x16x32_bf16 v[88:91], a[224:227], a[64:67], v[88:91]\n"
    "ds_read_b64_tr_b16 a[126:127], v4 offset:17416\n"
    "v_mfma_f32_16x16x32_bf16 v[92:95], a[232:235], a[64:67], v[92:95]\n"
    "ds_read_b64_tr_b16 a[128:129], v4 offset:256\n"
    "v_mfma_f32_16x16x32_bf16 v[92:95], a[236:239], a[68:71], v[92:95]\n"
    "ds_read_b64_tr_b16 a[130:131], v4 offset:1280\n"
    "v_mfma_f32_16x16x32_bf16 v[96:99], a[180:183], a[76:79], v[96:99]\n"
    "ds_read_b64_tr_b16 a[132:133], v4 offset:16640\n"
    "v_mfma_f32_16x16x32_bf16 v[96:99], a[176:179], a[72:75], v[96:99]\n"
    "ds_read_b64_tr_b16 a[134:135], v4 offset:17664\n"
    "v_mfma_f32_16x16x32_bf16 v[100:103], a[184:187], a[72:75], v[100:103]\n"
    "ds_read_b64_tr_b16 a[136:137], v4 offset:264\n"
    "v_mfma_f32_16x16x32_bf16 v[100:103], a[188:191], a[76:79], v[100:103]\n"
    "ds_read_b64_tr_b16 a[138:139], v4 offset:1288\n"
    "v_mfma_f32_16x16x32_bf16 v[104:107], a[196:199], a[76:79], v[104:107]\n"
    "ds_read_b64_tr_b16 a[140:141], v4 offset:16648\n"
    "v_mfma_f32_16x16x32_bf16 v[104:107], a[192:195], a[72:75], v[104:107]\n"
    "ds_read_b64_tr_b16 a[142:143], v4 offset:17672\n"
    "v_mfma_f32_16x16x32_bf16 v[108:111], a[200:203], a[72:75], v[108:111]\n"
    "ds_read_b64_tr_b16 a[144:145], v4 offset:512\n"
    "v_mfma_f32_16x16x32_bf16 v[108:111], a[204:207], a[76:79], v[108:111]\n"
    "ds_read_b64_tr_b16 a[146:147], v4 offset:1536\n"
    "v_mfma_f32_16x16x32_bf16 v[112:115], a[212:215], a[76:79], v[112:115]\n"
    "buffer_store_dword v30, v29, s[72:75], 0 offen offset:128 sc1\n"
    "v_mfma_f32_16x16x32_bf16 v[112:115], a[208:211], a[72:75], v[112:115]\n"
    "ds_read_b64_tr_b16 a[148:149], v4 offset:16896\n"
    "v_mfma_f32_16x16x32_bf16 v[116:119], a[216:219], a[72:75], v[116:119]\n"
    "ds_read_b64_tr_b16 a[150:151], v4 offset:17920\n"
    "v_mfma_f32_16x16x32_bf16 v[116:119], a[220:223], a[76:79], v[116:119]\n"
    "ds_read_b64_tr_b16 a[152:153], v4 offset:520\n"
    "v_mfma_f32_16x16x32_bf16 v[120:123], a[228:231], a[76:79], v[120:123]\n"
    "ds_read_b64_tr_b16 a[154:155], v4 offset:1544\n"
    "v_mfma_f32_16x16x32_bf16 v[120:123], a[224:227], a[72:75], v[120:123]\n"
    "buffer_load_dwordx4 v10, s[48:51], s58 offen offset:2048 nt lds\n"
    "v_mfma_f32_16x16x32_bf16 v[124:127], a[232:235], a[72:75], v[124:127]\n"
    "s_add_u32 m0, s57, 143360\n"
    "ds_read_b64_tr_b16 a[156:157], v4 offset:16904\n"
    "s_add_u32 s58, s58, 4096\n"
    "v_mfma_f32_16x16x32_bf16 v[124:127], a[236:239], a[76:79], v[124:127]\n"
    "ds_read_b64_tr_b16 a[158:159], v4 offset:17928\n"
    "v_mfma_f32_16x16x32_bf16 v[128:131], a[180:183], a[84:87], v[128:131]\n"
    "ds_read_b64_tr_b16 a[160:161], v4 offset:768\n"
    "v_mfma_f32_16x16x32_bf16 v[128:131], a[176:179], a[80:83], v[128:131]\n"
    "ds_read_b64_tr_b16 a[162:163], v4 offset:1792\n"
    "v_mfma_f32_16x16x32_bf16 v[132:135], a[184:187], a[80:83], v[132:135]\n"
    "ds_read_b64_tr_b16 a[164:165], v4 offset:17152\n"
    "v_mfma_f32_16x16x32_bf16 v[132:135], a[188:191], a[84:87], v[132:135]\n"
    "ds_read_b64_tr_b16 a[166:167], v4 offset:18176\n"
    "v_mfma_f32_16x16x32_bf16 v[136:139], a[196:199], a[84:87], v[136:139]\n"
    "ds_read_b64_tr_b16 a[168:169], v4 offset:776\n"
    "v_mfma_f32_16x16x32_bf16 v[136:139], a[192:195], a[80:83], v[136:139]\n"
    "ds_read_b64_tr_b16 a[170:171], v4 offset:1800\n"
    "v_mfma_f32_16x16x32_bf16 v[140:143], a[200:203], a[80:83], v[140:143]\n"
    "ds_read_b64_tr_b16 a[172:173], v4 offset:17160\n"
    "v_mfma_f32_16x16x32_bf16 v[140:143], a[204:207], a[84:87], v[140:143]\n"
    "ds_read_b64_tr_b16 a[174:175], v4 offset:18184\n"
    "v_lshrrev_b32 v4, 11, v1\n"
    "v_mfma_f32_16x16x32_bf16 v[144:147], a[212:215], a[84:87], v[144:147]\n"
    "v_mfma_f32_16x16x32_bf16 v[144:147], a[208:211], a[80:83], v[144:147]\n"
    "buffer_load_dwordx4 v10, s[48:51], s58 offen nt lds\n"
    "v_mfma_f32_16x16x32_bf16 v[148:151], a[216:219], a[80:83], v[148:151]\n"
    "v_mfma_f32_16x16x32_bf16 v[148:151], a[220:223], a[84:87], v[148:151]\n"
    "v_mfma_f32_16x16x32_bf16 v[152:155], a[228:231], a[84:87], v[152:155]\n"
    "v_mfma_f32_16x16x32_bf16 v[152:155], a[224:227], a[80:83], v[152:155]\n"
    "v_mfma_f32_16x16x32_bf16 v[156:159], a[232:235], a[80:83], v[156:159]\n"
    "v_mfma_f32_16x16x32_bf16 v[156:159], a[236:239], a[84:87], v[156:159]\n"
    "v_mfma_f32_16x16x32_bf16 v[160:163], a[180:183], a[92:95], v[160:163]\n"
    "v_mfma_f32_16x16x32_bf16 v[160:163], a[176:179], a[88:91], v[160:163]\n"
    "v_mfma_f32_16x16x32_bf16 v[164:167], a[184:187], a[88:91], v[164:167]\n"
    "v_mfma_f32_16x16x32_bf16 v[164:167], a[188:191], a[92:95], v[164:167]\n"
    "v_mfma_f32_16x16x32_bf16 v[168:171], a[196:199], a[92:95], v[168:171]\n"
    "v_mfma_f32_16x16x32_bf16 v[168:171], a[192:195], a[88:91], v[168:171]\n"
    "v_mfma_f32_16x16x32_bf16 v[172:175], a[200:203], a[88:91], v[172:175]\n"
    "v_mfma_f32_16x16x32_bf16 v[172:175], a[204:207], a[92:95], v[172:175]\n"
    "v_mfma_f32_16x16x32_bf16 v[176:179], a[212:215], a[92:95], v[176:179]\n"
    "v_mfma_f32_16x16x32_bf16 v[176:179], a[208:211], a[88:91], v[176:179]\n"
    "buffer_load_dwordx4 v10, s[48:51], s58 offen offset:2048 nt lds\n"
    "v_mfma_f32_16x16x32_bf16 v[180:183], a[216:219], a[88:91], v[180:183]\n"
    "s_add_u32 m0, s57, 0\n"
    "s_add_u32 s58, s65, 8192\n"
    "s_and_b32 s58, s58, 0x1ffff\n"
    "v_mfma_f32_16x16x32_bf16 v[180:183], a[220:223], a[92:95], v[180:183]\n"
    "v_mfma_f32_16x16x32_bf16 v[184:187], a[228:231], a[92:95], v[184:187]\n"
    "v_mfma_f32_16x16x32_bf16 v[184:187], a[224:227], a[88:91], v[184:187]\n"
    "v_mfma_f32_16x16x32_bf16 v[188:191], a[232:235], a[88:91], v[188:191]\n"
    "v_mfma_f32_16x16x32_bf16 v[188:191], a[236:239], a[92:95], v[188:191]\n"
    "v_mfma_f32_16x16x32_bf16 v[192:195], a[180:183], a[100:103], v[192:195]\n"
    "v_mfma_f32_16x16x32_bf16 v[192:195], a[176:179], a[96:99], v[192:195]\n"
    "v_mfma_f32_16x16x32_bf16 v[196:199], a[184:187], a[96:99], v[196:199]\n"
    "v_mfma_f32_16x16x32_bf16 v[196:199], a[188:191], a[100:103], v[196:199]\n"
    "v_mfma_f32_16x16x32_bf16 v[200:203], a[196:199], a[100:103], v[200:203]\n"
    "v_mfma_f32_16x16x32_bf16 v[200:203], a[192:195], a[96:99], v[200:203]\n"
    "v_mfma_f32_16x16x32_bf16 v[204:207], a[200:203], a[96:99], v[204:207]\n"
    "v_mfma_f32_16x16x32_bf16 v[204:207], a[204:207], a[100:103], v[204:207]\n"
    "v_mfma_f32_16x16x32_bf16 v[208:211], a[212:215], a[100:103], v[208:211]\n"
    "s_waitcnt vmcnt(5)\n"
    "v_mfma_f32_16x16x32_bf16 v[208:211], a[208:211], a[96:99], v[208:211]\n"
    "v_mfma_f32_16x16x32_bf16 v[212:215], a[216:219], a[96:99], v[212:215]\n"
    "buffer_load_dwordx4 v8, s[44:47], s58 offen nt lds\n"
    "v_mfma_f32_16x16x32_bf16 v[212:215], a[220:223], a[100:103], v[212:215]\n"
    "v_mfma_f32_16x16x32_bf16 v[216:219], a[228:231], a[100:103], v[216:219]\n"
    "v_mfma_f32_16x16x32_bf16 v[216:219], a[224:227], a[96:99], v[216:219]\n"
    "v_mfma_f32_16x16x32_bf16 v[220:223], a[232:235], a[96:99], v[220:223]\n"
    "v_mfma_f32_16x16x32_bf16 v[220:223], a[236:239], a[100:103], v[220:223]\n"
    "v_mfma_f32_16x16x32_bf16 v[224:227], a[180:183], a[108:111], v[224:227]\n"
    "v_mfma_f32_16x16x32_bf16 v[224:227], a[176:179], a[104:107], v[224:227]\n"
    "v_mfma_f32_16x16x32_bf16 v[228:231], a[184:187], a[104:107], v[228:231]\n"
    "v_mfma_f32_16x16x32_bf16 v[228:231], a[188:191], a[108:111], v[228:231]\n"
    "v_mfma_f32_16x16x32_bf16 v[232:235], a[196:199], a[108:111], v[232:235]\n"
    "v_mfma_f32_16x16x32_bf16 v[232:235], a[192:195], a[104:107], v[232:235]\n"
    "v_mfma_f32_16x16x32_bf16 v[236:239], a[200:203], a[104:107], v[236:239]\n"
    "v_mfma_f32_16x16x32_bf16 v[236:239], a[204:207], a[108:111], v[236:239]\n"
    "v_mfma_f32_16x16x32_bf16 v[240:243], a[212:215], a[108:111], v[240:243]\n"
    "v_mfma_f32_16x16x32_bf16 v[240:243], a[208:211], a[104:107], v[240:243]\n"
    "s_waitcnt vmcnt(5)\n"
    "v_mfma_f32_16x16x32_bf16 v[244:247], a[216:219], a[104:107], v[244:247]\n"
    "v_mfma_f32_16x16x32_bf16 v[244:247], a[220:223], a[108:111], v[244:247]\n"
    "buffer_load_dwordx4 v9, s[44:47], s58 offen offset:2048 nt lds\n"
    "v_mfma_f32_16x16x32_bf16 v[248:251], a[228:231], a[108:111], v[248:251]\n"
    "s_add_u32 m0, s57, 4096\n"
    "s_add_u32 s58, s58, 4096\n"
    "v_mfma_f32_16x16x32_bf16 v[248:251], a[224:227], a[104:107], v[248:251]\n"
    "v_mfma_f32_16x16x32_bf16 v[252:255], a[232:235], a[104:107], v[252:255]\n"
    "v_mfma_f32_16x16x32_bf16 v[252:255], a[236:239], a[108:111], v[252:255]\n"
    "s_waitcnt lgkmcnt(0)\n"
    "v_mfma_f32_16x16x32_bf16 v[128:131], a[116:119], a[28:31], v[128:131]\n"
    "s_waitcnt vmcnt(13)\n"
    "s_barrier\n"
    "ds_read_b128 a[56:59], v3 offset:0\n"
    "v_mfma_f32_16x16x32_bf16 v[128:131], a[112:115], a[24:27], v[128:131]\n"
    "ds_read_b128 a[60:63], v3 offset:512\n"
    "v_mfma_f32_16x16x32_bf16 v[132:135], a[120:123], a[24:27], v[132:135]\n"
    "ds_read_b128 a[64:67], v3 offset:2048\n"
    "v_mfma_f32_16x16x32_bf16 v[132:135], a[124:127], a[28:31], v[132:135]\n"
    "ds_read_b128 a[68:71], v3 offset:2560\n"
    "v_mfma_f32_16x16x32_bf16 v[136:139], a[132:135], a[28:31], v[136:139]\n"
    "ds_read_b128 a[72:75], v3 offset:4096\n"
    "v_mfma_f32_16x16x32_bf16 v[136:139], a[128:131], a[24:27], v[136:139]\n"
    "ds_read_b128 a[76:79], v3 offset:4608\n"
    "v_mfma_f32_16x16x32_bf16 v[140:143], a[136:139], a[24:27], v[140:143]\n"
    "ds_read_b128 a[80:83], v3 offset:6144\n"
    "v_mfma_f32_16x16x32_bf16 v[140:143], a[140:143], a[28:31], v[140:143]\n"
    "ds_read_b128 a[84:87], v3 offset:6656\n"
    "v_mfma_f32_16x16x32_bf16 v[144:147], a[148:151], a[28:31], v[144:147]\n"
    "ds_read_b128 a[88:91], v3 offset:8192\n"
    "v_mfma_f32_16x16x32_bf16 v[144:147], a[144:147], a[24:27], v[144:147]\n"
    "ds_read_b128 a[92:95], v3 offset:8704\n"
    "v_mfma_f32_16x16x32_bf16 v[148:151], a[152:155], a[24:27], v[148:151]\n"
    "s_waitcnt vmcnt(5)\n"
    "ds_read_b128 a[96:99], v3 offset:10240\n"
    "v_mfma_f32_16x16x32_bf16 v[148:151], a[156:159], a[28:31], v[148:151]\n"
    "ds_read_b128 a[100:103], v3 offset:10752\n"
    "v_mfma_f32_16x16x32_bf16 v[152:155], a[164:167], a[28:31], v[152:155]\n"
    "buffer_load_dwordx4 v8, s[44:47], s58 offen nt lds\n"
    "v_mfma_f32_16x16x32_bf16 v[152:155], a[160:163], a[24:27], v[152:155]\n"
    "ds_read_b128 a[104:107], v3 offset:12288\n"
    "v_mfma_f32_16x16x32_bf16 v[156:159], a[168:171], a[24:27], v[156:159]\n"
    "ds_read_b128 a[108:111], v3 offset:12800\n"
    "v_bfe_u32 v3, v1, 9, 2\n"
    "v_mfma_f32_16x16x32_bf16 v[156:159], a[172:175], a[28:31], v[156:159]\n"
    "v_lshlrev_b32 v3, 10, v3\n"
    "v_lshl_add_u32 v3, v6, 2, v3\n"
    "v_mfma_f32_16x16x32_bf16 v[160:163], a[116:119], a[36:39], v[160:163]\n"
    "v_add_u32 v3, s34, v3\n"
    "v_lshrrev_b32 v6, 7, v1\n"
    "v_mfma_f32_16x16x32_bf16 v[160:163], a[112:115], a[32:35], v[160:163]\n"
    "s_mov_b32 s34, 0xfffff111\n"
    "v_min_u32 v6, 16, v6\n"
    "v_lshrrev_b32 v6, v6, s34\n"
    "v_mfma_f32_16x16x32_bf16 v[164:167], a[120:123], a[32:35], v[164:167]\n"
    "v_and_b32 v6, 1, v6\n"
    "v_lshl_add_u32 v3, v6, 22, v3\n"
    "v_mfma_f32_16x16x32_bf16 v[164:167], a[124:127], a[36:39], v[164:167]\n"
    "v_xor_b32 v2, v2, v3\n"
    "v_lshrrev_b32 v6, 2, v1\n"
    "v_mfma_f32_16x16x32_bf16 v[168:171], a[132:135], a[36:39], v[168:171]\n"
    "v_mfma_f32_16x16x32_bf16 v[168:171], a[128:131], a[32:35], v[168:171]\n"
    "s_waitcnt vmcnt(10) lgkmcnt(0)\n"
    "s_barrier\n"
    "ds_read_b64_tr_b16 a[176:177], v5 offset:0\n"
    "v_mfma_f32_16x16x32_bf16 v[172:175], a[136:139], a[32:35], v[172:175]\n"
    "ds_read_b64_tr_b16 a[178:179], v5 offset:1024\n"
    "v_mfma_f32_16x16x32_bf16 v[172:175], a[140:143], a[36:39], v[172:175]\n"
    "ds_read_b64_tr_b16 a[180:181], v5 offset:16384\n"
    "v_mfma_f32_16x16x32_bf16 v[176:179], a[148:151], a[36:39], v[176:179]\n"
    "ds_read_b64_tr_b16 a[182:183], v5 offset:17408\n"
    "v_mfma_f32_16x16x32_bf16 v[176:179], a[144:147], a[32:35], v[176:179]\n"
    "ds_read_b64_tr_b16 a[184:185], v5 offset:8\n"
    "v_mfma_f32_16x16x32_bf16 v[180:183], a[152:155], a[32:35], v[180:183]\n"
    "s_waitcnt vmcnt(5)\n"
    "ds_read_b64_tr_b16 a[186:187], v5 offset:1032\n"
    "v_mfma_f32_16x16x32_bf16 v[180:183], a[156:159], a[36:39], v[180:183]\n"
    "ds_read_b64_tr_b16 a[188:189], v5 offset:16392\n"
    "v_mfma_f32_16x16x32_bf16 v[184:187], a[164:167], a[36:39], v[184:187]\n"
    "buffer_load_dwordx4 v9, s[44:47], s58 offen offset:2048 nt lds\n"
    "v_mfma_f32_16x16x32_bf16 v[184:187], a[160:163], a[32:35], v[184:187]\n"
    "s_add_u32 m0, s57, 8192\n"
    "ds_read_b64_tr_b16 a[190:191], v5 offset:17416\n"
    "s_add_u32 s58, s65, 8192\n"
    "v_mfma_f32_16x16x32_bf16 v[188:191], a[168:171], a[32:35], v[188:191]\n"
    "s_and_b32 s58, s58, 0x1ffff\n"
    "ds_read_b64_tr_b16 a[192:193], v5 offset:256\n"
    "v_mfma_f32_16x16x32_bf16 v[188:191], a[172:175], a[36:39], v[188:191]\n"
    "ds_read_b64_tr_b16 a[194:195], v5 offset:1280\n"
    "v_mfma_f32_16x16x32_bf16 v[192:195], a[116:119], a[44:47], v[192:195]\n"
    "ds_read_b64_tr_b16 a[196:197], v5 offset:16640\n"
    "v_mfma_f32_16x16x32_bf16 v[192:195], a[112:115], a[40:43], v[192:195]\n"
    "ds_read_b64_tr_b16 a[198:199], v5 offset:17664\n"
    "v_mfma_f32_16x16x32_bf16 v[196:199], a[120:123], a[40:43], v[196:199]\n"
    "ds_read_b64_tr_b16 a[200:201], v5 offset:264\n"
    "v_mfma_f32_16x16x32_bf16 v[196:199], a[124:127], a[44:47], v[196:199]\n"
    "ds_read_b64_tr_b16 a[202:203], v5 offset:1288\n"
    "v_mfma_f32_16x16x32_bf16 v[200:203], a[132:135], a[44:47], v[200:203]\n"
    "ds_read_b64_tr_b16 a[204:205], v5 offset:16648\n"
    "v_mfma_f32_16x16x32_bf16 v[200:203], a[128:131], a[40:43], v[200:203]\n"
    "ds_read_b64_tr_b16 a[206:207], v5 offset:17672\n"
    "v_mfma_f32_16x16x32_bf16 v[204:207], a[136:139], a[40:43], v[204:207]\n"
    "ds_read_b64_tr_b16 a[208:209], v5 offset:512\n"
    "v_mfma_f32_16x16x32_bf16 v[204:207], a[140:143], a[44:47], v[204:207]\n"
    "ds_read_b64_tr_b16 a[210:211], v5 offset:1536\n"
    "v_mfma_f32_16x16x32_bf16 v[208:211], a[148:151], a[44:47], v[208:211]\n"
    "ds_read_b64_tr_b16 a[212:213], v5 offset:16896\n"
    "v_mfma_f32_16x16x32_bf16 v[208:211], a[144:147], a[40:43], v[208:211]\n"
    "ds_read_b64_tr_b16 a[214:215], v5 offset:17920\n"
    "v_mfma_f32_16x16x32_bf16 v[212:215], a[152:155], a[40:43], v[212:215]\n"
    "ds_read_b64_tr_b16 a[216:217], v5 offset:520\n"
    "v_mfma_f32_16x16x32_bf16 v[212:215], a[156:159], a[44:47], v[212:215]\n"
    "s_waitcnt vmcnt(5)\n"
    "ds_read_b64_tr_b16 a[218:219], v5 offset:1544\n"
    "v_mfma_f32_16x16x32_bf16 v[216:219], a[164:167], a[44:47], v[216:219]\n"
    "ds_read_b64_tr_b16 a[220:221], v5 offset:16904\n"
    "v_mfma_f32_16x16x32_bf16 v[216:219], a[160:163], a[40:43], v[216:219]\n"
    "buffer_load_dwordx4 v10, s[48:51], s58 offen nt lds\n"
    "v_mfma_f32_16x16x32_bf16 v[220:223], a[168:171], a[40:43], v[220:223]\n"
    "ds_read_b64_tr_b16 a[222:223], v5 offset:17928\n"
    "v_mfma_f32_16x16x32_bf16 v[220:223], a[172:175], a[44:47], v[220:223]\n"
    "ds_read_b64_tr_b16 a[224:225], v5 offset:768\n"
    "v_mfma_f32_16x16x32_bf16 v[224:227], a[116:119], a[52:55], v[224:227]\n"
    "ds_read_b64_tr_b16 a[226:227], v5 offset:1792\n"
    "v_mfma_f32_16x16x32_bf16 v[224:227], a[112:115], a[48:51], v[224:227]\n"
    "ds_read_b64_tr_b16 a[228:229], v5 offset:17152\n"
    "v_mfma_f32_16x16x32_bf16 v[228:231], a[120:123], a[48:51], v[228:231]\n"
    "ds_read_b64_tr_b16 a[230:231], v5 offset:18176\n"
    "v_mfma_f32_16x16x32_bf16 v[228:231], a[124:127], a[52:55], v[228:231]\n"
    "ds_read_b64_tr_b16 a[232:233], v5 offset:776\n"
    "v_mfma_f32_16x16x32_bf16 v[232:235], a[132:135], a[52:55], v[232:235]\n"
    "ds_read_b64_tr_b16 a[234:235], v5 offset:1800\n"
    "v_mfma_f32_16x16x32_bf16 v[232:235], a[128:131], a[48:51], v[232:235]\n"
    "ds_read_b64_tr_b16 a[236:237], v5 offset:17160\n"
    "v_mfma_f32_16x16x32_bf16 v[236:239], a[136:139], a[48:51], v[236:239]\n"
    "ds_read_b64_tr_b16 a[238:239], v5 offset:18184\n"
    "v_bfe_u32 v5, v1, 7, 2\n"
    "v_mfma_f32_16x16x32_bf16 v[236:239], a[140:143], a[52:55], v[236:239]\n"
    "v_lshl_add_u32 v4, v4, 2, v5\n"
    "v_bfe_u32 v5, v1, 9, 2\n"
    "v_mfma_f32_16x16x32_bf16 v[240:243], a[148:151], a[52:55], v[240:243]\n"
    "v_xor_b32 v4, s67, v4\n"
    "v_lshlrev_b32 v7, 10, v5\n"
    "v_mfma_f32_16x16x32_bf16 v[240:243], a[144:147], a[48:51], v[240:243]\n"
    "s_lshl_b32 s67, s61, 12\n"
    "v_lshl_add_u32 v7, v4, 4, v7\n"
    "v_add_u32 v7, s67, v7\n"
    "v_mfma_f32_16x16x32_bf16 v[244:247], a[152:155], a[48:51], v[244:247]\n"
    "v_mfma_f32_16x16x32_bf16 v[244:247], a[156:159], a[52:55], v[244:247]\n"
    "v_mfma_f32_16x16x32_bf16 v[248:251], a[164:167], a[52:55], v[248:251]\n"
    "s_waitcnt vmcnt(5)\n"
    "v_mfma_f32_16x16x32_bf16 v[248:251], a[160:163], a[48:51], v[248:251]\n"
    "v_mfma_f32_16x16x32_bf16 v[252:255], a[168:171], a[48:51], v[252:255]\n"
    "buffer_load_dwordx4 v10, s[48:51], s58 offen offset:2048 nt lds\n"
    "v_mfma_f32_16x16x32_bf16 v[252:255], a[172:175], a[52:55], v[252:255]\n"
    "s_add_u32 m0, s57, 12288\n"
    "s_add_u32 s58, s58, 4096\n"
    "v_mfma_f32_16x16x32_bf16 v[32:35], a[116:119], a[4:7], v[32:35]\n"
    "v_mfma_f32_16x16x32_bf16 v[32:35], a[112:115], a[0:3], v[32:35]\n"
    "s_waitcnt lgkmcnt(1)\n"
    "v_mfma_f32_16x16x32_bf16 v[32:35], a[176:179], a[56:59], v[32:35]\n"
    "v_mfma_f32_16x16x32_bf16 v[32:35], a[180:183], a[60:63], v[32:35]\n"
    "v_mfma_f32_16x16x32_bf16 v[36:39], a[124:127], a[4:7], v[36:39]\n"
    "v_mfma_f32_16x16x32_bf16 v[36:39], a[120:123], a[0:3], v[36:39]\n"
    "v_mfma_f32_16x16x32_bf16 v[36:39], a[184:187], a[56:59], v[36:39]\n"
    "v_mfma_f32_16x16x32_bf16 v[36:39], a[188:191], a[60:63], v[36:39]\n"
    "v_mfma_f32_16x16x32_bf16 v[40:43], a[132:135], a[4:7], v[40:43]\n"
    "v_mfma_f32_16x16x32_bf16 v[40:43], a[128:131], a[0:3], v[40:43]\n"
    "v_mfma_f32_16x16x32_bf16 v[40:43], a[192:195], a[56:59], v[40:43]\n"
    "v_mfma_f32_16x16x32_bf16 v[40:43], a[196:199], a[60:63], v[40:43]\n"
    "v_mul_f32 v32, s59, v32\n"
    "v_mul_f32 v33, s59, v33\n"
    "v_mfma_f32_16x16x32_bf16 v[44:47], a[140:143], a[4:7], v[44:47]\n"
    "s_waitcnt vmcnt(5)\n"
    "v_mul_f32 v34, s59, v34\n"
    "v_mul_f32 v35, s59, v35\n"
    "v_mfma_f32_16x16x32_bf16 v[44:47], a[136:139], a[0:3], v[44:47]\n"
    "v_cvt_pk_bf16_f32 v32, v32, v33\n"
    "v_cvt_pk_bf16_f32 v33, v34, v35\n"
    "v_mfma_f32_16x16x32_bf16 v[44:47], a[200:203], a[56:59], v[44:47]\n"
    "buffer_load_dwordx4 v10, s[48:51], s58 offen nt lds\n"
    "v_mfma_f32_16x16x32_bf16 v[44:47], a[204:207], a[60:63], v[44:47]\n"
    "v_mul_f32 v38, s59, v38\n"
    "v_mul_f32 v39, s59, v39\n"
    "v_mfma_f32_16x16x32_bf16 v[48:51], a[148:151], a[4:7], v[48:51]\n"
    "v_cvt_pk_bf16_f32 v35, v38, v39\n"
    "v_mul_f32 v36, s59, v36\n"
    "v_mfma_f32_16x16x32_bf16 v[48:51], a[144:147], a[0:3], v[48:51]\n"
    "v_mul_f32 v37, s59, v37\n"
    "v_cvt_pk_bf16_f32 v34, v36, v37\n"
    "v_mfma_f32_16x16x32_bf16 v[48:51], a[208:211], a[56:59], v[48:51]\n"
    "v_mul_f32 v46, s59, v46\n"
    "v_mul_f32 v47, s59, v47\n"
    "v_mfma_f32_16x16x32_bf16 v[48:51], a[212:215], a[60:63], v[48:51]\n"
    "v_cvt_pk_bf16_f32 v39, v46, v47\n"
    "v_mul_f32 v40, s59, v40\n"
    "v_mfma_f32_16x16x32_bf16 v[52:55], a[156:159], a[4:7], v[52:55]\n"
    "v_mul_f32 v41, s59, v41\n"
    "v_mul_f32 v42, s59, v42\n"
    "v_mfma_f32_16x16x32_bf16 v[52:55], a[152:155], a[0:3], v[52:55]\n"
    "v_mul_f32 v43, s59, v43\n"
    "v_mul_f32 v44, s59, v44\n"
    "v_mfma_f32_16x16x32_bf16 v[52:55], a[216:219], a[56:59], v[52:55]\n"
    "v_mul_f32 v45, s59, v45\n"
    "v_cvt_pk_bf16_f32 v36, v40, v41\n"
    "v_mfma_f32_16x16x32_bf16 v[52:55], a[220:223], a[60:63], v[52:55]\n"
    "v_cvt_pk_bf16_f32 v37, v42, v43\n"
    "v_cvt_pk_bf16_f32 v38, v44, v45\n"
    "v_mfma_f32_16x16x32_bf16 v[56:59], a[164:167], a[4:7], v[56:59]\n"
    "v_mul_f32 v48, s59, v48\n"
    "v_mul_f32 v49, s59, v49\n"
    "v_mfma_f32_16x16x32_bf16 v[56:59], a[160:163], a[0:3], v[56:59]\n"
    "v_mul_f32 v50, s59, v50\n"
    "v_mul_f32 v51, s59, v51\n"
    "v_mfma_f32_16x16x32_bf16 v[56:59], a[224:227], a[56:59], v[56:59]\n"
    "v_mul_f32 v52, s59, v52\n"
    "v_mul_f32 v53, s59, v53\n"
    "v_mfma_f32_16x16x32_bf16 v[56:59], a[228:231], a[60:63], v[56:59]\n"
    "v_mul_f32 v54, s59, v54\n"
    "v_mul_f32 v55, s59, v55\n"
    "v_mfma_f32_16x16x32_bf16 v[60:63], a[172:175], a[4:7], v[60:63]\n"
    "v_cvt_pk_bf16_f32 v40, v48, v49\n"
    "v_cvt_pk_bf16_f32 v41, v50, v51\n"
    "v_mfma_f32_16x16x32_bf16 v[60:63], a[168:171], a[0:3], v[60:63]\n"
    "s_waitcnt vmcnt(5)\n"
    "v_cvt_pk_bf16_f32 v42, v52, v53\n"
    "v_cvt_pk_bf16_f32 v43, v54, v55\n"
    "v_mfma_f32_16x16x32_bf16 v[60:63], a[232:235], a[56:59], v[60:63]\n"
    "s_waitcnt lgkmcnt(0)\n"
    "v_mul_f32 v56, s59, v56\n"
    "v_mul_f32 v57, s59, v57\n"
    "v_mfma_f32_16x16x32_bf16 v[60:63], a[236:239], a[60:63], v[60:63]\n"
    "buffer_load_dwordx4 v10, s[48:51], s58 offen offset:2048 nt lds\n"
    "v_mfma_f32_16x16x32_bf16 v[64:67], a[116:119], a[12:15], v[64:67]\n"
    "s_add_u32 m0, s57, 32768\n"
    "v_mul_f32 v58, s59, v58\n"
    "v_mul_f32 v59, s59, v59\n"
    "v_mfma_f32_16x16x32_bf16 v[64:67], a[112:115], a[8:11], v[64:67]\n"
    "s_add_u32 s58, s65, 16384\n"
    "v_cvt_pk_bf16_f32 v44, v56, v57\n"
    "v_mul_f32 v60, s59, v60\n"
    "v_mfma_f32_16x16x32_bf16 v[64:67], a[176:179], a[64:67], v[64:67]\n"
    "s_and_b32 s58, s58, 0x1ffff\n"
    "v_mul_f32 v61, s59, v61\n"
    "v_cvt_pk_bf16_f32 v46, v60, v61\n"
    "v_mfma_f32_16x16x32_bf16 v[64:67], a[180:183], a[68:71], v[64:67]\n"
    "v_mul_f32 v62, s59, v62\n"
    "v_mul_f32 v63, s59, v63\n"
    "v_mfma_f32_16x16x32_bf16 v[68:71], a[124:127], a[12:15], v[68:71]\n"
    "v_cvt_pk_bf16_f32 v47, v62, v63\n"
    "v_cvt_pk_bf16_f32 v45, v58, v59\n"
    "v_mfma_f32_16x16x32_bf16 v[68:71], a[120:123], a[8:11], v[68:71]\n"
    "v_mfma_f32_16x16x32_bf16 v[68:71], a[184:187], a[64:67], v[68:71]\n"
    "v_mfma_f32_16x16x32_bf16 v[68:71], a[188:191], a[68:71], v[68:71]\n"
    "v_mul_f32 v64, s59, v64\n"
    "v_mul_f32 v65, s59, v65\n"
    "v_mfma_f32_16x16x32_bf16 v[72:75], a[132:135], a[12:15], v[72:75]\n"
    "v_mul_f32 v66, s59, v66\n"
    "v_mul_f32 v67, s59, v67\n"
    "v_mfma_f32_16x16x32_bf16 v[72:75], a[128:131], a[8:11], v[72:75]\n"
    "v_cvt_pk_bf16_f32 v48, v64, v65\n"
    "v_cvt_pk_bf16_f32 v49, v66, v67\n"
    "v_mfma_f32_16x16x32_bf16 v[72:75], a[192:195], a[64:67], v[72:75]\n"
    "v_mul_f32 v68, s59, v68\n"
    "v_mul_f32 v69, s59, v69\n"
    "v_mfma_f32_16x16x32_bf16 v[72:75], a[196:199], a[68:71], v[72:75]\n"
    "v_mul_f32 v70, s59, v70\n"
    "v_mul_f32 v71, s59, v71\n"
    "v_mfma_f32_16x16x32_bf16 v[76:79], a[140:143], a[12:15], v[76:79]\n"
    "v_cvt_pk_bf16_f32 v50, v68, v69\n"
    "v_cvt_pk_bf16_f32 v51, v70, v71\n"
    "v_mfma_f32_16x16x32_bf16 v[76:79], a[136:139], a[8:11], v[76:79]\n"
    "v_mfma_f32_16x16x32_bf16 v[76:79], a[200:203], a[64:67], v[76:79]\n"
    "s_waitcnt vmcnt(5)\n"
    "v_mul_f32 v72, s59, v72\n"
    "v_mul_f32 v73, s59, v73\n"
    "v_mfma_f32_16x16x32_bf16 v[76:79], a[204:207], a[68:71], v[76:79]\n"
    "v_mul_f32 v74, s59, v74\n"
    "v_mul_f32 v75, s59, v75\n"
    "v_mfma_f32_16x16x32_bf16 v[80:83], a[148:151], a[12:15], v[80:83]\n"
    "buffer_load_dwordx4 v8, s[44:47], s58 offen nt lds\n"
    "v_mfma_f32_16x16x32_bf16 v[80:83], a[144:147], a[8:11], v[80:83]\n"
    "v_cvt_pk_bf16_f32 v52, v72, v73\n"
    "v_cvt_pk_bf16_f32 v53, v74, v75\n"
    "v_mfma_f32_16x16x32_bf16 v[80:83], a[208:211], a[64:67], v[80:83]\n"
    "v_mul_f32 v76, s59, v76\n"
    "v_mul_f32 v77, s59, v77\n"
    "v_mfma_f32_16x16x32_bf16 v[80:83], a[212:215], a[68:71], v[80:83]\n"
    "v_mul_f32 v78, s59, v78\n"
    "v_mul_f32 v79, s59, v79\n"
    "v_mfma_f32_16x16x32_bf16 v[84:87], a[156:159], a[12:15], v[84:87]\n"
    "v_cvt_pk_bf16_f32 v54, v76, v77\n"
    "v_cvt_pk_bf16_f32 v55, v78, v79\n"
    "v_mfma_f32_16x16x32_bf16 v[84:87], a[152:155], a[8:11], v[84:87]\n"
    "v_mfma_f32_16x16x32_bf16 v[84:87], a[216:219], a[64:67], v[84:87]\n"
    "v_mfma_f32_16x16x32_bf16 v[84:87], a[220:223], a[68:71], v[84:87]\n"
    "v_mul_f32 v80, s59, v80\n"
    "v_mul_f32 v81, s59, v81\n"
    "v_mfma_f32_16x16x32_bf16 v[88:91], a[164:167], a[12:15], v[88:91]\n"
    "v_mul_f32 v82, s59, v82\n"
    "v_mul_f32 v83, s59, v83\n"
    "v_mfma_f32_16x16x32_bf16 v[88:91], a[160:163], a[8:11], v[88:91]\n"
    "v_cvt_pk_bf16_f32 v56, v80, v81\n"
    "v_cvt_pk_bf16_f32 v57, v82, v83\n"
    "v_mfma_f32_16x16x32_bf16 v[88:91], a[224:227], a[64:67], v[88:91]\n"
    "v_mul_f32 v84, s59, v84\n"
    "v_mul_f32 v85, s59, v85\n"
    "v_mfma_f32_16x16x32_bf16 v[88:91], a[228:231], a[68:71], v[88:91]\n"
    "v_mul_f32 v86, s59, v86\n"
    "v_mul_f32 v87, s59, v87\n"
    "v_mfma_f32_16x16x32_bf16 v[92:95], a[172:175], a[12:15], v[92:95]\n"
    "v_cvt_pk_bf16_f32 v58, v84, v85\n"
    "v_cvt_pk_bf16_f32 v59, v86, v87\n"
    "v_mfma_f32_16x16x32_bf16 v[92:95], a[168:171], a[8:11], v[92:95]\n"
    "v_mfma_f32_16x16x32_bf16 v[92:95], a[232:235], a[64:67], v[92:95]\n"
    "s_waitcnt vmcnt(5)\n"
    "v_mul_f32 v88, s59, v88\n"
    "v_mul_f32 v89, s59, v89\n"
    "v_mfma_f32_16x16x32_bf16 v[92:95], a[236:239], a[68:71], v[92:95]\n"
    "v_cvt_pk_bf16_f32 v60, v88, v89\n"
    "v_mul_f32 v90, s59, v90\n"
    "v_mfma_f32_16x16x32_bf16 v[96:99], a[116:119], a[20:23], v[96:99]\n"
    "buffer_load_dwordx4 v9, s[44:47], s58 offen offset:2048 nt lds\n"
    "v_mfma_f32_16x16x32_bf16 v[96:99], a[112:115], a[16:19], v[96:99]\n"
    "s_add_u32 m0, s57, 36864\n"
    "v_mul_f32 v91, s59, v91\n"
    "v_cvt_pk_bf16_f32 v61, v90, v91\n"
    "v_mfma_f32_16x16x32_bf16 v[96:99], a[176:179], a[72:75], v[96:99]\n"
    "s_add_u32 s58, s58, 4096\n"
    "v_mul_f32 v92, s59, v92\n"
    "v_mul_f32 v93, s59, v93\n"
    "v_mfma_f32_16x16x32_bf16 v[96:99], a[180:183], a[76:79], v[96:99]\n"
    "v_cvt_pk_bf16_f32 v62, v92, v93\n"
    "v_mul_f32 v94, s59, v94\n"
    "v_mfma_f32_16x16x32_bf16 v[100:103], a[124:127], a[20:23], v[100:103]\n"
    "s_waitcnt vmcnt(14)\n"
    "s_barrier\n"
    "ds_read_b128 a[240:243], v13 offset:0\n"
    "v_mfma_f32_16x16x32_bf16 v[100:103], a[120:123], a[16:19], v[100:103]\n"
    "ds_read_b128 a[244:247], v13 offset:256\n"
    "v_mul_f32 v95, s59, v95\n"
    "v_mfma_f32_16x16x32_bf16 v[100:103], a[184:187], a[72:75], v[100:103]\n"
    "ds_read_b128 a[248:251], v13 offset:512\n"
    "v_cvt_pk_bf16_f32 v63, v94, v95\n"
    "v_mfma_f32_16x16x32_bf16 v[100:103], a[188:191], a[76:79], v[100:103]\n"
    "ds_read_b128 a[252:255], v13 offset:768\n"
    "v_mul_f32 v96, s59, v96\n"
    "v_mfma_f32_16x16x32_bf16 v[104:107], a[132:135], a[20:23], v[104:107]\n"
    "v_mul_f32 v97, s59, v97\n"
    "v_cvt_pk_bf16_f32 v64, v96, v97\n"
    "v_mfma_f32_16x16x32_bf16 v[104:107], a[128:131], a[16:19], v[104:107]\n"
    "v_mul_f32 v98, s59, v98\n"
    "v_mul_f32 v99, s59, v99\n"
    "v_mfma_f32_16x16x32_bf16 v[104:107], a[192:195], a[72:75], v[104:107]\n"
    "v_mul_f32 v100, s59, v100\n"
    "v_mul_f32 v101, s59, v101\n"
    "v_mfma_f32_16x16x32_bf16 v[104:107], a[196:199], a[76:79], v[104:107]\n"
    "v_mul_f32 v102, s59, v102\n"
    "v_mul_f32 v103, s59, v103\n"
    "v_mfma_f32_16x16x32_bf16 v[108:111], a[140:143], a[20:23], v[108:111]\n"
    "v_cvt_pk_bf16_f32 v65, v98, v99\n"
    "v_cvt_pk_bf16_f32 v66, v100, v101\n"
    "v_mfma_f32_16x16x32_bf16 v[108:111], a[136:139], a[16:19], v[108:111]\n"
    "v_cvt_pk_bf16_f32 v67, v102, v103\n"
    "v_mfma_f32_16x16x32_bf16 v[108:111], a[200:203], a[72:75], v[108:111]\n"
    "v_mul_f32 v104, s59, v104\n"
    "v_mul_f32 v105, s59, v105\n"
    "v_mfma_f32_16x16x32_bf16 v[108:111], a[204:207], a[76:79], v[108:111]\n"
    "s_waitcnt vmcnt(5)\n"
    "v_mul_f32 v106, s59, v106\n"
    "v_mul_f32 v107, s59, v107\n"
    "v_mfma_f32_16x16x32_bf16 v[112:115], a[148:151], a[20:23], v[112:115]\n"
    "v_cvt_pk_bf16_f32 v68, v104, v105\n"
    "v_cvt_pk_bf16_f32 v69, v106, v107\n"
    "v_mfma_f32_16x16x32_bf16 v[112:115], a[144:147], a[16:19], v[112:115]\n"
    "buffer_load_dwordx4 v8, s[44:47], s58 offen nt lds\n"
    "v_mfma_f32_16x16x32_bf16 v[112:115], a[208:211], a[72:75], v[112:115]\n"
    "v_mul_f32 v108, s59, v108\n"
    "v_mul_f32 v109, s59, v109\n"
    "v_mfma_f32_16x16x32_bf16 v[112:115], a[212:215], a[76:79], v[112:115]\n"
    "v_cvt_pk_bf16_f32 v70, v108, v109\n"
    "v_mul_f32 v110, s59, v110\n"
    "v_mfma_f32_16x16x32_bf16 v[116:119], a[156:159], a[20:23], v[116:119]\n"
    "v_mul_f32 v111, s59, v111\n"
    "v_cvt_pk_bf16_f32 v71, v110, v111\n"
    "v_mfma_f32_16x16x32_bf16 v[116:119], a[152:155], a[16:19], v[116:119]\n"
    "v_mfma_f32_16x16x32_bf16 v[116:119], a[216:219], a[72:75], v[116:119]\n"
    "v_mfma_f32_16x16x32_bf16 v[116:119], a[220:223], a[76:79], v[116:119]\n"
    "v_mul_f32 v112, s59, v112\n"
    "v_mul_f32 v113, s59, v113\n"
    "v_mfma_f32_16x16x32_bf16 v[120:123], a[164:167], a[20:23], v[120:123]\n"
    "v_mul_f32 v114, s59, v114\n"
    "v_mul_f32 v115, s59, v115\n"
    "v_mfma_f32_16x16x32_bf16 v[120:123], a[160:163], a[16:19], v[120:123]\n"
    "v_cvt_pk_bf16_f32 v72, v112, v113\n"
    "v_cvt_pk_bf16_f32 v73, v114, v115\n"
    "v_mfma_f32_16x16x32_bf16 v[120:123], a[224:227], a[72:75], v[120:123]\n"
    "v_mul_f32 v116, s59, v116\n"
    "v_mul_f32 v117, s59, v117\n"
    "v_mfma_f32_16x16x32_bf16 v[120:123], a[228:231], a[76:79], v[120:123]\n"
    "v_mul_f32 v118, s59, v118\n"
    "v_mul_f32 v119, s59, v119\n"
    "v_mfma_f32_16x16x32_bf16 v[124:127], a[172:175], a[20:23], v[124:127]\n"
    "v_cvt_pk_bf16_f32 v74, v116, v117\n"
    "v_cvt_pk_bf16_f32 v75, v118, v119\n"
    "v_mfma_f32_16x16x32_bf16 v[124:127], a[168:171], a[16:19], v[124:127]\n"
    "v_mfma_f32_16x16x32_bf16 v[124:127], a[232:235], a[72:75], v[124:127]\n"
    "v_mfma_f32_16x16x32_bf16 v[124:127], a[236:239], a[76:79], v[124:127]\n"
    "v_mul_f32 v120, s59, v120\n"
    "v_mul_f32 v121, s59, v121\n"
    "v_mfma_f32_16x16x32_bf16 v[128:131], a[180:183], a[84:87], v[128:131]\n"
    "s_waitcnt vmcnt(5)\n"
    "v_mul_f32 v122, s59, v122\n"
    "v_mul_f32 v123, s59, v123\n"
    "v_mfma_f32_16x16x32_bf16 v[128:131], a[176:179], a[80:83], v[128:131]\n"
    "v_cvt_pk_bf16_f32 v76, v120, v121\n"
    "v_mul_f32 v124, s59, v124\n"
    "v_mfma_f32_16x16x32_bf16 v[132:135], a[184:187], a[80:83], v[132:135]\n"
    "buffer_load_dwordx4 v9, s[44:47], s58 offen offset:2048 nt lds\n"
    "v_mfma_f32_16x16x32_bf16 v[132:135], a[188:191], a[84:87], v[132:135]\n"
    "s_add_u32 m0, s57, 40960\n"
    "v_mul_f32 v125, s59, v125\n"
    "v_mul_f32 v126, s59, v126\n"
    "v_mfma_f32_16x16x32_bf16 v[136:139], a[196:199], a[84:87], v[136:139]\n"
    "s_add_u32 s58, s65, 16384\n"
    "v_mul_f32 v127, s59, v127\n"
    "v_cvt_pk_bf16_f32 v77, v122, v123\n"
    "v_mfma_f32_16x16x32_bf16 v[136:139], a[192:195], a[80:83], v[136:139]\n"
    "s_and_b32 s58, s58, 0x1ffff\n"
    "v_cvt_pk_bf16_f32 v78, v124, v125\n"
    "v_cvt_pk_bf16_f32 v79, v126, v127\n"
    "v_mfma_f32_16x16x32_bf16 v[140:143], a[200:203], a[80:83], v[140:143]\n"
    "v_mul_f32 v128, s59, v128\n"
    "v_mul_f32 v129, s59, v129\n"
    "v_mfma_f32_16x16x32_bf16 v[140:143], a[204:207], a[84:87], v[140:143]\n"
    "v_mul_f32 v130, s59, v130\n"
    "v_mul_f32 v131, s59, v131\n"
    "v_mfma_f32_16x16x32_bf16 v[144:147], a[212:215], a[84:87], v[144:147]\n"
    "v_mul_f32 v132, s59, v132\n"
    "v_mul_f32 v133, s59, v133\n"
    "v_mfma_f32_16x16x32_bf16 v[144:147], a[208:211], a[80:83], v[144:147]\n"
    "v_mul_f32 v134, s59, v134\n"
    "v_mul_f32 v135, s59, v135\n"
    "v_mfma_f32_16x16x32_bf16 v[148:151], a[216:219], a[80:83], v[148:151]\n"
    "v_cvt_pk_bf16_f32 v80, v128, v129\n"
    "v_cvt_pk_bf16_f32 v81, v130, v131\n"
    "v_mfma_f32_16x16x32_bf16 v[148:151], a[220:223], a[84:87], v[148:151]\n"
    "v_cvt_pk_bf16_f32 v82, v132, v133\n"
    "v_cvt_pk_bf16_f32 v83, v134, v135\n"
    "v_mfma_f32_16x16x32_bf16 v[152:155], a[228:231], a[84:87], v[152:155]\n"
    "v_mul_f32 v144, s59, v144\n"
    "v_mul_f32 v145, s59, v145\n"
    "v_mfma_f32_16x16x32_bf16 v[152:155], a[224:227], a[80:83], v[152:155]\n"
    "v_mul_f32 v146, s59, v146\n"
    "v_mul_f32 v147, s59, v147\n"
    "v_mfma_f32_16x16x32_bf16 v[156:159], a[232:235], a[80:83], v[156:159]\n"
    "v_cvt_pk_bf16_f32 v88, v144, v145\n"
    "v_cvt_pk_bf16_f32 v89, v146, v147\n"
    "v_mfma_f32_16x16x32_bf16 v[156:159], a[236:239], a[84:87], v[156:159]\n"
    "ds_read_b128 v[144:147], v27 offset:0\n"
    "v_mul_f32 v148, s59, v148\n"
    "v_mfma_f32_16x16x32_bf16 v[160:163], a[180:183], a[92:95], v[160:163]\n"
    "v_mul_f32 v149, s59, v149\n"
    "v_mul_f32 v150, s59, v150\n"
    "v_mfma_f32_16x16x32_bf16 v[160:163], a[176:179], a[88:91], v[160:163]\n"
    "s_waitcnt vmcnt(5)\n"
    "v_mul_f32 v151, s59, v151\n"
    "v_cvt_pk_bf16_f32 v90, v148, v149\n"
    "v_mfma_f32_16x16x32_bf16 v[164:167], a[184:187], a[88:91], v[164:167]\n"
    "v_cvt_pk_bf16_f32 v91, v150, v151\n"
    "v_mul_f32 v152, s59, v152\n"
    "v_mfma_f32_16x16x32_bf16 v[164:167], a[188:191], a[92:95], v[164:167]\n"
    "buffer_load_dwordx4 v10, s[48:51], s58 offen nt lds\n"
    "v_mfma_f32_16x16x32_bf16 v[168:171], a[196:199], a[92:95], v[168:171]\n"
    "ds_read_b128 v[148:151], v27 offset:256\n"
    "v_mul_f32 v153, s59, v153\n"
    "v_mfma_f32_16x16x32_bf16 v[168:171], a[192:195], a[88:91], v[168:171]\n"
    "v_mul_f32 v154, s59, v154\n"
    "v_mul_f32 v155, s59, v155\n"
    "v_mfma_f32_16x16x32_bf16 v[172:175], a[200:203], a[88:91], v[172:175]\n"
    "v_cvt_pk_bf16_f32 v92, v152, v153\n"
    "v_cvt_pk_bf16_f32 v93, v154, v155\n"
    "v_mfma_f32_16x16x32_bf16 v[172:175], a[204:207], a[92:95], v[172:175]\n"
    "s_waitcnt lgkmcnt(1)\n"
    "ds_read_b128 v[152:155], v27 offset:512\n"
    "v_xor_b32 v144, 0x80008000, v144\n"
    "v_mfma_f32_16x16x32_bf16 v[176:179], a[212:215], a[92:95], v[176:179]\n"
    "ds_read_b128 v[28:31], v27 offset:768\n"
    "v_xor_b32 v145, 0x80008000, v145\n"
    "v_mfma_f32_16x16x32_bf16 v[176:179], a[208:211], a[88:91], v[176:179]\n"
    "v_xor_b32 v146, 0x80008000, v146\n"
    "v_xor_b32 v147, 0x80008000, v147\n"
    "v_mfma_f32_16x16x32_bf16 v[180:183], a[216:219], a[88:91], v[180:183]\n"
    "v_mul_f32 v156, s59, v156\n"
    "v_mul_f32 v157, s59, v157\n"
    "v_mfma_f32_16x16x32_bf16 v[180:183], a[220:223], a[92:95], v[180:183]\n"
    "v_mul_f32 v158, s59, v158\n"
    "v_mul_f32 v159, s59, v159\n"
    "v_mfma_f32_16x16x32_bf16 v[184:187], a[228:231], a[92:95], v[184:187]\n"
    "s_waitcnt lgkmcnt(2)\n"
    "v_cvt_pk_bf16_f32 v94, v156, v157\n"
    "v_cvt_pk_bf16_f32 v95, v158, v159\n"
    "v_mfma_f32_16x16x32_bf16 v[184:187], a[224:227], a[88:91], v[184:187]\n"
    "v_xor_b32 v148, 0x80008000, v148\n"
    "v_xor_b32 v149, 0x80008000, v149\n"
    "v_mfma_f32_16x16x32_bf16 v[188:191], a[232:235], a[88:91], v[188:191]\n"
    "v_xor_b32 v150, 0x80008000, v150\n"
    "v_xor_b32 v151, 0x80008000, v151\n"
    "v_mfma_f32_16x16x32_bf16 v[188:191], a[236:239], a[92:95], v[188:191]\n"
    "s_waitcnt lgkmcnt(1)\n"
    "v_xor_b32 v152, 0x80008000, v152\n"
    "v_xor_b32 v153, 0x80008000, v153\n"
    "v_mfma_f32_16x16x32_bf16 v[192:195], a[180:183], a[100:103], v[192:195]\n"
    "s_waitcnt lgkmcnt(0)\n"
    "v_xor_b32 v154, 0x80008000, v154\n"
    "v_xor_b32 v155, 0x80008000, v155\n"
    "v_mfma_f32_16x16x32_bf16 v[192:195], a[176:179], a[96:99], v[192:195]\n"
    "s_waitcnt vmcnt(5)\n"
    "v_xor_b32 v28, 0x80008000, v28\n"
    "v_xor_b32 v29, 0x80008000, v29\n"
    "v_mfma_f32_16x16x32_bf16 v[196:199], a[184:187], a[96:99], v[196:199]\n"
    "v_xor_b32 v30, 0x80008000, v30\n"
    "v_xor_b32 v31, 0x80008000, v31\n"
    "v_mfma_f32_16x16x32_bf16 v[196:199], a[188:191], a[100:103], v[196:199]\n"
    "buffer_load_dwordx4 v10, s[48:51], s58 offen offset:2048 nt lds\n"
    "v_mfma_f32_16x16x32_bf16 v[200:203], a[196:199], a[100:103], v[200:203]\n"
    "s_add_u32 m0, s57, 45056\n"
    "v_mul_f32 v160, s59, v160\n"
    "v_mul_f32 v161, s59, v161\n"
    "v_mfma_f32_16x16x32_bf16 v[200:203], a[192:195], a[96:99], v[200:203]\n"
    "s_add_u32 s58, s58, 4096\n"
    "v_mul_f32 v162, s59, v162\n"
    "v_mul_f32 v163, s59, v163\n"
    "v_mfma_f32_16x16x32_bf16 v[204:207], a[200:203], a[96:99], v[204:207]\n"
    "v_cvt_pk_bf16_f32 v96, v160, v161\n"
    "v_cvt_pk_bf16_f32 v97, v162, v163\n"
    "v_mfma_f32_16x16x32_bf16 v[204:207], a[204:207], a[100:103], v[204:207]\n"
    "v_mul_f32 v184, s59, v184\n"
    "v_mul_f32 v185, s59, v185\n"
    "v_mfma_f32_16x16x32_bf16 v[208:211], a[212:215], a[100:103], v[208:211]\n"
    "v_mul_f32 v186, s59, v186\n"
    "v_mul_f32 v187, s59, v187\n"
    "v_mfma_f32_16x16x32_bf16 v[208:211], a[208:211], a[96:99], v[208:211]\n"
    "v_cvt_pk_bf16_f32 v108, v184, v185\n"
    "v_cvt_pk_bf16_f32 v109, v186, v187\n"
    "v_mfma_f32_16x16x32_bf16 v[212:215], a[216:219], a[96:99], v[212:215]\n"
    "v_mul_f32 v164, s59, v164\n"
    "v_mul_f32 v165, s59, v165\n"
    "v_mfma_f32_16x16x32_bf16 v[212:215], a[220:223], a[100:103], v[212:215]\n"
    "v_mul_f32 v166, s59, v166\n"
    "v_mul_f32 v167, s59, v167\n"
    "v_mfma_f32_16x16x32_bf16 v[216:219], a[228:231], a[100:103], v[216:219]\n"
    "v_cvt_pk_bf16_f32 v98, v164, v165\n"
    "v_cvt_pk_bf16_f32 v99, v166, v167\n"
    "v_mfma_f32_16x16x32_bf16 v[216:219], a[224:227], a[96:99], v[216:219]\n"
    "v_mul_f32 v188, s59, v188\n"
    "v_mul_f32 v189, s59, v189\n"
    "v_mfma_f32_16x16x32_bf16 v[220:223], a[232:235], a[96:99], v[220:223]\n"
    "v_mul_f32 v190, s59, v190\n"
    "v_mul_f32 v191, s59, v191\n"
    "v_mfma_f32_16x16x32_bf16 v[220:223], a[236:239], a[100:103], v[220:223]\n"
    "v_cvt_pk_bf16_f32 v110, v188, v189\n"
    "v_cvt_pk_bf16_f32 v111, v190, v191\n"
    "v_mfma_f32_16x16x32_bf16 v[224:227], a[180:183], a[108:111], v[224:227]\n"
    "v_mul_f32 v136, s59, v136\n"
    "v_mul_f32 v137, s59, v137\n"
    "v_mfma_f32_16x16x32_bf16 v[224:227], a[176:179], a[104:107], v[224:227]\n"
    "v_mul_f32 v138, s59, v138\n"
    "v_mul_f32 v139, s59, v139\n"
    "v_mfma_f32_16x16x32_bf16 v[228:231], a[184:187], a[104:107], v[228:231]\n"
    "s_waitcnt vmcnt(5)\n"
    "v_mul_f32 v140, s59, v140\n"
    "v_mul_f32 v141, s59, v141\n"
    "v_mfma_f32_16x16x32_bf16 v[228:231], a[188:191], a[108:111], v[228:231]\n"
    "v_mul_f32 v142, s59, v142\n"
    "v_mul_f32 v143, s59, v143\n"
    "v_mfma_f32_16x16x32_bf16 v[232:235], a[196:199], a[108:111], v[232:235]\n"
    "buffer_load_dwordx4 v10, s[48:51], s58 offen nt lds\n"
    "v_mfma_f32_16x16x32_bf16 v[232:235], a[192:195], a[104:107], v[232:235]\n"
    "v_mul_f32 v168, s59, v168\n"
    "v_mul_f32 v169, s59, v169\n"
    "v_mfma_f32_16x16x32_bf16 v[236:239], a[200:203], a[104:107], v[236:239]\n"
    "v_mul_f32 v170, s59, v170\n"
    "v_mul_f32 v171, s59, v171\n"
    "v_mfma_f32_16x16x32_bf16 v[236:239], a[204:207], a[108:111], v[236:239]\n"
    "v_cvt_pk_bf16_f32 v84, v136, v137\n"
    "v_cvt_pk_bf16_f32 v85, v138, v139\n"
    "v_mfma_f32_16x16x32_bf16 v[240:243], a[212:215], a[108:111], v[240:243]\n"
    "v_cvt_pk_bf16_f32 v86, v140, v141\n"
    "v_cvt_pk_bf16_f32 v87, v142, v143\n"
    "v_mfma_f32_16x16x32_bf16 v[240:243], a[208:211], a[104:107], v[240:243]\n"
    "v_cvt_pk_bf16_f32 v100, v168, v169\n"
    "v_cvt_pk_bf16_f32 v101, v170, v171\n"
    "v_mfma_f32_16x16x32_bf16 v[244:247], a[216:219], a[104:107], v[244:247]\n"
    "v_mul_f32 v192, s59, v192\n"
    "v_mul_f32 v193, s59, v193\n"
    "v_mfma_f32_16x16x32_bf16 v[244:247], a[220:223], a[108:111], v[244:247]\n"
    "v_mul_f32 v194, s59, v194\n"
    "v_mul_f32 v195, s59, v195\n"
    "v_mfma_f32_16x16x32_bf16 v[248:251], a[228:231], a[108:111], v[248:251]\n"
    "v_cvt_pk_bf16_f32 v112, v192, v193\n"
    "v_cvt_pk_bf16_f32 v113, v194, v195\n"
    "v_mfma_f32_16x16x32_bf16 v[248:251], a[224:227], a[104:107], v[248:251]\n"
    "ds_read_b128 a[224:227], v13 offset:4096\n"
    "v_mul_f32 v172, s59, v172\n"
    "v_mfma_f32_16x16x32_bf16 v[252:255], a[232:235], a[104:107], v[252:255]\n"
    "ds_read_b128 a[228:231], v13 offset:4352\n"
    "v_mul_f32 v173, s59, v173\n"
    "v_mfma_f32_16x16x32_bf16 v[252:255], a[236:239], a[108:111], v[252:255]\n"
    "ds_read_b128 a[232:235], v13 offset:4608\n"
    "v_mul_f32 v174, s59, v174\n"
    "v_mfma_f32_16x16x32_bf16 v[156:159], v[144:147], v[32:35], 0\n"
    "ds_read_b128 a[236:239], v13 offset:4864\n"
    "v_mul_f32 v175, s59, v175\n"
    "v_mfma_f32_16x16x32_bf16 v[156:159], v[148:151], v[36:39], v[156:159]\n"
    "v_cvt_pk_bf16_f32 v102, v172, v173\n"
    "v_cvt_pk_bf16_f32 v103, v174, v175\n"
    "v_mfma_f32_16x16x32_bf16 v[156:159], v[152:155], v[40:43], v[156:159]\n"
    "v_mul_f32 v176, s59, v176\n"
    "v_mul_f32 v177, s59, v177\n"
    "v_mfma_f32_16x16x32_bf16 v[156:159], v[28:31], v[44:47], v[156:159]\n"
    "s_waitcnt vmcnt(5)\n"
    "v_mul_f32 v178, s59, v178\n"
    "v_mul_f32 v179, s59, v179\n"
    "v_mfma_f32_16x16x32_bf16 v[160:163], v[144:147], v[48:51], 0\n"
    "v_mul_f32 v180, s59, v180\n"
    "v_mul_f32 v181, s59, v181\n"
    "v_mfma_f32_16x16x32_bf16 v[160:163], v[148:151], v[52:55], v[160:163]\n"
    "buffer_load_dwordx4 v10, s[48:51], s58 offen offset:2048 nt lds\n"
    "v_mfma_f32_16x16x32_bf16 v[160:163], v[152:155], v[56:59], v[160:163]\n"
    "s_add_u32 m0, s57, 65536\n"
    "v_mul_f32 v182, s59, v182\n"
    "v_mul_f32 v183, s59, v183\n"
    "v_mfma_f32_16x16x32_bf16 v[160:163], v[28:31], v[60:63], v[160:163]\n"
    "s_add_u32 s58, s65, 24576\n"
    "v_cvt_pk_bf16_f32 v104, v176, v177\n"
    "v_cvt_pk_bf16_f32 v105, v178, v179\n"
    "v_mfma_f32_16x16x32_bf16 v[184:187], a[240:243], v[32:35], v[156:159]\n"
    "s_and_b32 s58, s58, 0x1ffff\n"
    "v_cvt_pk_bf16_f32 v106, v180, v181\n"
    "v_cvt_pk_bf16_f32 v107, v182, v183\n"
    "v_mfma_f32_16x16x32_bf16 v[184:187], a[244:247], v[36:39], v[184:187]\n"
    "v_mul_f32 v196, s59, v196\n"
    "v_mul_f32 v197, s59, v197\n"
    "v_mfma_f32_16x16x32_bf16 v[184:187], a[248:251], v[40:43], v[184:187]\n"
    "v_mul_f32 v198, s59, v198\n"
    "v_mul_f32 v199, s59, v199\n"
    "v_mfma_f32_16x16x32_bf16 v[184:187], a[252:255], v[44:47], v[184:187]\n"
    "v_cvt_pk_bf16_f32 v114, v196, v197\n"
    "v_cvt_pk_bf16_f32 v115, v198, v199\n"
    "v_mfma_f32_16x16x32_bf16 v[164:167], v[144:147], v[64:67], 0\n"
    "v_mul_f32 v200, s59, v200\n"
    "v_mul_f32 v201, s59, v201\n"
    "v_mfma_f32_16x16x32_bf16 v[164:167], v[148:151], v[68:71], v[164:167]\n"
    "v_mul_f32 v202, s59, v202\n"
    "v_mul_f32 v203, s59, v203\n"
    "v_mfma_f32_16x16x32_bf16 v[164:167], v[152:155], v[72:75], v[164:167]\n"
    "v_mul_f32 v204, s59, v204\n"
    "v_mul_f32 v205, s59, v205\n"
    "v_mfma_f32_16x16x32_bf16 v[164:167], v[28:31], v[76:79], v[164:167]\n"
    "v_mul_f32 v206, s59, v206\n"
    "v_mul_f32 v207, s59, v207\n"
    "v_mfma_f32_16x16x32_bf16 v[188:191], a[252:255], v[60:63], v[160:163]\n"
    "v_cvt_pk_bf16_f32 v116, v200, v201\n"
    "v_cvt_pk_bf16_f32 v117, v202, v203\n"
    "v_mfma_f32_16x16x32_bf16 v[188:191], a[248:251], v[56:59], v[188:191]\n"
    "v_cvt_pk_bf16_f32 v118, v204, v205\n"
    "v_cvt_pk_bf16_f32 v119, v206, v207\n"
    "v_mfma_f32_16x16x32_bf16 v[188:191], a[244:247], v[52:55], v[188:191]\n"
    "v_mul_f32 v208, s59, v208\n"
    "v_mul_f32 v209, s59, v209\n"
    "v_mfma_f32_16x16x32_bf16 v[188:191], a[240:243], v[48:51], v[188:191]\n"
    "s_waitcnt vmcnt(5)\n"
    "v_mul_f32 v210, s59, v210\n"
    "v_mul_f32 v211, s59, v211\n"
    "v_mfma_f32_16x16x32_bf16 v[168:171], v[144:147], v[80:83], 0\n"
    "v_mul_f32 v212, s59, v212\n"
    "v_mul_f32 v213, s59, v213\n"
    "v_mfma_f32_16x16x32_bf16 v[168:171], v[148:151], v[84:87], v[168:171]\n"
    "buffer_load_dwordx4 v8, s[44:47], s58 offen nt lds\n"
    "v_mfma_f32_16x16x32_bf16 v[168:171], v[152:155], v[88:91], v[168:171]\n"
    "v_mul_f32 v214, s59, v214\n"
    "v_mul_f32 v215, s59, v215\n"
    "v_mfma_f32_16x16x32_bf16 v[168:171], v[28:31], v[92:95], v[168:171]\n"
    "v_cvt_pk_bf16_f32 v120, v208, v209\n"
    "v_cvt_pk_bf16_f32 v121, v210, v211\n"
    "v_mfma_f32_16x16x32_bf16 v[192:195], a[240:243], v[64:67], v[164:167]\n"
    "v_cvt_pk_bf16_f32 v122, v212, v213\n"
    "v_cvt_pk_bf16_f32 v123, v214, v215\n"
    "v_mfma_f32_16x16x32_bf16 v[192:195], a[244:247], v[68:71], v[192:195]\n"
    "v_exp_f32 v184, v184\n"
    "v_mfma_f32_16x16x32_bf16 v[192:195], a[248:251], v[72:75], v[192:195]\n"
    "v_mul_f32 v216, s59, v216\n"
    "v_mul_f32 v217, s59, v217\n"
    "v_mfma_f32_16x16x32_bf16 v[192:195], a[252:255], v[76:79], v[192:195]\n"
    "v_mul_f32 v218, s59, v218\n"
    "v_mul_f32 v219, s59, v219\n"
    "v_mfma_f32_16x16x32_bf16 v[172:175], v[144:147], v[96:99], 0\n"
    "v_mul_f32 v220, s59, v220\n"
    "v_mul_f32 v221, s59, v221\n"
    "v_mfma_f32_16x16x32_bf16 v[172:175], v[148:151], v[100:103], v[172:175]\n"
    "v_mul_f32 v222, s59, v222\n"
    "v_mul_f32 v223, s59, v223\n"
    "v_mfma_f32_16x16x32_bf16 v[172:175], v[152:155], v[104:107], v[172:175]\n"
    "v_exp_f32 v185, v185\n"
    "v_mfma_f32_16x16x32_bf16 v[172:175], v[28:31], v[108:111], v[172:175]\n"
    "v_cvt_pk_bf16_f32 v124, v216, v217\n"
    "v_cvt_pk_bf16_f32 v125, v218, v219\n"
    "v_mfma_f32_16x16x32_bf16 v[196:199], a[252:255], v[92:95], v[168:171]\n"
    "v_cvt_pk_bf16_f32 v126, v220, v221\n"
    "v_cvt_pk_bf16_f32 v127, v222, v223\n"
    "v_mfma_f32_16x16x32_bf16 v[196:199], a[248:251], v[88:91], v[196:199]\n"
    "v_exp_f32 v186, v186\n"
    "v_mfma_f32_16x16x32_bf16 v[196:199], a[244:247], v[84:87], v[196:199]\n"
    "v_add_f32 v20, v20, v184\n"
    "v_add_f32 v20, v20, v185\n"
    "v_mfma_f32_16x16x32_bf16 v[196:199], a[240:243], v[80:83], v[196:199]\n"
    "v_exp_f32 v187, v187\n"
    "v_mfma_f32_16x16x32_bf16 v[176:179], v[144:147], v[112:115], 0\n"
    "s_waitcnt vmcnt(5)\n"
    "v_add_f32 v20, v20, v186\n"
    "v_cvt_pk_bf16_f32 v200, v184, v185\n"
    "v_mfma_f32_16x16x32_bf16 v[176:179], v[148:151], v[116:119], v[176:179]\n"
    "v_cvt_pk_bf16_f32 v201, v186, v187\n"
    "v_add_f32 v20, v20, v187\n"
    "v_mfma_f32_16x16x32_bf16 v[176:179], v[152:155], v[120:123], v[176:179]\n"
    "buffer_load_dwordx4 v9, s[44:47], s58 offen offset:2048 nt lds\n"
    "v_mfma_f32_16x16x32_bf16 v[176:179], v[28:31], v[124:127], v[176:179]\n"
    "s_add_u32 m0, s57, 69632\n"
    "v_mul_f32 v224, s59, v224\n"
    "v_mul_f32 v225, s59, v225\n"
    "v_mfma_f32_16x16x32_bf16 v[184:187], a[240:243], v[96:99], v[172:175]\n"
    "s_add_u32 s58, s58, 4096\n"
    "v_mul_f32 v226, s59, v226\n"
    "v_mul_f32 v227, s59, v227\n"
    "v_mfma_f32_16x16x32_bf16 v[184:187], a[244:247], v[100:103], v[184:187]\n"
    "v_mul_f32 v228, s59, v228\n"
    "v_mul_f32 v229, s59, v229\n"
    "v_mfma_f32_16x16x32_bf16 v[184:187], a[248:251], v[104:107], v[184:187]\n"
    "v_mul_f32 v230, s59, v230\n"
    "v_mul_f32 v231, s59, v231\n"
    "v_cvt_pk_bf16_f32 v128, v224, v225\n"
    "v_cvt_pk_bf16_f32 v129, v226, v227\n"
    "v_cvt_pk_bf16_f32 v130, v228, v229\n"
    "v_cvt_pk_bf16_f32 v131, v230, v231\n"
    "v_mfma_f32_16x16x32_bf16 v[184:187], a[252:255], v[108:111], v[184:187]\n"
    "v_mul_f32 v232, s59, v232\n"
    "v_mul_f32 v233, s59, v233\n"
    "v_mul_f32 v234, s59, v234\n"
    "v_mul_f32 v235, s59, v235\n"
    "v_mul_f32 v236, s59, v236\n"
    "v_mul_f32 v237, s59, v237\n"
    "v_mul_f32 v238, s59, v238\n"
    "v_mul_f32 v239, s59, v239\n"
    "v_cvt_pk_bf16_f32 v132, v232, v233\n"
    "v_cvt_pk_bf16_f32 v133, v234, v235\n"
    "v_cvt_pk_bf16_f32 v134, v236, v237\n"
    "v_cvt_pk_bf16_f32 v135, v238, v239\n"
    "v_mfma_f32_16x16x32_bf16 v[180:183], v[144:147], v[128:131], 0\n"
    "v_mul_f32 v240, s59, v240\n"
    "v_mul_f32 v241, s59, v241\n"
    "v_mul_f32 v242, s59, v242\n"
    "v_mul_f32 v243, s59, v243\n"
    "v_mul_f32 v244, s59, v244\n"
    "v_mul_f32 v245, s59, v245\n"
    "v_mul_f32 v246, s59, v246\n"
    "v_mul_f32 v247, s59, v247\n"
    "v_cvt_pk_bf16_f32 v136, v240, v241\n"
    "v_cvt_pk_bf16_f32 v137, v242, v243\n"
    "v_cvt_pk_bf16_f32 v138, v244, v245\n"
    "v_cvt_pk_bf16_f32 v139, v246, v247\n"
    "v_mfma_f32_16x16x32_bf16 v[180:183], v[148:151], v[132:135], v[180:183]\n"
    "v_exp_f32 v188, v188\n"
    "v_mul_f32 v248, s59, v248\n"
    "v_mul_f32 v249, s59, v249\n"
    "v_mul_f32 v250, s59, v250\n"
    "v_mul_f32 v251, s59, v251\n"
    "v_mul_f32 v252, s59, v252\n"
    "v_mul_f32 v253, s59, v253\n"
    "v_mul_f32 v254, s59, v254\n"
    "v_mul_f32 v255, s59, v255\n"
    "v_cvt_pk_bf16_f32 v140, v248, v249\n"
    "v_cvt_pk_bf16_f32 v141, v250, v251\n"
    "v_cvt_pk_bf16_f32 v142, v252, v253\n"
    "v_cvt_pk_bf16_f32 v143, v254, v255\n"
    "v_exp_f32 v189, v189\n"
    "v_mfma_f32_16x16x32_bf16 v[180:183], v[152:155], v[136:139], v[180:183]\n"
    "v_exp_f32 v190, v190\n"
    "v_add_f32 v21, v21, v188\n"
    "v_exp_f32 v191, v191\n"
    "v_add_f32 v21, v21, v189\n"
    "v_mfma_f32_16x16x32_bf16 v[180:183], v[28:31], v[140:143], v[180:183]\n"
    "s_waitcnt vmcnt(5)\n"
    "v_add_f32 v21, v21, v190\n"
    "v_cvt_pk_bf16_f32 v204, v188, v189\n"
    "v_cvt_pk_bf16_f32 v205, v190, v191\n"
    "v_add_f32 v21, v21, v191\n"
    "v_mfma_f32_16x16x32_bf16 v[188:191], a[252:255], v[124:127], v[176:179]\n"
    "v_exp_f32 v192, v192\n"
    "v_mfma_f32_16x16x32_bf16 v[188:191], a[248:251], v[120:123], v[188:191]\n"
    "buffer_load_dwordx4 v8, s[44:47], s58 offen nt lds\n"
    "v_exp_f32 v193, v193\n"
    "v_mfma_f32_16x16x32_bf16 v[188:191], a[244:247], v[116:119], v[188:191]\n"
    "v_exp_f32 v194, v194\n"
    "v_add_f32 v22, v22, v192\n"
    "v_exp_f32 v195, v195\n"
    "v_add_f32 v22, v22, v193\n"
    "v_mfma_f32_16x16x32_bf16 v[188:191], a[240:243], v[112:115], v[188:191]\n"
    "v_add_f32 v22, v22, v194\n"
    "v_cvt_pk_bf16_f32 v208, v192, v193\n"
    "v_cvt_pk_bf16_f32 v209, v194, v195\n"
    "v_add_f32 v22, v22, v195\n"
    "v_mfma_f32_16x16x32_bf16 v[192:195], a[240:243], v[128:131], v[180:183]\n"
    "v_exp_f32 v196, v196\n"
    "v_mfma_f32_16x16x32_bf16 v[192:195], a[244:247], v[132:135], v[192:195]\n"
    "v_exp_f32 v197, v197\n"
    "v_mfma_f32_16x16x32_bf16 v[192:195], a[248:251], v[136:139], v[192:195]\n"
    "v_exp_f32 v198, v198\n"
    "v_add_f32 v23, v23, v196\n"
    "v_exp_f32 v199, v199\n"
    "v_add_f32 v23, v23, v197\n"
    "v_mfma_f32_16x16x32_bf16 v[192:195], a[252:255], v[140:143], v[192:195]\n"
    "s_waitcnt lgkmcnt(0)\n"
    "v_add_f32 v23, v23, v198\n"
    "v_cvt_pk_bf16_f32 v212, v196, v197\n"
    "v_cvt_pk_bf16_f32 v213, v198, v199\n"
    "v_add_f32 v23, v23, v199\n"
    "v_mfma_f32_16x16x32_bf16 v[196:199], a[224:227], v[32:35], v[156:159]\n"
    "v_exp_f32 v184, v184\n"
    "v_mfma_f32_16x16x32_bf16 v[196:199], a[228:231], v[36:39], v[196:199]\n"
    "v_exp_f32 v185, v185\n"
    "v_mfma_f32_16x16x32_bf16 v[196:199], a[232:235], v[40:43], v[196:199]\n"
    "s_waitcnt vmcnt(5)\n"
    "v_exp_f32 v186, v186\n"
    "v_add_f32 v24, v24, v184\n"
    "v_exp_f32 v187, v187\n"
    "v_add_f32 v24, v24, v185\n"
    "v_mfma_f32_16x16x32_bf16 v[196:199], a[236:239], v[44:47], v[196:199]\n"
    "v_add_f32 v24, v24, v186\n"
    "v_cvt_pk_bf16_f32 v216, v184, v185\n"
    "v_cvt_pk_bf16_f32 v217, v186, v187\n"
    "v_add_f32 v24, v24, v187\n"
    "v_mfma_f32_16x16x32_bf16 v[184:187], a[236:239], v[60:63], v[160:163]\n"
    "buffer_load_dwordx4 v9, s[44:47], s58 offen offset:2048 nt lds\n"
    "v_mfma_f32_16x16x32_bf16 v[184:187], a[232:235], v[56:59], v[184:187]\n"
    "s_add_u32 m0, s57, 73728\n"
    "v_exp_f32 v188, v188\n"
    "v_exp_f32 v189, v189\n"
    "v_mfma_f32_16x16x32_bf16 v[184:187], a[228:231], v[52:55], v[184:187]\n"
    "s_add_u32 s58, s65, 24576\n"
    "v_exp_f32 v190, v190\n"
    "v_add_f32 v25, v25, v188\n"
    "v_exp_f32 v191, v191\n"
    "v_add_f32 v25, v25, v189\n"
    "v_mfma_f32_16x16x32_bf16 v[184:187], a[224:227], v[48:51], v[184:187]\n"
    "s_waitcnt vmcnt(16)\n"
    "s_barrier\n"
    "ds_read_b128 a[240:243], v11 offset:0\n"
    "v_add_f32 v25, v25, v190\n"
    "v_cvt_pk_bf16_f32 v220, v188, v189\n"
    "v_cvt_pk_bf16_f32 v221, v190, v191\n"
    "v_add_f32 v25, v25, v191\n"
    "v_mfma_f32_16x16x32_bf16 v[188:191], a[224:227], v[64:67], v[164:167]\n"
    "s_and_b32 s58, s58, 0x1ffff\n"
    "v_exp_f32 v192, v192\n"
    "v_mfma_f32_16x16x32_bf16 v[188:191], a[228:231], v[68:71], v[188:191]\n"
    "v_exp_f32 v193, v193\n"
    "v_mfma_f32_16x16x32_bf16 v[188:191], a[232:235], v[72:75], v[188:191]\n"
    "v_exp_f32 v194, v194\n"
    "v_add_f32 v26, v26, v192\n"
    "v_exp_f32 v195, v195\n"
    "v_add_f32 v26, v26, v193\n"
    "v_mfma_f32_16x16x32_bf16 v[188:191], a[236:239], v[76:79], v[188:191]\n"
    "ds_read_b128 a[244:247], v11 offset:256\n"
    "v_add_f32 v26, v26, v194\n"
    "v_cvt_pk_bf16_f32 v224, v192, v193\n"
    "v_cvt_pk_bf16_f32 v225, v194, v195\n"
    "v_add_f32 v26, v26, v195\n"
    "v_mfma_f32_16x16x32_bf16 v[192:195], a[236:239], v[92:95], v[168:171]\n"
    "v_exp_f32 v196, v196\n"
    "v_mfma_f32_16x16x32_bf16 v[192:195], a[232:235], v[88:91], v[192:195]\n"
    "s_waitcnt vmcnt(5)\n"
    "v_exp_f32 v197, v197\n"
    "v_mfma_f32_16x16x32_bf16 v[192:195], a[228:231], v[84:87], v[192:195]\n"
    "v_exp_f32 v198, v198\n"
    "v_add_f32 v20, v20, v196\n"
    "v_exp_f32 v199, v199\n"
    "v_add_f32 v20, v20, v197\n"
    "v_mfma_f32_16x16x32_bf16 v[192:195], a[224:227], v[80:83], v[192:195]\n"
    "buffer_load_dwordx4 v10, s[48:51], s58 offen nt lds\n"
    "v_add_f32 v20, v20, v198\n"
    "v_cvt_pk_bf16_f32 v202, v196, v197\n"
    "v_cvt_pk_bf16_f32 v203, v198, v199\n"
    "v_add_f32 v20, v20, v199\n"
    "v_mfma_f32_16x16x32_bf16 v[196:199], a[224:227], v[96:99], v[172:175]\n"
    "v_exp_f32 v184, v184\n"
    "v_mfma_f32_16x16x32_bf16 v[196:199], a[228:231], v[100:103], v[196:199]\n"
    "v_exp_f32 v185, v185\n"
    "v_mfma_f32_16x16x32_bf16 v[196:199], a[232:235], v[104:107], v[196:199]\n"
    "v_exp_f32 v186, v186\n"
    "v_add_f32 v21, v21, v184\n"
    "v_exp_f32 v187, v187\n"
    "v_add_f32 v21, v21, v185\n"
    "v_mfma_f32_16x16x32_bf16 v[196:199], a[236:239], v[108:111], v[196:199]\n"
    "ds_read_b128 a[248:251], v11 offset:512\n"
    "v_add_f32 v21, v21, v186\n"
    "v_cvt_pk_bf16_f32 v206, v184, v185\n"
    "v_cvt_pk_bf16_f32 v207, v186, v187\n"
    "v_add_f32 v21, v21, v187\n"
    "v_mfma_f32_16x16x32_bf16 v[184:187], a[236:239], v[124:127], v[176:179]\n"
    "v_exp_f32 v188, v188\n"
    "v_mfma_f32_16x16x32_bf16 v[184:187], a[232:235], v[120:123], v[184:187]\n"
    "v_exp_f32 v189, v189\n"
    "v_mfma_f32_16x16x32_bf16 v[184:187], a[228:231], v[116:119], v[184:187]\n"
    "v_exp_f32 v190, v190\n"
    "v_add_f32 v22, v22, v188\n"
    "ds_read_b128 a[252:255], v11 offset:768\n"
    "v_exp_f32 v191, v191\n"
    "v_add_f32 v22, v22, v189\n"
    "v_mfma_f32_16x16x32_bf16 v[184:187], a[224:227], v[112:115], v[184:187]\n"
    "ds_read_b64_tr_b16 v[144:145], v16 offset:0\n"
    "v_add_f32 v22, v22, v190\n"
    "v_cvt_pk_bf16_f32 v210, v188, v189\n"
    "v_cvt_pk_bf16_f32 v211, v190, v191\n"
    "v_add_f32 v22, v22, v191\n"
    "v_mfma_f32_16x16x32_bf16 v[188:191], a[224:227], v[128:131], v[180:183]\n"
    "s_waitcnt vmcnt(5)\n"
    "v_exp_f32 v192, v192\n"
    "v_mfma_f32_16x16x32_bf16 v[188:191], a[228:231], v[132:135], v[188:191]\n"
    "v_exp_f32 v193, v193\n"
    "v_mfma_f32_16x16x32_bf16 v[188:191], a[232:235], v[136:139], v[188:191]\n"
    "buffer_load_dwordx4 v10, s[48:51], s58 offen offset:2048 nt lds\n"
    "v_exp_f32 v194, v194\n"
    "v_add_f32 v23, v23, v192\n"
    "v_exp_f32 v195, v195\n"
    "v_add_f32 v23, v23, v193\n"
    "v_mfma_f32_16x16x32_bf16 v[188:191], a[236:239], v[140:143], v[188:191]\n"
    "s_waitcnt lgkmcnt(3)\n"
    "ds_read_b64_tr_b16 v[146:147], v16 offset:4096\n"
    "v_add_f32 v23, v23, v194\n"
    "v_cvt_pk_bf16_f32 v214, v192, v193\n"
    "v_cvt_pk_bf16_f32 v215, v194, v195\n"
    "v_add_f32 v23, v23, v195\n"
    "v_mfma_f32_16x16x32_bf16 v[192:195], a[240:243], v[32:35], v[156:159]\n"
    "s_add_u32 m0, s57, 77824\n"
    "v_exp_f32 v196, v196\n"
    "v_mfma_f32_16x16x32_bf16 v[192:195], a[244:247], v[36:39], v[192:195]\n"
    "s_waitcnt lgkmcnt(3)\n"
    "v_exp_f32 v197, v197\n"
    "v_mfma_f32_16x16x32_bf16 v[192:195], a[248:251], v[40:43], v[192:195]\n"
    "s_waitcnt lgkmcnt(2)\n"
    "v_exp_f32 v198, v198\n"
    "v_add_f32 v24, v24, v196\n"
    "v_exp_f32 v199, v199\n"
    "v_add_f32 v24, v24, v197\n"
    "v_mfma_f32_16x16x32_bf16 v[192:195], a[252:255], v[44:47], v[192:195]\n"
    "s_add_u32 s58, s58, 4096\n"
    "ds_read_b64_tr_b16 v[148:149], v19 offset:0\n"
    "v_add_f32 v24, v24, v198\n"
    "v_cvt_pk_bf16_f32 v218, v196, v197\n"
    "v_cvt_pk_bf16_f32 v219, v198, v199\n"
    "v_add_f32 v24, v24, v199\n"
    "v_mfma_f32_16x16x32_bf16 v[196:199], a[252:255], v[60:63], v[160:163]\n"
    "v_exp_f32 v184, v184\n"
    "v_mfma_f32_16x16x32_bf16 v[196:199], a[248:251], v[56:59], v[196:199]\n"
    "v_exp_f32 v185, v185\n"
    "v_mfma_f32_16x16x32_bf16 v[196:199], a[244:247], v[52:55], v[196:199]\n"
    "v_exp_f32 v186, v186\n"
    "v_mfma_f32_16x16x32_bf16 v[196:199], a[240:243], v[48:51], v[196:199]\n"
    "s_waitcnt lgkmcnt(1)\n"
    "v_exp_f32 v187, v187\n"
    "v_mfma_f32_16x16x32_bf16 a[0:3], v[144:147], v[200:203], 0\n"
    "s_waitcnt vmcnt(5)\n"
    "v_exp_f32 v188, v188\n"
    "v_mfma_f32_16x16x32_bf16 a[4:7], v[144:147], v[204:207], 0\n"
    "buffer_load_dwordx4 v10, s[48:51], s58 offen nt lds\n"
    "v_mfma_f32_16x16x32_bf16 a[8:11], v[144:147], v[208:211], 0\n"
    "v_exp_f32 v189, v189\n"
    "v_mfma_f32_16x16x32_bf16 a[12:15], v[144:147], v[212:215], 0\n"
    "v_exp_f32 v190, v190\n"
    "v_cvt_pk_bf16_f32 v222, v184, v185\n"
    "v_cvt_pk_bf16_f32 v223, v186, v187\n"
    "v_exp_f32 v191, v191\n"
    "v_mfma_f32_16x16x32_bf16 a[16:19], v[144:147], v[216:219], 0\n"
    "ds_read_b64_tr_b16 v[150:151], v19 offset:4096\n"
    "v_cvt_pk_bf16_f32 v226, v188, v189\n"
    "v_cvt_pk_bf16_f32 v227, v190, v191\n"
    "v_mfma_f32_16x16x32_bf16 a[20:23], v[144:147], v[220:223], 0\n"
    "ds_read_b64_tr_b16 v[152:153], v16 offset:256\n"
    "v_add_f32 v25, v25, v184\n"
    "v_add_f32 v25, v25, v185\n"
    "v_mfma_f32_16x16x32_bf16 a[24:27], v[144:147], v[224:227], 0\n"
    "ds_read_b64_tr_b16 v[154:155], v16 offset:4352\n"
    "v_add_f32 v25, v25, v186\n"
    "v_add_f32 v25, v25, v187\n"
    "v_mfma_f32_16x16x32_bf16 v[184:187], a[240:243], v[64:67], v[164:167]\n"
    "ds_read_b64_tr_b16 v[28:29], v19 offset:256\n"
    "v_add_f32 v26, v26, v188\n"
    "v_mfma_f32_16x16x32_bf16 v[184:187], a[244:247], v[68:71], v[184:187]\n"
    "ds_read_b64_tr_b16 v[30:31], v19 offset:4352\n"
    "v_add_f32 v26, v26, v189\n"
    "v_mfma_f32_16x16x32_bf16 v[184:187], a[248:251], v[72:75], v[184:187]\n"
    "ds_read_b128 a[224:227], v11 offset:4096\n"
    "v_add_f32 v26, v26, v190\n"
    "v_mfma_f32_16x16x32_bf16 v[184:187], a[252:255], v[76:79], v[184:187]\n"
    "s_waitcnt vmcnt(5)\n"
    "ds_read_b128 a[228:231], v11 offset:4352\n"
    "v_add_f32 v26, v26, v191\n"
    "v_mfma_f32_16x16x32_bf16 v[188:191], a[252:255], v[92:95], v[168:171]\n"
    "v_exp_f32 v192, v192\n"
    "v_mfma_f32_16x16x32_bf16 v[188:191], a[248:251], v[88:91], v[188:191]\n"
    "buffer_load_dwordx4 v10, s[48:51], s58 offen offset:2048 nt lds\n"
    "v_mfma_f32_16x16x32_bf16 v[188:191], a[244:247], v[84:87], v[188:191]\n"
    "s_add_u32 m0, s57, 98304\n"
    "v_exp_f32 v193, v193\n"
    "v_mfma_f32_16x16x32_bf16 v[188:191], a[240:243], v[80:83], v[188:191]\n"
    "s_waitcnt lgkmcnt(6)\n"
    "v_exp_f32 v194, v194\n"
    "v_mfma_f32_16x16x32_bf16 a[28:31], v[148:151], v[200:203], 0\n"
    "s_add_u32 s58, s65, 32768\n"
    "ds_read_b128 a[232:235], v11 offset:4608\n"
    "v_add_f32 v20, v20, v192\n"
    "v_mfma_f32_16x16x32_bf16 a[32:35], v[148:151], v[204:207], 0\n"
    "s_and_b32 s58, s58, 0x1ffff\n"
    "v_exp_f32 v195, v195\n"
    "v_mfma_f32_16x16x32_bf16 a[36:39], v[148:151], v[208:211], 0\n"
    "ds_read_b128 a[236:239], v11 offset:4864\n"
    "v_add_f32 v20, v20, v193\n"
    "v_mfma_f32_16x16x32_bf16 a[40:43], v[148:151], v[212:215], 0\n"
    "ds_read_b64_tr_b16 v[144:145], v16 offset:512\n"
    "v_add_f32 v20, v20, v194\n"
    "v_mfma_f32_16x16x32_bf16 a[44:47], v[148:151], v[216:219], 0\n"
    "ds_read_b64_tr_b16 v[146:147], v16 offset:4608\n"
    "v_cvt_pk_bf16_f32 v228, v192, v193\n"
    "v_mfma_f32_16x16x32_bf16 a[48:51], v[148:151], v[220:223], 0\n"
    "v_cvt_pk_bf16_f32 v229, v194, v195\n"
    "v_add_f32 v20, v20, v195\n"
    "v_mfma_f32_16x16x32_bf16 a[52:55], v[148:151], v[224:227], 0\n"
    "v_exp_f32 v196, v196\n"
    "v_mfma_f32_16x16x32_bf16 v[192:195], a[240:243], v[96:99], v[172:175]\n"
    "v_exp_f32 v197, v197\n"
    "v_mfma_f32_16x16x32_bf16 v[192:195], a[244:247], v[100:103], v[192:195]\n"
    "v_exp_f32 v198, v198\n"
    "v_mfma_f32_16x16x32_bf16 v[192:195], a[248:251], v[104:107], v[192:195]\n"
    "ds_read_b64_tr_b16 v[148:149], v19 offset:512\n"
    "v_add_f32 v21, v21, v196\n"
    "v_exp_f32 v199, v199\n"
    "v_add_f32 v21, v21, v197\n"
    "v_mfma_f32_16x16x32_bf16 v[192:195], a[252:255], v[108:111], v[192:195]\n"
    "ds_read_b64_tr_b16 v[150:151], v19 offset:4608\n"
    "v_add_f32 v21, v21, v198\n"
    "v_cvt_pk_bf16_f32 v232, v196, v197\n"
    "v_cvt_pk_bf16_f32 v233, v198, v199\n"
    "v_add_f32 v21, v21, v199\n"
    "v_mfma_f32_16x16x32_bf16 v[196:199], a[252:255], v[124:127], v[176:179]\n"
    "v_exp_f32 v184, v184\n"
    "v_mfma_f32_16x16x32_bf16 v[196:199], a[248:251], v[120:123], v[196:199]\n"
    "s_waitcnt vmcnt(5)\n"
    "v_exp_f32 v185, v185\n"
    "v_mfma_f32_16x16x32_bf16 v[196:199], a[244:247], v[116:119], v[196:199]\n"
    "v_exp_f32 v186, v186\n"
    "v_mfma_f32_16x16x32_bf16 v[196:199], a[240:243], v[112:115], v[196:199]\n"
    "s_waitcnt lgkmcnt(4)\n"
    "v_add_f32 v22, v22, v184\n"
    "v_add_f32 v22, v22, v185\n"
    "v_mfma_f32_16x16x32_bf16 a[56:59], v[152:155], v[200:203], 0\n"
    "buffer_load_dwordx4 v8, s[44:47], s58 offen lds\n"
    "v_mfma_f32_16x16x32_bf16 a[60:63], v[152:155], v[204:207], 0\n"
    "v_exp_f32 v187, v187\n"
    "v_mfma_f32_16x16x32_bf16 a[64:67], v[152:155], v[208:211], 0\n"
    "v_add_f32 v22, v22, v186\n"
    "v_cvt_pk_bf16_f32 v236, v184, v185\n"
    "v_mfma_f32_16x16x32_bf16 a[68:71], v[152:155], v[212:215], 0\n"
    "v_cvt_pk_bf16_f32 v237, v186, v187\n"
    "v_add_f32 v22, v22, v187\n"
    "v_mfma_f32_16x16x32_bf16 a[72:75], v[152:155], v[216:219], 0\n"
    "v_exp_f32 v188, v188\n"
    "v_mfma_f32_16x16x32_bf16 a[76:79], v[152:155], v[220:223], 0\n"
    "v_exp_f32 v189, v189\n"
    "v_mfma_f32_16x16x32_bf16 a[80:83], v[152:155], v[224:227], 0\n"
    "v_exp_f32 v190, v190\n"
    "v_mfma_f32_16x16x32_bf16 v[184:187], a[240:243], v[128:131], v[180:183]\n"
    "ds_read_b64_tr_b16 v[152:153], v16 offset:768\n"
    "v_add_f32 v23, v23, v188\n"
    "v_mfma_f32_16x16x32_bf16 v[184:187], a[244:247], v[132:135], v[184:187]\n"
    "v_exp_f32 v191, v191\n"
    "v_mfma_f32_16x16x32_bf16 v[184:187], a[248:251], v[136:139], v[184:187]\n"
    "ds_read_b64_tr_b16 v[154:155], v16 offset:4864\n"
    "v_add_f32 v23, v23, v189\n"
    "v_mfma_f32_16x16x32_bf16 v[184:187], a[252:255], v[140:143], v[184:187]\n"
    "v_add_f32 v23, v23, v190\n"
    "v_cvt_pk_bf16_f32 v240, v188, v189\n"
    "v_mfma_f32_16x16x32_bf16 a[84:87], v[28:31], v[200:203], 0\n"
    "v_cvt_pk_bf16_f32 v241, v190, v191\n"
    "v_add_f32 v23, v23, v191\n"
    "v_mfma_f32_16x16x32_bf16 a[88:91], v[28:31], v[204:207], 0\n"
    "v_exp_f32 v192, v192\n"
    "v_mfma_f32_16x16x32_bf16 a[92:95], v[28:31], v[208:211], 0\n"
    "s_waitcnt vmcnt(5)\n"
    "v_exp_f32 v193, v193\n"
    "v_mfma_f32_16x16x32_bf16 a[96:99], v[28:31], v[212:215], 0\n"
    "v_exp_f32 v194, v194\n"
    "v_mfma_f32_16x16x32_bf16 a[100:103], v[28:31], v[216:219], 0\n"
    "buffer_load_dwordx4 v9, s[44:47], s58 offen offset:2048 lds\n"
    "v_mfma_f32_16x16x32_bf16 a[104:107], v[28:31], v[220:223], 0\n"
    "s_add_u32 m0, s57, 102400\n"
    "v_add_f32 v24, v24, v192\n"
    "v_add_f32 v24, v24, v193\n"
    "v_mfma_f32_16x16x32_bf16 a[108:111], v[28:31], v[224:227], 0\n"
    "s_add_u32 s58, s58, 4096\n"
    "v_exp_f32 v195, v195\n"
    "v_mfma_f32_16x16x32_bf16 v[188:191], a[224:227], v[32:35], v[156:159]\n"
    "ds_read_b64_tr_b16 v[28:29], v19 offset:768\n"
    "v_add_f32 v24, v24, v194\n"
    "v_mfma_f32_16x16x32_bf16 v[188:191], a[228:231], v[36:39], v[188:191]\n"
    "ds_read_b64_tr_b16 v[30:31], v19 offset:4864\n"
    "v_cvt_pk_bf16_f32 v244, v192, v193\n"
    "v_mfma_f32_16x16x32_bf16 v[188:191], a[232:235], v[40:43], v[188:191]\n"
    "v_cvt_pk_bf16_f32 v245, v194, v195\n"
    "v_add_f32 v24, v24, v195\n"
    "v_mfma_f32_16x16x32_bf16 v[188:191], a[236:239], v[44:47], v[188:191]\n"
    "v_exp_f32 v196, v196\n"
    "v_mfma_f32_16x16x32_bf16 v[192:195], a[236:239], v[60:63], v[160:163]\n"
    "v_exp_f32 v197, v197\n"
    "v_mfma_f32_16x16x32_bf16 v[192:195], a[232:235], v[56:59], v[192:195]\n"
    "v_exp_f32 v198, v198\n"
    "v_mfma_f32_16x16x32_bf16 v[192:195], a[228:231], v[52:55], v[192:195]\n"
    "s_memrealtime s[24:25]\n"
    "v_add_f32 v25, v25, v196\n"
    "v_add_f32 v25, v25, v197\n"
    "v_mfma_f32_16x16x32_bf16 v[192:195], a[224:227], v[48:51], v[192:195]\n"
    "s_waitcnt lgkmcnt(2)\n"
    "v_exp_f32 v199, v199\n"
    "v_mfma_f32_16x16x32_bf16 a[112:115], v[144:147], v[200:203], 0\n"
    "v_add_f32 v25, v25, v198\n"
    "v_cvt_pk_bf16_f32 v248, v196, v197\n"
    "v_mfma_f32_16x16x32_bf16 a[116:119], v[144:147], v[204:207], 0\n"
    "v_cvt_pk_bf16_f32 v249, v198, v199\n"
    "v_add_f32 v25, v25, v199\n"
    "v_mfma_f32_16x16x32_bf16 a[120:123], v[144:147], v[208:211], 0\n"
    "v_exp_f32 v184, v184\n"
    "v_mfma_f32_16x16x32_bf16 a[124:127], v[144:147], v[212:215], 0\n"
    "v_exp_f32 v185, v185\n"
    "v_mfma_f32_16x16x32_bf16 a[128:131], v[144:147], v[216:219], 0\n"
    "s_waitcnt vmcnt(5)\n"
    "v_exp_f32 v186, v186\n"
    "v_mfma_f32_16x16x32_bf16 a[132:135], v[144:147], v[220:223], 0\n"
    "v_add_f32 v26, v26, v184\n"
    "v_add_f32 v26, v26, v185\n"
    "v_mfma_f32_16x16x32_bf16 a[136:139], v[144:147], v[224:227], 0\n"
    "buffer_load_dwordx4 v8, s[44:47], s58 offen lds\n"
    "v_mfma_f32_16x16x32_bf16 v[196:199], a[224:227], v[64:67], v[164:167]\n"
    "v_exp_f32 v187, v187\n"
    "v_mfma_f32_16x16x32_bf16 v[196:199], a[228:231], v[68:71], v[196:199]\n"
    "s_waitcnt vmcnt(15) lgkmcnt(0)\n"
    "s_barrier\n"
    "ds_read_b128 a[240:243], v11 offset:32768\n"
    "v_mfma_f32_16x16x32_bf16 v[196:199], a[232:235], v[72:75], v[196:199]\n"
    "s_add_u32 s56, s24, 125\n"
    "ds_read_b128 a[244:247], v11 offset:33024\n"
    "v_add_f32 v26, v26, v186\n"
    "v_mfma_f32_16x16x32_bf16 v[196:199], a[236:239], v[76:79], v[196:199]\n"
    "ds_read_b128 a[248:251], v11 offset:33280\n"
    "v_cvt_pk_bf16_f32 v252, v184, v185\n"
    "v_cvt_pk_bf16_f32 v253, v186, v187\n"
    "v_add_f32 v26, v26, v187\n"
    "v_mfma_f32_16x16x32_bf16 v[184:187], a[236:239], v[92:95], v[168:171]\n"
    "v_exp_f32 v188, v188\n"
    "v_mfma_f32_16x16x32_bf16 v[184:187], a[232:235], v[88:91], v[184:187]\n"
    "v_exp_f32 v189, v189\n"
    "v_mfma_f32_16x16x32_bf16 v[184:187], a[228:231], v[84:87], v[184:187]\n"
    "v_exp_f32 v190, v190\n"
    "v_mfma_f32_16x16x32_bf16 v[184:187], a[224:227], v[80:83], v[184:187]\n"
    "ds_read_b128 a[252:255], v11 offset:33536\n"
    "v_add_f32 v20, v20, v188\n"
    "v_mfma_f32_16x16x32_bf16 a[140:143], v[148:151], v[200:203], 0\n"
    "v_exp_f32 v191, v191\n"
    "v_mfma_f32_16x16x32_bf16 a[144:147], v[148:151], v[204:207], 0\n"
    "ds_read_b64_tr_b16 v[144:145], v14 offset:0\n"
    "v_add_f32 v20, v20, v189\n"
    "v_mfma_f32_16x16x32_bf16 a[148:151], v[148:151], v[208:211], 0\n"
    "ds_read_b64_tr_b16 v[146:147], v14 offset:4096\n"
    "v_add_f32 v20, v20, v190\n"
    "v_mfma_f32_16x16x32_bf16 a[152:155], v[148:151], v[212:215], 0\n"
    "v_cvt_pk_bf16_f32 v230, v188, v189\n"
    "v_cvt_pk_bf16_f32 v231, v190, v191\n"
    "v_mfma_f32_16x16x32_bf16 a[156:159], v[148:151], v[216:219], 0\n"
    "v_exp_f32 v192, v192\n"
    "v_mfma_f32_16x16x32_bf16 a[160:163], v[148:151], v[220:223], 0\n"
    "v_add_f32 v20, v20, v191\n"
    "v_add_f32 v21, v21, v192\n"
    "v_mfma_f32_16x16x32_bf16 a[164:167], v[148:151], v[224:227], 0\n"
    "s_waitcnt vmcnt(5)\n"
    "v_exp_f32 v193, v193\n"
    "v_mfma_f32_16x16x32_bf16 v[188:191], a[224:227], v[96:99], v[172:175]\n"
    "v_exp_f32 v194, v194\n"
    "v_mfma_f32_16x16x32_bf16 v[188:191], a[228:231], v[100:103], v[188:191]\n"
    "buffer_load_dwordx4 v9, s[44:47], s58 offen offset:2048 lds\n"
    "v_mfma_f32_16x16x32_bf16 v[188:191], a[232:235], v[104:107], v[188:191]\n"
    "s_add_u32 m0, s57, 131072\n"
    "v_exp_f32 v195, v195\n"
    "v_add_f32 v21, v21, v193\n"
    "v_mfma_f32_16x16x32_bf16 v[188:191], a[236:239], v[108:111], v[188:191]\n"
    "s_add_u32 s58, s65, 40960\n"
    "ds_read_b64_tr_b16 v[148:149], v17 offset:0\n"
    "v_add_f32 v21, v21, v194\n"
    "v_cvt_pk_bf16_f32 v234, v192, v193\n"
    "v_cvt_pk_bf16_f32 v235, v194, v195\n"
    "v_add_f32 v21, v21, v195\n"
    "v_mfma_f32_16x16x32_bf16 v[192:195], a[236:239], v[124:127], v[176:179]\n"
    "s_and_b32 s58, s58, 0x1ffff\n"
    "v_exp_f32 v196, v196\n"
    "v_mfma_f32_16x16x32_bf16 v[192:195], a[232:235], v[120:123], v[192:195]\n"
    "v_exp_f32 v197, v197\n"
    "v_mfma_f32_16x16x32_bf16 v[192:195], a[228:231], v[116:119], v[192:195]\n"
    "v_exp_f32 v198, v198\n"
    "v_mfma_f32_16x16x32_bf16 v[192:195], a[224:227], v[112:115], v[192:195]\n"
    "ds_read_b64_tr_b16 v[150:151], v17 offset:4096\n"
    "v_add_f32 v22, v22, v196\n"
    "v_mfma_f32_16x16x32_bf16 a[168:171], v[152:155], v[200:203], 0\n"
    "v_exp_f32 v199, v199\n"
    "v_mfma_f32_16x16x32_bf16 a[172:175], v[152:155], v[204:207], 0\n"
    "v_add_f32 v22, v22, v197\n"
    "v_add_f32 v22, v22, v198\n"
    "v_mfma_f32_16x16x32_bf16 a[176:179], v[152:155], v[208:211], 0\n"
    "v_cvt_pk_bf16_f32 v238, v196, v197\n"
    "v_cvt_pk_bf16_f32 v239, v198, v199\n"
    "v_mfma_f32_16x16x32_bf16 a[180:183], v[152:155], v[212:215], 0\n"
    "v_exp_f32 v184, v184\n"
    "v_mfma_f32_16x16x32_bf16 a[184:187], v[152:155], v[216:219], 0\n"
    "v_add_f32 v22, v22, v199\n"
    "v_add_f32 v23, v23, v184\n"
    "v_mfma_f32_16x16x32_bf16 a[188:191], v[152:155], v[220:223], 0\n"
    "v_exp_f32 v185, v185\n"
    "v_mfma_f32_16x16x32_bf16 a[192:195], v[152:155], v[224:227], 0\n"
    "v_exp_f32 v186, v186\n"
    "v_mfma_f32_16x16x32_bf16 v[196:199], a[224:227], v[128:131], v[180:183]\n"
    "v_exp_f32 v187, v187\n"
    "v_mfma_f32_16x16x32_bf16 v[196:199], a[228:231], v[132:135], v[196:199]\n"
    "s_waitcnt vmcnt(5)\n"
    "ds_read_b64_tr_b16 v[152:153], v14 offset:256\n"
    "v_add_f32 v23, v23, v185\n"
    "v_mfma_f32_16x16x32_bf16 v[196:199], a[232:235], v[136:139], v[196:199]\n"
    "ds_read_b64_tr_b16 v[154:155], v14 offset:4352\n"
    "v_add_f32 v23, v23, v186\n"
    "v_mfma_f32_16x16x32_bf16 v[196:199], a[236:239], v[140:143], v[196:199]\n"
    "buffer_load_dwordx4 v8, s[44:47], s58 offen lds\n"
    "v_mfma_f32_16x16x32_bf16 a[196:199], v[28:31], v[200:203], 0\n"
    "ds_read_b128 a[224:227], v11 offset:36864\n"
    "v_cvt_pk_bf16_f32 v242, v184, v185\n"
    "v_mfma_f32_16x16x32_bf16 a[200:203], v[28:31], v[204:207], 0\n"
    "ds_read_b128 a[228:231], v11 offset:37120\n"
    "v_cvt_pk_bf16_f32 v243, v186, v187\n"
    "v_mfma_f32_16x16x32_bf16 a[204:207], v[28:31], v[208:211], 0\n"
    "ds_read_b128 a[232:235], v11 offset:37376\n"
    "v_add_f32 v23, v23, v187\n"
    "v_mfma_f32_16x16x32_bf16 a[208:211], v[28:31], v[212:215], 0\n"
    "v_exp_f32 v188, v188\n"
    "v_mfma_f32_16x16x32_bf16 a[212:215], v[28:31], v[216:219], 0\n"
    "v_exp_f32 v189, v189\n"
    "v_mfma_f32_16x16x32_bf16 a[216:219], v[28:31], v[220:223], 0\n"
    "v_exp_f32 v190, v190\n"
    "v_mfma_f32_16x16x32_bf16 a[220:223], v[28:31], v[224:227], 0\n"
    "s_waitcnt lgkmcnt(5)\n"
    "ds_read_b128 a[236:239], v11 offset:37632\n"
    "v_add_f32 v24, v24, v188\n"
    "v_mfma_f32_16x16x32_bf16 v[184:187], a[240:243], v[32:35], v[156:159]\n"
    "v_exp_f32 v191, v191\n"
    "v_mfma_f32_16x16x32_bf16 v[184:187], a[244:247], v[36:39], v[184:187]\n"
    "ds_read_b64_tr_b16 v[28:29], v17 offset:256\n"
    "v_add_f32 v24, v24, v189\n"
    "v_mfma_f32_16x16x32_bf16 v[184:187], a[248:251], v[40:43], v[184:187]\n"
    "ds_read_b64_tr_b16 v[30:31], v17 offset:4352\n"
    "v_add_f32 v24, v24, v190\n"
    "v_mfma_f32_16x16x32_bf16 v[184:187], a[252:255], v[44:47], v[184:187]\n"
    "v_cvt_pk_bf16_f32 v246, v188, v189\n"
    "v_cvt_pk_bf16_f32 v247, v190, v191\n"
    "v_add_f32 v24, v24, v191\n"
    "v_mfma_f32_16x16x32_bf16 v[188:191], a[252:255], v[60:63], v[160:163]\n"
    "v_exp_f32 v192, v192\n"
    "v_mfma_f32_16x16x32_bf16 v[188:191], a[248:251], v[56:59], v[188:191]\n"
    "s_waitcnt vmcnt(5)\n"
    "v_exp_f32 v193, v193\n"
    "v_mfma_f32_16x16x32_bf16 v[188:191], a[244:247], v[52:55], v[188:191]\n"
    "v_exp_f32 v194, v194\n"
    "v_mfma_f32_16x16x32_bf16 v[188:191], a[240:243], v[48:51], v[188:191]\n"
    "buffer_load_dwordx4 v9, s[44:47], s58 offen offset:2048 lds\n"
    "v_mfma_f32_16x16x32_bf16 a[0:3], v[144:147], v[228:231], a[0:3]\n"
    "s_add_u32 m0, s57, 135168\n"
    "v_exp_f32 v195, v195\n"
    "v_mfma_f32_16x16x32_bf16 a[4:7], v[144:147], v[232:235], a[4:7]\n"
    "s_add_u32 s58, s58, 4096\n"
    "v_exp_f32 v196, v196\n"
    "v_mfma_f32_16x16x32_bf16 a[8:11], v[144:147], v[236:239], a[8:11]\n"
    "v_exp_f32 v197, v197\n"
    "v_mfma_f32_16x16x32_bf16 a[12:15], v[144:147], v[240:243], a[12:15]\n"
    "v_exp_f32 v198, v198\n"
    "v_cvt_pk_bf16_f32 v250, v192, v193\n"
    "v_cvt_pk_bf16_f32 v251, v194, v195\n"
    "v_exp_f32 v199, v199\n"
    "v_mfma_f32_16x16x32_bf16 a[16:19], v[144:147], v[244:247], a[16:19]\n"
    "v_cvt_pk_bf16_f32 v254, v196, v197\n"
    "v_cvt_pk_bf16_f32 v255, v198, v199\n"
    "v_mfma_f32_16x16x32_bf16 a[20:23], v[144:147], v[248:251], a[20:23]\n"
    "v_add_f32 v25, v25, v192\n"
    "v_add_f32 v25, v25, v193\n"
    "v_mfma_f32_16x16x32_bf16 a[24:27], v[144:147], v[252:255], a[24:27]\n"
    "v_add_f32 v25, v25, v194\n"
    "v_add_f32 v25, v25, v195\n"
    "v_mfma_f32_16x16x32_bf16 v[192:195], a[240:243], v[64:67], v[164:167]\n"
    "ds_read_b64_tr_b16 v[144:145], v14 offset:512\n"
    "v_add_f32 v26, v26, v196\n"
    "v_mfma_f32_16x16x32_bf16 v[192:195], a[244:247], v[68:71], v[192:195]\n"
    "ds_read_b64_tr_b16 v[146:147], v14 offset:4608\n"
    "v_add_f32 v26, v26, v197\n"
    "v_mfma_f32_16x16x32_bf16 v[192:195], a[248:251], v[72:75], v[192:195]\n"
    "v_exp_f32 v184, v184\n"
    "v_mfma_f32_16x16x32_bf16 v[192:195], a[252:255], v[76:79], v[192:195]\n"
    "v_add_f32 v26, v26, v198\n"
    "v_add_f32 v26, v26, v199\n"
    "v_mfma_f32_16x16x32_bf16 v[196:199], a[252:255], v[92:95], v[168:171]\n"
    "v_exp_f32 v185, v185\n"
    "v_mfma_f32_16x16x32_bf16 v[196:199], a[248:251], v[88:91], v[196:199]\n"
    "s_waitcnt vmcnt(5)\n"
    "v_exp_f32 v186, v186\n"
    "v_mfma_f32_16x16x32_bf16 v[196:199], a[244:247], v[84:87], v[196:199]\n"
    "v_add_f32 v20, v20, v184\n"
    "v_add_f32 v20, v20, v185\n"
    "v_mfma_f32_16x16x32_bf16 v[196:199], a[240:243], v[80:83], v[196:199]\n"
    "buffer_load_dwordx4 v8, s[44:47], s58 offen lds\n"
    "v_mfma_f32_16x16x32_bf16 a[28:31], v[148:151], v[228:231], a[28:31]\n"
    "v_exp_f32 v187, v187\n"
    "v_mfma_f32_16x16x32_bf16 a[32:35], v[148:151], v[232:235], a[32:35]\n"
    "v_add_f32 v20, v20, v186\n"
    "v_cvt_pk_bf16_f32 v200, v184, v185\n"
    "v_mfma_f32_16x16x32_bf16 a[36:39], v[148:151], v[236:239], a[36:39]\n"
    "v_cvt_pk_bf16_f32 v201, v186, v187\n"
    "v_add_f32 v20, v20, v187\n"
    "v_mfma_f32_16x16x32_bf16 a[40:43], v[148:151], v[240:243], a[40:43]\n"
    "v_exp_f32 v188, v188\n"
    "v_mfma_f32_16x16x32_bf16 a[44:47], v[148:151], v[244:247], a[44:47]\n"
    "v_exp_f32 v189, v189\n"
    "v_mfma_f32_16x16x32_bf16 a[48:51], v[148:151], v[248:251], a[48:51]\n"
    "v_exp_f32 v190, v190\n"
    "v_mfma_f32_16x16x32_bf16 a[52:55], v[148:151], v[252:255], a[52:55]\n"
    "ds_read_b64_tr_b16 v[148:149], v17 offset:512\n"
    "v_add_f32 v21, v21, v188\n"
    "v_mfma_f32_16x16x32_bf16 v[184:187], a[240:243], v[96:99], v[172:175]\n"
    "v_exp_f32 v191, v191\n"
    "v_mfma_f32_16x16x32_bf16 v[184:187], a[244:247], v[100:103], v[184:187]\n"
    "ds_read_b64_tr_b16 v[150:151], v17 offset:4608\n"
    "v_add_f32 v21, v21, v189\n"
    "v_mfma_f32_16x16x32_bf16 v[184:187], a[248:251], v[104:107], v[184:187]\n"
    "v_add_f32 v21, v21, v190\n"
    "v_cvt_pk_bf16_f32 v204, v188, v189\n"
    "v_mfma_f32_16x16x32_bf16 v[184:187], a[252:255], v[108:111], v[184:187]\n"
    "v_cvt_pk_bf16_f32 v205, v190, v191\n"
    "v_add_f32 v21, v21, v191\n"
    "v_mfma_f32_16x16x32_bf16 v[188:191], a[252:255], v[124:127], v[176:179]\n"
    "v_exp_f32 v192, v192\n"
    "v_mfma_f32_16x16x32_bf16 v[188:191], a[248:251], v[120:123], v[188:191]\n"
    "s_waitcnt vmcnt(5)\n"
    "v_exp_f32 v193, v193\n"
    "v_mfma_f32_16x16x32_bf16 v[188:191], a[244:247], v[116:119], v[188:191]\n"
    "v_exp_f32 v194, v194\n"
    "v_mfma_f32_16x16x32_bf16 v[188:191], a[240:243], v[112:115], v[188:191]\n"
    "s_waitcnt lgkmcnt(2)\n"
    "v_add_f32 v22, v22, v192\n"
    "v_add_f32 v22, v22, v193\n"
    "v_mfma_f32_16x16x32_bf16 a[56:59], v[152:155], v[228:231], a[56:59]\n"
    "buffer_load_dwordx4 v9, s[44:47], s58 offen offset:2048 lds\n"
    "v_mfma_f32_16x16x32_bf16 a[60:63], v[152:155], v[232:235], a[60:63]\n"
    "s_add_u32 m0, s57, 106496\n"
    "v_exp_f32 v195, v195\n"
    "v_mfma_f32_16x16x32_bf16 a[64:67], v[152:155], v[236:239], a[64:67]\n"
    "s_add_u32 s58, s65, 32768\n"
    "v_add_f32 v22, v22, v194\n"
    "v_cvt_pk_bf16_f32 v208, v192, v193\n"
    "v_mfma_f32_16x16x32_bf16 a[68:71], v[152:155], v[240:243], a[68:71]\n"
    "s_and_b32 s58, s58, 0x1ffff\n"
    "v_cvt_pk_bf16_f32 v209, v194, v195\n"
    "v_add_f32 v22, v22, v195\n"
    "v_mfma_f32_16x16x32_bf16 a[72:75], v[152:155], v[244:247], a[72:75]\n"
    "v_exp_f32 v196, v196\n"
    "v_mfma_f32_16x16x32_bf16 a[76:79], v[152:155], v[248:251], a[76:79]\n"
    "v_exp_f32 v197, v197\n"
    "v_mfma_f32_16x16x32_bf16 a[80:83], v[152:155], v[252:255], a[80:83]\n"
    "v_exp_f32 v198, v198\n"
    "v_mfma_f32_16x16x32_bf16 v[192:195], a[240:243], v[128:131], v[180:183]\n"
    "ds_read_b64_tr_b16 v[152:153], v14 offset:768\n"
    "v_add_f32 v23, v23, v196\n"
    "v_mfma_f32_16x16x32_bf16 v[192:195], a[244:247], v[132:135], v[192:195]\n"
    "v_exp_f32 v199, v199\n"
    "v_mfma_f32_16x16x32_bf16 v[192:195], a[248:251], v[136:139], v[192:195]\n"
    "ds_read_b64_tr_b16 v[154:155], v14 offset:4864\n"
    "v_add_f32 v23, v23, v197\n"
    "v_mfma_f32_16x16x32_bf16 v[192:195], a[252:255], v[140:143], v[192:195]\n"
    "v_add_f32 v23, v23, v198\n"
    "v_cvt_pk_bf16_f32 v212, v196, v197\n"
    "v_mfma_f32_16x16x32_bf16 a[84:87], v[28:31], v[228:231], a[84:87]\n"
    "v_cvt_pk_bf16_f32 v213, v198, v199\n"
    "v_add_f32 v23, v23, v199\n"
    "v_mfma_f32_16x16x32_bf16 a[88:91], v[28:31], v[232:235], a[88:91]\n"
    "s_waitcnt vmcnt(5)\n"
    "v_exp_f32 v184, v184\n"
    "v_mfma_f32_16x16x32_bf16 a[92:95], v[28:31], v[236:239], a[92:95]\n"
    "v_exp_f32 v185, v185\n"
    "v_mfma_f32_16x16x32_bf16 a[96:99], v[28:31], v[240:243], a[96:99]\n"
    "buffer_load_dwordx4 v10, s[48:51], s58 offen nt lds\n"
    "v_mfma_f32_16x16x32_bf16 a[100:103], v[28:31], v[244:247], a[100:103]\n"
    "v_exp_f32 v186, v186\n"
    "v_mfma_f32_16x16x32_bf16 a[104:107], v[28:31], v[248:251], a[104:107]\n"
    "v_add_f32 v24, v24, v184\n"
    "v_add_f32 v24, v24, v185\n"
    "v_mfma_f32_16x16x32_bf16 a[108:111], v[28:31], v[252:255], a[108:111]\n"
    "v_exp_f32 v187, v187\n"
    "v_mfma_f32_16x16x32_bf16 v[196:199], a[224:227], v[32:35], v[156:159]\n"
    "ds_read_b64_tr_b16 v[28:29], v17 offset:768\n"
    "v_add_f32 v24, v24, v186\n"
    "v_mfma_f32_16x16x32_bf16 v[196:199], a[228:231], v[36:39], v[196:199]\n"
    "ds_read_b64_tr_b16 v[30:31], v17 offset:4864\n"
    "v_cvt_pk_bf16_f32 v216, v184, v185\n"
    "v_mfma_f32_16x16x32_bf16 v[196:199], a[232:235], v[40:43], v[196:199]\n"
    "v_cvt_pk_bf16_f32 v217, v186, v187\n"
    "v_add_f32 v24, v24, v187\n"
    "v_mfma_f32_16x16x32_bf16 v[196:199], a[236:239], v[44:47], v[196:199]\n"
    "v_exp_f32 v188, v188\n"
    "v_mfma_f32_16x16x32_bf16 v[184:187], a[236:239], v[60:63], v[160:163]\n"
    "v_exp_f32 v189, v189\n"
    "v_mfma_f32_16x16x32_bf16 v[184:187], a[232:235], v[56:59], v[184:187]\n"
    "v_exp_f32 v190, v190\n"
    "v_mfma_f32_16x16x32_bf16 v[184:187], a[228:231], v[52:55], v[184:187]\n"
    "s_memrealtime s[24:25]\n"
    "v_add_f32 v25, v25, v188\n"
    "v_add_f32 v25, v25, v189\n"
    "v_mfma_f32_16x16x32_bf16 v[184:187], a[224:227], v[48:51], v[184:187]\n"
    "v_exp_f32 v191, v191\n"
    "v_mfma_f32_16x16x32_bf16 a[112:115], v[144:147], v[228:231], a[112:115]\n"
    "v_add_f32 v25, v25, v190\n"
    "v_cvt_pk_bf16_f32 v220, v188, v189\n"
    "v_mfma_f32_16x16x32_bf16 a[116:119], v[144:147], v[232:235], a[116:119]\n"
    "v_cvt_pk_bf16_f32 v221, v190, v191\n"
    "v_add_f32 v25, v25, v191\n"
    "v_mfma_f32_16x16x32_bf16 a[120:123], v[144:147], v[236:239], a[120:123]\n"
    "s_waitcnt vmcnt(5)\n"
    "v_exp_f32 v192, v192\n"
    "v_mfma_f32_16x16x32_bf16 a[124:127], v[144:147], v[240:243], a[124:127]\n"
    "v_exp_f32 v193, v193\n"
    "v_mfma_f32_16x16x32_bf16 a[128:131], v[144:147], v[244:247], a[128:131]\n"
    "buffer_load_dwordx4 v10, s[48:51], s58 offen offset:2048 nt lds\n"
    "v_mfma_f32_16x16x32_bf16 a[132:135], v[144:147], v[248:251], a[132:135]\n"
    "s_add_u32 m0, s57, 110592\n"
    "v_exp_f32 v194, v194\n"
    "v_mfma_f32_16x16x32_bf16 a[136:139], v[144:147], v[252:255], a[136:139]\n"
    "s_add_u32 s58, s58, 4096\n"
    "v_add_f32 v26, v26, v192\n"
    "v_add_f32 v26, v26, v193\n"
    "v_mfma_f32_16x16x32_bf16 v[188:191], a[224:227], v[64:67], v[164:167]\n"
    "v_exp_f32 v195, v195\n"
    "v_mfma_f32_16x16x32_bf16 v[188:191], a[228:231], v[68:71], v[188:191]\n"
    "s_waitcnt vmcnt(14) lgkmcnt(0)\n"
    "s_barrier\n"
    "ds_read_b128 a[240:243], v12 offset:0\n"
    "v_mfma_f32_16x16x32_bf16 v[188:191], a[232:235], v[72:75], v[188:191]\n"
    "s_sub_i32 s25, s26, s56\n"
    "ds_read_b128 a[244:247], v12 offset:256\n"
    "v_add_f32 v26, v26, v194\n"
    "v_mfma_f32_16x16x32_bf16 v[188:191], a[236:239], v[76:79], v[188:191]\n"
    "s_max_i32 s25, s25, -3\n"
    "ds_read_b128 a[248:251], v12 offset:512\n"
    "v_cvt_pk_bf16_f32 v224, v192, v193\n"
    "v_cvt_pk_bf16_f32 v225, v194, v195\n"
    "v_add_f32 v26, v26, v195\n"
    "v_mfma_f32_16x16x32_bf16 v[192:195], a[236:239], v[92:95], v[168:171]\n"
    "s_min_i32 s25, s25, 0\n"
    "v_exp_f32 v196, v196\n"
    "v_mfma_f32_16x16x32_bf16 v[192:195], a[232:235], v[88:91], v[192:195]\n"
    "s_add_u32 s56, s56, s25\n"
    "v_exp_f32 v197, v197\n"
    "v_mfma_f32_16x16x32_bf16 v[192:195], a[228:231], v[84:87], v[192:195]\n"
    "s_sub_i32 s25, s56, s24\n"
    "v_exp_f32 v198, v198\n"
    "v_mfma_f32_16x16x32_bf16 v[192:195], a[224:227], v[80:83], v[192:195]\n"
    "s_max_i32 s25, s25, -10\n"
    "ds_read_b128 a[252:255], v12 offset:768\n"
    "v_add_f32 v20, v20, v196\n"
    "v_mfma_f32_16x16x32_bf16 a[140:143], v[148:151], v[228:231], a[140:143]\n"
    "s_min_i32 s25, s25, 62\n"
    "v_exp_f32 v199, v199\n"
    "v_mfma_f32_16x16x32_bf16 a[144:147], v[148:151], v[232:235], a[144:147]\n"
    "s_add_u32 s56, s24, s25\n"
    "ds_read_b64_tr_b16 v[144:145], v14 offset:32768\n"
    "v_add_f32 v20, v20, v197\n"
    "v_mfma_f32_16x16x32_bf16 a[148:151], v[148:151], v[236:239], a[148:151]\n"
    "s_add_u32 s56, s56, 125\n"
    "ds_read_b64_tr_b16 v[146:147], v14 offset:36864\n"
    "v_add_f32 v20, v20, v198\n"
    "v_mfma_f32_16x16x32_bf16 a[152:155], v[148:151], v[240:243], a[152:155]\n"
    "s_cmp_gt_i32 s25, 2\ns_cbranch_scc1 L_ta2_%=\nL_tb2_%=:\n"
    "v_cvt_pk_bf16_f32 v202, v196, v197\n"
    "v_cvt_pk_bf16_f32 v203, v198, v199\n"
    "v_mfma_f32_16x16x32_bf16 a[156:159], v[148:151], v[244:247], a[156:159]\n"
    "s_add_u32 s26, s26, 125\n"
    "v_exp_f32 v184, v184\n"
    "v_mfma_f32_16x16x32_bf16 a[160:163], v[148:151], v[248:251], a[160:163]\n"
    "s_waitcnt vmcnt(5)\n"
    "v_add_f32 v20, v20, v199\n"
    "v_add_f32 v21, v21, v184\n"
    "v_mfma_f32_16x16x32_bf16 a[164:167], v[148:151], v[252:255], a[164:167]\n"
    "buffer_load_dwordx4 v10, s[48:51], s58 offen nt lds\n"
    "v_mfma_f32_16x16x32_bf16 v[196:199], a[224:227], v[96:99], v[172:175]\n"
    "v_exp_f32 v185, v185\n"
    "v_mfma_f32_16x16x32_bf16 v[196:199], a[228:231], v[100:103], v[196:199]\n"
    "v_exp_f32 v186, v186\n"
    "v_mfma_f32_16x16x32_bf16 v[196:199], a[232:235], v[104:107], v[196:199]\n"
    "v_exp_f32 v187, v187\n"
    "v_add_f32 v21, v21, v185\n"
    "v_mfma_f32_16x16x32_bf16 v[196:199], a[236:239], v[108:111], v[196:199]\n"
    "ds_read_b64_tr_b16 v[148:149], v17 offset:32768\n"
    "v_add_f32 v21, v21, v186\n"
    "v_cvt_pk_bf16_f32 v206, v184, v185\n"
    "v_cvt_pk_bf16_f32 v207, v186, v187\n"
    "v_add_f32 v21, v21, v187\n"
    "v_mfma_f32_16x16x32_bf16 v[184:187], a[236:239], v[124:127], v[176:179]\n"
    "v_exp_f32 v188, v188\n"
    "v_mfma_f32_16x16x32_bf16 v[184:187], a[232:235], v[120:123], v[184:187]\n"
    "v_exp_f32 v189, v189\n"
    "v_mfma_f32_16x16x32_bf16 v[184:187], a[228:231], v[116:119], v[184:187]\n"
    "v_exp_f32 v190, v190\n"
    "v_mfma_f32_16x16x32_bf16 v[184:187], a[224:227], v[112:115], v[184:187]\n"
    "ds_read_b64_tr_b16 v[150:151], v17 offset:36864\n"
    "v_add_f32 v22, v22, v188\n"
    "v_mfma_f32_16x16x32_bf16 a[168:171], v[152:155], v[228:231], a[168:171]\n"
    "v_exp_f32 v191, v191\n"
    "v_mfma_f32_16x16x32_bf16 a[172:175], v[152:155], v[232:235], a[172:175]\n"
    "v_add_f32 v22, v22, v189\n"
    "v_add_f32 v22, v22, v190\n"
    "v_mfma_f32_16x16x32_bf16 a[176:179], v[152:155], v[236:239], a[176:179]\n"
    "s_waitcnt vmcnt(5)\n"
    "v_cvt_pk_bf16_f32 v210, v188, v189\n"
    "v_cvt_pk_bf16_f32 v211, v190, v191\n"
    "v_mfma_f32_16x16x32_bf16 a[180:183], v[152:155], v[240:243], a[180:183]\n"
    "v_exp_f32 v192, v192\n"
    "v_mfma_f32_16x16x32_bf16 a[184:187], v[152:155], v[244:247], a[184:187]\n"
    "buffer_load_dwordx4 v10, s[48:51], s58 offen offset:2048 nt lds\n"
    "v_mfma_f32_16x16x32_bf16 a[188:191], v[152:155], v[248:251], a[188:191]\n"
    "s_add_u32 m0, s57, 0\n"
    "v_add_f32 v22, v22, v191\n"
    "v_add_f32 v23, v23, v192\n"
    "v_mfma_f32_16x16x32_bf16 a[192:195], v[152:155], v[252:255], a[192:195]\n"
    "s_add_u32 s58, s65, 49152\n"
    "v_exp_f32 v193, v193\n"
    "v_mfma_f32_16x16x32_bf16 v[188:191], a[224:227], v[128:131], v[180:183]\n"
    "s_and_b32 s58, s58, 0x1ffff\n"
    "v_exp_f32 v194, v194\n"
    "v_mfma_f32_16x16x32_bf16 v[188:191], a[228:231], v[132:135], v[188:191]\n"
    "v_exp_f32 v195, v195\n"
    "v_mfma_f32_16x16x32_bf16 v[188:191], a[232:235], v[136:139], v[188:191]\n"
    "ds_read_b64_tr_b16 v[152:153], v14 offset:33024\n"
    "v_add_f32 v23, v23, v193\n"
    "v_mfma_f32_16x16x32_bf16 v[188:191], a[236:239], v[140:143], v[188:191]\n"
    "ds_read_b64_tr_b16 v[154:155], v14 offset:37120\n"
    "v_add_f32 v23, v23, v194\n"
    "v_mfma_f32_16x16x32_bf16 a[196:199], v[28:31], v[228:231], a[196:199]\n"
    "ds_read_b128 a[224:227], v12 offset:4096\n"
    "v_cvt_pk_bf16_f32 v214, v192, v193\n"
    "v_mfma_f32_16x16x32_bf16 a[200:203], v[28:31], v[232:235], a[200:203]\n"
    "ds_read_b128 a[228:231], v12 offset:4352\n"
    "v_cvt_pk_bf16_f32 v215, v194, v195\n"
    "v_mfma_f32_16x16x32_bf16 a[204:207], v[28:31], v[236:239], a[204:207]\n"
    "ds_read_b128 a[232:235], v12 offset:4608\n"
    "v_add_f32 v23, v23, v195\n"
    "v_mfma_f32_16x16x32_bf16 a[208:211], v[28:31], v[240:243], a[208:211]\n"
    "v_exp_f32 v196, v196\n"
    "v_mfma_f32_16x16x32_bf16 a[212:215], v[28:31], v[244:247], a[212:215]\n"
    "v_exp_f32 v197, v197\n"
    "v_mfma_f32_16x16x32_bf16 a[216:219], v[28:31], v[248:251], a[216:219]\n"
    "v_exp_f32 v198, v198\n"
    "v_mfma_f32_16x16x32_bf16 a[220:223], v[28:31], v[252:255], a[220:223]\n"
    "s_waitcnt lgkmcnt(5)\n"
    "ds_read_b128 a[236:239], v12 offset:4864\n"
    "v_add_f32 v24, v24, v196\n"
    "v_mfma_f32_16x16x32_bf16 v[192:195], a[240:243], v[32:35], v[156:159]\n"
    "s_waitcnt vmcnt(5)\n"
    "v_exp_f32 v199, v199\n"
    "v_mfma_f32_16x16x32_bf16 v[192:195], a[244:247], v[36:39], v[192:195]\n"
    "buffer_load_dwordx4 v8, s[44:47], s58 offen lds\n"
    "v_mfma_f32_16x16x32_bf16 v[192:195], a[248:251], v[40:43], v[192:195]\n"
    "ds_read_b64_tr_b16 v[28:29], v17 offset:33024\n"
    "v_add_f32 v24, v24, v197\n"
    "v_mfma_f32_16x16x32_bf16 v[192:195], a[252:255], v[44:47], v[192:195]\n"
    "ds_read_b64_tr_b16 v[30:31], v17 offset:37120\n"
    "v_add_f32 v24, v24, v198\n"
    "v_cvt_pk_bf16_f32 v218, v196, v197\n"
    "v_cvt_pk_bf16_f32 v219, v198, v199\n"
    "v_add_f32 v24, v24, v199\n"
    "v_mfma_f32_16x16x32_bf16 v[196:199], a[252:255], v[60:63], v[160:163]\n"
    "v_exp_f32 v184, v184\n"
    "v_mfma_f32_16x16x32_bf16 v[196:199], a[248:251], v[56:59], v[196:199]\n"
    "v_exp_f32 v185, v185\n"
    "v_mfma_f32_16x16x32_bf16 v[196:199], a[244:247], v[52:55], v[196:199]\n"
    "v_exp_f32 v186, v186\n"
    "v_mfma_f32_16x16x32_bf16 v[196:199], a[240:243], v[48:51], v[196:199]\n"
    "v_exp_f32 v187, v187\n"
    "v_mfma_f32_16x16x32_bf16 a[0:3], v[144:147], v[200:203], a[0:3]\n"
    "v_exp_f32 v188, v188\n"
    "v_mfma_f32_16x16x32_bf16 a[4:7], v[144:147], v[204:207], a[4:7]\n"
    "v_exp_f32 v189, v189\n"
    "v_mfma_f32_16x16x32_bf16 a[8:11], v[144:147], v[208:211], a[8:11]\n"
    "v_exp_f32 v190, v190\n"
    "v_mfma_f32_16x16x32_bf16 a[12:15], v[144:147], v[212:215], a[12:15]\n"
    "v_exp_f32 v191, v191\n"
    "v_cvt_pk_bf16_f32 v222, v184, v185\n"
    "v_cvt_pk_bf16_f32 v223, v186, v187\n"
    "v_mfma_f32_16x16x32_bf16 a[16:19], v[144:147], v[216:219], a[16:19]\n"
    "v_cvt_pk_bf16_f32 v226, v188, v189\n"
    "v_cvt_pk_bf16_f32 v227, v190, v191\n"
    "v_mfma_f32_16x16x32_bf16 a[20:23], v[144:147], v[220:223], a[20:23]\n"
    "v_add_f32 v25, v25, v184\n"
    "v_add_f32 v25, v25, v185\n"
    "v_mfma_f32_16x16x32_bf16 a[24:27], v[144:147], v[224:227], a[24:27]\n"
    "s_waitcnt vmcnt(5)\n"
    "v_add_f32 v25, v25, v186\n"
    "v_add_f32 v25, v25, v187\n"
    "v_mfma_f32_16x16x32_bf16 v[184:187], a[240:243], v[64:67], v[164:167]\n"
    "ds_read_b64_tr_b16 v[144:145], v14 offset:33280\n"
    "v_add_f32 v26, v26, v188\n"
    "v_mfma_f32_16x16x32_bf16 v[184:187], a[244:247], v[68:71], v[184:187]\n"
    "buffer_load_dwordx4 v9, s[44:47], s58 offen offset:2048 lds\n"
    "v_mfma_f32_16x16x32_bf16 v[184:187], a[248:251], v[72:75], v[184:187]\n"
    "s_add_u32 m0, s57, 4096\n"
    "ds_read_b64_tr_b16 v[146:147], v14 offset:37376\n"
    "v_add_f32 v26, v26, v189\n"
    "v_mfma_f32_16x16x32_bf16 v[184:187], a[252:255], v[76:79], v[184:187]\n"
    "s_add_u32 s58, s58, 4096\n"
    "v_add_f32 v26, v26, v190\n"
    "v_add_f32 v26, v26, v191\n"
    "v_mfma_f32_16x16x32_bf16 v[188:191], a[252:255], v[92:95], v[168:171]\n"
    "v_exp_f32 v192, v192\n"
    "v_mfma_f32_16x16x32_bf16 v[188:191], a[248:251], v[88:91], v[188:191]\n"
    "v_exp_f32 v193, v193\n"
    "v_mfma_f32_16x16x32_bf16 v[188:191], a[244:247], v[84:87], v[188:191]\n"
    "v_exp_f32 v194, v194\n"
    "v_mfma_f32_16x16x32_bf16 v[188:191], a[240:243], v[80:83], v[188:191]\n"
    "v_add_f32 v20, v20, v192\n"
    "v_add_f32 v20, v20, v193\n"
    "v_mfma_f32_16x16x32_bf16 a[28:31], v[148:151], v[200:203], a[28:31]\n"
    "v_exp_f32 v195, v195\n"
    "v_mfma_f32_16x16x32_bf16 a[32:35], v[148:151], v[204:207], a[32:35]\n"
    "v_add_f32 v20, v20, v194\n"
    "v_cvt_pk_bf16_f32 v228, v192, v193\n"
    "v_mfma_f32_16x16x32_bf16 a[36:39], v[148:151], v[208:211], a[36:39]\n"
    "v_cvt_pk_bf16_f32 v229, v194, v195\n"
    "v_add_f32 v20, v20, v195\n"
    "v_mfma_f32_16x16x32_bf16 a[40:43], v[148:151], v[212:215], a[40:43]\n"
    "v_exp_f32 v196, v196\n"
    "v_mfma_f32_16x16x32_bf16 a[44:47], v[148:151], v[216:219], a[44:47]\n"
    "v_exp_f32 v197, v197\n"
    "v_mfma_f32_16x16x32_bf16 a[48:51], v[148:151], v[220:223], a[48:51]\n"
    "v_exp_f32 v198, v198\n"
    "v_mfma_f32_16x16x32_bf16 a[52:55], v[148:151], v[224:227], a[52:55]\n"
    "ds_read_b64_tr_b16 v[148:149], v17 offset:33280\n"
    "v_add_f32 v21, v21, v196\n"
    "v_mfma_f32_16x16x32_bf16 v[192:195], a[240:243], v[96:99], v[172:175]\n"
    "s_waitcnt vmcnt(5)\n"
    "v_exp_f32 v199, v199\n"
    "v_mfma_f32_16x16x32_bf16 v[192:195], a[244:247], v[100:103], v[192:195]\n"
    "ds_read_b64_tr_b16 v[150:151], v17 offset:37376\n"
    "v_add_f32 v21, v21, v197\n"
    "v_mfma_f32_16x16x32_bf16 v[192:195], a[248:251], v[104:107], v[192:195]\n"
    "buffer_load_dwordx4 v8, s[44:47], s58 offen lds\n"
    "v_mfma_f32_16x16x32_bf16 v[192:195], a[252:255], v[108:111], v[192:195]\n"
    "v_add_f32 v21, v21, v198\n"
    "v_cvt_pk_bf16_f32 v232, v196, v197\n"
    "v_cvt_pk_bf16_f32 v233, v198, v199\n"
    "v_add_f32 v21, v21, v199\n"
    "v_mfma_f32_16x16x32_bf16 v[196:199], a[252:255], v[124:127], v[176:179]\n"
    "v_exp_f32 v184, v184\n"
    "v_mfma_f32_16x16x32_bf16 v[196:199], a[248:251], v[120:123], v[196:199]\n"
    "v_exp_f32 v185, v185\n"
    "v_mfma_f32_16x16x32_bf16 v[196:199], a[244:247], v[116:119], v[196:199]\n"
    "v_exp_f32 v186, v186\n"
    "v_mfma_f32_16x16x32_bf16 v[196:199], a[240:243], v[112:115], v[196:199]\n"
    "s_waitcnt lgkmcnt(2)\n"
    "v_add_f32 v22, v22, v184\n"
    "v_add_f32 v22, v22, v185\n"
    "v_mfma_f32_16x16x32_bf16 a[56:59], v[152:155], v[200:203], a[56:59]\n"
    "v_exp_f32 v187, v187\n"
    "v_mfma_f32_16x16x32_bf16 a[60:63], v[152:155], v[204:207], a[60:63]\n"
    "v_add_f32 v22, v22, v186\n"
    "v_cvt_pk_bf16_f32 v236, v184, v185\n"
    "v_mfma_f32_16x16x32_bf16 a[64:67], v[152:155], v[208:211], a[64:67]\n"
    "v_cvt_pk_bf16_f32 v237, v186, v187\n"
    "v_add_f32 v22, v22, v187\n"
    "v_mfma_f32_16x16x32_bf16 a[68:71], v[152:155], v[212:215], a[68:71]\n"
    "v_exp_f32 v188, v188\n"
    "v_mfma_f32_16x16x32_bf16 a[72:75], v[152:155], v[216:219], a[72:75]\n"
    "v_exp_f32 v189, v189\n"
    "v_mfma_f32_16x16x32_bf16 a[76:79], v[152:155], v[220:223], a[76:79]\n"
    "v_exp_f32 v190, v190\n"
    "v_mfma_f32_16x16x32_bf16 a[80:83], v[152:155], v[224:227], a[80:83]\n"
    "ds_read_b64_tr_b16 v[152:153], v14 offset:33536\n"
    "v_add_f32 v23, v23, v188\n"
    "v_mfma_f32_16x16x32_bf16 v[184:187], a[240:243], v[128:131], v[180:183]\n"
    "s_waitcnt vmcnt(5)\n"
    "v_exp_f32 v191, v191\n"
    "v_mfma_f32_16x16x32_bf16 v[184:187], a[244:247], v[132:135], v[184:187]\n"
    "ds_read_b64_tr_b16 v[154:155], v14 offset:37632\n"
    "v_add_f32 v23, v23, v189\n"
    "v_mfma_f32_16x16x32_bf16 v[184:187], a[248:251], v[136:139], v[184:187]\n"
    "buffer_load_dwordx4 v9, s[44:47], s58 offen offset:2048 lds\n"
    "v_mfma_f32_16x16x32_bf16 v[184:187], a[252:255], v[140:143], v[184:187]\n"
    "s_add_u32 m0, s57, 139264\n"
    "v_add_f32 v23, v23, v190\n"
    "v_cvt_pk_bf16_f32 v240, v188, v189\n"
    "v_mfma_f32_16x16x32_bf16 a[84:87], v[28:31], v[200:203], a[84:87]\n"
    "s_add_u32 s58, s65, 40960\n"
    "v_cvt_pk_bf16_f32 v241, v190, v191\n"
    "v_add_f32 v23, v23, v191\n"
    "v_mfma_f32_16x16x32_bf16 a[88:91], v[28:31], v[204:207], a[88:91]\n"
    "s_and_b32 s58, s58, 0x1ffff\n"
    "v_exp_f32 v192, v192\n"
    "v_mfma_f32_16x16x32_bf16 a[92:95], v[28:31], v[208:211], a[92:95]\n"
    "v_exp_f32 v193, v193\n"
    "v_mfma_f32_16x16x32_bf16 a[96:99], v[28:31], v[212:215], a[96:99]\n"
    "v_exp_f32 v194, v194\n"
    "v_mfma_f32_16x16x32_bf16 a[100:103], v[28:31], v[216:219], a[100:103]\n"
    "v_add_f32 v24, v24, v192\n"
    "v_add_f32 v24, v24, v193\n"
    "v_mfma_f32_16x16x32_bf16 a[104:107], v[28:31], v[220:223], a[104:107]\n"
    "v_exp_f32 v195, v195\n"
    "v_mfma_f32_16x16x32_bf16 a[108:111], v[28:31], v[224:227], a[108:111]\n"
    "ds_read_b64_tr_b16 v[28:29], v17 offset:33536\n"
    "v_add_f32 v24, v24, v194\n"
    "v_mfma_f32_16x16x32_bf16 v[188:191], a[224:227], v[32:35], v[156:159]\n"
    "ds_read_b64_tr_b16 v[30:31], v17 offset:37632\n"
    "v_cvt_pk_bf16_f32 v244, v192, v193\n"
    "v_mfma_f32_16x16x32_bf16 v[188:191], a[228:231], v[36:39], v[188:191]\n"
    "v_cvt_pk_bf16_f32 v245, v194, v195\n"
    "v_add_f32 v24, v24, v195\n"
    "v_mfma_f32_16x16x32_bf16 v[188:191], a[232:235], v[40:43], v[188:191]\n"
    "v_exp_f32 v196, v196\n"
    "v_mfma_f32_16x16x32_bf16 v[188:191], a[236:239], v[44:47], v[188:191]\n"
    "v_exp_f32 v197, v197\n"
    "v_mfma_f32_16x16x32_bf16 v[192:195], a[236:239], v[60:63], v[160:163]\n"
    "s_waitcnt vmcnt(5)\n"
    "v_exp_f32 v198, v198\n"
    "v_mfma_f32_16x16x32_bf16 v[192:195], a[232:235], v[56:59], v[192:195]\n"
    "v_add_f32 v25, v25, v196\n"
    "v_add_f32 v25, v25, v197\n"
    "v_mfma_f32_16x16x32_bf16 v[192:195], a[228:231], v[52:55], v[192:195]\n"
    "buffer_load_dwordx4 v10, s[48:51], s58 offen nt lds\n"
    "v_mfma_f32_16x16x32_bf16 v[192:195], a[224:227], v[48:51], v[192:195]\n"
    "s_memrealtime s[24:25]\n"
    "v_exp_f32 v199, v199\n"
    "v_mfma_f32_16x16x32_bf16 a[112:115], v[144:147], v[200:203], a[112:115]\n"
    "v_add_f32 v25, v25, v198\n"
    "v_cvt_pk_bf16_f32 v248, v196, v197\n"
    "v_mfma_f32_16x16x32_bf16 a[116:119], v[144:147], v[204:207], a[116:119]\n"
    "v_cvt_pk_bf16_f32 v249, v198, v199\n"
    "v_add_f32 v25, v25, v199\n"
    "v_mfma_f32_16x16x32_bf16 a[120:123], v[144:147], v[208:211], a[120:123]\n"
    "v_exp_f32 v184, v184\n"
    "v_mfma_f32_16x16x32_bf16 a[124:127], v[144:147], v[212:215], a[124:127]\n"
    "v_exp_f32 v185, v185\n"
    "v_mfma_f32_16x16x32_bf16 a[128:131], v[144:147], v[216:219], a[128:131]\n"
    "v_exp_f32 v186, v186\n"
    "v_mfma_f32_16x16x32_bf16 a[132:135], v[144:147], v[220:223], a[132:135]\n"
    "v_add_f32 v26, v26, v184\n"
    "v_add_f32 v26, v26, v185\n"
    "v_mfma_f32_16x16x32_bf16 a[136:139], v[144:147], v[224:227], a[136:139]\n"
    "v_exp_f32 v187, v187\n"
    "v_mfma_f32_16x16x32_bf16 v[196:199], a[224:227], v[64:67], v[164:167]\n"
    "v_add_f32 v26, v26, v186\n"
    "v_cvt_pk_bf16_f32 v252, v184, v185\n"
    "v_mfma_f32_16x16x32_bf16 v[196:199], a[228:231], v[68:71], v[196:199]\n"
    "s_waitcnt vmcnt(13) lgkmcnt(0)\n"
    "s_barrier\n"
    "ds_read_b128 a[240:243], v12 offset:32768\n"
    "v_mfma_f32_16x16x32_bf16 v[196:199], a[232:235], v[72:75], v[196:199]\n"
    "s_sub_i32 s25, s26, s56\n"
    "ds_read_b128 a[244:247], v12 offset:33024\n"
    "v_cvt_pk_bf16_f32 v253, v186, v187\n"
    "v_mfma_f32_16x16x32_bf16 v[196:199], a[236:239], v[76:79], v[196:199]\n"
    "s_max_i32 s25, s25, -3\n"
    "ds_read_b128 a[248:251], v12 offset:33280\n"
    "v_add_f32 v26, v26, v187\n"
    "v_mfma_f32_16x16x32_bf16 v[184:187], a[236:239], v[92:95], v[168:171]\n"
    "s_min_i32 s25, s25, 0\n"
    "v_exp_f32 v188, v188\n"
    "v_mfma_f32_16x16x32_bf16 v[184:187], a[232:235], v[88:91], v[184:187]\n"
    "s_add_u32 s56, s56, s25\n"
    "v_exp_f32 v189, v189\n"
    "v_mfma_f32_16x16x32_bf16 v[184:187], a[228:231], v[84:87], v[184:187]\n"
    "s_sub_i32 s25, s56, s24\n"
    "v_exp_f32 v190, v190\n"
    "v_mfma_f32_16x16x32_bf16 v[184:187], a[224:227], v[80:83], v[184:187]\n"
    "s_max_i32 s25, s25, -10\n"
    "ds_read_b128 a[252:255], v12 offset:33536\n"
    "v_add_f32 v20, v20, v188\n"
    "v_mfma_f32_16x16x32_bf16 a[140:143], v[148:151], v[200:203], a[140:143]\n"
    "s_min_i32 s25, s25, 62\n"
    "v_exp_f32 v191, v191\n"
    "v_mfma_f32_16x16x32_bf16 a[144:147], v[148:151], v[204:207], a[144:147]\n"
    "s_add_u32 s56, s24, s25\n"
    "ds_read_b64_tr_b16 v[144:145], v15 offset:0\n"
    "v_add_f32 v20, v20, v189\n"
    "v_mfma_f32_16x16x32_bf16 a[148:151], v[148:151], v[208:211], a[148:151]\n"
    "s_add_u32 s56, s56, 125\n"
    "ds_read_b64_tr_b16 v[146:147], v15 offset:4096\n"
    "v_add_f32 v20, v20, v190\n"
    "v_mfma_f32_16x16x32_bf16 a[152:155], v[148:151], v[212:215], a[152:155]\n"
    "s_cmp_gt_i32 s25, 2\ns_cbranch_scc1 L_ta3_%=\nL_tb3_%=:\n"
    "v_cvt_pk_bf16_f32 v230, v188, v189\n"
    "v_cvt_pk_bf16_f32 v231, v190, v191\n"
    "v_mfma_f32_16x16x32_bf16 a[156:159], v[148:151], v[216:219], a[156:159]\n"
    "s_add_u32 s26, s26, 125\n"
    "v_exp_f32 v192, v192\n"
    "v_mfma_f32_16x16x32_bf16 a[160:163], v[148:151], v[220:223], a[160:163]\n"
    "s_waitcnt vmcnt(5)\n"
    "v_add_f32 v20, v20, v191\n"
    "v_add_f32 v21, v21, v192\n"
    "v_mfma_f32_16x16x32_bf16 a[164:167], v[148:151], v[224:227], a[164:167]\n"
    "buffer_load_dwordx4 v10, s[48:51], s58 offen offset:2048 nt lds\n"
    "v_mfma_f32_16x16x32_bf16 v[188:191], a[224:227], v[96:99], v[172:175]\n"
    "s_add_u32 m0, s57, 143360\n"
    "v_exp_f32 v193, v193\n"
    "v_mfma_f32_16x16x32_bf16 v[188:191], a[228:231], v[100:103], v[188:191]\n"
    "s_add_u32 s58, s58, 4096\n"
    "v_exp_f32 v194, v194\n"
    "v_mfma_f32_16x16x32_bf16 v[188:191], a[232:235], v[104:107], v[188:191]\n"
    "v_exp_f32 v195, v195\n"
    "v_add_f32 v21, v21, v193\n"
    "v_mfma_f32_16x16x32_bf16 v[188:191], a[236:239], v[108:111], v[188:191]\n"
    "ds_read_b64_tr_b16 v[148:149], v18 offset:0\n"
    "v_add_f32 v21, v21, v194\n"
    "v_cvt_pk_bf16_f32 v234, v192, v193\n"
    "v_cvt_pk_bf16_f32 v235, v194, v195\n"
    "v_add_f32 v21, v21, v195\n"
    "v_mfma_f32_16x16x32_bf16 v[192:195], a[236:239], v[124:127], v[176:179]\n"
    "s_waitcnt vmcnt(5)\n"
    "v_exp_f32 v196, v196\n"
    "v_mfma_f32_16x16x32_bf16 v[192:195], a[232:235], v[120:123], v[192:195]\n"
    "v_exp_f32 v197, v197\n"
    "v_mfma_f32_16x16x32_bf16 v[192:195], a[228:231], v[116:119], v[192:195]\n"
    "buffer_load_dwordx4 v10, s[48:51], s58 offen nt lds\n"
    "v_mfma_f32_16x16x32_bf16 v[192:195], a[224:227], v[112:115], v[192:195]\n"
    "v_exp_f32 v198, v198\n"
    "v_mfma_f32_16x16x32_bf16 a[168:171], v[152:155], v[200:203], a[168:171]\n"
    "ds_read_b64_tr_b16 v[150:151], v18 offset:4096\n"
    "v_add_f32 v22, v22, v196\n"
    "v_mfma_f32_16x16x32_bf16 a[172:175], v[152:155], v[204:207], a[172:175]\n"
    "v_exp_f32 v199, v199\n"
    "v_mfma_f32_16x16x32_bf16 a[176:179], v[152:155], v[208:211], a[176:179]\n"
    "v_add_f32 v22, v22, v197\n"
    "v_add_f32 v22, v22, v198\n"
    "v_mfma_f32_16x16x32_bf16 a[180:183], v[152:155], v[212:215], a[180:183]\n"
    "v_cvt_pk_bf16_f32 v238, v196, v197\n"
    "v_cvt_pk_bf16_f32 v239, v198, v199\n"
    "v_mfma_f32_16x16x32_bf16 a[184:187], v[152:155], v[216:219], a[184:187]\n"
    "v_exp_f32 v184, v184\n"
    "v_mfma_f32_16x16x32_bf16 a[188:191], v[152:155], v[220:223], a[188:191]\n"
    "v_add_f32 v22, v22, v199\n"
    "v_add_f32 v23, v23, v184\n"
    "v_mfma_f32_16x16x32_bf16 a[192:195], v[152:155], v[224:227], a[192:195]\n"
    "v_exp_f32 v185, v185\n"
    "v_mfma_f32_16x16x32_bf16 v[196:199], a[224:227], v[128:131], v[180:183]\n"
    "v_exp_f32 v186, v186\n"
    "v_mfma_f32_16x16x32_bf16 v[196:199], a[228:231], v[132:135], v[196:199]\n"
    "v_exp_f32 v187, v187\n"
    "v_mfma_f32_16x16x32_bf16 v[196:199], a[232:235], v[136:139], v[196:199]\n"
    "ds_read_b64_tr_b16 v[152:153], v15 offset:256\n"
    "v_add_f32 v23, v23, v185\n"
    "v_mfma_f32_16x16x32_bf16 v[196:199], a[236:239], v[140:143], v[196:199]\n"
    "ds_read_b64_tr_b16 v[154:155], v15 offset:4352\n"
    "v_add_f32 v23, v23, v186\n"
    "v_mfma_f32_16x16x32_bf16 a[196:199], v[28:31], v[200:203], a[196:199]\n"
    "ds_read_b128 a[224:227], v12 offset:36864\n"
    "v_cvt_pk_bf16_f32 v242, v184, v185\n"
    "v_mfma_f32_16x16x32_bf16 a[200:203], v[28:31], v[204:207], a[200:203]\n"
    "s_waitcnt vmcnt(5)\n"
    "ds_read_b128 a[228:231], v12 offset:37120\n"
    "v_cvt_pk_bf16_f32 v243, v186, v187\n"
    "v_mfma_f32_16x16x32_bf16 a[204:207], v[28:31], v[208:211], a[204:207]\n"
    "ds_read_b128 a[232:235], v12 offset:37376\n"
    "v_add_f32 v23, v23, v187\n"
    "v_mfma_f32_16x16x32_bf16 a[208:211], v[28:31], v[212:215], a[208:211]\n"
    "buffer_load_dwordx4 v10, s[48:51], s58 offen offset:2048 nt lds\n"
    "v_mfma_f32_16x16x32_bf16 a[212:215], v[28:31], v[216:219], a[212:215]\n"
    "s_add_u32 m0, s57, 32768\n"
    "v_exp_f32 v188, v188\n"
    "v_mfma_f32_16x16x32_bf16 a[216:219], v[28:31], v[220:223], a[216:219]\n"
    "s_add_u32 s58, s65, 57344\n"
    "v_exp_f32 v189, v189\n"
    "v_mfma_f32_16x16x32_bf16 a[220:223], v[28:31], v[224:227], a[220:223]\n"
    "s_waitcnt lgkmcnt(5)\n"
    "v_exp_f32 v190, v190\n"
    "v_mfma_f32_16x16x32_bf16 v[184:187], a[240:243], v[32:35], v[156:159]\n"
    "s_and_b32 s58, s58, 0x1ffff\n"
    "ds_read_b128 a[236:239], v12 offset:37632\n"
    "v_add_f32 v24, v24, v188\n"
    "v_mfma_f32_16x16x32_bf16 v[184:187], a[244:247], v[36:39], v[184:187]\n"
    "v_exp_f32 v191, v191\n"
    "v_mfma_f32_16x16x32_bf16 v[184:187], a[248:251], v[40:43], v[184:187]\n"
    "ds_read_b64_tr_b16 v[28:29], v18 offset:256\n"
    "v_add_f32 v24, v24, v189\n"
    "v_mfma_f32_16x16x32_bf16 v[184:187], a[252:255], v[44:47], v[184:187]\n"
    "ds_read_b64_tr_b16 v[30:31], v18 offset:4352\n"
    "v_add_f32 v24, v24, v190\n"
    "v_cvt_pk_bf16_f32 v246, v188, v189\n"
    "v_cvt_pk_bf16_f32 v247, v190, v191\n"
    "v_add_f32 v24, v24, v191\n"
    "v_mfma_f32_16x16x32_bf16 v[188:191], a[252:255], v[60:63], v[160:163]\n"
    "v_exp_f32 v192, v192\n"
    "v_mfma_f32_16x16x32_bf16 v[188:191], a[248:251], v[56:59], v[188:191]\n"
    "v_exp_f32 v193, v193\n"
    "v_mfma_f32_16x16x32_bf16 v[188:191], a[244:247], v[52:55], v[188:191]\n"
    "v_exp_f32 v194, v194\n"
    "v_mfma_f32_16x16x32_bf16 v[188:191], a[240:243], v[48:51], v[188:191]\n"
    "v_exp_f32 v195, v195\n"
    "v_mfma_f32_16x16x32_bf16 a[0:3], v[144:147], v[228:231], a[0:3]\n"
    "v_exp_f32 v196, v196\n"
    "v_mfma_f32_16x16x32_bf16 a[4:7], v[144:147], v[232:235], a[4:7]\n"
    "s_waitcnt vmcnt(5)\n"
    "v_exp_f32 v197, v197\n"
    "v_mfma_f32_16x16x32_bf16 a[8:11], v[144:147], v[236:239], a[8:11]\n"
    "v_exp_f32 v198, v198\n"
    "v_mfma_f32_16x16x32_bf16 a[12:15], v[144:147], v[240:243], a[12:15]\n"
    "buffer_load_dwordx4 v8, s[44:47], s58 offen lds\n"
    "v_cvt_pk_bf16_f32 v250, v192, v193\n"
    "v_cvt_pk_bf16_f32 v251, v194, v195\n"
    "v_exp_f32 v199, v199\n"
    "v_mfma_f32_16x16x32_bf16 a[16:19], v[144:147], v[244:247], a[16:19]\n"
    "v_cvt_pk_bf16_f32 v254, v196, v197\n"
    "v_cvt_pk_bf16_f32 v255, v198, v199\n"
    "v_mfma_f32_16x16x32_bf16 a[20:23], v[144:147], v[248:251], a[20:23]\n"
    "v_add_f32 v25, v25, v192\n"
    "v_add_f32 v25, v25, v193\n"
    "v_mfma_f32_16x16x32_bf16 a[24:27], v[144:147], v[252:255], a[24:27]\n"
    "v_add_f32 v25, v25, v194\n"
    "v_add_f32 v25, v25, v195\n"
    "v_mfma_f32_16x16x32_bf16 v[192:195], a[240:243], v[64:67], v[164:167]\n"
    "ds_read_b64_tr_b16 v[144:145], v15 offset:512\n"
    "v_add_f32 v26, v26, v196\n"
    "v_mfma_f32_16x16x32_bf16 v[192:195], a[244:247], v[68:71], v[192:195]\n"
    "ds_read_b64_tr_b16 v[146:147], v15 offset:4608\n"
    "v_add_f32 v26, v26, v197\n"
    "v_mfma_f32_16x16x32_bf16 v[192:195], a[248:251], v[72:75], v[192:195]\n"
    "v_exp_f32 v184, v184\n"
    "v_mfma_f32_16x16x32_bf16 v[192:195], a[252:255], v[76:79], v[192:195]\n"
    "v_add_f32 v26, v26, v198\n"
    "v_add_f32 v26, v26, v199\n"
    "v_mfma_f32_16x16x32_bf16 v[196:199], a[252:255], v[92:95], v[168:171]\n"
    "v_exp_f32 v185, v185\n"
    "v_mfma_f32_16x16x32_bf16 v[196:199], a[248:251], v[88:91], v[196:199]\n"
    "v_exp_f32 v186, v186\n"
    "v_mfma_f32_16x16x32_bf16 v[196:199], a[244:247], v[84:87], v[196:199]\n"
    "v_add_f32 v20, v20, v184\n"
    "v_add_f32 v20, v20, v185\n"
    "v_mfma_f32_16x16x32_bf16 v[196:199], a[240:243], v[80:83], v[196:199]\n"
    "v_exp_f32 v187, v187\n"
    "v_mfma_f32_16x16x32_bf16 a[28:31], v[148:151], v[228:231], a[28:31]\n"
    "v_add_f32 v20, v20, v186\n"
    "v_cvt_pk_bf16_f32 v200, v184, v185\n"
    "v_mfma_f32_16x16x32_bf16 a[32:35], v[148:151], v[232:235], a[32:35]\n"
    "s_waitcnt vmcnt(5)\n"
    "v_cvt_pk_bf16_f32 v201, v186, v187\n"
    "v_add_f32 v20, v20, v187\n"
    "v_mfma_f32_16x16x32_bf16 a[36:39], v[148:151], v[236:239], a[36:39]\n"
    "v_exp_f32 v188, v188\n"
    "v_mfma_f32_16x16x32_bf16 a[40:43], v[148:151], v[240:243], a[40:43]\n"
    "buffer_load_dwordx4 v9, s[44:47], s58 offen offset:2048 lds\n"
    "v_mfma_f32_16x16x32_bf16 a[44:47], v[148:151], v[244:247], a[44:47]\n"
    "s_add_u32 m0, s57, 36864\n"
    "v_exp_f32 v189, v189\n"
    "v_mfma_f32_16x16x32_bf16 a[48:51], v[148:151], v[248:251], a[48:51]\n"
    "s_add_u32 s58, s58, 4096\n"
    "v_exp_f32 v190, v190\n"
    "v_mfma_f32_16x16x32_bf16 a[52:55], v[148:151], v[252:255], a[52:55]\n"
    "ds_read_b64_tr_b16 v[148:149], v18 offset:512\n"
    "v_add_f32 v21, v21, v188\n"
    "v_mfma_f32_16x16x32_bf16 v[184:187], a[240:243], v[96:99], v[172:175]\n"
    "v_exp_f32 v191, v191\n"
    "v_mfma_f32_16x16x32_bf16 v[184:187], a[244:247], v[100:103], v[184:187]\n"
    "ds_read_b64_tr_b16 v[150:151], v18 offset:4608\n"
    "v_add_f32 v21, v21, v189\n"
    "v_mfma_f32_16x16x32_bf16 v[184:187], a[248:251], v[104:107], v[184:187]\n"
    "v_add_f32 v21, v21, v190\n"
    "v_cvt_pk_bf16_f32 v204, v188, v189\n"
    "v_mfma_f32_16x16x32_bf16 v[184:187], a[252:255], v[108:111], v[184:187]\n"
    "v_cvt_pk_bf16_f32 v205, v190, v191\n"
    "v_add_f32 v21, v21, v191\n"
    "v_mfma_f32_16x16x32_bf16 v[188:191], a[252:255], v[124:127], v[176:179]\n"
    "v_exp_f32 v192, v192\n"
    "v_mfma_f32_16x16x32_bf16 v[188:191], a[248:251], v[120:123], v[188:191]\n"
    "v_exp_f32 v193, v193\n"
    "v_mfma_f32_16x16x32_bf16 v[188:191], a[244:247], v[116:119], v[188:191]\n"
    "v_exp_f32 v194, v194\n"
    "v_mfma_f32_16x16x32_bf16 v[188:191], a[240:243], v[112:115], v[188:191]\n"
    "s_waitcnt lgkmcnt(2)\n"
    "v_add_f32 v22, v22, v192\n"
    "v_add_f32 v22, v22, v193\n"
    "v_mfma_f32_16x16x32_bf16 a[56:59], v[152:155], v[228:231], a[56:59]\n"
    "v_exp_f32 v195, v195\n"
    "v_mfma_f32_16x16x32_bf16 a[60:63], v[152:155], v[232:235], a[60:63]\n"
    "s_waitcnt vmcnt(5)\n"
    "v_add_f32 v22, v22, v194\n"
    "v_cvt_pk_bf16_f32 v208, v192, v193\n"
    "v_mfma_f32_16x16x32_bf16 a[64:67], v[152:155], v[236:239], a[64:67]\n"
    "v_cvt_pk_bf16_f32 v209, v194, v195\n"
    "v_add_f32 v22, v22, v195\n"
    "v_mfma_f32_16x16x32_bf16 a[68:71], v[152:155], v[240:243], a[68:71]\n"
    "buffer_load_dwordx4 v8, s[44:47], s58 offen lds\n"
    "v_mfma_f32_16x16x32_bf16 a[72:75], v[152:155], v[244:247], a[72:75]\n"
    "v_exp_f32 v196, v196\n"
    "v_mfma_f32_16x16x32_bf16 a[76:79], v[152:155], v[248:251], a[76:79]\n"
    "v_exp_f32 v197, v197\n"
    "v_mfma_f32_16x16x32_bf16 a[80:83], v[152:155], v[252:255], a[80:83]\n"
    "v_exp_f32 v198, v198\n"
    "v_mfma_f32_16x16x32_bf16 v[192:195], a[240:243], v[128:131], v[180:183]\n"
    "ds_read_b64_tr_b16 v[152:153], v15 offset:768\n"
    "v_add_f32 v23, v23, v196\n"
    "v_mfma_f32_16x16x32_bf16 v[192:195], a[244:247], v[132:135], v[192:195]\n"
    "v_exp_f32 v199, v199\n"
    "v_mfma_f32_16x16x32_bf16 v[192:195], a[248:251], v[136:139], v[192:195]\n"
    "ds_read_b64_tr_b16 v[154:155], v15 offset:4864\n"
    "v_add_f32 v23, v23, v197\n"
    "v_mfma_f32_16x16x32_bf16 v[192:195], a[252:255], v[140:143], v[192:195]\n"
    "v_add_f32 v23, v23, v198\n"
    "v_cvt_pk_bf16_f32 v212, v196, v197\n"
    "v_mfma_f32_16x16x32_bf16 a[84:87], v[28:31], v[228:231], a[84:87]\n"
    "v_cvt_pk_bf16_f32 v213, v198, v199\n"
    "v_add_f32 v23, v23, v199\n"
    "v_mfma_f32_16x16x32_bf16 a[88:91], v[28:31], v[232:235], a[88:91]\n"
    "v_exp_f32 v184, v184\n"
    "v_mfma_f32_16x16x32_bf16 a[92:95], v[28:31], v[236:239], a[92:95]\n"
    "v_exp_f32 v185, v185\n"
    "v_mfma_f32_16x16x32_bf16 a[96:99], v[28:31], v[240:243], a[96:99]\n"
    "v_exp_f32 v186, v186\n"
    "v_mfma_f32_16x16x32_bf16 a[100:103], v[28:31], v[244:247], a[100:103]\n"
    "v_add_f32 v24, v24, v184\n"
    "v_add_f32 v24, v24, v185\n"
    "v_mfma_f32_16x16x32_bf16 a[104:107], v[28:31], v[248:251], a[104:107]\n"
    "s_waitcnt vmcnt(5)\n"
    "v_exp_f32 v187, v187\n"
    "v_mfma_f32_16x16x32_bf16 a[108:111], v[28:31], v[252:255], a[108:111]\n"
    "ds_read_b64_tr_b16 v[28:29], v18 offset:768\n"
    "v_add_f32 v24, v24, v186\n"
    "v_mfma_f32_16x16x32_bf16 v[196:199], a[224:227], v[32:35], v[156:159]\n"
    "buffer_load_dwordx4 v9, s[44:47], s58 offen offset:2048 lds\n"
    "v_mfma_f32_16x16x32_bf16 v[196:199], a[228:231], v[36:39], v[196:199]\n"
    "s_add_u32 m0, s57, 8192\n"
    "ds_read_b64_tr_b16 v[30:31], v18 offset:4864\n"
    "v_cvt_pk_bf16_f32 v216, v184, v185\n"
    "v_mfma_f32_16x16x32_bf16 v[196:199], a[232:235], v[40:43], v[196:199]\n"
    "s_add_u32 s58, s65, 49152\n"
    "v_cvt_pk_bf16_f32 v217, v186, v187\n"
    "v_add_f32 v24, v24, v187\n"
    "v_mfma_f32_16x16x32_bf16 v[196:199], a[236:239], v[44:47], v[196:199]\n"
    "s_and_b32 s58, s58, 0x1ffff\n"
    "v_exp_f32 v188, v188\n"
    "v_mfma_f32_16x16x32_bf16 v[184:187], a[236:239], v[60:63], v[160:163]\n"
    "v_exp_f32 v189, v189\n"
    "v_mfma_f32_16x16x32_bf16 v[184:187], a[232:235], v[56:59], v[184:187]\n"
    "v_exp_f32 v190, v190\n"
    "v_mfma_f32_16x16x32_bf16 v[184:187], a[228:231], v[52:55], v[184:187]\n"
    "s_memrealtime s[24:25]\n"
    "v_add_f32 v25, v25, v188\n"
    "v_add_f32 v25, v25, v189\n"
    "v_mfma_f32_16x16x32_bf16 v[184:187], a[224:227], v[48:51], v[184:187]\n"
    "v_exp_f32 v191, v191\n"
    "v_mfma_f32_16x16x32_bf16 a[112:115], v[144:147], v[228:231], a[112:115]\n"
    "v_add_f32 v25, v25, v190\n"
    "v_cvt_pk_bf16_f32 v220, v188, v189\n"
    "v_mfma_f32_16x16x32_bf16 a[116:119], v[144:147], v[232:235], a[116:119]\n"
    "v_cvt_pk_bf16_f32 v221, v190, v191\n"
    "v_add_f32 v25, v25, v191\n"
    "v_mfma_f32_16x16x32_bf16 a[120:123], v[144:147], v[236:239], a[120:123]\n"
    "v_exp_f32 v192, v192\n"
    "v_mfma_f32_16x16x32_bf16 a[124:127], v[144:147], v[240:243], a[124:127]\n"
    "v_exp_f32 v193, v193\n"
    "v_mfma_f32_16x16x32_bf16 a[128:131], v[144:147], v[244:247], a[128:131]\n"
    "v_exp_f32 v194, v194\n"
    "v_mfma_f32_16x16x32_bf16 a[132:135], v[144:147], v[248:251], a[132:135]\n"
    "v_add_f32 v26, v26, v192\n"
    "v_add_f32 v26, v26, v193\n"
    "v_mfma_f32_16x16x32_bf16 a[136:139], v[144:147], v[252:255], a[136:139]\n"
    "s_waitcnt vmcnt(5)\n"
    "v_exp_f32 v195, v195\n"
    "v_mfma_f32_16x16x32_bf16 v[188:191], a[224:227], v[64:67], v[164:167]\n"
    "v_add_f32 v26, v26, v194\n"
    "v_cvt_pk_bf16_f32 v224, v192, v193\n"
    "v_mfma_f32_16x16x32_bf16 v[188:191], a[228:231], v[68:71], v[188:191]\n"
    "s_waitcnt vmcnt(12) lgkmcnt(0)\n"
    "s_barrier\n"
    "ds_read_b128 a[240:243], v13 offset:0\n"
    "v_mfma_f32_16x16x32_bf16 v[188:191], a[232:235], v[72:75], v[188:191]\n"
    "buffer_load_dwordx4 v10, s[48:51], s58 offen nt lds\n"
    "v_mfma_f32_16x16x32_bf16 v[188:191], a[236:239], v[76:79], v[188:191]\n"
    "s_sub_i32 s25, s26, s56\n"
    "ds_read_b128 a[244:247], v13 offset:256\n"
    "v_cvt_pk_bf16_f32 v225, v194, v195\n"
    "v_add_f32 v26, v26, v195\n"
    "v_mfma_f32_16x16x32_bf16 v[192:195], a[236:239], v[92:95], v[168:171]\n"
    "s_max_i32 s25, s25, -3\n"
    "v_exp_f32 v196, v196\n"
    "v_mfma_f32_16x16x32_bf16 v[192:195], a[232:235], v[88:91], v[192:195]\n"
    "s_min_i32 s25, s25, 0\n"
    "v_exp_f32 v197, v197\n"
    "v_mfma_f32_16x16x32_bf16 v[192:195], a[228:231], v[84:87], v[192:195]\n"
    "s_add_u32 s56, s56, s25\n"
    "v_exp_f32 v198, v198\n"
    "v_mfma_f32_16x16x32_bf16 v[192:195], a[224:227], v[80:83], v[192:195]\n"
    "s_sub_i32 s25, s56, s24\n"
    "ds_read_b128 a[248:251], v13 offset:512\n"
    "v_add_f32 v20, v20, v196\n"
    "v_mfma_f32_16x16x32_bf16 a[140:143], v[148:151], v[228:231], a[140:143]\n"
    "s_max_i32 s25, s25, -10\n"
    "v_exp_f32 v199, v199\n"
    "v_mfma_f32_16x16x32_bf16 a[144:147], v[148:151], v[232:235], a[144:147]\n"
    "s_min_i32 s25, s25, 62\n"
    "ds_read_b128 a[252:255], v13 offset:768\n"
    "v_add_f32 v20, v20, v197\n"
    "v_mfma_f32_16x16x32_bf16 a[148:151], v[148:151], v[236:239], a[148:151]\n"
    "s_add_u32 s56, s24, s25\n"
    "ds_read_b64_tr_b16 v[144:145], v15 offset:32768\n"
    "v_add_f32 v20, v20, v198\n"
    "v_mfma_f32_16x16x32_bf16 a[152:155], v[148:151], v[240:243], a[152:155]\n"
    "s_add_u32 s56, s56, 125\n"
    "ds_read_b64_tr_b16 v[146:147], v15 offset:36864\n"
    "v_cvt_pk_bf16_f32 v202, v196, v197\n"
    "v_mfma_f32_16x16x32_bf16 a[156:159], v[148:151], v[244:247], a[156:159]\n"
    "s_cmp_gt_i32 s25, 2\ns_cbranch_scc1 L_ta4_%=\nL_tb4_%=:\n"
    "v_cvt_pk_bf16_f32 v203, v198, v199\n"
    "v_add_f32 v20, v20, v199\n"
    "v_mfma_f32_16x16x32_bf16 a[160:163], v[148:151], v[248:251], a[160:163]\n"
    "s_add_u32 s26, s26, 125\n"
    "v_exp_f32 v184, v184\n"
    "v_mfma_f32_16x16x32_bf16 a[164:167], v[148:151], v[252:255], a[164:167]\n"
    "s_waitcnt vmcnt(5)\n"
    "v_exp_f32 v185, v185\n"
    "v_mfma_f32_16x16x32_bf16 v[196:199], a[224:227], v[96:99], v[172:175]\n"
    "v_exp_f32 v186, v186\n"
    "v_mfma_f32_16x16x32_bf16 v[196:199], a[228:231], v[100:103], v[196:199]\n"
    "buffer_load_dwordx4 v10, s[48:51], s58 offen offset:2048 nt lds\n"
    "v_mfma_f32_16x16x32_bf16 v[196:199], a[232:235], v[104:107], v[196:199]\n"
    "s_add_u32 m0, s57, 12288\n"
    "ds_read_b64_tr_b16 v[148:149], v18 offset:32768\n"
    "v_add_f32 v21, v21, v184\n"
    "v_exp_f32 v187, v187\n"
    "v_add_f32 v21, v21, v185\n"
    "v_mfma_f32_16x16x32_bf16 v[196:199], a[236:239], v[108:111], v[196:199]\n"
    "s_add_u32 s58, s58, 4096\n"
    "ds_read_b64_tr_b16 v[150:151], v18 offset:36864\n"
    "v_add_f32 v21, v21, v186\n"
    "v_cvt_pk_bf16_f32 v206, v184, v185\n"
    "v_cvt_pk_bf16_f32 v207, v186, v187\n"
    "v_add_f32 v21, v21, v187\n"
    "v_mfma_f32_16x16x32_bf16 v[184:187], a[236:239], v[124:127], v[176:179]\n"
    "v_exp_f32 v188, v188\n"
    "v_mfma_f32_16x16x32_bf16 v[184:187], a[232:235], v[120:123], v[184:187]\n"
    "v_exp_f32 v189, v189\n"
    "v_mfma_f32_16x16x32_bf16 v[184:187], a[228:231], v[116:119], v[184:187]\n"
    "v_exp_f32 v190, v190\n"
    "v_mfma_f32_16x16x32_bf16 v[184:187], a[224:227], v[112:115], v[184:187]\n"
    "v_add_f32 v22, v22, v188\n"
    "v_add_f32 v22, v22, v189\n"
    "v_mfma_f32_16x16x32_bf16 a[168:171], v[152:155], v[228:231], a[168:171]\n"
    "v_exp_f32 v191, v191\n"
    "v_mfma_f32_16x16x32_bf16 a[172:175], v[152:155], v[232:235], a[172:175]\n"
    "v_add_f32 v22, v22, v190\n"
    "v_cvt_pk_bf16_f32 v210, v188, v189\n"
    "v_mfma_f32_16x16x32_bf16 a[176:179], v[152:155], v[236:239], a[176:179]\n"
    "v_cvt_pk_bf16_f32 v211, v190, v191\n"
    "v_add_f32 v22, v22, v191\n"
    "v_mfma_f32_16x16x32_bf16 a[180:183], v[152:155], v[240:243], a[180:183]\n"
    "v_exp_f32 v192, v192\n"
    "v_mfma_f32_16x16x32_bf16 a[184:187], v[152:155], v[244:247], a[184:187]\n"
    "v_exp_f32 v193, v193\n"
    "v_mfma_f32_16x16x32_bf16 a[188:191], v[152:155], v[248:251], a[188:191]\n"
    "v_exp_f32 v194, v194\n"
    "v_mfma_f32_16x16x32_bf16 a[192:195], v[152:155], v[252:255], a[192:195]\n"
    "s_waitcnt vmcnt(5)\n"
    "ds_read_b64_tr_b16 v[152:153], v15 offset:33024\n"
    "v_add_f32 v23, v23, v192\n"
    "v_mfma_f32_16x16x32_bf16 v[188:191], a[224:227], v[128:131], v[180:183]\n"
    "v_exp_f32 v195, v195\n"
    "v_mfma_f32_16x16x32_bf16 v[188:191], a[228:231], v[132:135], v[188:191]\n"
    "buffer_load_dwordx4 v10, s[48:51], s58 offen nt lds\n"
    "v_mfma_f32_16x16x32_bf16 v[188:191], a[232:235], v[136:139], v[188:191]\n"
    "ds_read_b64_tr_b16 v[154:155], v15 offset:37120\n"
    "v_add_f32 v23, v23, v193\n"
    "v_mfma_f32_16x16x32_bf16 v[188:191], a[236:239], v[140:143], v[188:191]\n"
    "ds_read_b128 a[224:227], v13 offset:4096\n"
    "v_add_f32 v23, v23, v194\n"
    "v_mfma_f32_16x16x32_bf16 a[196:199], v[28:31], v[228:231], a[196:199]\n"
    "ds_read_b128 a[228:231], v13 offset:4352\n"
    "v_cvt_pk_bf16_f32 v214, v192, v193\n"
    "v_mfma_f32_16x16x32_bf16 a[200:203], v[28:31], v[232:235], a[200:203]\n"
    "ds_read_b128 a[232:235], v13 offset:4608\n"
    "v_cvt_pk_bf16_f32 v215, v194, v195\n"
    "v_mfma_f32_16x16x32_bf16 a[204:207], v[28:31], v[236:239], a[204:207]\n"
    "ds_read_b128 a[236:239], v13 offset:4864\n"
    "v_add_f32 v23, v23, v195\n"
    "v_mfma_f32_16x16x32_bf16 a[208:211], v[28:31], v[240:243], a[208:211]\n"
    "v_exp_f32 v196, v196\n"
    "v_mfma_f32_16x16x32_bf16 a[212:215], v[28:31], v[244:247], a[212:215]\n"
    "v_exp_f32 v197, v197\n"
    "v_mfma_f32_16x16x32_bf16 a[216:219], v[28:31], v[248:251], a[216:219]\n"
    "v_exp_f32 v198, v198\n"
    "v_mfma_f32_16x16x32_bf16 a[220:223], v[28:31], v[252:255], a[220:223]\n"
    "s_waitcnt lgkmcnt(6)\n"
    "ds_read_b64_tr_b16 v[28:29], v18 offset:33024\n"
    "v_add_f32 v24, v24, v196\n"
    "v_mfma_f32_16x16x32_bf16 v[192:195], a[240:243], v[32:35], v[156:159]\n"
    "v_exp_f32 v199, v199\n"
    "v_mfma_f32_16x16x32_bf16 v[192:195], a[244:247], v[36:39], v[192:195]\n"
    "ds_read_b64_tr_b16 v[30:31], v18 offset:37120\n"
    "v_add_f32 v24, v24, v197\n"
    "v_mfma_f32_16x16x32_bf16 v[192:195], a[248:251], v[40:43], v[192:195]\n"
    "v_add_f32 v24, v24, v198\n"
    "v_cvt_pk_bf16_f32 v218, v196, v197\n"
    "v_mfma_f32_16x16x32_bf16 v[192:195], a[252:255], v[44:47], v[192:195]\n"
    "s_waitcnt vmcnt(5)\n"
    "v_cvt_pk_bf16_f32 v219, v198, v199\n"
    "v_add_f32 v24, v24, v199\n"
    "v_mfma_f32_16x16x32_bf16 v[196:199], a[252:255], v[60:63], v[160:163]\n"
    "v_exp_f32 v184, v184\n"
    "v_mfma_f32_16x16x32_bf16 v[196:199], a[248:251], v[56:59], v[196:199]\n"
    "buffer_load_dwordx4 v10, s[48:51], s58 offen offset:2048 nt lds\n"
    "v_mfma_f32_16x16x32_bf16 v[196:199], a[244:247], v[52:55], v[196:199]\n"
    "s_add_u32 m0, s57, 65536\n"
    "v_exp_f32 v185, v185\n"
    "v_mfma_f32_16x16x32_bf16 v[196:199], a[240:243], v[48:51], v[196:199]\n"
    "s_add_u32 s58, s65, 65536\n"
    "v_exp_f32 v186, v186\n"
    "v_mfma_f32_16x16x32_bf16 a[0:3], v[144:147], v[200:203], a[0:3]\n"
    "s_and_b32 s58, s58, 0x1ffff\n"
    "v_exp_f32 v187, v187\n"
    "v_mfma_f32_16x16x32_bf16 a[4:7], v[144:147], v[204:207], a[4:7]\n"
    "v_exp_f32 v188, v188\n"
    "v_mfma_f32_16x16x32_bf16 a[8:11], v[144:147], v[208:211], a[8:11]\n"
    "v_exp_f32 v189, v189\n"
    "v_mfma_f32_16x16x32_bf16 a[12:15], v[144:147], v[212:215], a[12:15]\n"
    "v_exp_f32 v190, v190\n"
    "v_cvt_pk_bf16_f32 v222, v184, v185\n"
    "v_cvt_pk_bf16_f32 v223, v186, v187\n"
    "v_exp_f32 v191, v191\n"
    "v_mfma_f32_16x16x32_bf16 a[16:19], v[144:147], v[216:219], a[16:19]\n"
    "v_cvt_pk_bf16_f32 v226, v188, v189\n"
    "v_cvt_pk_bf16_f32 v227, v190, v191\n"
    "v_mfma_f32_16x16x32_bf16 a[20:23], v[144:147], v[220:223], a[20:23]\n"
    "v_add_f32 v25, v25, v184\n"
    "v_add_f32 v25, v25, v185\n"
    "v_mfma_f32_16x16x32_bf16 a[24:27], v[144:147], v[224:227], a[24:27]\n"
    "v_add_f32 v25, v25, v186\n"
    "v_add_f32 v25, v25, v187\n"
    "v_mfma_f32_16x16x32_bf16 v[184:187], a[240:243], v[64:67], v[164:167]\n"
    "ds_read_b64_tr_b16 v[144:145], v15 offset:33280\n"
    "v_add_f32 v26, v26, v188\n"
    "v_mfma_f32_16x16x32_bf16 v[184:187], a[244:247], v[68:71], v[184:187]\n"
    "ds_read_b64_tr_b16 v[146:147], v15 offset:37376\n"
    "v_add_f32 v26, v26, v189\n"
    "v_mfma_f32_16x16x32_bf16 v[184:187], a[248:251], v[72:75], v[184:187]\n"
    "v_exp_f32 v192, v192\n"
    "v_mfma_f32_16x16x32_bf16 v[184:187], a[252:255], v[76:79], v[184:187]\n"
    "s_waitcnt vmcnt(5)\n"
    "v_add_f32 v26, v26, v190\n"
    "v_add_f32 v26, v26, v191\n"
    "v_mfma_f32_16x16x32_bf16 v[188:191], a[252:255], v[92:95], v[168:171]\n"
    "v_exp_f32 v193, v193\n"
    "v_mfma_f32_16x16x32_bf16 v[188:191], a[248:251], v[88:91], v[188:191]\n"
    "buffer_load_dwordx4 v8, s[44:47], s58 offen lds\n"
    "v_mfma_f32_16x16x32_bf16 v[188:191], a[244:247], v[84:87], v[188:191]\n"
    "v_exp_f32 v194, v194\n"
    "v_mfma_f32_16x16x32_bf16 v[188:191], a[240:243], v[80:83], v[188:191]\n"
    "v_add_f32 v20, v20, v192\n"
    "v_add_f32 v20, v20, v193\n"
    "v_mfma_f32_16x16x32_bf16 a[28:31], v[148:151], v[200:203], a[28:31]\n"
    "v_exp_f32 v195, v195\n"
    "v_mfma_f32_16x16x32_bf16 a[32:35], v[148:151], v[204:207], a[32:35]\n"
    "v_add_f32 v20, v20, v194\n"
    "v_cvt_pk_bf16_f32 v228, v192, v193\n"
    "v_mfma_f32_16x16x32_bf16 a[36:39], v[148:151], v[208:211], a[36:39]\n"
    "v_cvt_pk_bf16_f32 v229, v194, v195\n"
    "v_add_f32 v20, v20, v195\n"
    "v_mfma_f32_16x16x32_bf16 a[40:43], v[148:151], v[212:215], a[40:43]\n"
    "v_exp_f32 v196, v196\n"
    "v_mfma_f32_16x16x32_bf16 a[44:47], v[148:151], v[216:219], a[44:47]\n"
    "v_exp_f32 v197, v197\n"
    "v_mfma_f32_16x16x32_bf16 a[48:51], v[148:151], v[220:223], a[48:51]\n"
    "v_exp_f32 v198, v198\n"
    "v_mfma_f32_16x16x32_bf16 a[52:55], v[148:151], v[224:227], a[52:55]\n"
    "ds_read_b64_tr_b16 v[148:149], v18 offset:33280\n"
    "v_add_f32 v21, v21, v196\n"
    "v_mfma_f32_16x16x32_bf16 v[192:195], a[240:243], v[96:99], v[172:175]\n"
    "v_exp_f32 v199, v199\n"
    "v_mfma_f32_16x16x32_bf16 v[192:195], a[244:247], v[100:103], v[192:195]\n"
    "ds_read_b64_tr_b16 v[150:151], v18 offset:37376\n"
    "v_add_f32 v21, v21, v197\n"
    "v_mfma_f32_16x16x32_bf16 v[192:195], a[248:251], v[104:107], v[192:195]\n"
    "v_add_f32 v21, v21, v198\n"
    "v_cvt_pk_bf16_f32 v232, v196, v197\n"
    "v_mfma_f32_16x16x32_bf16 v[192:195], a[252:255], v[108:111], v[192:195]\n"
    "v_cvt_pk_bf16_f32 v233, v198, v199\n"
    "v_add_f32 v21, v21, v199\n"
    "v_mfma_f32_16x16x32_bf16 v[196:199], a[252:255], v[124:127], v[176:179]\n"
    "s_waitcnt vmcnt(5)\n"
    "v_exp_f32 v184, v184\n"
    "v_mfma_f32_16x16x32_bf16 v[196:199], a[248:251], v[120:123], v[196:199]\n"
    "v_exp_f32 v185, v185\n"
    "v_mfma_f32_16x16x32_bf16 v[196:199], a[244:247], v[116:119], v[196:199]\n"
    "buffer_load_dwordx4 v9, s[44:47], s58 offen offset:2048 lds\n"
    "v_mfma_f32_16x16x32_bf16 v[196:199], a[240:243], v[112:115], v[196:199]\n"
    "s_waitcnt lgkmcnt(2)\n"
    "v_exp_f32 v186, v186\n"
    "v_mfma_f32_16x16x32_bf16 a[56:59], v[152:155], v[200:203], a[56:59]\n"
    "s_add_u32 m0, s57, 69632\n"
    "v_add_f32 v22, v22, v184\n"
    "v_add_f32 v22, v22, v185\n"
    "v_mfma_f32_16x16x32_bf16 a[60:63], v[152:155], v[204:207], a[60:63]\n"
    "s_add_u32 s58, s58, 4096\n"
    "v_exp_f32 v187, v187\n"
    "v_mfma_f32_16x16x32_bf16 a[64:67], v[152:155], v[208:211], a[64:67]\n"
    "v_add_f32 v22, v22, v186\n"
    "v_cvt_pk_bf16_f32 v236, v184, v185\n"
    "v_mfma_f32_16x16x32_bf16 a[68:71], v[152:155], v[212:215], a[68:71]\n"
    "v_cvt_pk_bf16_f32 v237, v186, v187\n"
    "v_add_f32 v22, v22, v187\n"
    "v_mfma_f32_16x16x32_bf16 a[72:75], v[152:155], v[216:219], a[72:75]\n"
    "v_exp_f32 v188, v188\n"
    "v_mfma_f32_16x16x32_bf16 a[76:79], v[152:155], v[220:223], a[76:79]\n"
    "v_exp_f32 v189, v189\n"
    "v_mfma_f32_16x16x32_bf16 a[80:83], v[152:155], v[224:227], a[80:83]\n"
    "v_exp_f32 v190, v190\n"
    "v_mfma_f32_16x16x32_bf16 v[184:187], a[240:243], v[128:131], v[180:183]\n"
    "ds_read_b64_tr_b16 v[152:153], v15 offset:33536\n"
    "v_add_f32 v23, v23, v188\n"
    "v_mfma_f32_16x16x32_bf16 v[184:187], a[244:247], v[132:135], v[184:187]\n"
    "v_exp_f32 v191, v191\n"
    "v_mfma_f32_16x16x32_bf16 v[184:187], a[248:251], v[136:139], v[184:187]\n"
    "ds_read_b64_tr_b16 v[154:155], v15 offset:37632\n"
    "v_add_f32 v23, v23, v189\n"
    "v_mfma_f32_16x16x32_bf16 v[184:187], a[252:255], v[140:143], v[184:187]\n"
    "v_add_f32 v23, v23, v190\n"
    "v_cvt_pk_bf16_f32 v240, v188, v189\n"
    "v_mfma_f32_16x16x32_bf16 a[84:87], v[28:31], v[200:203], a[84:87]\n"
    "s_waitcnt vmcnt(5)\n"
    "v_cvt_pk_bf16_f32 v241, v190, v191\n"
    "v_add_f32 v23, v23, v191\n"
    "v_mfma_f32_16x16x32_bf16 a[88:91], v[28:31], v[204:207], a[88:91]\n"
    "v_exp_f32 v192, v192\n"
    "v_mfma_f32_16x16x32_bf16 a[92:95], v[28:31], v[208:211], a[92:95]\n"
    "buffer_load_dwordx4 v8, s[44:47], s58 offen lds\n"
    "v_mfma_f32_16x16x32_bf16 a[96:99], v[28:31], v[212:215], a[96:99]\n"
    "v_exp_f32 v193, v193\n"
    "v_mfma_f32_16x16x32_bf16 a[100:103], v[28:31], v[216:219], a[100:103]\n"
    "v_exp_f32 v194, v194\n"
    "v_mfma_f32_16x16x32_bf16 a[104:107], v[28:31], v[220:223], a[104:107]\n"
    "v_add_f32 v24, v24, v192\n"
    "v_add_f32 v24, v24, v193\n"
    "v_mfma_f32_16x16x32_bf16 a[108:111], v[28:31], v[224:227], a[108:111]\n"
    "v_exp_f32 v195, v195\n"
    "v_mfma_f32_16x16x32_bf16 v[188:191], a[224:227], v[32:35], v[156:159]\n"
    "ds_read_b64_tr_b16 v[28:29], v18 offset:33536\n"
    "v_add_f32 v24, v24, v194\n"
    "v_mfma_f32_16x16x32_bf16 v[188:191], a[228:231], v[36:39], v[188:191]\n"
    "ds_read_b64_tr_b16 v[30:31], v18 offset:37632\n"
    "v_cvt_pk_bf16_f32 v244, v192, v193\n"
    "v_mfma_f32_16x16x32_bf16 v[188:191], a[232:235], v[40:43], v[188:191]\n"
    "v_cvt_pk_bf16_f32 v245, v194, v195\n"
    "v_add_f32 v24, v24, v195\n"
    "v_mfma_f32_16x16x32_bf16 v[188:191], a[236:239], v[44:47], v[188:191]\n"
    "v_exp_f32 v196, v196\n"
    "v_mfma_f32_16x16x32_bf16 v[192:195], a[236:239], v[60:63], v[160:163]\n"
    "v_exp_f32 v197, v197\n"
    "v_mfma_f32_16x16x32_bf16 v[192:195], a[232:235], v[56:59], v[192:195]\n"
    "v_exp_f32 v198, v198\n"
    "v_mfma_f32_16x16x32_bf16 v[192:195], a[228:231], v[52:55], v[192:195]\n"
    "s_memrealtime s[24:25]\n"
    "v_add_f32 v25, v25, v196\n"
    "v_add_f32 v25, v25, v197\n"
    "v_mfma_f32_16x16x32_bf16 v[192:195], a[224:227], v[48:51], v[192:195]\n"
    "v_exp_f32 v199, v199\n"
    "v_mfma_f32_16x16x32_bf16 a[112:115], v[144:147], v[200:203], a[112:115]\n"
    "s_waitcnt vmcnt(5)\n"
    "v_add_f32 v25, v25, v198\n"
    "v_cvt_pk_bf16_f32 v248, v196, v197\n"
    "v_mfma_f32_16x16x32_bf16 a[116:119], v[144:147], v[204:207], a[116:119]\n"
    "v_cvt_pk_bf16_f32 v249, v198, v199\n"
    "v_add_f32 v25, v25, v199\n"
    "v_mfma_f32_16x16x32_bf16 a[120:123], v[144:147], v[208:211], a[120:123]\n"
    "buffer_load_dwordx4 v9, s[44:47], s58 offen offset:2048 lds\n"
    "v_mfma_f32_16x16x32_bf16 a[124:127], v[144:147], v[212:215], a[124:127]\n"
    "s_add_u32 m0, s57, 40960\n"
    "v_exp_f32 v184, v184\n"
    "v_mfma_f32_16x16x32_bf16 a[128:131], v[144:147], v[216:219], a[128:131]\n"
    "s_add_u32 s58, s65, 57344\n"
    "v_exp_f32 v185, v185\n"
    "v_mfma_f32_16x16x32_bf16 a[132:135], v[144:147], v[220:223], a[132:135]\n"
    "s_and_b32 s58, s58, 0x1ffff\n"
    "v_exp_f32 v186, v186\n"
    "v_mfma_f32_16x16x32_bf16 a[136:139], v[144:147], v[224:227], a[136:139]\n"
    "v_add_f32 v26, v26, v184\n"
    "v_add_f32 v26, v26, v185\n"
    "v_mfma_f32_16x16x32_bf16 v[196:199], a[224:227], v[64:67], v[164:167]\n"
    "v_exp_f32 v187, v187\n"
    "v_mfma_f32_16x16x32_bf16 v[196:199], a[228:231], v[68:71], v[196:199]\n"
    "s_waitcnt vmcnt(12) lgkmcnt(0)\n"
    "s_barrier\n"
    "ds_read_b128 a[240:243], v11 offset:0\n"
    "v_mfma_f32_16x16x32_bf16 v[196:199], a[232:235], v[72:75], v[196:199]\n"
    "s_sub_i32 s25, s26, s56\n"
    "ds_read_b128 a[244:247], v11 offset:256\n"
    "v_add_f32 v26, v26, v186\n"
    "v_mfma_f32_16x16x32_bf16 v[196:199], a[236:239], v[76:79], v[196:199]\n"
    "s_max_i32 s25, s25, -3\n"
    "ds_read_b128 a[248:251], v11 offset:512\n"
    "v_cvt_pk_bf16_f32 v252, v184, v185\n"
    "v_cvt_pk_bf16_f32 v253, v186, v187\n"
    "v_add_f32 v26, v26, v187\n"
    "v_mfma_f32_16x16x32_bf16 v[184:187], a[236:239], v[92:95], v[168:171]\n"
    "buffer_load_dword v0, v10, s[28:31], s66 offen\n"
    "v_mfma_f32_16x16x32_bf16 v[184:187], a[232:235], v[88:91], v[184:187]\n"
    "s_min_i32 s25, s25, 0\n"
    "v_exp_f32 v188, v188\n"
    "v_mfma_f32_16x16x32_bf16 v[184:187], a[228:231], v[84:87], v[184:187]\n"
    "s_add_u32 s56, s56, s25\n"
    "v_exp_f32 v189, v189\n"
    "v_mfma_f32_16x16x32_bf16 v[184:187], a[224:227], v[80:83], v[184:187]\n"
    "s_sub_i32 s25, s56, s24\n"
    "v_exp_f32 v190, v190\n"
    "v_mfma_f32_16x16x32_bf16 a[140:143], v[148:151], v[200:203], a[140:143]\n"
    "s_max_i32 s25, s25, -10\n"
    "ds_read_b128 a[252:255], v11 offset:768\n"
    "v_add_f32 v20, v20, v188\n"
    "v_mfma_f32_16x16x32_bf16 a[144:147], v[148:151], v[204:207], a[144:147]\n"
    "s_min_i32 s25, s25, 62\n"
    "v_exp_f32 v191, v191\n"
    "v_mfma_f32_16x16x32_bf16 a[148:151], v[148:151], v[208:211], a[148:151]\n"
    "s_add_u32 s56, s24, s25\n"
    "ds_read_b64_tr_b16 v[144:145], v16 offset:0\n"
    "v_add_f32 v20, v20, v189\n"
    "v_mfma_f32_16x16x32_bf16 a[152:155], v[148:151], v[212:215], a[152:155]\n"
    "s_add_u32 s56, s56, 125\n"
    "ds_read_b64_tr_b16 v[146:147], v16 offset:4096\n"
    "v_add_f32 v20, v20, v190\n"
    "v_mfma_f32_16x16x32_bf16 a[156:159], v[148:151], v[216:219], a[156:159]\n"
    "s_cmp_gt_i32 s25, 2\ns_cbranch_scc1 L_ta5_%=\nL_tb5_%=:\n"
    "v_cvt_pk_bf16_f32 v230, v188, v189\n"
    "v_cvt_pk_bf16_f32 v231, v190, v191\n"
    "v_mfma_f32_16x16x32_bf16 a[160:163], v[148:151], v[220:223], a[160:163]\n"
    "s_add_u32 s26, s26, 125\n"
    "v_exp_f32 v192, v192\n"
    "v_mfma_f32_16x16x32_bf16 a[164:167], v[148:151], v[224:227], a[164:167]\n"
    "s_waitcnt vmcnt(5)\n"
    "ds_read_b64_tr_b16 v[148:149], v19 offset:0\n"
    "v_add_f32 v20, v20, v191\n"
    "v_mfma_f32_16x16x32_bf16 v[188:191], a[224:227], v[96:99], v[172:175]\n"
    "buffer_load_dwordx4 v10, s[48:51], s58 offen nt lds\n"
    "v_mfma_f32_16x16x32_bf16 v[188:191], a[228:231], v[100:103], v[188:191]\n"
    "v_exp_f32 v193, v193\n"
    "v_mfma_f32_16x16x32_bf16 v[188:191], a[232:235], v[104:107], v[188:191]\n"
    "v_exp_f32 v194, v194\n"
    "v_add_f32 v21, v21, v192\n"
    "v_exp_f32 v195, v195\n"
    "v_add_f32 v21, v21, v193\n"
    "v_mfma_f32_16x16x32_bf16 v[188:191], a[236:239], v[108:111], v[188:191]\n"
    "ds_read_b64_tr_b16 v[150:151], v19 offset:4096\n"
    "v_add_f32 v21, v21, v194\n"
    "v_cvt_pk_bf16_f32 v234, v192, v193\n"
    "v_cvt_pk_bf16_f32 v235, v194, v195\n"
    "v_add_f32 v21, v21, v195\n"
    "v_mfma_f32_16x16x32_bf16 v[192:195], a[236:239], v[124:127], v[176:179]\n"
    "v_exp_f32 v196, v196\n"
    "v_mfma_f32_16x16x32_bf16 v[192:195], a[232:235], v[120:123], v[192:195]\n"
    "v_exp_f32 v197, v197\n"
    "v_mfma_f32_16x16x32_bf16 v[192:195], a[228:231], v[116:119], v[192:195]\n"
    "v_exp_f32 v198, v198\n"
    "v_mfma_f32_16x16x32_bf16 v[192:195], a[224:227], v[112:115], v[192:195]\n"
    "v_add_f32 v22, v22, v196\n"
    "v_add_f32 v22, v22, v197\n"
    "v_mfma_f32_16x16x32_bf16 a[168:171], v[152:155], v[200:203], a[168:171]\n"
    "s_waitcnt vmcnt(5)\n"
    "v_exp_f32 v199, v199\n"
    "v_mfma_f32_16x16x32_bf16 a[172:175], v[152:155], v[204:207], a[172:175]\n"
    "v_add_f32 v22, v22, v198\n"
    "v_cvt_pk_bf16_f32 v238, v196, v197\n"
    "v_mfma_f32_16x16x32_bf16 a[176:179], v[152:155], v[208:211], a[176:179]\n"
    "buffer_load_dwordx4 v10, s[48:51], s58 offen offset:2048 nt lds\n"
    "v_mfma_f32_16x16x32_bf16 a[180:183], v[152:155], v[212:215], a[180:183]\n"
    "s_add_u32 m0, s57, 45056\n"
    "v_cvt_pk_bf16_f32 v239, v198, v199\n"
    "v_add_f32 v22, v22, v199\n"
    "v_mfma_f32_16x16x32_bf16 a[184:187], v[152:155], v[216:219], a[184:187]\n"
    "s_add_u32 s58, s58, 4096\n"
    "v_exp_f32 v184, v184\n"
    "v_mfma_f32_16x16x32_bf16 a[188:191], v[152:155], v[220:223], a[188:191]\n"
    "v_exp_f32 v185, v185\n"
    "v_mfma_f32_16x16x32_bf16 a[192:195], v[152:155], v[224:227], a[192:195]\n"
    "v_exp_f32 v186, v186\n"
    "v_mfma_f32_16x16x32_bf16 v[196:199], a[224:227], v[128:131], v[180:183]\n"
    "ds_read_b64_tr_b16 v[152:153], v16 offset:256\n"
    "v_add_f32 v23, v23, v184\n"
    "v_mfma_f32_16x16x32_bf16 v[196:199], a[228:231], v[132:135], v[196:199]\n"
    "v_exp_f32 v187, v187\n"
    "v_mfma_f32_16x16x32_bf16 v[196:199], a[232:235], v[136:139], v[196:199]\n"
    "ds_read_b64_tr_b16 v[154:155], v16 offset:4352\n"
    "v_add_f32 v23, v23, v185\n"
    "v_mfma_f32_16x16x32_bf16 v[196:199], a[236:239], v[140:143], v[196:199]\n"
    "ds_read_b128 a[224:227], v11 offset:4096\n"
    "v_add_f32 v23, v23, v186\n"
    "v_mfma_f32_16x16x32_bf16 a[196:199], v[28:31], v[200:203], a[196:199]\n"
    "ds_read_b128 a[228:231], v11 offset:4352\n"
    "v_cvt_pk_bf16_f32 v242, v184, v185\n"
    "v_mfma_f32_16x16x32_bf16 a[200:203], v[28:31], v[204:207], a[200:203]\n"
    "ds_read_b128 a[232:235], v11 offset:4608\n"
    "v_cvt_pk_bf16_f32 v243, v186, v187\n"
    "v_mfma_f32_16x16x32_bf16 a[204:207], v[28:31], v[208:211], a[204:207]\n"
    "ds_read_b128 a[236:239], v11 offset:4864\n"
    "v_add_f32 v23, v23, v187\n"
    "v_mfma_f32_16x16x32_bf16 a[208:211], v[28:31], v[212:215], a[208:211]\n"
    "v_exp_f32 v188, v188\n"
    "v_mfma_f32_16x16x32_bf16 a[212:215], v[28:31], v[216:219], a[212:215]\n"
    "v_exp_f32 v189, v189\n"
    "v_mfma_f32_16x16x32_bf16 a[216:219], v[28:31], v[220:223], a[216:219]\n"
    "s_waitcnt vmcnt(5)\n"
    "v_exp_f32 v190, v190\n"
    "v_mfma_f32_16x16x32_bf16 a[220:223], v[28:31], v[224:227], a[220:223]\n"
    "s_waitcnt lgkmcnt(6)\n"
    "ds_read_b64_tr_b16 v[28:29], v19 offset:256\n"
    "v_add_f32 v24, v24, v188\n"
    "v_mfma_f32_16x16x32_bf16 v[184:187], a[240:243], v[32:35], v[156:159]\n"
    "buffer_load_dwordx4 v10, s[48:51], s58 offen nt lds\n"
    "v_mfma_f32_16x16x32_bf16 v[184:187], a[244:247], v[36:39], v[184:187]\n"
    "v_exp_f32 v191, v191\n"
    "v_mfma_f32_16x16x32_bf16 v[184:187], a[248:251], v[40:43], v[184:187]\n"
    "ds_read_b64_tr_b16 v[30:31], v19 offset:4352\n"
    "v_add_f32 v24, v24, v189\n"
    "v_mfma_f32_16x16x32_bf16 v[184:187], a[252:255], v[44:47], v[184:187]\n"
    "v_add_f32 v24, v24, v190\n"
    "v_cvt_pk_bf16_f32 v246, v188, v189\n"
    "v_cvt_pk_bf16_f32 v247, v190, v191\n"
    "v_add_f32 v24, v24, v191\n"
    "v_mfma_f32_16x16x32_bf16 v[188:191], a[252:255], v[60:63], v[160:163]\n"
    "v_exp_f32 v192, v192\n"
    "v_mfma_f32_16x16x32_bf16 v[188:191], a[248:251], v[56:59], v[188:191]\n"
    "v_exp_f32 v193, v193\n"
    "v_mfma_f32_16x16x32_bf16 v[188:191], a[244:247], v[52:55], v[188:191]\n"
    "v_exp_f32 v194, v194\n"
    "v_mfma_f32_16x16x32_bf16 v[188:191], a[240:243], v[48:51], v[188:191]\n"
    "v_exp_f32 v195, v195\n"
    "v_mfma_f32_16x16x32_bf16 a[0:3], v[144:147], v[228:231], a[0:3]\n"
    "v_exp_f32 v196, v196\n"
    "v_mfma_f32_16x16x32_bf16 a[4:7], v[144:147], v[232:235], a[4:7]\n"
    "v_exp_f32 v197, v197\n"
    "v_mfma_f32_16x16x32_bf16 a[8:11], v[144:147], v[236:239], a[8:11]\n"
    "v_exp_f32 v198, v198\n"
    "v_mfma_f32_16x16x32_bf16 a[12:15], v[144:147], v[240:243], a[12:15]\n"
    "v_exp_f32 v199, v199\n"
    "v_cvt_pk_bf16_f32 v250, v192, v193\n"
    "v_cvt_pk_bf16_f32 v251, v194, v195\n"
    "v_mfma_f32_16x16x32_bf16 a[16:19], v[144:147], v[244:247], a[16:19]\n"
    "v_cvt_pk_bf16_f32 v254, v196, v197\n"
    "v_cvt_pk_bf16_f32 v255, v198, v199\n"
    "v_mfma_f32_16x16x32_bf16 a[20:23], v[144:147], v[248:251], a[20:23]\n"
    "s_waitcnt vmcnt(5)\n"
    "v_add_f32 v25, v25, v192\n"
    "v_add_f32 v25, v25, v193\n"
    "v_mfma_f32_16x16x32_bf16 a[24:27], v[144:147], v[252:255], a[24:27]\n"
    "v_add_f32 v25, v25, v194\n"
    "v_add_f32 v25, v25, v195\n"
    "v_mfma_f32_16x16x32_bf16 v[192:195], a[240:243], v[64:67], v[164:167]\n"
    "buffer_load_dwordx4 v10, s[48:51], s58 offen offset:2048 nt lds\n"
    "v_mfma_f32_16x16x32_bf16 v[192:195], a[244:247], v[68:71], v[192:195]\n"
    "s_add_u32 m0, s57, 98304\n"
    "ds_read_b64_tr_b16 v[144:145], v16 offset:512\n"
    "v_add_f32 v26, v26, v196\n"
    "v_mfma_f32_16x16x32_bf16 v[192:195], a[248:251], v[72:75], v[192:195]\n"
    "s_add_u32 s58, s65, 73728\n"
    "ds_read_b64_tr_b16 v[146:147], v16 offset:4608\n"
    "v_add_f32 v26, v26, v197\n"
    "v_mfma_f32_16x16x32_bf16 v[192:195], a[252:255], v[76:79], v[192:195]\n"
    "s_and_b32 s58, s58, 0x1ffff\n"
    "v_add_f32 v26, v26, v198\n"
    "v_add_f32 v26, v26, v199\n"
    "v_mfma_f32_16x16x32_bf16 v[196:199], a[252:255], v[92:95], v[168:171]\n"
    "v_exp_f32 v184, v184\n"
    "v_mfma_f32_16x16x32_bf16 v[196:199], a[248:251], v[88:91], v[196:199]\n"
    "v_exp_f32 v185, v185\n"
    "v_mfma_f32_16x16x32_bf16 v[196:199], a[244:247], v[84:87], v[196:199]\n"
    "v_exp_f32 v186, v186\n"
    "v_mfma_f32_16x16x32_bf16 v[196:199], a[240:243], v[80:83], v[196:199]\n"
    "v_add_f32 v20, v20, v184\n"
    "v_add_f32 v20, v20, v185\n"
    "v_mfma_f32_16x16x32_bf16 a[28:31], v[148:151], v[228:231], a[28:31]\n"
    "v_exp_f32 v187, v187\n"
    "v_mfma_f32_16x16x32_bf16 a[32:35], v[148:151], v[232:235], a[32:35]\n"
    "v_add_f32 v20, v20, v186\n"
    "v_cvt_pk_bf16_f32 v200, v184, v185\n"
    "v_mfma_f32_16x16x32_bf16 a[36:39], v[148:151], v[236:239], a[36:39]\n"
    "v_cvt_pk_bf16_f32 v201, v186, v187\n"
    "v_add_f32 v20, v20, v187\n"
    "v_mfma_f32_16x16x32_bf16 a[40:43], v[148:151], v[240:243], a[40:43]\n"
    "v_exp_f32 v188, v188\n"
    "v_mfma_f32_16x16x32_bf16 a[44:47], v[148:151], v[244:247], a[44:47]\n"
    "v_exp_f32 v189, v189\n"
    "v_mfma_f32_16x16x32_bf16 a[48:51], v[148:151], v[248:251], a[48:51]\n"
    "s_waitcnt vmcnt(5)\n"
    "v_exp_f32 v190, v190\n"
    "v_mfma_f32_16x16x32_bf16 a[52:55], v[148:151], v[252:255], a[52:55]\n"
    "ds_read_b64_tr_b16 v[148:149], v19 offset:512\n"
    "v_add_f32 v21, v21, v188\n"
    "v_mfma_f32_16x16x32_bf16 v[184:187], a[240:243], v[96:99], v[172:175]\n"
    "buffer_load_dwordx4 v8, s[44:47], s58 offen lds\n"
    "v_mfma_f32_16x16x32_bf16 v[184:187], a[244:247], v[100:103], v[184:187]\n"
    "v_exp_f32 v191, v191\n"
    "v_mfma_f32_16x16x32_bf16 v[184:187], a[248:251], v[104:107], v[184:187]\n"
    "ds_read_b64_tr_b16 v[150:151], v19 offset:4608\n"
    "v_add_f32 v21, v21, v189\n"
    "v_mfma_f32_16x16x32_bf16 v[184:187], a[252:255], v[108:111], v[184:187]\n"
    "v_add_f32 v21, v21, v190\n"
    "v_cvt_pk_bf16_f32 v204, v188, v189\n"
    "v_cvt_pk_bf16_f32 v205, v190, v191\n"
    "v_add_f32 v21, v21, v191\n"
    "v_mfma_f32_16x16x32_bf16 v[188:191], a[252:255], v[124:127], v[176:179]\n"
    "v_exp_f32 v192, v192\n"
    "v_mfma_f32_16x16x32_bf16 v[188:191], a[248:251], v[120:123], v[188:191]\n"
    "v_exp_f32 v193, v193\n"
    "v_mfma_f32_16x16x32_bf16 v[188:191], a[244:247], v[116:119], v[188:191]\n"
    "v_exp_f32 v194, v194\n"
    "v_mfma_f32_16x16x32_bf16 v[188:191], a[240:243], v[112:115], v[188:191]\n"
    "s_waitcnt lgkmcnt(2)\n"
    "v_add_f32 v22, v22, v192\n"
    "v_add_f32 v22, v22, v193\n"
    "v_mfma_f32_16x16x32_bf16 a[56:59], v[152:155], v[228:231], a[56:59]\n"
    "v_exp_f32 v195, v195\n"
    "v_mfma_f32_16x16x32_bf16 a[60:63], v[152:155], v[232:235], a[60:63]\n"
    "v_add_f32 v22, v22, v194\n"
    "v_cvt_pk_bf16_f32 v208, v192, v193\n"
    "v_mfma_f32_16x16x32_bf16 a[64:67], v[152:155], v[236:239], a[64:67]\n"
    "v_cvt_pk_bf16_f32 v209, v194, v195\n"
    "v_add_f32 v22, v22, v195\n"
    "v_mfma_f32_16x16x32_bf16 a[68:71], v[152:155], v[240:243], a[68:71]\n"
    "v_exp_f32 v196, v196\n"
    "v_mfma_f32_16x16x32_bf16 a[72:75], v[152:155], v[244:247], a[72:75]\n"
    "v_exp_f32 v197, v197\n"
    "v_mfma_f32_16x16x32_bf16 a[76:79], v[152:155], v[248:251], a[76:79]\n"
    "s_waitcnt vmcnt(5)\n"
    "v_exp_f32 v198, v198\n"
    "v_mfma_f32_16x16x32_bf16 a[80:83], v[152:155], v[252:255], a[80:83]\n"
    "ds_read_b64_tr_b16 v[152:153], v16 offset:768\n"
    "v_add_f32 v23, v23, v196\n"
    "v_mfma_f32_16x16x32_bf16 v[192:195], a[240:243], v[128:131], v[180:183]\n"
    "buffer_load_dwordx4 v9, s[44:47], s58 offen offset:2048 lds\n"
    "v_mfma_f32_16x16x32_bf16 v[192:195], a[244:247], v[132:135], v[192:195]\n"
    "s_add_u32 m0, s57, 102400\n"
    "v_exp_f32 v199, v199\n"
    "v_mfma_f32_16x16x32_bf16 v[192:195], a[248:251], v[136:139], v[192:195]\n"
    "s_add_u32 s58, s58, 4096\n"
    "ds_read_b64_tr_b16 v[154:155], v16 offset:4864\n"
    "v_add_f32 v23, v23, v197\n"
    "v_mfma_f32_16x16x32_bf16 v[192:195], a[252:255], v[140:143], v[192:195]\n"
    "v_add_f32 v23, v23, v198\n"
    "v_cvt_pk_bf16_f32 v212, v196, v197\n"
    "v_mfma_f32_16x16x32_bf16 a[84:87], v[28:31], v[228:231], a[84:87]\n"
    "v_cvt_pk_bf16_f32 v213, v198, v199\n"
    "v_add_f32 v23, v23, v199\n"
    "v_mfma_f32_16x16x32_bf16 a[88:91], v[28:31], v[232:235], a[88:91]\n"
    "v_exp_f32 v184, v184\n"
    "v_mfma_f32_16x16x32_bf16 a[92:95], v[28:31], v[236:239], a[92:95]\n"
    "v_exp_f32 v185, v185\n"
    "v_mfma_f32_16x16x32_bf16 a[96:99], v[28:31], v[240:243], a[96:99]\n"
    "v_exp_f32 v186, v186\n"
    "v_mfma_f32_16x16x32_bf16 a[100:103], v[28:31], v[244:247], a[100:103]\n"
    "v_add_f32 v24, v24, v184\n"
    "v_add_f32 v24, v24, v185\n"
    "v_mfma_f32_16x16x32_bf16 a[104:107], v[28:31], v[248:251], a[104:107]\n"
    "v_exp_f32 v187, v187\n"
    "v_mfma_f32_16x16x32_bf16 a[108:111], v[28:31], v[252:255], a[108:111]\n"
    "ds_read_b64_tr_b16 v[28:29], v19 offset:768\n"
    "v_add_f32 v24, v24, v186\n"
    "v_mfma_f32_16x16x32_bf16 v[196:199], a[224:227], v[32:35], v[156:159]\n"
    "ds_read_b64_tr_b16 v[30:31], v19 offset:4864\n"
    "v_cvt_pk_bf16_f32 v216, v184, v185\n"
    "v_mfma_f32_16x16x32_bf16 v[196:199], a[228:231], v[36:39], v[196:199]\n"
    "v_cvt_pk_bf16_f32 v217, v186, v187\n"
    "v_add_f32 v24, v24, v187\n"
    "v_mfma_f32_16x16x32_bf16 v[196:199], a[232:235], v[40:43], v[196:199]\n"
    "s_waitcnt vmcnt(5)\n"
    "v_exp_f32 v188, v188\n"
    "v_mfma_f32_16x16x32_bf16 v[196:199], a[236:239], v[44:47], v[196:199]\n"
    "v_exp_f32 v189, v189\n"
    "v_mfma_f32_16x16x32_bf16 v[184:187], a[236:239], v[60:63], v[160:163]\n"
    "buffer_load_dwordx4 v8, s[44:47], s58 offen lds\n"
    "v_mfma_f32_16x16x32_bf16 v[184:187], a[232:235], v[56:59], v[184:187]\n"
    "v_exp_f32 v190, v190\n"
    "v_mfma_f32_16x16x32_bf16 v[184:187], a[228:231], v[52:55], v[184:187]\n"
    "s_memrealtime s[24:25]\n"
    "v_add_f32 v25, v25, v188\n"
    "v_add_f32 v25, v25, v189\n"
    "v_mfma_f32_16x16x32_bf16 v[184:187], a[224:227], v[48:51], v[184:187]\n"
    "v_exp_f32 v191, v191\n"
    "v_mfma_f32_16x16x32_bf16 a[112:115], v[144:147], v[228:231], a[112:115]\n"
    "v_add_f32 v25, v25, v190\n"
    "v_cvt_pk_bf16_f32 v220, v188, v189\n"
    "v_mfma_f32_16x16x32_bf16 a[116:119], v[144:147], v[232:235], a[116:119]\n"
    "v_cvt_pk_bf16_f32 v221, v190, v191\n"
    "v_add_f32 v25, v25, v191\n"
    "v_mfma_f32_16x16x32_bf16 a[120:123], v[144:147], v[236:239], a[120:123]\n"
    "v_exp_f32 v192, v192\n"
    "v_mfma_f32_16x16x32_bf16 a[124:127], v[144:147], v[240:243], a[124:127]\n"
    "v_exp_f32 v193, v193\n"
    "v_mfma_f32_16x16x32_bf16 a[128:131], v[144:147], v[244:247], a[128:131]\n"
    "v_exp_f32 v194, v194\n"
    "v_mfma_f32_16x16x32_bf16 a[132:135], v[144:147], v[248:251], a[132:135]\n"
    "v_add_f32 v26, v26, v192\n"
    "v_add_f32 v26, v26, v193\n"
    "v_mfma_f32_16x16x32_bf16 a[136:139], v[144:147], v[252:255], a[136:139]\n"
    "v_exp_f32 v195, v195\n"
    "v_mfma_f32_16x16x32_bf16 v[188:191], a[224:227], v[64:67], v[164:167]\n"
    "v_add_f32 v26, v26, v194\n"
    "v_cvt_pk_bf16_f32 v224, v192, v193\n"
    "v_mfma_f32_16x16x32_bf16 v[188:191], a[228:231], v[68:71], v[188:191]\n"
    "s_waitcnt vmcnt(11) lgkmcnt(0)\n"
    "s_barrier\n"
    "ds_read_b128 a[240:243], v11 offset:32768\n"
    "v_mfma_f32_16x16x32_bf16 v[188:191], a[232:235], v[72:75], v[188:191]\n"
    "s_sub_i32 s25, s26, s56\n"
    "ds_read_b128 a[244:247], v11 offset:33024\n"
    "v_cvt_pk_bf16_f32 v225, v194, v195\n"
    "v_mfma_f32_16x16x32_bf16 v[188:191], a[236:239], v[76:79], v[188:191]\n"
    "s_max_i32 s25, s25, -3\n"
    "ds_read_b128 a[248:251], v11 offset:33280\n"
    "v_add_f32 v26, v26, v195\n"
    "v_mfma_f32_16x16x32_bf16 v[192:195], a[236:239], v[92:95], v[168:171]\n"
    "s_min_i32 s25, s25, 0\n"
    "v_exp_f32 v196, v196\n"
    "v_mfma_f32_16x16x32_bf16 v[192:195], a[232:235], v[88:91], v[192:195]\n"
    "s_add_u32 s56, s56, s25\n"
    "v_exp_f32 v197, v197\n"
    "v_mfma_f32_16x16x32_bf16 v[192:195], a[228:231], v[84:87], v[192:195]\n"
    "s_sub_i32 s25, s56, s24\n"
    "v_exp_f32 v198, v198\n"
    "v_mfma_f32_16x16x32_bf16 v[192:195], a[224:227], v[80:83], v[192:195]\n"
    "s_max_i32 s25, s25, -10\n"
    "ds_read_b128 a[252:255], v11 offset:33536\n"
    "v_add_f32 v20, v20, v196\n"
    "v_mfma_f32_16x16x32_bf16 a[140:143], v[148:151], v[228:231], a[140:143]\n"
    "s_min_i32 s25, s25, 62\n"
    "v_exp_f32 v199, v199\n"
    "v_mfma_f32_16x16x32_bf16 a[144:147], v[148:151], v[232:235], a[144:147]\n"
    "s_add_u32 s56, s24, s25\n"
    "ds_read_b64_tr_b16 v[144:145], v14 offset:0\n"
    "v_add_f32 v20, v20, v197\n"
    "v_mfma_f32_16x16x32_bf16 a[148:151], v[148:151], v[236:239], a[148:151]\n"
    "s_add_u32 s56, s56, 125\n"
    "ds_read_b64_tr_b16 v[146:147], v14 offset:4096\n"
    "v_add_f32 v20, v20, v198\n"
    "v_mfma_f32_16x16x32_bf16 a[152:155], v[148:151], v[240:243], a[152:155]\n"
    "s_cmp_gt_i32 s25, 2\ns_cbranch_scc1 L_ta6_%=\nL_tb6_%=:\n"
    "v_cvt_pk_bf16_f32 v202, v196, v197\n"
    "v_cvt_pk_bf16_f32 v203, v198, v199\n"
    "v_mfma_f32_16x16x32_bf16 a[156:159], v[148:151], v[244:247], a[156:159]\n"
    "s_add_u32 s26, s26, 125\n"
    "v_exp_f32 v184, v184\n"
    "v_mfma_f32_16x16x32_bf16 a[160:163], v[148:151], v[248:251], a[160:163]\n"
    "s_waitcnt vmcnt(5)\n"
    "v_add_f32 v20, v20, v199\n"
    "v_add_f32 v21, v21, v184\n"
    "v_mfma_f32_16x16x32_bf16 a[164:167], v[148:151], v[252:255], a[164:167]\n"
    "buffer_load_dwordx4 v9, s[44:47], s58 offen offset:2048 lds\n"
    "v_mfma_f32_16x16x32_bf16 v[196:199], a[224:227], v[96:99], v[172:175]\n"
    "s_add_u32 m0, s57, 73728\n"
    "v_exp_f32 v185, v185\n"
    "v_mfma_f32_16x16x32_bf16 v[196:199], a[228:231], v[100:103], v[196:199]\n"
    "s_add_u32 s58, s65, 65536\n"
    "v_exp_f32 v186, v186\n"
    "v_mfma_f32_16x16x32_bf16 v[196:199], a[232:235], v[104:107], v[196:199]\n"
    "s_and_b32 s58, s58, 0x1ffff\n"
    "v_exp_f32 v187, v187\n"
    "v_add_f32 v21, v21, v185\n"
    "v_mfma_f32_16x16x32_bf16 v[196:199], a[236:239], v[108:111], v[196:199]\n"
    "s_waitcnt vmcnt(5)\n"
    "ds_read_b64_tr_b16 v[148:149], v17 offset:0\n"
    "v_add_f32 v21, v21, v186\n"
    "v_cvt_pk_bf16_f32 v206, v184, v185\n"
    "v_cvt_pk_bf16_f32 v207, v186, v187\n"
    "v_add_f32 v21, v21, v187\n"
    "v_mfma_f32_16x16x32_bf16 v[184:187], a[236:239], v[124:127], v[176:179]\n"
    "v_exp_f32 v188, v188\n"
    "v_mfma_f32_16x16x32_bf16 v[184:187], a[232:235], v[120:123], v[184:187]\n"
    "buffer_load_dwordx4 v10, s[48:51], s58 offen nt lds\n"
    "v_mfma_f32_16x16x32_bf16 v[184:187], a[228:231], v[116:119], v[184:187]\n"
    "v_exp_f32 v189, v189\n"
    "v_mfma_f32_16x16x32_bf16 v[184:187], a[224:227], v[112:115], v[184:187]\n"
    "v_exp_f32 v190, v190\n"
    "v_mfma_f32_16x16x32_bf16 a[168:171], v[152:155], v[228:231], a[168:171]\n"
    "ds_read_b64_tr_b16 v[150:151], v17 offset:4096\n"
    "v_add_f32 v22, v22, v188\n"
    "v_mfma_f32_16x16x32_bf16 a[172:175], v[152:155], v[232:235], a[172:175]\n"
    "v_exp_f32 v191, v191\n"
    "v_mfma_f32_16x16x32_bf16 a[176:179], v[152:155], v[236:239], a[176:179]\n"
    "v_add_f32 v22, v22, v189\n"
    "v_add_f32 v22, v22, v190\n"
    "v_mfma_f32_16x16x32_bf16 a[180:183], v[152:155], v[240:243], a[180:183]\n"
    "v_cvt_pk_bf16_f32 v210, v188, v189\n"
    "v_cvt_pk_bf16_f32 v211, v190, v191\n"
    "v_mfma_f32_16x16x32_bf16 a[184:187], v[152:155], v[244:247], a[184:187]\n"
    "v_exp_f32 v192, v192\n"
    "v_mfma_f32_16x16x32_bf16 a[188:191], v[152:155], v[248:251], a[188:191]\n"
    "v_add_f32 v22, v22, v191\n"
    "v_add_f32 v23, v23, v192\n"
    "v_mfma_f32_16x16x32_bf16 a[192:195], v[152:155], v[252:255], a[192:195]\n"
    "v_exp_f32 v193, v193\n"
    "v_mfma_f32_16x16x32_bf16 v[188:191], a[224:227], v[128:131], v[180:183]\n"
    "v_exp_f32 v194, v194\n"
    "v_mfma_f32_16x16x32_bf16 v[188:191], a[228:231], v[132:135], v[188:191]\n"
    "v_exp_f32 v195, v195\n"
    "v_mfma_f32_16x16x32_bf16 v[188:191], a[232:235], v[136:139], v[188:191]\n"
    "ds_read_b64_tr_b16 v[152:153], v14 offset:256\n"
    "v_add_f32 v23, v23, v193\n"
    "v_mfma_f32_16x16x32_bf16 v[188:191], a[236:239], v[140:143], v[188:191]\n"
    "s_waitcnt vmcnt(5)\n"
    "ds_read_b64_tr_b16 v[154:155], v14 offset:4352\n"
    "v_add_f32 v23, v23, v194\n"
    "v_mfma_f32_16x16x32_bf16 a[196:199], v[28:31], v[228:231], a[196:199]\n"
    "ds_read_b128 a[224:227], v11 offset:36864\n"
    "v_cvt_pk_bf16_f32 v214, v192, v193\n"
    "v_mfma_f32_16x16x32_bf16 a[200:203], v[28:31], v[232:235], a[200:203]\n"
    "buffer_load_dwordx4 v10, s[48:51], s58 offen offset:2048 nt lds\n"
    "v_mfma_f32_16x16x32_bf16 a[204:207], v[28:31], v[236:239], a[204:207]\n"
    "s_add_u32 m0, s57, 77824\n"
    "ds_read_b128 a[228:231], v11 offset:37120\n"
    "v_cvt_pk_bf16_f32 v215, v194, v195\n"
    "v_mfma_f32_16x16x32_bf16 a[208:211], v[28:31], v[240:243], a[208:211]\n"
    "s_add_u32 s58, s58, 4096\n"
    "ds_read_b128 a[232:235], v11 offset:37376\n"
    "v_add_f32 v23, v23, v195\n"
    "v_mfma_f32_16x16x32_bf16 a[212:215], v[28:31], v[244:247], a[212:215]\n"
    "v_exp_f32 v196, v196\n"
    "v_mfma_f32_16x16x32_bf16 a[216:219], v[28:31], v[248:251], a[216:219]\n"
    "v_exp_f32 v197, v197\n"
    "v_mfma_f32_16x16x32_bf16 a[220:223], v[28:31], v[252:255], a[220:223]\n"
    "s_waitcnt lgkmcnt(5)\n"
    "v_exp_f32 v198, v198\n"
    "v_mfma_f32_16x16x32_bf16 v[192:195], a[240:243], v[32:35], v[156:159]\n"
    "ds_read_b128 a[236:239], v11 offset:37632\n"
    "v_add_f32 v24, v24, v196\n"
    "v_mfma_f32_16x16x32_bf16 v[192:195], a[244:247], v[36:39], v[192:195]\n"
    "v_exp_f32 v199, v199\n"
    "v_mfma_f32_16x16x32_bf16 v[192:195], a[248:251], v[40:43], v[192:195]\n"
    "ds_read_b64_tr_b16 v[28:29], v17 offset:256\n"
    "v_add_f32 v24, v24, v197\n"
    "v_mfma_f32_16x16x32_bf16 v[192:195], a[252:255], v[44:47], v[192:195]\n"
    "ds_read_b64_tr_b16 v[30:31], v17 offset:4352\n"
    "v_add_f32 v24, v24, v198\n"
    "v_cvt_pk_bf16_f32 v218, v196, v197\n"
    "v_cvt_pk_bf16_f32 v219, v198, v199\n"
    "v_add_f32 v24, v24, v199\n"
    "v_mfma_f32_16x16x32_bf16 v[196:199], a[252:255], v[60:63], v[160:163]\n"
    "v_exp_f32 v184, v184\n"
    "v_mfma_f32_16x16x32_bf16 v[196:199], a[248:251], v[56:59], v[196:199]\n"
    "v_exp_f32 v185, v185\n"
    "v_mfma_f32_16x16x32_bf16 v[196:199], a[244:247], v[52:55], v[196:199]\n"
    "v_exp_f32 v186, v186\n"
    "v_mfma_f32_16x16x32_bf16 v[196:199], a[240:243], v[48:51], v[196:199]\n"
    "s_waitcnt vmcnt(5)\n"
    "v_exp_f32 v187, v187\n"
    "v_mfma_f32_16x16x32_bf16 a[0:3], v[144:147], v[200:203], a[0:3]\n"
    "v_exp_f32 v188, v188\n"
    "v_mfma_f32_16x16x32_bf16 a[4:7], v[144:147], v[204:207], a[4:7]\n"
    "buffer_load_dwordx4 v10, s[48:51], s58 offen nt lds\n"
    "v_mfma_f32_16x16x32_bf16 a[8:11], v[144:147], v[208:211], a[8:11]\n"
    "v_exp_f32 v189, v189\n"
    "v_mfma_f32_16x16x32_bf16 a[12:15], v[144:147], v[212:215], a[12:15]\n"
    "v_exp_f32 v190, v190\n"
    "v_cvt_pk_bf16_f32 v222, v184, v185\n"
    "v_cvt_pk_bf16_f32 v223, v186, v187\n"
    "v_exp_f32 v191, v191\n"
    "v_mfma_f32_16x16x32_bf16 a[16:19], v[144:147], v[216:219], a[16:19]\n"
    "v_cvt_pk_bf16_f32 v226, v188, v189\n"
    "v_cvt_pk_bf16_f32 v227, v190, v191\n"
    "v_mfma_f32_16x16x32_bf16 a[20:23], v[144:147], v[220:223], a[20:23]\n"
    "v_add_f32 v25, v25, v184\n"
    "v_add_f32 v25, v25, v185\n"
    "v_mfma_f32_16x16x32_bf16 a[24:27], v[144:147], v[224:227], a[24:27]\n"
    "v_add_f32 v25, v25, v186\n"
    "v_add_f32 v25, v25, v187\n"
    "v_mfma_f32_16x16x32_bf16 v[184:187], a[240:243], v[64:67], v[164:167]\n"
    "ds_read_b64_tr_b16 v[144:145], v14 offset:512\n"
    "v_add_f32 v26, v26, v188\n"
    "v_mfma_f32_16x16x32_bf16 v[184:187], a[244:247], v[68:71], v[184:187]\n"
    "ds_read_b64_tr_b16 v[146:147], v14 offset:4608\n"
    "v_add_f32 v26, v26, v189\n"
    "v_mfma_f32_16x16x32_bf16 v[184:187], a[248:251], v[72:75], v[184:187]\n"
    "v_exp_f32 v192, v192\n"
    "v_mfma_f32_16x16x32_bf16 v[184:187], a[252:255], v[76:79], v[184:187]\n"
    "v_add_f32 v26, v26, v190\n"
    "v_add_f32 v26, v26, v191\n"
    "v_mfma_f32_16x16x32_bf16 v[188:191], a[252:255], v[92:95], v[168:171]\n"
    "v_exp_f32 v193, v193\n"
    "v_mfma_f32_16x16x32_bf16 v[188:191], a[248:251], v[88:91], v[188:191]\n"
    "v_exp_f32 v194, v194\n"
    "v_mfma_f32_16x16x32_bf16 v[188:191], a[244:247], v[84:87], v[188:191]\n"
    "v_add_f32 v20, v20, v192\n"
    "v_add_f32 v20, v20, v193\n"
    "v_mfma_f32_16x16x32_bf16 v[188:191], a[240:243], v[80:83], v[188:191]\n"
    "s_waitcnt vmcnt(5)\n"
    "v_exp_f32 v195, v195\n"
    "v_mfma_f32_16x16x32_bf16 a[28:31], v[148:151], v[200:203], a[28:31]\n"
    "v_add_f32 v20, v20, v194\n"
    "v_cvt_pk_bf16_f32 v228, v192, v193\n"
    "v_mfma_f32_16x16x32_bf16 a[32:35], v[148:151], v[204:207], a[32:35]\n"
    "buffer_load_dwordx4 v10, s[48:51], s58 offen offset:2048 nt lds\n"
    "v_mfma_f32_16x16x32_bf16 a[36:39], v[148:151], v[208:211], a[36:39]\n"
    "s_add_u32 m0, s57, 131072\n"
    "v_cvt_pk_bf16_f32 v229, v194, v195\n"
    "v_add_f32 v20, v20, v195\n"
    "v_mfma_f32_16x16x32_bf16 a[40:43], v[148:151], v[212:215], a[40:43]\n"
    "s_add_u32 s58, s65, 81920\n"
    "v_exp_f32 v196, v196\n"
    "v_mfma_f32_16x16x32_bf16 a[44:47], v[148:151], v[216:219], a[44:47]\n"
    "s_and_b32 s58, s58, 0x1ffff\n"
    "v_exp_f32 v197, v197\n"
    "v_mfma_f32_16x16x32_bf16 a[48:51], v[148:151], v[220:223], a[48:51]\n"
    "v_exp_f32 v198, v198\n"
    "v_mfma_f32_16x16x32_bf16 a[52:55], v[148:151], v[224:227], a[52:55]\n"
    "ds_read_b64_tr_b16 v[148:149], v17 offset:512\n"
    "v_add_f32 v21, v21, v196\n"
    "v_mfma_f32_16x16x32_bf16 v[192:195], a[240:243], v[96:99], v[172:175]\n"
    "v_exp_f32 v199, v199\n"
    "v_mfma_f32_16x16x32_bf16 v[192:195], a[244:247], v[100:103], v[192:195]\n"
    "ds_read_b64_tr_b16 v[150:151], v17 offset:4608\n"
    "v_add_f32 v21, v21, v197\n"
    "v_mfma_f32_16x16x32_bf16 v[192:195], a[248:251], v[104:107], v[192:195]\n"
    "v_add_f32 v21, v21, v198\n"
    "v_cvt_pk_bf16_f32 v232, v196, v197\n"
    "v_mfma_f32_16x16x32_bf16 v[192:195], a[252:255], v[108:111], v[192:195]\n"
    "v_cvt_pk_bf16_f32 v233, v198, v199\n"
    "v_add_f32 v21, v21, v199\n"
    "v_mfma_f32_16x16x32_bf16 v[196:199], a[252:255], v[124:127], v[176:179]\n"
    "v_exp_f32 v184, v184\n"
    "v_mfma_f32_16x16x32_bf16 v[196:199], a[248:251], v[120:123], v[196:199]\n"
    "v_exp_f32 v185, v185\n"
    "v_mfma_f32_16x16x32_bf16 v[196:199], a[244:247], v[116:119], v[196:199]\n"
    "v_exp_f32 v186, v186\n"
    "v_mfma_f32_16x16x32_bf16 v[196:199], a[240:243], v[112:115], v[196:199]\n"
    "s_waitcnt lgkmcnt(2)\n"
    "v_add_f32 v22, v22, v184\n"
    "v_add_f32 v22, v22, v185\n"
    "v_mfma_f32_16x16x32_bf16 a[56:59], v[152:155], v[200:203], a[56:59]\n"
    "s_waitcnt vmcnt(5)\n"
    "v_exp_f32 v187, v187\n"
    "v_mfma_f32_16x16x32_bf16 a[60:63], v[152:155], v[204:207], a[60:63]\n"
    "v_add_f32 v22, v22, v186\n"
    "v_cvt_pk_bf16_f32 v236, v184, v185\n"
    "v_mfma_f32_16x16x32_bf16 a[64:67], v[152:155], v[208:211], a[64:67]\n"
    "buffer_load_dwordx4 v8, s[44:47], s58 offen lds\n"
    "v_mfma_f32_16x16x32_bf16 a[68:71], v[152:155], v[212:215], a[68:71]\n"
    "v_cvt_pk_bf16_f32 v237, v186, v187\n"
    "v_add_f32 v22, v22, v187\n"
    "v_mfma_f32_16x16x32_bf16 a[72:75], v[152:155], v[216:219], a[72:75]\n"
    "v_exp_f32 v188, v188\n"
    "v_mfma_f32_16x16x32_bf16 a[76:79], v[152:155], v[220:223], a[76:79]\n"
    "v_exp_f32 v189, v189\n"
    "v_mfma_f32_16x16x32_bf16 a[80:83], v[152:155], v[224:227], a[80:83]\n"
    "v_exp_f32 v190, v190\n"
    "v_mfma_f32_16x16x32_bf16 v[184:187], a[240:243], v[128:131], v[180:183]\n"
    "ds_read_b64_tr_b16 v[152:153], v14 offset:768\n"
    "v_add_f32 v23, v23, v188\n"
    "v_mfma_f32_16x16x32_bf16 v[184:187], a[244:247], v[132:135], v[184:187]\n"
    "v_exp_f32 v191, v191\n"
    "v_mfma_f32_16x16x32_bf16 v[184:187], a[248:251], v[136:139], v[184:187]\n"
    "ds_read_b64_tr_b16 v[154:155], v14 offset:4864\n"
    "v_add_f32 v23, v23, v189\n"
    "v_mfma_f32_16x16x32_bf16 v[184:187], a[252:255], v[140:143], v[184:187]\n"
    "v_add_f32 v23, v23, v190\n"
    "v_cvt_pk_bf16_f32 v240, v188, v189\n"
    "v_mfma_f32_16x16x32_bf16 a[84:87], v[28:31], v[200:203], a[84:87]\n"
    "v_cvt_pk_bf16_f32 v241, v190, v191\n"
    "v_add_f32 v23, v23, v191\n"
    "v_mfma_f32_16x16x32_bf16 a[88:91], v[28:31], v[204:207], a[88:91]\n"
    "v_exp_f32 v192, v192\n"
    "v_mfma_f32_16x16x32_bf16 a[92:95], v[28:31], v[208:211], a[92:95]\n"
    "v_exp_f32 v193, v193\n"
    "v_mfma_f32_16x16x32_bf16 a[96:99], v[28:31], v[212:215], a[96:99]\n"
    "v_exp_f32 v194, v194\n"
    "v_mfma_f32_16x16x32_bf16 a[100:103], v[28:31], v[216:219], a[100:103]\n"
    "s_waitcnt vmcnt(5)\n"
    "v_add_f32 v24, v24, v192\n"
    "v_add_f32 v24, v24, v193\n"
    "v_mfma_f32_16x16x32_bf16 a[104:107], v[28:31], v[220:223], a[104:107]\n"
    "v_exp_f32 v195, v195\n"
    "v_mfma_f32_16x16x32_bf16 a[108:111], v[28:31], v[224:227], a[108:111]\n"
    "buffer_load_dwordx4 v9, s[44:47], s58 offen offset:2048 lds\n"
    "v_mfma_f32_16x16x32_bf16 v[188:191], a[224:227], v[32:35], v[156:159]\n"
    "s_add_u32 m0, s57, 135168\n"
    "ds_read_b64_tr_b16 v[28:29], v17 offset:768\n"
    "v_add_f32 v24, v24, v194\n"
    "v_mfma_f32_16x16x32_bf16 v[188:191], a[228:231], v[36:39], v[188:191]\n"
    "s_add_u32 s58, s58, 4096\n"
    "ds_read_b64_tr_b16 v[30:31], v17 offset:4864\n"
    "v_cvt_pk_bf16_f32 v244, v192, v193\n"
    "v_mfma_f32_16x16x32_bf16 v[188:191], a[232:235], v[40:43], v[188:191]\n"
    "v_cvt_pk_bf16_f32 v245, v194, v195\n"
    "v_add_f32 v24, v24, v195\n"
    "v_mfma_f32_16x16x32_bf16 v[188:191], a[236:239], v[44:47], v[188:191]\n"
    "v_exp_f32 v196, v196\n"
    "v_mfma_f32_16x16x32_bf16 v[192:195], a[236:239], v[60:63], v[160:163]\n"
    "v_exp_f32 v197, v197\n"
    "v_mfma_f32_16x16x32_bf16 v[192:195], a[232:235], v[56:59], v[192:195]\n"
    "v_exp_f32 v198, v198\n"
    "v_mfma_f32_16x16x32_bf16 v[192:195], a[228:231], v[52:55], v[192:195]\n"
    "s_memrealtime s[24:25]\n"
    "v_add_f32 v25, v25, v196\n"
    "v_add_f32 v25, v25, v197\n"
    "v_mfma_f32_16x16x32_bf16 v[192:195], a[224:227], v[48:51], v[192:195]\n"
    "v_exp_f32 v199, v199\n"
    "v_mfma_f32_16x16x32_bf16 a[112:115], v[144:147], v[200:203], a[112:115]\n"
    "v_add_f32 v25, v25, v198\n"
    "v_cvt_pk_bf16_f32 v248, v196, v197\n"
    "v_mfma_f32_16x16x32_bf16 a[116:119], v[144:147], v[204:207], a[116:119]\n"
    "v_cvt_pk_bf16_f32 v249, v198, v199\n"
    "v_add_f32 v25, v25, v199\n"
    "v_mfma_f32_16x16x32_bf16 a[120:123], v[144:147], v[208:211], a[120:123]\n"
    "v_exp_f32 v184, v184\n"
    "v_mfma_f32_16x16x32_bf16 a[124:127], v[144:147], v[212:215], a[124:127]\n"
    "v_exp_f32 v185, v185\n"
    "v_mfma_f32_16x16x32_bf16 a[128:131], v[144:147], v[216:219], a[128:131]\n"
    "s_waitcnt vmcnt(5)\n"
    "v_exp_f32 v186, v186\n"
    "v_mfma_f32_16x16x32_bf16 a[132:135], v[144:147], v[220:223], a[132:135]\n"
    "v_add_f32 v26, v26, v184\n"
    "v_add_f32 v26, v26, v185\n"
    "v_mfma_f32_16x16x32_bf16 a[136:139], v[144:147], v[224:227], a[136:139]\n"
    "buffer_load_dwordx4 v8, s[44:47], s58 offen lds\n"
    "v_mfma_f32_16x16x32_bf16 v[196:199], a[224:227], v[64:67], v[164:167]\n"
    "v_exp_f32 v187, v187\n"
    "v_mfma_f32_16x16x32_bf16 v[196:199], a[228:231], v[68:71], v[196:199]\n"
    "s_waitcnt vmcnt(11) lgkmcnt(0)\n"
    "s_barrier\n"
    "ds_read_b128 a[240:243], v12 offset:0\n"
    "v_mfma_f32_16x16x32_bf16 v[196:199], a[232:235], v[72:75], v[196:199]\n"
    "s_sub_i32 s25, s26, s56\n"
    "ds_read_b128 a[244:247], v12 offset:256\n"
    "v_add_f32 v26, v26, v186\n"
    "v_mfma_f32_16x16x32_bf16 v[196:199], a[236:239], v[76:79], v[196:199]\n"
    "s_max_i32 s25, s25, -3\n"
    "ds_read_b128 a[248:251], v12 offset:512\n"
    "v_cvt_pk_bf16_f32 v252, v184, v185\n"
    "v_cvt_pk_bf16_f32 v253, v186, v187\n"
    "v_add_f32 v26, v26, v187\n"
    "v_mfma_f32_16x16x32_bf16 v[184:187], a[236:239], v[92:95], v[168:171]\n"
    "s_min_i32 s25, s25, 0\n"
    "v_exp_f32 v188, v188\n"
    "v_mfma_f32_16x16x32_bf16 v[184:187], a[232:235], v[88:91], v[184:187]\n"
    "s_add_u32 s56, s56, s25\n"
    "v_exp_f32 v189, v189\n"
    "v_mfma_f32_16x16x32_bf16 v[184:187], a[228:231], v[84:87], v[184:187]\n"
    "s_sub_i32 s25, s56, s24\n"
    "v_exp_f32 v190, v190\n"
    "v_mfma_f32_16x16x32_bf16 v[184:187], a[224:227], v[80:83], v[184:187]\n"
    "s_max_i32 s25, s25, -10\n"
    "ds_read_b128 a[252:255], v12 offset:768\n"
    "v_add_f32 v20, v20, v188\n"
    "v_mfma_f32_16x16x32_bf16 a[140:143], v[148:151], v[200:203], a[140:143]\n"
    "s_min_i32 s25, s25, 62\n"
    "v_exp_f32 v191, v191\n"
    "v_mfma_f32_16x16x32_bf16 a[144:147], v[148:151], v[204:207], a[144:147]\n"
    "s_add_u32 s56, s24, s25\n"
    "ds_read_b64_tr_b16 v[144:145], v14 offset:32768\n"
    "v_add_f32 v20, v20, v189\n"
    "v_mfma_f32_16x16x32_bf16 a[148:151], v[148:151], v[208:211], a[148:151]\n"
    "s_add_u32 s56, s56, 125\n"
    "ds_read_b64_tr_b16 v[146:147], v14 offset:36864\n"
    "v_add_f32 v20, v20, v190\n"
    "v_mfma_f32_16x16x32_bf16 a[152:155], v[148:151], v[212:215], a[152:155]\n"
    "s_cmp_gt_i32 s25, 2\ns_cbranch_scc1 L_ta7_%=\nL_tb7_%=:\n"
    "v_cvt_pk_bf16_f32 v230, v188, v189\n"
    "v_cvt_pk_bf16_f32 v231, v190, v191\n"
    "v_mfma_f32_16x16x32_bf16 a[156:159], v[148:151], v[216:219], a[156:159]\n"
    "s_add_u32 s26, s26, 125\n"
    "v_exp_f32 v192, v192\n"
    "v_mfma_f32_16x16x32_bf16 a[160:163], v[148:151], v[220:223], a[160:163]\n"
    "s_waitcnt vmcnt(5)\n"
    "v_add_f32 v20, v20, v191\n"
    "v_add_f32 v21, v21, v192\n"
    "v_mfma_f32_16x16x32_bf16 a[164:167], v[148:151], v[224:227], a[164:167]\n"
    "buffer_load_dwordx4 v9, s[44:47], s58 offen offset:2048 lds\n"
    "v_mfma_f32_16x16x32_bf16 v[188:191], a[224:227], v[96:99], v[172:175]\n"
    "s_add_u32 m0, s57, 106496\n"
    "v_exp_f32 v193, v193\n"
    "v_mfma_f32_16x16x32_bf16 v[188:191], a[228:231], v[100:103], v[188:191]\n"
    "s_add_u32 s58, s65, 73728\n"
    "v_exp_f32 v194, v194\n"
    "v_mfma_f32_16x16x32_bf16 v[188:191], a[232:235], v[104:107], v[188:191]\n"
    "s_and_b32 s58, s58, 0x1ffff\n"
    "v_exp_f32 v195, v195\n"
    "v_add_f32 v21, v21, v193\n"
    "v_mfma_f32_16x16x32_bf16 v[188:191], a[236:239], v[108:111], v[188:191]\n"
    "ds_read_b64_tr_b16 v[148:149], v17 offset:32768\n"
    "v_add_f32 v21, v21, v194\n"
    "v_cvt_pk_bf16_f32 v234, v192, v193\n"
    "v_cvt_pk_bf16_f32 v235, v194, v195\n"
    "v_add_f32 v21, v21, v195\n"
    "v_mfma_f32_16x16x32_bf16 v[192:195], a[236:239], v[124:127], v[176:179]\n"
    "v_exp_f32 v196, v196\n"
    "v_mfma_f32_16x16x32_bf16 v[192:195], a[232:235], v[120:123], v[192:195]\n"
    "v_exp_f32 v197, v197\n"
    "v_mfma_f32_16x16x32_bf16 v[192:195], a[228:231], v[116:119], v[192:195]\n"
    "v_exp_f32 v198, v198\n"
    "v_mfma_f32_16x16x32_bf16 v[192:195], a[224:227], v[112:115], v[192:195]\n"
    "ds_read_b64_tr_b16 v[150:151], v17 offset:36864\n"
    "v_add_f32 v22, v22, v196\n"
    "v_mfma_f32_16x16x32_bf16 a[168:171], v[152:155], v[200:203], a[168:171]\n"
    "v_exp_f32 v199, v199\n"
    "v_mfma_f32_16x16x32_bf16 a[172:175], v[152:155], v[204:207], a[172:175]\n"
    "v_add_f32 v22, v22, v197\n"
    "v_add_f32 v22, v22, v198\n"
    "v_mfma_f32_16x16x32_bf16 a[176:179], v[152:155], v[208:211], a[176:179]\n"
    "v_cvt_pk_bf16_f32 v238, v196, v197\n"
    "v_cvt_pk_bf16_f32 v239, v198, v199\n"
    "v_mfma_f32_16x16x32_bf16 a[180:183], v[152:155], v[212:215], a[180:183]\n"
    "v_exp_f32 v184, v184\n"
    "v_mfma_f32_16x16x32_bf16 a[184:187], v[152:155], v[216:219], a[184:187]\n"
    "s_waitcnt vmcnt(5)\n"
    "v_add_f32 v22, v22, v199\n"
    "v_add_f32 v23, v23, v184\n"
    "v_mfma_f32_16x16x32_bf16 a[188:191], v[152:155], v[220:223], a[188:191]\n"
    "v_exp_f32 v185, v185\n"
    "v_mfma_f32_16x16x32_bf16 a[192:195], v[152:155], v[224:227], a[192:195]\n"
    "buffer_load_dwordx4 v10, s[48:51], s58 offen nt lds\n"
    "v_mfma_f32_16x16x32_bf16 v[196:199], a[224:227], v[128:131], v[180:183]\n"
    "v_exp_f32 v186, v186\n"
    "v_mfma_f32_16x16x32_bf16 v[196:199], a[228:231], v[132:135], v[196:199]\n"
    "v_exp_f32 v187, v187\n"
    "v_mfma_f32_16x16x32_bf16 v[196:199], a[232:235], v[136:139], v[196:199]\n"
    "ds_read_b64_tr_b16 v[152:153], v14 offset:33024\n"
    "v_add_f32 v23, v23, v185\n"
    "v_mfma_f32_16x16x32_bf16 v[196:199], a[236:239], v[140:143], v[196:199]\n"
    "ds_read_b64_tr_b16 v[154:155], v14 offset:37120\n"
    "v_add_f32 v23, v23, v186\n"
    "v_mfma_f32_16x16x32_bf16 a[196:199], v[28:31], v[200:203], a[196:199]\n"
    "ds_read_b128 a[224:227], v12 offset:4096\n"
    "v_cvt_pk_bf16_f32 v242, v184, v185\n"
    "v_mfma_f32_16x16x32_bf16 a[200:203], v[28:31], v[204:207], a[200:203]\n"
    "ds_read_b128 a[228:231], v12 offset:4352\n"
    "v_cvt_pk_bf16_f32 v243, v186, v187\n"
    "v_mfma_f32_16x16x32_bf16 a[204:207], v[28:31], v[208:211], a[204:207]\n"
    "ds_read_b128 a[232:235], v12 offset:4608\n"
    "v_add_f32 v23, v23, v187\n"
    "v_mfma_f32_16x16x32_bf16 a[208:211], v[28:31], v[212:215], a[208:211]\n"
    "v_exp_f32 v188, v188\n"
    "v_mfma_f32_16x16x32_bf16 a[212:215], v[28:31], v[216:219], a[212:215]\n"
    "v_exp_f32 v189, v189\n"
    "v_mfma_f32_16x16x32_bf16 a[216:219], v[28:31], v[220:223], a[216:219]\n"
    "v_exp_f32 v190, v190\n"
    "v_mfma_f32_16x16x32_bf16 a[220:223], v[28:31], v[224:227], a[220:223]\n"
    "s_waitcnt lgkmcnt(5)\n"
    "ds_read_b128 a[236:239], v12 offset:4864\n"
    "v_add_f32 v24, v24, v188\n"
    "v_mfma_f32_16x16x32_bf16 v[184:187], a[240:243], v[32:35], v[156:159]\n"
    "v_exp_f32 v191, v191\n"
    "v_mfma_f32_16x16x32_bf16 v[184:187], a[244:247], v[36:39], v[184:187]\n"
    "ds_read_b64_tr_b16 v[28:29], v17 offset:33024\n"
    "v_add_f32 v24, v24, v189\n"
    "v_mfma_f32_16x16x32_bf16 v[184:187], a[248:251], v[40:43], v[184:187]\n"
    "s_waitcnt vmcnt(5)\n"
    "ds_read_b64_tr_b16 v[30:31], v17 offset:37120\n"
    "v_add_f32 v24, v24, v190\n"
    "v_mfma_f32_16x16x32_bf16 v[184:187], a[252:255], v[44:47], v[184:187]\n"
    "v_cvt_pk_bf16_f32 v246, v188, v189\n"
    "v_cvt_pk_bf16_f32 v247, v190, v191\n"
    "v_add_f32 v24, v24, v191\n"
    "v_mfma_f32_16x16x32_bf16 v[188:191], a[252:255], v[60:63], v[160:163]\n"
    "buffer_load_dwordx4 v10, s[48:51], s58 offen offset:2048 nt lds\n"
    "v_mfma_f32_16x16x32_bf16 v[188:191], a[248:251], v[56:59], v[188:191]\n"
    "s_add_u32 m0, s57, 110592\n"
    "v_exp_f32 v192, v192\n"
    "v_mfma_f32_16x16x32_bf16 v[188:191], a[244:247], v[52:55], v[188:191]\n"
    "s_add_u32 s58, s58, 4096\n"
    "v_exp_f32 v193, v193\n"
    "v_mfma_f32_16x16x32_bf16 v[188:191], a[240:243], v[48:51], v[188:191]\n"
    "v_exp_f32 v194, v194\n"
    "v_mfma_f32_16x16x32_bf16 a[0:3], v[144:147], v[228:231], a[0:3]\n"
    "v_exp_f32 v195, v195\n"
    "v_mfma_f32_16x16x32_bf16 a[4:7], v[144:147], v[232:235], a[4:7]\n"
    "v_exp_f32 v196, v196\n"
    "v_mfma_f32_16x16x32_bf16 a[8:11], v[144:147], v[236:239], a[8:11]\n"
    "v_exp_f32 v197, v197\n"
    "v_mfma_f32_16x16x32_bf16 a[12:15], v[144:147], v[240:243], a[12:15]\n"
    "v_exp_f32 v198, v198\n"
    "v_cvt_pk_bf16_f32 v250, v192, v193\n"
    "v_cvt_pk_bf16_f32 v251, v194, v195\n"
    "v_exp_f32 v199, v199\n"
    "v_mfma_f32_16x16x32_bf16 a[16:19], v[144:147], v[244:247], a[16:19]\n"
    "v_cvt_pk_bf16_f32 v254, v196, v197\n"
    "v_cvt_pk_bf16_f32 v255, v198, v199\n"
    "v_mfma_f32_16x16x32_bf16 a[20:23], v[144:147], v[248:251], a[20:23]\n"
    "v_add_f32 v25, v25, v192\n"
    "v_add_f32 v25, v25, v193\n"
    "v_mfma_f32_16x16x32_bf16 a[24:27], v[144:147], v[252:255], a[24:27]\n"
    "v_add_f32 v25, v25, v194\n"
    "v_add_f32 v25, v25, v195\n"
    "v_mfma_f32_16x16x32_bf16 v[192:195], a[240:243], v[64:67], v[164:167]\n"
    "ds_read_b64_tr_b16 v[144:145], v14 offset:33280\n"
    "v_add_f32 v26, v26, v196\n"
    "v_mfma_f32_16x16x32_bf16 v[192:195], a[244:247], v[68:71], v[192:195]\n"
    "ds_read_b64_tr_b16 v[146:147], v14 offset:37376\n"
    "v_add_f32 v26, v26, v197\n"
    "v_mfma_f32_16x16x32_bf16 v[192:195], a[248:251], v[72:75], v[192:195]\n"
    "s_waitcnt vmcnt(5)\n"
    "v_exp_f32 v184, v184\n"
    "v_mfma_f32_16x16x32_bf16 v[192:195], a[252:255], v[76:79], v[192:195]\n"
    "v_add_f32 v26, v26, v198\n"
    "v_add_f32 v26, v26, v199\n"
    "v_mfma_f32_16x16x32_bf16 v[196:199], a[252:255], v[92:95], v[168:171]\n"
    "buffer_load_dwordx4 v10, s[48:51], s58 offen nt lds\n"
    "v_mfma_f32_16x16x32_bf16 v[196:199], a[248:251], v[88:91], v[196:199]\n"
    "v_exp_f32 v185, v185\n"
    "v_mfma_f32_16x16x32_bf16 v[196:199], a[244:247], v[84:87], v[196:199]\n"
    "v_exp_f32 v186, v186\n"
    "v_mfma_f32_16x16x32_bf16 v[196:199], a[240:243], v[80:83], v[196:199]\n"
    "v_add_f32 v20, v20, v184\n"
    "v_add_f32 v20, v20, v185\n"
    "v_mfma_f32_16x16x32_bf16 a[28:31], v[148:151], v[228:231], a[28:31]\n"
    "v_exp_f32 v187, v187\n"
    "v_mfma_f32_16x16x32_bf16 a[32:35], v[148:151], v[232:235], a[32:35]\n"
    "v_add_f32 v20, v20, v186\n"
    "v_cvt_pk_bf16_f32 v200, v184, v185\n"
    "v_mfma_f32_16x16x32_bf16 a[36:39], v[148:151], v[236:239], a[36:39]\n"
    "v_cvt_pk_bf16_f32 v201, v186, v187\n"
    "v_add_f32 v20, v20, v187\n"
    "v_mfma_f32_16x16x32_bf16 a[40:43], v[148:151], v[240:243], a[40:43]\n"
    "v_exp_f32 v188, v188\n"
    "v_mfma_f32_16x16x32_bf16 a[44:47], v[148:151], v[244:247], a[44:47]\n"
    "v_exp_f32 v189, v189\n"
    "v_mfma_f32_16x16x32_bf16 a[48:51], v[148:151], v[248:251], a[48:51]\n"
    "v_exp_f32 v190, v190\n"
    "v_mfma_f32_16x16x32_bf16 a[52:55], v[148:151], v[252:255], a[52:55]\n"
    "ds_read_b64_tr_b16 v[148:149], v17 offset:33280\n"
    "v_add_f32 v21, v21, v188\n"
    "v_mfma_f32_16x16x32_bf16 v[184:187], a[240:243], v[96:99], v[172:175]\n"
    "v_exp_f32 v191, v191\n"
    "v_mfma_f32_16x16x32_bf16 v[184:187], a[244:247], v[100:103], v[184:187]\n"
    "ds_read_b64_tr_b16 v[150:151], v17 offset:37376\n"
    "v_add_f32 v21, v21, v189\n"
    "v_mfma_f32_16x16x32_bf16 v[184:187], a[248:251], v[104:107], v[184:187]\n"
    "s_waitcnt vmcnt(5)\n"
    "v_add_f32 v21, v21, v190\n"
    "v_cvt_pk_bf16_f32 v204, v188, v189\n"
    "v_mfma_f32_16x16x32_bf16 v[184:187], a[252:255], v[108:111], v[184:187]\n"
    "v_cvt_pk_bf16_f32 v205, v190, v191\n"
    "v_add_f32 v21, v21, v191\n"
    "v_mfma_f32_16x16x32_bf16 v[188:191], a[252:255], v[124:127], v[176:179]\n"
    "buffer_load_dwordx4 v10, s[48:51], s58 offen offset:2048 nt lds\n"
    "v_mfma_f32_16x16x32_bf16 v[188:191], a[248:251], v[120:123], v[188:191]\n"
    "s_add_u32 m0, s57, 0\n"
    "v_exp_f32 v192, v192\n"
    "v_mfma_f32_16x16x32_bf16 v[188:191], a[244:247], v[116:119], v[188:191]\n"
    "s_add_u32 s58, s65, 90112\n"
    "v_exp_f32 v193, v193\n"
    "v_mfma_f32_16x16x32_bf16 v[188:191], a[240:243], v[112:115], v[188:191]\n"
    "s_waitcnt lgkmcnt(2)\n"
    "v_exp_f32 v194, v194\n"
    "v_mfma_f32_16x16x32_bf16 a[56:59], v[152:155], v[228:231], a[56:59]\n"
    "s_and_b32 s58, s58, 0x1ffff\n"
    "v_add_f32 v22, v22, v192\n"
    "v_add_f32 v22, v22, v193\n"
    "v_mfma_f32_16x16x32_bf16 a[60:63], v[152:155], v[232:235], a[60:63]\n"
    "v_exp_f32 v195, v195\n"
    "v_mfma_f32_16x16x32_bf16 a[64:67], v[152:155], v[236:239], a[64:67]\n"
    "v_add_f32 v22, v22, v194\n"
    "v_cvt_pk_bf16_f32 v208, v192, v193\n"
    "v_mfma_f32_16x16x32_bf16 a[68:71], v[152:155], v[240:243], a[68:71]\n"
    "v_cvt_pk_bf16_f32 v209, v194, v195\n"
    "v_add_f32 v22, v22, v195\n"
    "v_mfma_f32_16x16x32_bf16 a[72:75], v[152:155], v[244:247], a[72:75]\n"
    "v_exp_f32 v196, v196\n"
    "v_mfma_f32_16x16x32_bf16 a[76:79], v[152:155], v[248:251], a[76:79]\n"
    "v_exp_f32 v197, v197\n"
    "v_mfma_f32_16x16x32_bf16 a[80:83], v[152:155], v[252:255], a[80:83]\n"
    "v_exp_f32 v198, v198\n"
    "v_mfma_f32_16x16x32_bf16 v[192:195], a[240:243], v[128:131], v[180:183]\n"
    "ds_read_b64_tr_b16 v[152:153], v14 offset:33536\n"
    "v_add_f32 v23, v23, v196\n"
    "v_mfma_f32_16x16x32_bf16 v[192:195], a[244:247], v[132:135], v[192:195]\n"
    "v_exp_f32 v199, v199\n"
    "v_mfma_f32_16x16x32_bf16 v[192:195], a[248:251], v[136:139], v[192:195]\n"
    "s_waitcnt vmcnt(5)\n"
    "ds_read_b64_tr_b16 v[154:155], v14 offset:37632\n"
    "v_add_f32 v23, v23, v197\n"
    "v_mfma_f32_16x16x32_bf16 v[192:195], a[252:255], v[140:143], v[192:195]\n"
    "v_add_f32 v23, v23, v198\n"
    "v_cvt_pk_bf16_f32 v212, v196, v197\n"
    "v_mfma_f32_16x16x32_bf16 a[84:87], v[28:31], v[228:231], a[84:87]\n"
    "buffer_load_dwordx4 v8, s[44:47], s58 offen lds\n"
    "v_mfma_f32_16x16x32_bf16 a[88:91], v[28:31], v[232:235], a[88:91]\n"
    "v_cvt_pk_bf16_f32 v213, v198, v199\n"
    "v_add_f32 v23, v23, v199\n"
    "v_mfma_f32_16x16x32_bf16 a[92:95], v[28:31], v[236:239], a[92:95]\n"
    "v_exp_f32 v184, v184\n"
    "v_mfma_f32_16x16x32_bf16 a[96:99], v[28:31], v[240:243], a[96:99]\n"
    "v_exp_f32 v185, v185\n"
    "v_mfma_f32_16x16x32_bf16 a[100:103], v[28:31], v[244:247], a[100:103]\n"
    "v_exp_f32 v186, v186\n"
    "v_mfma_f32_16x16x32_bf16 a[104:107], v[28:31], v[248:251], a[104:107]\n"
    "v_add_f32 v24, v24, v184\n"
    "v_add_f32 v24, v24, v185\n"
    "v_mfma_f32_16x16x32_bf16 a[108:111], v[28:31], v[252:255], a[108:111]\n"
    "v_exp_f32 v187, v187\n"
    "v_mfma_f32_16x16x32_bf16 v[196:199], a[224:227], v[32:35], v[156:159]\n"
    "ds_read_b64_tr_b16 v[28:29], v17 offset:33536\n"
    "v_add_f32 v24, v24, v186\n"
    "v_mfma_f32_16x16x32_bf16 v[196:199], a[228:231], v[36:39], v[196:199]\n"
    "ds_read_b64_tr_b16 v[30:31], v17 offset:37632\n"
    "v_cvt_pk_bf16_f32 v216, v184, v185\n"
    "v_mfma_f32_16x16x32_bf16 v[196:199], a[232:235], v[40:43], v[196:199]\n"
    "v_cvt_pk_bf16_f32 v217, v186, v187\n"
    "v_add_f32 v24, v24, v187\n"
    "v_mfma_f32_16x16x32_bf16 v[196:199], a[236:239], v[44:47], v[196:199]\n"
    "v_exp_f32 v188, v188\n"
    "v_mfma_f32_16x16x32_bf16 v[184:187], a[236:239], v[60:63], v[160:163]\n"
    "v_exp_f32 v189, v189\n"
    "v_mfma_f32_16x16x32_bf16 v[184:187], a[232:235], v[56:59], v[184:187]\n"
    "v_exp_f32 v190, v190\n"
    "v_mfma_f32_16x16x32_bf16 v[184:187], a[228:231], v[52:55], v[184:187]\n"
    "s_memrealtime s[24:25]\n"
    "v_add_f32 v25, v25, v188\n"
    "v_add_f32 v25, v25, v189\n"
    "v_mfma_f32_16x16x32_bf16 v[184:187], a[224:227], v[48:51], v[184:187]\n"
    "s_waitcnt vmcnt(5)\n"
    "v_exp_f32 v191, v191\n"
    "v_mfma_f32_16x16x32_bf16 a[112:115], v[144:147], v[228:231], a[112:115]\n"
    "buffer_load_dwordx4 v9, s[44:47], s58 offen offset:2048 lds\n"
    "v_mfma_f32_16x16x32_bf16 a[116:119], v[144:147], v[232:235], a[116:119]\n"
    "s_add_u32 m0, s57, 4096\n"
    "v_add_f32 v25, v25, v190\n"
    "v_cvt_pk_bf16_f32 v220, v188, v189\n"
    "v_mfma_f32_16x16x32_bf16 a[120:123], v[144:147], v[236:239], a[120:123]\n"
    "s_add_u32 s58, s58, 4096\n"
    "v_cvt_pk_bf16_f32 v221, v190, v191\n"
    "v_add_f32 v25, v25, v191\n"
    "v_mfma_f32_16x16x32_bf16 a[124:127], v[144:147], v[240:243], a[124:127]\n"
    "v_exp_f32 v192, v192\n"
    "v_mfma_f32_16x16x32_bf16 a[128:131], v[144:147], v[244:247], a[128:131]\n"
    "v_exp_f32 v193, v193\n"
    "v_mfma_f32_16x16x32_bf16 a[132:135], v[144:147], v[248:251], a[132:135]\n"
    "v_exp_f32 v194, v194\n"
    "v_mfma_f32_16x16x32_bf16 a[136:139], v[144:147], v[252:255], a[136:139]\n"
    "v_add_f32 v26, v26, v192\n"
    "v_add_f32 v26, v26, v193\n"
    "v_mfma_f32_16x16x32_bf16 v[188:191], a[224:227], v[64:67], v[164:167]\n"
    "v_exp_f32 v195, v195\n"
    "v_mfma_f32_16x16x32_bf16 v[188:191], a[228:231], v[68:71], v[188:191]\n"
    "s_waitcnt vmcnt(10) lgkmcnt(0)\n"
    "s_barrier\n"
    "ds_read_b128 a[240:243], v12 offset:32768\n"
    "v_mfma_f32_16x16x32_bf16 v[188:191], a[232:235], v[72:75], v[188:191]\n"
    "s_sub_i32 s25, s26, s56\n"
    "ds_read_b128 a[244:247], v12 offset:33024\n"
    "v_add_f32 v26, v26, v194\n"
    "v_mfma_f32_16x16x32_bf16 v[188:191], a[236:239], v[76:79], v[188:191]\n"
    "s_max_i32 s25, s25, -3\n"
    "ds_read_b128 a[248:251], v12 offset:33280\n"
    "v_cvt_pk_bf16_f32 v224, v192, v193\n"
    "v_cvt_pk_bf16_f32 v225, v194, v195\n"
    "v_add_f32 v26, v26, v195\n"
    "v_mfma_f32_16x16x32_bf16 v[192:195], a[236:239], v[92:95], v[168:171]\n"
    "s_min_i32 s25, s25, 0\n"
    "v_exp_f32 v196, v196\n"
    "v_mfma_f32_16x16x32_bf16 v[192:195], a[232:235], v[88:91], v[192:195]\n"
    "s_add_u32 s56, s56, s25\n"
    "v_exp_f32 v197, v197\n"
    "v_mfma_f32_16x16x32_bf16 v[192:195], a[228:231], v[84:87], v[192:195]\n"
    "s_sub_i32 s25, s56, s24\n"
    "v_exp_f32 v198, v198\n"
    "v_mfma_f32_16x16x32_bf16 v[192:195], a[224:227], v[80:83], v[192:195]\n"
    "s_max_i32 s25, s25, -10\n"
    "ds_read_b128 a[252:255], v12 offset:33536\n"
    "v_add_f32 v20, v20, v196\n"
    "v_mfma_f32_16x16x32_bf16 a[140:143], v[148:151], v[228:231], a[140:143]\n"
    "s_min_i32 s25, s25, 62\n"
    "v_exp_f32 v199, v199\n"
    "v_mfma_f32_16x16x32_bf16 a[144:147], v[148:151], v[232:235], a[144:147]\n"
    "s_add_u32 s56, s24, s25\n"
    "ds_read_b64_tr_b16 v[144:145], v15 offset:0\n"
    "v_add_f32 v20, v20, v197\n"
    "v_mfma_f32_16x16x32_bf16 a[148:151], v[148:151], v[236:239], a[148:151]\n"
    "s_add_u32 s56, s56, 125\n"
    "ds_read_b64_tr_b16 v[146:147], v15 offset:4096\n"
    "v_add_f32 v20, v20, v198\n"
    "v_mfma_f32_16x16x32_bf16 a[152:155], v[148:151], v[240:243], a[152:155]\n"
    "s_cmp_gt_i32 s25, 2\ns_cbranch_scc1 L_ta8_%=\nL_tb8_%=:\n"
    "v_cvt_pk_bf16_f32 v202, v196, v197\n"
    "v_cvt_pk_bf16_f32 v203, v198, v199\n"
    "v_mfma_f32_16x16x32_bf16 a[156:159], v[148:151], v[244:247], a[156:159]\n"
    "s_add_u32 s26, s26, 125\n"
    "v_exp_f32 v184, v184\n"
    "v_mfma_f32_16x16x32_bf16 a[160:163], v[148:151], v[248:251], a[160:163]\n"
    "s_waitcnt vmcnt(5)\n"
    "v_add_f32 v20, v20, v199\n"
    "v_add_f32 v21, v21, v184\n"
    "v_mfma_f32_16x16x32_bf16 a[164:167], v[148:151], v[252:255], a[164:167]\n"
    "buffer_load_dwordx4 v8, s[44:47], s58 offen lds\n"
    "v_mfma_f32_16x16x32_bf16 v[196:199], a[224:227], v[96:99], v[172:175]\n"
    "v_exp_f32 v185, v185\n"
    "v_mfma_f32_16x16x32_bf16 v[196:199], a[228:231], v[100:103], v[196:199]\n"
    "v_exp_f32 v186, v186\n"
    "v_mfma_f32_16x16x32_bf16 v[196:199], a[232:235], v[104:107], v[196:199]\n"
    "v_exp_f32 v187, v187\n"
    "v_add_f32 v21, v21, v185\n"
    "v_mfma_f32_16x16x32_bf16 v[196:199], a[236:239], v[108:111], v[196:199]\n"
    "ds_read_b64_tr_b16 v[148:149], v18 offset:0\n"
    "v_add_f32 v21, v21, v186\n"
    "v_cvt_pk_bf16_f32 v206, v184, v185\n"
    "v_cvt_pk_bf16_f32 v207, v186, v187\n"
    "v_add_f32 v21, v21, v187\n"
    "v_mfma_f32_16x16x32_bf16 v[184:187], a[236:239], v[124:127], v[176:179]\n"
    "v_exp_f32 v188, v188\n"
    "v_mfma_f32_16x16x32_bf16 v[184:187], a[232:235], v[120:123], v[184:187]\n"
    "v_exp_f32 v189, v189\n"
    "v_mfma_f32_16x16x32_bf16 v[184:187], a[228:231], v[116:119], v[184:187]\n"
    "v_exp_f32 v190, v190\n"
    "v_mfma_f32_16x16x32_bf16 v[184:187], a[224:227], v[112:115], v[184:187]\n"
    "s_waitcnt vmcnt(5)\n"
    "ds_read_b64_tr_b16 v[150:151], v18 offset:4096\n"
    "v_add_f32 v22, v22, v188\n"
    "v_mfma_f32_16x16x32_bf16 a[168:171], v[152:155], v[228:231], a[168:171]\n"
    "v_exp_f32 v191, v191\n"
    "v_mfma_f32_16x16x32_bf16 a[172:175], v[152:155], v[232:235], a[172:175]\n"
    "buffer_load_dwordx4 v9, s[44:47], s58 offen offset:2048 lds\n"
    "v_mfma_f32_16x16x32_bf16 a[176:179], v[152:155], v[236:239], a[176:179]\n"
    "s_add_u32 m0, s57, 139264\n"
    "v_add_f32 v22, v22, v189\n"
    "v_add_f32 v22, v22, v190\n"
    "v_mfma_f32_16x16x32_bf16 a[180:183], v[152:155], v[240:243], a[180:183]\n"
    "s_add_u32 s58, s65, 81920\n"
    "v_cvt_pk_bf16_f32 v210, v188, v189\n"
    "v_cvt_pk_bf16_f32 v211, v190, v191\n"
    "v_mfma_f32_16x16x32_bf16 a[184:187], v[152:155], v[244:247], a[184:187]\n"
    "s_and_b32 s58, s58, 0x1ffff\n"
    "v_exp_f32 v192, v192\n"
    "v_mfma_f32_16x16x32_bf16 a[188:191], v[152:155], v[248:251], a[188:191]\n"
    "v_add_f32 v22, v22, v191\n"
    "v_add_f32 v23, v23, v192\n"
    "v_mfma_f32_16x16x32_bf16 a[192:195], v[152:155], v[252:255], a[192:195]\n"
    "v_exp_f32 v193, v193\n"
    "v_mfma_f32_16x16x32_bf16 v[188:191], a[224:227], v[128:131], v[180:183]\n"
    "v_exp_f32 v194, v194\n"
    "v_mfma_f32_16x16x32_bf16 v[188:191], a[228:231], v[132:135], v[188:191]\n"
    "v_exp_f32 v195, v195\n"
    "v_mfma_f32_16x16x32_bf16 v[188:191], a[232:235], v[136:139], v[188:191]\n"
    "ds_read_b64_tr_b16 v[152:153], v15 offset:256\n"
    "v_add_f32 v23, v23, v193\n"
    "v_mfma_f32_16x16x32_bf16 v[188:191], a[236:239], v[140:143], v[188:191]\n"
    "ds_read_b64_tr_b16 v[154:155], v15 offset:4352\n"
    "v_add_f32 v23, v23, v194\n"
    "v_mfma_f32_16x16x32_bf16 a[196:199], v[28:31], v[228:231], a[196:199]\n"
    "ds_read_b128 a[224:227], v12 offset:36864\n"
    "v_cvt_pk_bf16_f32 v214, v192, v193\n"
    "v_mfma_f32_16x16x32_bf16 a[200:203], v[28:31], v[232:235], a[200:203]\n"
    "ds_read_b128 a[228:231], v12 offset:37120\n"
    "v_cvt_pk_bf16_f32 v215, v194, v195\n"
    "v_mfma_f32_16x16x32_bf16 a[204:207], v[28:31], v[236:239], a[204:207]\n"
    "ds_read_b128 a[232:235], v12 offset:37376\n"
    "v_add_f32 v23, v23, v195\n"
    "v_mfma_f32_16x16x32_bf16 a[208:211], v[28:31], v[240:243], a[208:211]\n"
    "s_waitcnt vmcnt(5)\n"
    "v_exp_f32 v196, v196\n"
    "v_mfma_f32_16x16x32_bf16 a[212:215], v[28:31], v[244:247], a[212:215]\n"
    "v_exp_f32 v197, v197\n"
    "v_mfma_f32_16x16x32_bf16 a[216:219], v[28:31], v[248:251], a[216:219]\n"
    "buffer_load_dwordx4 v10, s[48:51], s58 offen nt lds\n"
    "v_mfma_f32_16x16x32_bf16 a[220:223], v[28:31], v[252:255], a[220:223]\n"
    "s_waitcnt lgkmcnt(5)\n"
    "v_exp_f32 v198, v198\n"
    "v_mfma_f32_16x16x32_bf16 v[192:195], a[240:243], v[32:35], v[156:159]\n"
    "ds_read_b128 a[236:239], v12 offset:37632\n"
    "v_add_f32 v24, v24, v196\n"
    "v_mfma_f32_16x16x32_bf16 v[192:195], a[244:247], v[36:39], v[192:195]\n"
    "v_exp_f32 v199, v199\n"
    "v_mfma_f32_16x16x32_bf16 v[192:195], a[248:251], v[40:43], v[192:195]\n"
    "ds_read_b64_tr_b16 v[28:29], v18 offset:256\n"
    "v_add_f32 v24, v24, v197\n"
    "v_mfma_f32_16x16x32_bf16 v[192:195], a[252:255], v[44:47], v[192:195]\n"
    "ds_read_b64_tr_b16 v[30:31], v18 offset:4352\n"
    "v_add_f32 v24, v24, v198\n"
    "v_cvt_pk_bf16_f32 v218, v196, v197\n"
    "v_cvt_pk_bf16_f32 v219, v198, v199\n"
    "v_add_f32 v24, v24, v199\n"
    "v_mfma_f32_16x16x32_bf16 v[196:199], a[252:255], v[60:63], v[160:163]\n"
    "v_exp_f32 v184, v184\n"
    "v_mfma_f32_16x16x32_bf16 v[196:199], a[248:251], v[56:59], v[196:199]\n"
    "v_exp_f32 v185, v185\n"
    "v_mfma_f32_16x16x32_bf16 v[196:199], a[244:247], v[52:55], v[196:199]\n"
    "v_exp_f32 v186, v186\n"
    "v_mfma_f32_16x16x32_bf16 v[196:199], a[240:243], v[48:51], v[196:199]\n"
    "v_exp_f32 v187, v187\n"
    "v_mfma_f32_16x16x32_bf16 a[0:3], v[144:147], v[200:203], a[0:3]\n"
    "v_exp_f32 v188, v188\n"
    "v_mfma_f32_16x16x32_bf16 a[4:7], v[144:147], v[204:207], a[4:7]\n"
    "v_exp_f32 v189, v189\n"
    "v_mfma_f32_16x16x32_bf16 a[8:11], v[144:147], v[208:211], a[8:11]\n"
    "v_exp_f32 v190, v190\n"
    "v_mfma_f32_16x16x32_bf16 a[12:15], v[144:147], v[212:215], a[12:15]\n"
    "s_waitcnt vmcnt(5)\n"
    "v_exp_f32 v191, v191\n"
    "v_cvt_pk_bf16_f32 v222, v184, v185\n"
    "v_cvt_pk_bf16_f32 v223, v186, v187\n"
    "v_mfma_f32_16x16x32_bf16 a[16:19], v[144:147], v[216:219], a[16:19]\n"
    "v_cvt_pk_bf16_f32 v226, v188, v189\n"
    "v_cvt_pk_bf16_f32 v227, v190, v191\n"
    "v_mfma_f32_16x16x32_bf16 a[20:23], v[144:147], v[220:223], a[20:23]\n"
    "buffer_load_dwordx4 v10, s[48:51], s58 offen offset:2048 nt lds\n"
    "v_add_f32 v25, v25, v184\n"
    "v_add_f32 v25, v25, v185\n"
    "v_mfma_f32_16x16x32_bf16 a[24:27], v[144:147], v[224:227], a[24:27]\n"
    "s_add_u32 m0, s57, 143360\n"
    "v_add_f32 v25, v25, v186\n"
    "v_add_f32 v25, v25, v187\n"
    "v_mfma_f32_16x16x32_bf16 v[184:187], a[240:243], v[64:67], v[164:167]\n"
    "s_add_u32 s58, s58, 4096\n"
    "ds_read_b64_tr_b16 v[144:145], v15 offset:512\n"
    "v_add_f32 v26, v26, v188\n"
    "v_mfma_f32_16x16x32_bf16 v[184:187], a[244:247], v[68:71], v[184:187]\n"
    "ds_read_b64_tr_b16 v[146:147], v15 offset:4608\n"
    "v_add_f32 v26, v26, v189\n"
    "v_mfma_f32_16x16x32_bf16 v[184:187], a[248:251], v[72:75], v[184:187]\n"
    "v_exp_f32 v192, v192\n"
    "v_mfma_f32_16x16x32_bf16 v[184:187], a[252:255], v[76:79], v[184:187]\n"
    "v_add_f32 v26, v26, v190\n"
    "v_add_f32 v26, v26, v191\n"
    "v_mfma_f32_16x16x32_bf16 v[188:191], a[252:255], v[92:95], v[168:171]\n"
    "v_exp_f32 v193, v193\n"
    "v_mfma_f32_16x16x32_bf16 v[188:191], a[248:251], v[88:91], v[188:191]\n"
    "v_exp_f32 v194, v194\n"
    "v_mfma_f32_16x16x32_bf16 v[188:191], a[244:247], v[84:87], v[188:191]\n"
    "v_add_f32 v20, v20, v192\n"
    "v_add_f32 v20, v20, v193\n"
    "v_mfma_f32_16x16x32_bf16 v[188:191], a[240:243], v[80:83], v[188:191]\n"
    "v_exp_f32 v195, v195\n"
    "v_mfma_f32_16x16x32_bf16 a[28:31], v[148:151], v[200:203], a[28:31]\n"
    "v_add_f32 v20, v20, v194\n"
    "v_cvt_pk_bf16_f32 v228, v192, v193\n"
    "v_mfma_f32_16x16x32_bf16 a[32:35], v[148:151], v[204:207], a[32:35]\n"
    "v_cvt_pk_bf16_f32 v229, v194, v195\n"
    "v_add_f32 v20, v20, v195\n"
    "v_mfma_f32_16x16x32_bf16 a[36:39], v[148:151], v[208:211], a[36:39]\n"
    "v_exp_f32 v196, v196\n"
    "v_mfma_f32_16x16x32_bf16 a[40:43], v[148:151], v[212:215], a[40:43]\n"
    "s_waitcnt vmcnt(5)\n"
    "v_exp_f32 v197, v197\n"
    "v_mfma_f32_16x16x32_bf16 a[44:47], v[148:151], v[216:219], a[44:47]\n"
    "v_exp_f32 v198, v198\n"
    "v_mfma_f32_16x16x32_bf16 a[48:51], v[148:151], v[220:223], a[48:51]\n"
    "buffer_load_dwordx4 v10, s[48:51], s58 offen nt lds\n"
    "v_mfma_f32_16x16x32_bf16 a[52:55], v[148:151], v[224:227], a[52:55]\n"
    "ds_read_b64_tr_b16 v[148:149], v18 offset:512\n"
    "v_add_f32 v21, v21, v196\n"
    "v_mfma_f32_16x16x32_bf16 v[192:195], a[240:243], v[96:99], v[172:175]\n"
    "v_exp_f32 v199, v199\n"
    "v_mfma_f32_16x16x32_bf16 v[192:195], a[244:247], v[100:103], v[192:195]\n"
    "ds_read_b64_tr_b16 v[150:151], v18 offset:4608\n"
    "v_add_f32 v21, v21, v197\n"
    "v_mfma_f32_16x16x32_bf16 v[192:195], a[248:251], v[104:107], v[192:195]\n"
    "v_add_f32 v21, v21, v198\n"
    "v_cvt_pk_bf16_f32 v232, v196, v197\n"
    "v_mfma_f32_16x16x32_bf16 v[192:195], a[252:255], v[108:111], v[192:195]\n"
    "v_cvt_pk_bf16_f32 v233, v198, v199\n"
    "v_add_f32 v21, v21, v199\n"
    "v_mfma_f32_16x16x32_bf16 v[196:199], a[252:255], v[124:127], v[176:179]\n"
    "v_exp_f32 v184, v184\n"
    "v_mfma_f32_16x16x32_bf16 v[196:199], a[248:251], v[120:123], v[196:199]\n"
    "v_exp_f32 v185, v185\n"
    "v_mfma_f32_16x16x32_bf16 v[196:199], a[244:247], v[116:119], v[196:199]\n"
    "v_exp_f32 v186, v186\n"
    "v_mfma_f32_16x16x32_bf16 v[196:199], a[240:243], v[112:115], v[196:199]\n"
    "s_waitcnt lgkmcnt(2)\n"
    "v_add_f32 v22, v22, v184\n"
    "v_add_f32 v22, v22, v185\n"
    "v_mfma_f32_16x16x32_bf16 a[56:59], v[152:155], v[200:203], a[56:59]\n"
    "v_exp_f32 v187, v187\n"
    "v_mfma_f32_16x16x32_bf16 a[60:63], v[152:155], v[204:207], a[60:63]\n"
    "v_add_f32 v22, v22, v186\n"
    "v_cvt_pk_bf16_f32 v236, v184, v185\n"
    "v_mfma_f32_16x16x32_bf16 a[64:67], v[152:155], v[208:211], a[64:67]\n"
    "v_cvt_pk_bf16_f32 v237, v186, v187\n"
    "v_add_f32 v22, v22, v187\n"
    "v_mfma_f32_16x16x32_bf16 a[68:71], v[152:155], v[212:215], a[68:71]\n"
    "v_exp_f32 v188, v188\n"
    "v_mfma_f32_16x16x32_bf16 a[72:75], v[152:155], v[216:219], a[72:75]\n"
    "s_waitcnt vmcnt(5)\n"
    "v_exp_f32 v189, v189\n"
    "v_mfma_f32_16x16x32_bf16 a[76:79], v[152:155], v[220:223], a[76:79]\n"
    "v_exp_f32 v190, v190\n"
    "v_mfma_f32_16x16x32_bf16 a[80:83], v[152:155], v[224:227], a[80:83]\n"
    "buffer_load_dwordx4 v10, s[48:51], s58 offen offset:2048 nt lds\n"
    "v_mfma_f32_16x16x32_bf16 v[184:187], a[240:243], v[128:131], v[180:183]\n"
    "s_add_u32 m0, s57, 32768\n"
    "ds_read_b64_tr_b16 v[152:153], v15 offset:768\n"
    "v_add_f32 v23, v23, v188\n"
    "v_mfma_f32_16x16x32_bf16 v[184:187], a[244:247], v[132:135], v[184:187]\n"
    "s_add_u32 s58, s65, 98304\n"
    "v_exp_f32 v191, v191\n"
    "v_mfma_f32_16x16x32_bf16 v[184:187], a[248:251], v[136:139], v[184:187]\n"
    "s_and_b32 s58, s58, 0x1ffff\n"
    "ds_read_b64_tr_b16 v[154:155], v15 offset:4864\n"
    "v_add_f32 v23, v23, v189\n"
    "v_mfma_f32_16x16x32_bf16 v[184:187], a[252:255], v[140:143], v[184:187]\n"
    "v_add_f32 v23, v23, v190\n"
    "v_cvt_pk_bf16_f32 v240, v188, v189\n"
    "v_mfma_f32_16x16x32_bf16 a[84:87], v[28:31], v[200:203], a[84:87]\n"
    "v_cvt_pk_bf16_f32 v241, v190, v191\n"
    "v_add_f32 v23, v23, v191\n"
    "v_mfma_f32_16x16x32_bf16 a[88:91], v[28:31], v[204:207], a[88:91]\n"
    "v_exp_f32 v192, v192\n"
    "v_mfma_f32_16x16x32_bf16 a[92:95], v[28:31], v[208:211], a[92:95]\n"
    "v_exp_f32 v193, v193\n"
    "v_mfma_f32_16x16x32_bf16 a[96:99], v[28:31], v[212:215], a[96:99]\n"
    "v_exp_f32 v194, v194\n"
    "v_mfma_f32_16x16x32_bf16 a[100:103], v[28:31], v[216:219], a[100:103]\n"
    "v_add_f32 v24, v24, v192\n"
    "v_add_f32 v24, v24, v193\n"
    "v_mfma_f32_16x16x32_bf16 a[104:107], v[28:31], v[220:223], a[104:107]\n"
    "v_exp_f32 v195, v195\n"
    "v_mfma_f32_16x16x32_bf16 a[108:111], v[28:31], v[224:227], a[108:111]\n"
    "ds_read_b64_tr_b16 v[28:29], v18 offset:768\n"
    "v_add_f32 v24, v24, v194\n"
    "v_mfma_f32_16x16x32_bf16 v[188:191], a[224:227], v[32:35], v[156:159]\n"
    "ds_read_b64_tr_b16 v[30:31], v18 offset:4864\n"
    "v_cvt_pk_bf16_f32 v244, v192, v193\n"
    "v_mfma_f32_16x16x32_bf16 v[188:191], a[228:231], v[36:39], v[188:191]\n"
    "s_waitcnt vmcnt(5)\n"
    "v_cvt_pk_bf16_f32 v245, v194, v195\n"
    "v_add_f32 v24, v24, v195\n"
    "v_mfma_f32_16x16x32_bf16 v[188:191], a[232:235], v[40:43], v[188:191]\n"
    "v_exp_f32 v196, v196\n"
    "v_mfma_f32_16x16x32_bf16 v[188:191], a[236:239], v[44:47], v[188:191]\n"
    "buffer_load_dwordx4 v8, s[44:47], s58 offen nt lds\n"
    "v_mfma_f32_16x16x32_bf16 v[192:195], a[236:239], v[60:63], v[160:163]\n"
    "v_exp_f32 v197, v197\n"
    "v_mfma_f32_16x16x32_bf16 v[192:195], a[232:235], v[56:59], v[192:195]\n"
    "v_exp_f32 v198, v198\n"
    "v_mfma_f32_16x16x32_bf16 v[192:195], a[228:231], v[52:55], v[192:195]\n"
    "s_memrealtime s[24:25]\n"
    "v_add_f32 v25, v25, v196\n"
    "v_add_f32 v25, v25, v197\n"
    "v_mfma_f32_16x16x32_bf16 v[192:195], a[224:227], v[48:51], v[192:195]\n"
    "v_exp_f32 v199, v199\n"
    "v_mfma_f32_16x16x32_bf16 a[112:115], v[144:147], v[200:203], a[112:115]\n"
    "v_add_f32 v25, v25, v198\n"
    "v_cvt_pk_bf16_f32 v248, v196, v197\n"
    "v_mfma_f32_16x16x32_bf16 a[116:119], v[144:147], v[204:207], a[116:119]\n"
    "v_cvt_pk_bf16_f32 v249, v198, v199\n"
    "v_add_f32 v25, v25, v199\n"
    "v_mfma_f32_16x16x32_bf16 a[120:123], v[144:147], v[208:211], a[120:123]\n"
    "v_exp_f32 v184, v184\n"
    "v_mfma_f32_16x16x32_bf16 a[124:127], v[144:147], v[212:215], a[124:127]\n"
    "v_exp_f32 v185, v185\n"
    "v_mfma_f32_16x16x32_bf16 a[128:131], v[144:147], v[216:219], a[128:131]\n"
    "v_exp_f32 v186, v186\n"
    "v_mfma_f32_16x16x32_bf16 a[132:135], v[144:147], v[220:223], a[132:135]\n"
    "v_add_f32 v26, v26, v184\n"
    "v_add_f32 v26, v26, v185\n"
    "v_mfma_f32_16x16x32_bf16 a[136:139], v[144:147], v[224:227], a[136:139]\n"
    "v_exp_f32 v187, v187\n"
    "v_mfma_f32_16x16x32_bf16 v[196:199], a[224:227], v[64:67], v[164:167]\n"
    "v_add_f32 v26, v26, v186\n"
    "v_cvt_pk_bf16_f32 v252, v184, v185\n"
    "v_mfma_f32_16x16x32_bf16 v[196:199], a[228:231], v[68:71], v[196:199]\n"
    "s_waitcnt vmcnt(9) lgkmcnt(0)\n"
    "s_barrier\n"
    "ds_read_b128 a[240:243], v13 offset:0\n"
    "v_mfma_f32_16x16x32_bf16 v[196:199], a[232:235], v[72:75], v[196:199]\n"
    "s_sub_i32 s25, s26, s56\n"
    "ds_read_b128 a[244:247], v13 offset:256\n"
    "v_cvt_pk_bf16_f32 v253, v186, v187\n"
    "v_mfma_f32_16x16x32_bf16 v[196:199], a[236:239], v[76:79], v[196:199]\n"
    "s_max_i32 s25, s25, -3\n"
    "ds_read_b128 a[248:251], v13 offset:512\n"
    "v_add_f32 v26, v26, v187\n"
    "v_mfma_f32_16x16x32_bf16 v[184:187], a[236:239], v[92:95], v[168:171]\n"
    "s_min_i32 s25, s25, 0\n"
    "v_exp_f32 v188, v188\n"
    "v_mfma_f32_16x16x32_bf16 v[184:187], a[232:235], v[88:91], v[184:187]\n"
    "s_add_u32 s56, s56, s25\n"
    "v_exp_f32 v189, v189\n"
    "v_mfma_f32_16x16x32_bf16 v[184:187], a[228:231], v[84:87], v[184:187]\n"
    "s_sub_i32 s25, s56, s24\n"
    "v_exp_f32 v190, v190\n"
    "v_mfma_f32_16x16x32_bf16 v[184:187], a[224:227], v[80:83], v[184:187]\n"
    "s_max_i32 s25, s25, -10\n"
    "ds_read_b128 a[252:255], v13 offset:768\n"
    "v_add_f32 v20, v20, v188\n"
    "v_mfma_f32_16x16x32_bf16 a[140:143], v[148:151], v[200:203], a[140:143]\n"
    "s_min_i32 s25, s25, 62\n"
    "v_exp_f32 v191, v191\n"
    "v_mfma_f32_16x16x32_bf16 a[144:147], v[148:151], v[204:207], a[144:147]\n"
    "s_add_u32 s56, s24, s25\n"
    "ds_read_b64_tr_b16 v[144:145], v15 offset:32768\n"
    "v_add_f32 v20, v20, v189\n"
    "v_mfma_f32_16x16x32_bf16 a[148:151], v[148:151], v[208:211], a[148:151]\n"
    "s_add_u32 s56, s56, 125\n"
    "ds_read_b64_tr_b16 v[146:147], v15 offset:36864\n"
    "v_add_f32 v20, v20, v190\n"
    "v_mfma_f32_16x16x32_bf16 a[152:155], v[148:151], v[212:215], a[152:155]\n"
    "s_cmp_gt_i32 s25, 2\ns_cbranch_scc1 L_ta9_%=\nL_tb9_%=:\n"
    "v_cvt_pk_bf16_f32 v230, v188, v189\n"
    "v_cvt_pk_bf16_f32 v231, v190, v191\n"
    "v_mfma_f32_16x16x32_bf16 a[156:159], v[148:151], v[216:219], a[156:159]\n"
    "s_add_u32 s26, s26, 125\n"
    "v_exp_f32 v192, v192\n"
    "v_mfma_f32_16x16x32_bf16 a[160:163], v[148:151], v[220:223], a[160:163]\n"
    "s_waitcnt vmcnt(5)\n"
    "v_add_f32 v20, v20, v191\n"
    "v_add_f32 v21, v21, v192\n"
    "v_mfma_f32_16x16x32_bf16 a[164:167], v[148:151], v[224:227], a[164:167]\n"
    "buffer_load_dwordx4 v9, s[44:47], s58 offen offset:2048 nt lds\n"
    "v_mfma_f32_16x16x32_bf16 v[188:191], a[224:227], v[96:99], v[172:175]\n"
    "s_add_u32 m0, s57, 36864\n"
    "v_exp_f32 v193, v193\n"
    "v_mfma_f32_16x16x32_bf16 v[188:191], a[228:231], v[100:103], v[188:191]\n"
    "s_waitcnt vmcnt(5)\n"
    "v_exp_f32 v194, v194\n"
    "v_mfma_f32_16x16x32_bf16 v[188:191], a[232:235], v[104:107], v[188:191]\n"
    "s_add_u32 s58, s58, 4096\n"
    "v_exp_f32 v195, v195\n"
    "v_add_f32 v21, v21, v193\n"
    "v_mfma_f32_16x16x32_bf16 v[188:191], a[236:239], v[108:111], v[188:191]\n"
    "ds_read_b64_tr_b16 v[148:149], v18 offset:32768\n"
    "v_add_f32 v21, v21, v194\n"
    "v_cvt_pk_bf16_f32 v234, v192, v193\n"
    "v_cvt_pk_bf16_f32 v235, v194, v195\n"
    "v_add_f32 v21, v21, v195\n"
    "v_mfma_f32_16x16x32_bf16 v[192:195], a[236:239], v[124:127], v[176:179]\n"
    "buffer_load_dwordx4 v8, s[44:47], s58 offen nt lds\n"
    "v_mfma_f32_16x16x32_bf16 v[192:195], a[232:235], v[120:123], v[192:195]\n"
    "v_exp_f32 v196, v196\n"
    "v_mfma_f32_16x16x32_bf16 v[192:195], a[228:231], v[116:119], v[192:195]\n"
    "v_exp_f32 v197, v197\n"
    "v_mfma_f32_16x16x32_bf16 v[192:195], a[224:227], v[112:115], v[192:195]\n"
    "v_exp_f32 v198, v198\n"
    "v_mfma_f32_16x16x32_bf16 a[168:171], v[152:155], v[200:203], a[168:171]\n"
    "ds_read_b64_tr_b16 v[150:151], v18 offset:36864\n"
    "v_add_f32 v22, v22, v196\n"
    "v_mfma_f32_16x16x32_bf16 a[172:175], v[152:155], v[204:207], a[172:175]\n"
    "v_exp_f32 v199, v199\n"
    "v_mfma_f32_16x16x32_bf16 a[176:179], v[152:155], v[208:211], a[176:179]\n"
    "v_add_f32 v22, v22, v197\n"
    "v_add_f32 v22, v22, v198\n"
    "v_mfma_f32_16x16x32_bf16 a[180:183], v[152:155], v[212:215], a[180:183]\n"
    "v_cvt_pk_bf16_f32 v238, v196, v197\n"
    "v_cvt_pk_bf16_f32 v239, v198, v199\n"
    "v_mfma_f32_16x16x32_bf16 a[184:187], v[152:155], v[216:219], a[184:187]\n"
    "v_exp_f32 v184, v184\n"
    "v_mfma_f32_16x16x32_bf16 a[188:191], v[152:155], v[220:223], a[188:191]\n"
    "v_add_f32 v22, v22, v199\n"
    "v_add_f32 v23, v23, v184\n"
    "v_mfma_f32_16x16x32_bf16 a[192:195], v[152:155], v[224:227], a[192:195]\n"
    "v_exp_f32 v185, v185\n"
    "v_mfma_f32_16x16x32_bf16 v[196:199], a[224:227], v[128:131], v[180:183]\n"
    "v_exp_f32 v186, v186\n"
    "v_mfma_f32_16x16x32_bf16 v[196:199], a[228:231], v[132:135], v[196:199]\n"
    "s_waitcnt vmcnt(5)\n"
    "v_exp_f32 v187, v187\n"
    "v_mfma_f32_16x16x32_bf16 v[196:199], a[232:235], v[136:139], v[196:199]\n"
    "ds_read_b64_tr_b16 v[152:153], v15 offset:33024\n"
    "v_add_f32 v23, v23, v185\n"
    "v_mfma_f32_16x16x32_bf16 v[196:199], a[236:239], v[140:143], v[196:199]\n"
    "buffer_load_dwordx4 v9, s[44:47], s58 offen offset:2048 nt lds\n"
    "v_mfma_f32_16x16x32_bf16 a[196:199], v[28:31], v[200:203], a[196:199]\n"
    "s_add_u32 m0, s57, 8192\n"
    "ds_read_b64_tr_b16 v[154:155], v15 offset:37120\n"
    "v_add_f32 v23, v23, v186\n"
    "v_mfma_f32_16x16x32_bf16 a[200:203], v[28:31], v[204:207], a[200:203]\n"
    "s_add_u32 s58, s65, 90112\n"
    "ds_read_b128 a[224:227], v13 offset:4096\n"
    "v_cvt_pk_bf16_f32 v242, v184, v185\n"
    "v_mfma_f32_16x16x32_bf16 a[204:207], v[28:31], v[208:211], a[204:207]\n"
    "s_and_b32 s58, s58, 0x1ffff\n"
    "ds_read_b128 a[228:231], v13 offset:4352\n"
    "v_cvt_pk_bf16_f32 v243, v186, v187\n"
    "v_mfma_f32_16x16x32_bf16 a[208:211], v[28:31], v[212:215], a[208:211]\n"
    "ds_read_b128 a[232:235], v13 offset:4608\n"
    "v_add_f32 v23, v23, v187\n"
    "v_mfma_f32_16x16x32_bf16 a[212:215], v[28:31], v[216:219], a[212:215]\n"
    "v_exp_f32 v188, v188\n"
    "v_mfma_f32_16x16x32_bf16 a[216:219], v[28:31], v[220:223], a[216:219]\n"
    "v_exp_f32 v189, v189\n"
    "v_mfma_f32_16x16x32_bf16 a[220:223], v[28:31], v[224:227], a[220:223]\n"
    "s_waitcnt lgkmcnt(5)\n"
    "v_exp_f32 v190, v190\n"
    "v_mfma_f32_16x16x32_bf16 v[184:187], a[240:243], v[32:35], v[156:159]\n"
    "ds_read_b128 a[236:239], v13 offset:4864\n"
    "v_add_f32 v24, v24, v188\n"
    "v_mfma_f32_16x16x32_bf16 v[184:187], a[244:247], v[36:39], v[184:187]\n"
    "v_exp_f32 v191, v191\n"
    "v_mfma_f32_16x16x32_bf16 v[184:187], a[248:251], v[40:43], v[184:187]\n"
    "ds_read_b64_tr_b16 v[28:29], v18 offset:33024\n"
    "v_add_f32 v24, v24, v189\n"
    "v_mfma_f32_16x16x32_bf16 v[184:187], a[252:255], v[44:47], v[184:187]\n"
    "ds_read_b64_tr_b16 v[30:31], v18 offset:37120\n"
    "v_add_f32 v24, v24, v190\n"
    "v_cvt_pk_bf16_f32 v246, v188, v189\n"
    "v_cvt_pk_bf16_f32 v247, v190, v191\n"
    "v_add_f32 v24, v24, v191\n"
    "v_mfma_f32_16x16x32_bf16 v[188:191], a[252:255], v[60:63], v[160:163]\n"
    "v_exp_f32 v192, v192\n"
    "v_mfma_f32_16x16x32_bf16 v[188:191], a[248:251], v[56:59], v[188:191]\n"
    "v_exp_f32 v193, v193\n"
    "v_mfma_f32_16x16x32_bf16 v[188:191], a[244:247], v[52:55], v[188:191]\n"
    "s_waitcnt vmcnt(5)\n"
    "v_exp_f32 v194, v194\n"
    "v_mfma_f32_16x16x32_bf16 v[188:191], a[240:243], v[48:51], v[188:191]\n"
    "v_exp_f32 v195, v195\n"
    "v_mfma_f32_16x16x32_bf16 a[0:3], v[144:147], v[228:231], a[0:3]\n"
    "buffer_load_dwordx4 v10, s[48:51], s58 offen nt lds\n"
    "v_mfma_f32_16x16x32_bf16 a[4:7], v[144:147], v[232:235], a[4:7]\n"
    "v_exp_f32 v196, v196\n"
    "v_mfma_f32_16x16x32_bf16 a[8:11], v[144:147], v[236:239], a[8:11]\n"
    "v_exp_f32 v197, v197\n"
    "v_mfma_f32_16x16x32_bf16 a[12:15], v[144:147], v[240:243], a[12:15]\n"
    "v_exp_f32 v198, v198\n"
    "v_cvt_pk_bf16_f32 v250, v192, v193\n"
    "v_cvt_pk_bf16_f32 v251, v194, v195\n"
    "v_exp_f32 v199, v199\n"
    "v_mfma_f32_16x16x32_bf16 a[16:19], v[144:147], v[244:247], a[16:19]\n"
    "v_cvt_pk_bf16_f32 v254, v196, v197\n"
    "v_cvt_pk_bf16_f32 v255, v198, v199\n"
    "v_mfma_f32_16x16x32_bf16 a[20:23], v[144:147], v[248:251], a[20:23]\n"
    "v_add_f32 v25, v25, v192\n"
    "v_add_f32 v25, v25, v193\n"
    "v_mfma_f32_16x16x32_bf16 a[24:27], v[144:147], v[252:255], a[24:27]\n"
    "v_add_f32 v25, v25, v194\n"
    "v_add_f32 v25, v25, v195\n"
    "v_mfma_f32_16x16x32_bf16 v[192:195], a[240:243], v[64:67], v[164:167]\n"
    "ds_read_b64_tr_b16 v[144:145], v15 offset:33280\n"
    "v_add_f32 v26, v26, v196\n"
    "v_mfma_f32_16x16x32_bf16 v[192:195], a[244:247], v[68:71], v[192:195]\n"
    "ds_read_b64_tr_b16 v[146:147], v15 offset:37376\n"
    "v_add_f32 v26, v26, v197\n"
    "v_mfma_f32_16x16x32_bf16 v[192:195], a[248:251], v[72:75], v[192:195]\n"
    "v_exp_f32 v184, v184\n"
    "v_mfma_f32_16x16x32_bf16 v[192:195], a[252:255], v[76:79], v[192:195]\n"
    "v_add_f32 v26, v26, v198\n"
    "v_add_f32 v26, v26, v199\n"
    "v_mfma_f32_16x16x32_bf16 v[196:199], a[252:255], v[92:95], v[168:171]\n"
    "v_exp_f32 v185, v185\n"
    "v_mfma_f32_16x16x32_bf16 v[196:199], a[248:251], v[88:91], v[196:199]\n"
    "v_exp_f32 v186, v186\n"
    "v_mfma_f32_16x16x32_bf16 v[196:199], a[244:247], v[84:87], v[196:199]\n"
    "s_waitcnt vmcnt(5)\n"
    "v_add_f32 v20, v20, v184\n"
    "v_add_f32 v20, v20, v185\n"
    "v_mfma_f32_16x16x32_bf16 v[196:199], a[240:243], v[80:83], v[196:199]\n"
    "v_exp_f32 v187, v187\n"
    "v_mfma_f32_16x16x32_bf16 a[28:31], v[148:151], v[228:231], a[28:31]\n"
    "buffer_load_dwordx4 v10, s[48:51], s58 offen offset:2048 nt lds\n"
    "v_mfma_f32_16x16x32_bf16 a[32:35], v[148:151], v[232:235], a[32:35]\n"
    "s_add_u32 m0, s57, 12288\n"
    "v_add_f32 v20, v20, v186\n"
    "v_cvt_pk_bf16_f32 v200, v184, v185\n"
    "v_mfma_f32_16x16x32_bf16 a[36:39], v[148:151], v[236:239], a[36:39]\n"
    "s_add_u32 s58, s58, 4096\n"
    "v_cvt_pk_bf16_f32 v201, v186, v187\n"
    "v_add_f32 v20, v20, v187\n"
    "v_mfma_f32_16x16x32_bf16 a[40:43], v[148:151], v[240:243], a[40:43]\n"
    "v_exp_f32 v188, v188\n"
    "v_mfma_f32_16x16x32_bf16 a[44:47], v[148:151], v[244:247], a[44:47]\n"
    "v_exp_f32 v189, v189\n"
    "v_mfma_f32_16x16x32_bf16 a[48:51], v[148:151], v[248:251], a[48:51]\n"
    "v_exp_f32 v190, v190\n"
    "v_mfma_f32_16x16x32_bf16 a[52:55], v[148:151], v[252:255], a[52:55]\n"
    "ds_read_b64_tr_b16 v[148:149], v18 offset:33280\n"
    "v_add_f32 v21, v21, v188\n"
    "v_mfma_f32_16x16x32_bf16 v[184:187], a[240:243], v[96:99], v[172:175]\n"
    "v_exp_f32 v191, v191\n"
    "v_mfma_f32_16x16x32_bf16 v[184:187], a[244:247], v[100:103], v[184:187]\n"
    "ds_read_b64_tr_b16 v[150:151], v18 offset:37376\n"
    "v_add_f32 v21, v21, v189\n"
    "v_mfma_f32_16x16x32_bf16 v[184:187], a[248:251], v[104:107], v[184:187]\n"
    "v_add_f32 v21, v21, v190\n"
    "v_cvt_pk_bf16_f32 v204, v188, v189\n"
    "v_mfma_f32_16x16x32_bf16 v[184:187], a[252:255], v[108:111], v[184:187]\n"
    "v_cvt_pk_bf16_f32 v205, v190, v191\n"
    "v_add_f32 v21, v21, v191\n"
    "v_mfma_f32_16x16x32_bf16 v[188:191], a[252:255], v[124:127], v[176:179]\n"
    "v_exp_f32 v192, v192\n"
    "v_mfma_f32_16x16x32_bf16 v[188:191], a[248:251], v[120:123], v[188:191]\n"
    "v_exp_f32 v193, v193\n"
    "v_mfma_f32_16x16x32_bf16 v[188:191], a[244:247], v[116:119], v[188:191]\n"
    "s_waitcnt vmcnt(5)\n"
    "v_exp_f32 v194, v194\n"
    "v_mfma_f32_16x16x32_bf16 v[188:191], a[240:243], v[112:115], v[188:191]\n"
    "s_waitcnt lgkmcnt(2)\n"
    "v_add_f32 v22, v22, v192\n"
    "v_add_f32 v22, v22, v193\n"
    "v_mfma_f32_16x16x32_bf16 a[56:59], v[152:155], v[228:231], a[56:59]\n"
    "buffer_load_dwordx4 v10, s[48:51], s58 offen nt lds\n"
    "v_mfma_f32_16x16x32_bf16 a[60:63], v[152:155], v[232:235], a[60:63]\n"
    "v_exp_f32 v195, v195\n"
    "v_mfma_f32_16x16x32_bf16 a[64:67], v[152:155], v[236:239], a[64:67]\n"
    "v_add_f32 v22, v22, v194\n"
    "v_cvt_pk_bf16_f32 v208, v192, v193\n"
    "v_mfma_f32_16x16x32_bf16 a[68:71], v[152:155], v[240:243], a[68:71]\n"
    "v_cvt_pk_bf16_f32 v209, v194, v195\n"
    "v_add_f32 v22, v22, v195\n"
    "v_mfma_f32_16x16x32_bf16 a[72:75], v[152:155], v[244:247], a[72:75]\n"
    "v_exp_f32 v196, v196\n"
    "v_mfma_f32_16x16x32_bf16 a[76:79], v[152:155], v[248:251], a[76:79]\n"
    "v_exp_f32 v197, v197\n"
    "v_mfma_f32_16x16x32_bf16 a[80:83], v[152:155], v[252:255], a[80:83]\n"
    "v_exp_f32 v198, v198\n"
    "v_mfma_f32_16x16x32_bf16 v[192:195], a[240:243], v[128:131], v[180:183]\n"
    "ds_read_b64_tr_b16 v[152:153], v15 offset:33536\n"
    "v_add_f32 v23, v23, v196\n"
    "v_mfma_f32_16x16x32_bf16 v[192:195], a[244:247], v[132:135], v[192:195]\n"
    "v_exp_f32 v199, v199\n"
    "v_mfma_f32_16x16x32_bf16 v[192:195], a[248:251], v[136:139], v[192:195]\n"
    "ds_read_b64_tr_b16 v[154:155], v15 offset:37632\n"
    "v_add_f32 v23, v23, v197\n"
    "v_mfma_f32_16x16x32_bf16 v[192:195], a[252:255], v[140:143], v[192:195]\n"
    "v_add_f32 v23, v23, v198\n"
    "v_cvt_pk_bf16_f32 v212, v196, v197\n"
    "v_mfma_f32_16x16x32_bf16 a[84:87], v[28:31], v[228:231], a[84:87]\n"
    "v_cvt_pk_bf16_f32 v213, v198, v199\n"
    "v_add_f32 v23, v23, v199\n"
    "v_mfma_f32_16x16x32_bf16 a[88:91], v[28:31], v[232:235], a[88:91]\n"
    "v_exp_f32 v184, v184\n"
    "v_mfma_f32_16x16x32_bf16 a[92:95], v[28:31], v[236:239], a[92:95]\n"
    "s_waitcnt vmcnt(5)\n"
    "v_exp_f32 v185, v185\n"
    "v_mfma_f32_16x16x32_bf16 a[96:99], v[28:31], v[240:243], a[96:99]\n"
    "v_exp_f32 v186, v186\n"
    "v_mfma_f32_16x16x32_bf16 a[100:103], v[28:31], v[244:247], a[100:103]\n"
    "buffer_load_dwordx4 v10, s[48:51], s58 offen offset:2048 nt lds\n"
    "v_mfma_f32_16x16x32_bf16 a[104:107], v[28:31], v[248:251], a[104:107]\n"
    "s_add_u32 m0, s57, 65536\n"
    "v_add_f32 v24, v24, v184\n"
    "v_add_f32 v24, v24, v185\n"
    "v_mfma_f32_16x16x32_bf16 a[108:111], v[28:31], v[252:255], a[108:111]\n"
    "s_add_u32 s58, s65, 106496\n"
    "v_exp_f32 v187, v187\n"
    "v_mfma_f32_16x16x32_bf16 v[196:199], a[224:227], v[32:35], v[156:159]\n"
    "s_and_b32 s58, s58, 0x1ffff\n"
    "ds_read_b64_tr_b16 v[28:29], v18 offset:33536\n"
    "v_add_f32 v24, v24, v186\n"
    "v_mfma_f32_16x16x32_bf16 v[196:199], a[228:231], v[36:39], v[196:199]\n"
    "ds_read_b64_tr_b16 v[30:31], v18 offset:37632\n"
    "v_cvt_pk_bf16_f32 v216, v184, v185\n"
    "v_mfma_f32_16x16x32_bf16 v[196:199], a[232:235], v[40:43], v[196:199]\n"
    "v_cvt_pk_bf16_f32 v217, v186, v187\n"
    "v_add_f32 v24, v24, v187\n"
    "v_mfma_f32_16x16x32_bf16 v[196:199], a[236:239], v[44:47], v[196:199]\n"
    "v_exp_f32 v188, v188\n"
    "v_mfma_f32_16x16x32_bf16 v[184:187], a[236:239], v[60:63], v[160:163]\n"
    "v_exp_f32 v189, v189\n"
    "v_mfma_f32_16x16x32_bf16 v[184:187], a[232:235], v[56:59], v[184:187]\n"
    "v_exp_f32 v190, v190\n"
    "v_mfma_f32_16x16x32_bf16 v[184:187], a[228:231], v[52:55], v[184:187]\n"
    "s_memrealtime s[24:25]\n"
    "v_add_f32 v25, v25, v188\n"
    "v_add_f32 v25, v25, v189\n"
    "v_mfma_f32_16x16x32_bf16 v[184:187], a[224:227], v[48:51], v[184:187]\n"
    "v_exp_f32 v191, v191\n"
    "v_mfma_f32_16x16x32_bf16 a[112:115], v[144:147], v[228:231], a[112:115]\n"
    "v_add_f32 v25, v25, v190\n"
    "v_cvt_pk_bf16_f32 v220, v188, v189\n"
    "v_mfma_f32_16x16x32_bf16 a[116:119], v[144:147], v[232:235], a[116:119]\n"
    "v_cvt_pk_bf16_f32 v221, v190, v191\n"
    "v_add_f32 v25, v25, v191\n"
    "v_mfma_f32_16x16x32_bf16 a[120:123], v[144:147], v[236:239], a[120:123]\n"
    "s_waitcnt vmcnt(5)\n"
    "v_exp_f32 v192, v192\n"
    "v_mfma_f32_16x16x32_bf16 a[124:127], v[144:147], v[240:243], a[124:127]\n"
    "v_exp_f32 v193, v193\n"
    "v_mfma_f32_16x16x32_bf16 a[128:131], v[144:147], v[244:247], a[128:131]\n"
    "buffer_load_dwordx4 v8, s[44:47], s58 offen nt lds\n"
    "v_mfma_f32_16x16x32_bf16 a[132:135], v[144:147], v[248:251], a[132:135]\n"
    "v_exp_f32 v194, v194\n"
    "v_mfma_f32_16x16x32_bf16 a[136:139], v[144:147], v[252:255], a[136:139]\n"
    "v_add_f32 v26, v26, v192\n"
    "v_add_f32 v26, v26, v193\n"
    "v_mfma_f32_16x16x32_bf16 v[188:191], a[224:227], v[64:67], v[164:167]\n"
    "v_exp_f32 v195, v195\n"
    "v_mfma_f32_16x16x32_bf16 v[188:191], a[228:231], v[68:71], v[188:191]\n"
    "s_waitcnt vmcnt(9) lgkmcnt(0)\n"
    "s_barrier\n"
    "ds_read_b128 a[240:243], v11 offset:0\n"
    "v_mfma_f32_16x16x32_bf16 v[188:191], a[232:235], v[72:75], v[188:191]\n"
    "s_sub_i32 s25, s26, s56\n"
    "ds_read_b128 a[244:247], v11 offset:256\n"
    "v_add_f32 v26, v26, v194\n"
    "v_mfma_f32_16x16x32_bf16 v[188:191], a[236:239], v[76:79], v[188:191]\n"
    "s_max_i32 s25, s25, -3\n"
    "ds_read_b128 a[248:251], v11 offset:512\n"
    "v_cvt_pk_bf16_f32 v224, v192, v193\n"
    "v_cvt_pk_bf16_f32 v225, v194, v195\n"
    "v_add_f32 v26, v26, v195\n"
    "v_mfma_f32_16x16x32_bf16 v[192:195], a[236:239], v[92:95], v[168:171]\n"
    "s_min_i32 s25, s25, 0\n"
    "v_exp_f32 v196, v196\n"
    "v_mfma_f32_16x16x32_bf16 v[192:195], a[232:235], v[88:91], v[192:195]\n"
    "s_add_u32 s56, s56, s25\n"
    "v_exp_f32 v197, v197\n"
    "v_mfma_f32_16x16x32_bf16 v[192:195], a[228:231], v[84:87], v[192:195]\n"
    "s_sub_i32 s25, s56, s24\n"
    "v_exp_f32 v198, v198\n"
    "v_mfma_f32_16x16x32_bf16 v[192:195], a[224:227], v[80:83], v[192:195]\n"
    "s_max_i32 s25, s25, -10\n"
    "ds_read_b128 a[252:255], v11 offset:768\n"
    "v_add_f32 v20, v20, v196\n"
    "v_mfma_f32_16x16x32_bf16 a[140:143], v[148:151], v[228:231], a[140:143]\n"
    "s_min_i32 s25, s25, 62\n"
    "v_exp_f32 v199, v199\n"
    "v_mfma_f32_16x16x32_bf16 a[144:147], v[148:151], v[232:235], a[144:147]\n"
    "s_add_u32 s56, s24, s25\n"
    "ds_read_b64_tr_b16 v[144:145], v16 offset:0\n"
    "v_add_f32 v20, v20, v197\n"
    "v_mfma_f32_16x16x32_bf16 a[148:151], v[148:151], v[236:239], a[148:151]\n"
    "s_add_u32 s56, s56, 125\n"
    "ds_read_b64_tr_b16 v[146:147], v16 offset:4096\n"
    "v_add_f32 v20, v20, v198\n"
    "v_mfma_f32_16x16x32_bf16 a[152:155], v[148:151], v[240:243], a[152:155]\n"
    "s_cmp_gt_i32 s25, 2\ns_cbranch_scc1 L_ta10_%=\nL_tb10_%=:\n"
    "v_cvt_pk_bf16_f32 v202, v196, v197\n"
    "v_cvt_pk_bf16_f32 v203, v198, v199\n"
    "v_mfma_f32_16x16x32_bf16 a[156:159], v[148:151], v[244:247], a[156:159]\n"
    "s_add_u32 s26, s26, 125\n"
    "v_exp_f32 v184, v184\n"
    "v_mfma_f32_16x16x32_bf16 a[160:163], v[148:151], v[248:251], a[160:163]\n"
    "s_waitcnt vmcnt(5)\n"
    "v_add_f32 v20, v20, v199\n"
    "v_add_f32 v21, v21, v184\n"
    "v_mfma_f32_16x16x32_bf16 a[164:167], v[148:151], v[252:255], a[164:167]\n"
    "buffer_load_dwordx4 v9, s[44:47], s58 offen offset:2048 nt lds\n"
    "v_mfma_f32_16x16x32_bf16 v[196:199], a[224:227], v[96:99], v[172:175]\n"
    "s_add_u32 m0, s57, 69632\n"
    "v_exp_f32 v185, v185\n"
    "v_mfma_f32_16x16x32_bf16 v[196:199], a[228:231], v[100:103], v[196:199]\n"
    "s_add_u32 s58, s58, 4096\n"
    "v_exp_f32 v186, v186\n"
    "v_mfma_f32_16x16x32_bf16 v[196:199], a[232:235], v[104:107], v[196:199]\n"
    "v_exp_f32 v187, v187\n"
    "v_add_f32 v21, v21, v185\n"
    "v_mfma_f32_16x16x32_bf16 v[196:199], a[236:239], v[108:111], v[196:199]\n"
    "ds_read_b64_tr_b16 v[148:149], v19 offset:0\n"
    "v_add_f32 v21, v21, v186\n"
    "v_cvt_pk_bf16_f32 v206, v184, v185\n"
    "v_cvt_pk_bf16_f32 v207, v186, v187\n"
    "v_add_f32 v21, v21, v187\n"
    "v_mfma_f32_16x16x32_bf16 v[184:187], a[236:239], v[124:127], v[176:179]\n"
    "v_exp_f32 v188, v188\n"
    "v_mfma_f32_16x16x32_bf16 v[184:187], a[232:235], v[120:123], v[184:187]\n"
    "v_exp_f32 v189, v189\n"
    "v_mfma_f32_16x16x32_bf16 v[184:187], a[228:231], v[116:119], v[184:187]\n"
    "v_exp_f32 v190, v190\n"
    "v_mfma_f32_16x16x32_bf16 v[184:187], a[224:227], v[112:115], v[184:187]\n"
    "ds_read_b64_tr_b16 v[150:151], v19 offset:4096\n"
    "v_add_f32 v22, v22, v188\n"
    "v_mfma_f32_16x16x32_bf16 a[168:171], v[152:155], v[228:231], a[168:171]\n"
    "v_exp_f32 v191, v191\n"
    "v_mfma_f32_16x16x32_bf16 a[172:175], v[152:155], v[232:235], a[172:175]\n"
    "v_add_f32 v22, v22, v189\n"
    "v_add_f32 v22, v22, v190\n"
    "v_mfma_f32_16x16x32_bf16 a[176:179], v[152:155], v[236:239], a[176:179]\n"
    "v_cvt_pk_bf16_f32 v210, v188, v189\n"
    "v_cvt_pk_bf16_f32 v211, v190, v191\n"
    "v_mfma_f32_16x16x32_bf16 a[180:183], v[152:155], v[240:243], a[180:183]\n"
    "s_waitcnt vmcnt(5)\n"
    "v_exp_f32 v192, v192\n"
    "v_mfma_f32_16x16x32_bf16 a[184:187], v[152:155], v[244:247], a[184:187]\n"
    "v_add_f32 v22, v22, v191\n"
    "v_add_f32 v23, v23, v192\n"
    "v_mfma_f32_16x16x32_bf16 a[188:191], v[152:155], v[248:251], a[188:191]\n"
    "buffer_load_dwordx4 v8, s[44:47], s58 offen nt lds\n"
    "v_mfma_f32_16x16x32_bf16 a[192:195], v[152:155], v[252:255], a[192:195]\n"
    "v_exp_f32 v193, v193\n"
    "v_mfma_f32_16x16x32_bf16 v[188:191], a[224:227], v[128:131], v[180:183]\n"
    "v_exp_f32 v194, v194\n"
    "v_mfma_f32_16x16x32_bf16 v[188:191], a[228:231], v[132:135], v[188:191]\n"
    "v_exp_f32 v195, v195\n"
    "v_mfma_f32_16x16x32_bf16 v[188:191], a[232:235], v[136:139], v[188:191]\n"
    "ds_read_b64_tr_b16 v[152:153], v16 offset:256\n"
    "v_add_f32 v23, v23, v193\n"
    "v_mfma_f32_16x16x32_bf16 v[188:191], a[236:239], v[140:143], v[188:191]\n"
    "ds_read_b64_tr_b16 v[154:155], v16 offset:4352\n"
    "v_add_f32 v23, v23, v194\n"
    "v_mfma_f32_16x16x32_bf16 a[196:199], v[28:31], v[228:231], a[196:199]\n"
    "ds_read_b128 a[224:227], v11 offset:4096\n"
    "v_cvt_pk_bf16_f32 v214, v192, v193\n"
    "v_mfma_f32_16x16x32_bf16 a[200:203], v[28:31], v[232:235], a[200:203]\n"
    "ds_read_b128 a[228:231], v11 offset:4352\n"
    "v_cvt_pk_bf16_f32 v215, v194, v195\n"
    "v_mfma_f32_16x16x32_bf16 a[204:207], v[28:31], v[236:239], a[204:207]\n"
    "ds_read_b128 a[232:235], v11 offset:4608\n"
    "v_add_f32 v23, v23, v195\n"
    "v_mfma_f32_16x16x32_bf16 a[208:211], v[28:31], v[240:243], a[208:211]\n"
    "v_exp_f32 v196, v196\n"
    "v_mfma_f32_16x16x32_bf16 a[212:215], v[28:31], v[244:247], a[212:215]\n"
    "v_exp_f32 v197, v197\n"
    "v_mfma_f32_16x16x32_bf16 a[216:219], v[28:31], v[248:251], a[216:219]\n"
    "v_exp_f32 v198, v198\n"
    "v_mfma_f32_16x16x32_bf16 a[220:223], v[28:31], v[252:255], a[220:223]\n"
    "s_waitcnt lgkmcnt(5)\n"
    "ds_read_b128 a[236:239], v11 offset:4864\n"
    "v_add_f32 v24, v24, v196\n"
    "v_mfma_f32_16x16x32_bf16 v[192:195], a[240:243], v[32:35], v[156:159]\n"
    "s_waitcnt vmcnt(5)\n"
    "v_exp_f32 v199, v199\n"
    "v_mfma_f32_16x16x32_bf16 v[192:195], a[244:247], v[36:39], v[192:195]\n"
    "ds_read_b64_tr_b16 v[28:29], v19 offset:256\n"
    "v_add_f32 v24, v24, v197\n"
    "v_mfma_f32_16x16x32_bf16 v[192:195], a[248:251], v[40:43], v[192:195]\n"
    "buffer_load_dwordx4 v9, s[44:47], s58 offen offset:2048 nt lds\n"
    "v_mfma_f32_16x16x32_bf16 v[192:195], a[252:255], v[44:47], v[192:195]\n"
    "s_add_u32 m0, s57, 40960\n"
    "ds_read_b64_tr_b16 v[30:31], v19 offset:4352\n"
    "v_add_f32 v24, v24, v198\n"
    "v_cvt_pk_bf16_f32 v218, v196, v197\n"
    "v_cvt_pk_bf16_f32 v219, v198, v199\n"
    "v_add_f32 v24, v24, v199\n"
    "v_mfma_f32_16x16x32_bf16 v[196:199], a[252:255], v[60:63], v[160:163]\n"
    "s_add_u32 s58, s65, 98304\n"
    "v_exp_f32 v184, v184\n"
    "v_mfma_f32_16x16x32_bf16 v[196:199], a[248:251], v[56:59], v[196:199]\n"
    "s_and_b32 s58, s58, 0x1ffff\n"
    "v_exp_f32 v185, v185\n"
    "v_mfma_f32_16x16x32_bf16 v[196:199], a[244:247], v[52:55], v[196:199]\n"
    "v_exp_f32 v186, v186\n"
    "v_mfma_f32_16x16x32_bf16 v[196:199], a[240:243], v[48:51], v[196:199]\n"
    "v_exp_f32 v187, v187\n"
    "v_mfma_f32_16x16x32_bf16 a[0:3], v[144:147], v[200:203], a[0:3]\n"
    "v_exp_f32 v188, v188\n"
    "v_mfma_f32_16x16x32_bf16 a[4:7], v[144:147], v[204:207], a[4:7]\n"
    "v_exp_f32 v189, v189\n"
    "v_mfma_f32_16x16x32_bf16 a[8:11], v[144:147], v[208:211], a[8:11]\n"
    "v_exp_f32 v190, v190\n"
    "v_mfma_f32_16x16x32_bf16 a[12:15], v[144:147], v[212:215], a[12:15]\n"
    "v_exp_f32 v191, v191\n"
    "v_cvt_pk_bf16_f32 v222, v184, v185\n"
    "v_cvt_pk_bf16_f32 v223, v186, v187\n"
    "v_mfma_f32_16x16x32_bf16 a[16:19], v[144:147], v[216:219], a[16:19]\n"
    "v_cvt_pk_bf16_f32 v226, v188, v189\n"
    "v_cvt_pk_bf16_f32 v227, v190, v191\n"
    "v_mfma_f32_16x16x32_bf16 a[20:23], v[144:147], v[220:223], a[20:23]\n"
    "v_add_f32 v25, v25, v184\n"
    "v_add_f32 v25, v25, v185\n"
    "v_mfma_f32_16x16x32_bf16 a[24:27], v[144:147], v[224:227], a[24:27]\n"
    "v_add_f32 v25, v25, v186\n"
    "v_add_f32 v25, v25, v187\n"
    "v_mfma_f32_16x16x32_bf16 v[184:187], a[240:243], v[64:67], v[164:167]\n"
    "s_waitcnt vmcnt(5)\n"
    "ds_read_b64_tr_b16 v[144:145], v16 offset:512\n"
    "v_add_f32 v26, v26, v188\n"
    "v_mfma_f32_16x16x32_bf16 v[184:187], a[244:247], v[68:71], v[184:187]\n"
    "ds_read_b64_tr_b16 v[146:147], v16 offset:4608\n"
    "v_add_f32 v26, v26, v189\n"
    "v_mfma_f32_16x16x32_bf16 v[184:187], a[248:251], v[72:75], v[184:187]\n"
    "buffer_load_dwordx4 v10, s[48:51], s58 offen nt lds\n"
    "v_mfma_f32_16x16x32_bf16 v[184:187], a[252:255], v[76:79], v[184:187]\n"
    "v_add_f32 v26, v26, v190\n"
    "v_add_f32 v26, v26, v191\n"
    "v_mfma_f32_16x16x32_bf16 v[188:191], a[252:255], v[92:95], v[168:171]\n"
    "v_exp_f32 v192, v192\n"
    "v_mfma_f32_16x16x32_bf16 v[188:191], a[248:251], v[88:91], v[188:191]\n"
    "v_exp_f32 v193, v193\n"
    "v_mfma_f32_16x16x32_bf16 v[188:191], a[244:247], v[84:87], v[188:191]\n"
    "v_exp_f32 v194, v194\n"
    "v_mfma_f32_16x16x32_bf16 v[188:191], a[240:243], v[80:83], v[188:191]\n"
    "v_add_f32 v20, v20, v192\n"
    "v_add_f32 v20, v20, v193\n"
    "v_mfma_f32_16x16x32_bf16 a[28:31], v[148:151], v[200:203], a[28:31]\n"
    "v_exp_f32 v195, v195\n"
    "v_mfma_f32_16x16x32_bf16 a[32:35], v[148:151], v[204:207], a[32:35]\n"
    "v_add_f32 v20, v20, v194\n"
    "v_cvt_pk_bf16_f32 v228, v192, v193\n"
    "v_mfma_f32_16x16x32_bf16 a[36:39], v[148:151], v[208:211], a[36:39]\n"
    "v_cvt_pk_bf16_f32 v229, v194, v195\n"
    "v_add_f32 v20, v20, v195\n"
    "v_mfma_f32_16x16x32_bf16 a[40:43], v[148:151], v[212:215], a[40:43]\n"
    "v_exp_f32 v196, v196\n"
    "v_mfma_f32_16x16x32_bf16 a[44:47], v[148:151], v[216:219], a[44:47]\n"
    "v_exp_f32 v197, v197\n"
    "v_mfma_f32_16x16x32_bf16 a[48:51], v[148:151], v[220:223], a[48:51]\n"
    "v_exp_f32 v198, v198\n"
    "v_mfma_f32_16x16x32_bf16 a[52:55], v[148:151], v[224:227], a[52:55]\n"
    "ds_read_b64_tr_b16 v[148:149], v19 offset:512\n"
    "v_add_f32 v21, v21, v196\n"
    "v_mfma_f32_16x16x32_bf16 v[192:195], a[240:243], v[96:99], v[172:175]\n"
    "s_waitcnt vmcnt(5)\n"
    "v_exp_f32 v199, v199\n"
    "v_mfma_f32_16x16x32_bf16 v[192:195], a[244:247], v[100:103], v[192:195]\n"
    "ds_read_b64_tr_b16 v[150:151], v19 offset:4608\n"
    "v_add_f32 v21, v21, v197\n"
    "v_mfma_f32_16x16x32_bf16 v[192:195], a[248:251], v[104:107], v[192:195]\n"
    "buffer_load_dwordx4 v10, s[48:51], s58 offen offset:2048 nt lds\n"
    "v_mfma_f32_16x16x32_bf16 v[192:195], a[252:255], v[108:111], v[192:195]\n"
    "s_add_u32 m0, s57, 45056\n"
    "v_add_f32 v21, v21, v198\n"
    "v_cvt_pk_bf16_f32 v232, v196, v197\n"
    "v_cvt_pk_bf16_f32 v233, v198, v199\n"
    "v_add_f32 v21, v21, v199\n"
    "v_mfma_f32_16x16x32_bf16 v[196:199], a[252:255], v[124:127], v[176:179]\n"
    "s_add_u32 s58, s58, 4096\n"
    "v_exp_f32 v184, v184\n"
    "v_mfma_f32_16x16x32_bf16 v[196:199], a[248:251], v[120:123], v[196:199]\n"
    "v_exp_f32 v185, v185\n"
    "v_mfma_f32_16x16x32_bf16 v[196:199], a[244:247], v[116:119], v[196:199]\n"
    "v_exp_f32 v186, v186\n"
    "v_mfma_f32_16x16x32_bf16 v[196:199], a[240:243], v[112:115], v[196:199]\n"
    "s_waitcnt lgkmcnt(2)\n"
    "v_add_f32 v22, v22, v184\n"
    "v_add_f32 v22, v22, v185\n"
    "v_mfma_f32_16x16x32_bf16 a[56:59], v[152:155], v[200:203], a[56:59]\n"
    "v_exp_f32 v187, v187\n"
    "v_mfma_f32_16x16x32_bf16 a[60:63], v[152:155], v[204:207], a[60:63]\n"
    "v_add_f32 v22, v22, v186\n"
    "v_cvt_pk_bf16_f32 v236, v184, v185\n"
    "v_mfma_f32_16x16x32_bf16 a[64:67], v[152:155], v[208:211], a[64:67]\n"
    "v_cvt_pk_bf16_f32 v237, v186, v187\n"
    "v_add_f32 v22, v22, v187\n"
    "v_mfma_f32_16x16x32_bf16 a[68:71], v[152:155], v[212:215], a[68:71]\n"
    "v_exp_f32 v188, v188\n"
    "v_mfma_f32_16x16x32_bf16 a[72:75], v[152:155], v[216:219], a[72:75]\n"
    "v_exp_f32 v189, v189\n"
    "v_mfma_f32_16x16x32_bf16 a[76:79], v[152:155], v[220:223], a[76:79]\n"
    "v_exp_f32 v190, v190\n"
    "v_mfma_f32_16x16x32_bf16 a[80:83], v[152:155], v[224:227], a[80:83]\n"
    "ds_read_b64_tr_b16 v[152:153], v16 offset:768\n"
    "v_add_f32 v23, v23, v188\n"
    "v_mfma_f32_16x16x32_bf16 v[184:187], a[240:243], v[128:131], v[180:183]\n"
    "v_exp_f32 v191, v191\n"
    "v_mfma_f32_16x16x32_bf16 v[184:187], a[244:247], v[132:135], v[184:187]\n"
    "s_waitcnt vmcnt(5)\n"
    "ds_read_b64_tr_b16 v[154:155], v16 offset:4864\n"
    "v_add_f32 v23, v23, v189\n"
    "v_mfma_f32_16x16x32_bf16 v[184:187], a[248:251], v[136:139], v[184:187]\n"
    "v_add_f32 v23, v23, v190\n"
    "v_cvt_pk_bf16_f32 v240, v188, v189\n"
    "v_mfma_f32_16x16x32_bf16 v[184:187], a[252:255], v[140:143], v[184:187]\n"
    "buffer_load_dwordx4 v10, s[48:51], s58 offen nt lds\n"
    "v_mfma_f32_16x16x32_bf16 a[84:87], v[28:31], v[200:203], a[84:87]\n"
    "v_cvt_pk_bf16_f32 v241, v190, v191\n"
    "v_add_f32 v23, v23, v191\n"
    "v_mfma_f32_16x16x32_bf16 a[88:91], v[28:31], v[204:207], a[88:91]\n"
    "v_exp_f32 v192, v192\n"
    "v_mfma_f32_16x16x32_bf16 a[92:95], v[28:31], v[208:211], a[92:95]\n"
    "v_exp_f32 v193, v193\n"
    "v_mfma_f32_16x16x32_bf16 a[96:99], v[28:31], v[212:215], a[96:99]\n"
    "v_exp_f32 v194, v194\n"
    "v_mfma_f32_16x16x32_bf16 a[100:103], v[28:31], v[216:219], a[100:103]\n"
    "v_add_f32 v24, v24, v192\n"
    "v_add_f32 v24, v24, v193\n"
    "v_mfma_f32_16x16x32_bf16 a[104:107], v[28:31], v[220:223], a[104:107]\n"
    "v_exp_f32 v195, v195\n"
    "v_mfma_f32_16x16x32_bf16 a[108:111], v[28:31], v[224:227], a[108:111]\n"
    "ds_read_b64_tr_b16 v[28:29], v19 offset:768\n"
    "v_add_f32 v24, v24, v194\n"
    "v_mfma_f32_16x16x32_bf16 v[188:191], a[224:227], v[32:35], v[156:159]\n"
    "ds_read_b64_tr_b16 v[30:31], v19 offset:4864\n"
    "v_cvt_pk_bf16_f32 v244, v192, v193\n"
    "v_mfma_f32_16x16x32_bf16 v[188:191], a[228:231], v[36:39], v[188:191]\n"
    "v_cvt_pk_bf16_f32 v245, v194, v195\n"
    "v_add_f32 v24, v24, v195\n"
    "v_mfma_f32_16x16x32_bf16 v[188:191], a[232:235], v[40:43], v[188:191]\n"
    "v_exp_f32 v196, v196\n"
    "v_mfma_f32_16x16x32_bf16 v[188:191], a[236:239], v[44:47], v[188:191]\n"
    "v_exp_f32 v197, v197\n"
    "v_mfma_f32_16x16x32_bf16 v[192:195], a[236:239], v[60:63], v[160:163]\n"
    "v_exp_f32 v198, v198\n"
    "v_mfma_f32_16x16x32_bf16 v[192:195], a[232:235], v[56:59], v[192:195]\n"
    "s_waitcnt vmcnt(5)\n"
    "v_add_f32 v25, v25, v196\n"
    "v_add_f32 v25, v25, v197\n"
    "v_mfma_f32_16x16x32_bf16 v[192:195], a[228:231], v[52:55], v[192:195]\n"
    "s_memrealtime s[24:25]\n"
    "v_exp_f32 v199, v199\n"
    "v_mfma_f32_16x16x32_bf16 v[192:195], a[224:227], v[48:51], v[192:195]\n"
    "buffer_load_dwordx4 v10, s[48:51], s58 offen offset:2048 nt lds\n"
    "v_mfma_f32_16x16x32_bf16 a[112:115], v[144:147], v[200:203], a[112:115]\n"
    "s_add_u32 m0, s57, 98304\n"
    "v_add_f32 v25, v25, v198\n"
    "v_cvt_pk_bf16_f32 v248, v196, v197\n"
    "v_mfma_f32_16x16x32_bf16 a[116:119], v[144:147], v[204:207], a[116:119]\n"
    "s_add_u32 s58, s65, 114688\n"
    "v_cvt_pk_bf16_f32 v249, v198, v199\n"
    "v_add_f32 v25, v25, v199\n"
    "v_mfma_f32_16x16x32_bf16 a[120:123], v[144:147], v[208:211], a[120:123]\n"
    "s_and_b32 s58, s58, 0x1ffff\n"
    "v_exp_f32 v184, v184\n"
    "v_mfma_f32_16x16x32_bf16 a[124:127], v[144:147], v[212:215], a[124:127]\n"
    "v_exp_f32 v185, v185\n"
    "v_mfma_f32_16x16x32_bf16 a[128:131], v[144:147], v[216:219], a[128:131]\n"
    "v_exp_f32 v186, v186\n"
    "v_mfma_f32_16x16x32_bf16 a[132:135], v[144:147], v[220:223], a[132:135]\n"
    "v_add_f32 v26, v26, v184\n"
    "v_add_f32 v26, v26, v185\n"
    "v_mfma_f32_16x16x32_bf16 a[136:139], v[144:147], v[224:227], a[136:139]\n"
    "v_exp_f32 v187, v187\n"
    "v_mfma_f32_16x16x32_bf16 v[196:199], a[224:227], v[64:67], v[164:167]\n"
    "v_add_f32 v26, v26, v186\n"
    "v_cvt_pk_bf16_f32 v252, v184, v185\n"
    "v_mfma_f32_16x16x32_bf16 v[196:199], a[228:231], v[68:71], v[196:199]\n"
    "s_waitcnt vmcnt(8) lgkmcnt(0)\n"
    "s_barrier\n"
    "ds_read_b128 a[240:243], v11 offset:32768\n"
    "v_mfma_f32_16x16x32_bf16 v[196:199], a[232:235], v[72:75], v[196:199]\n"
    "s_sub_i32 s25, s26, s56\n"
    "ds_read_b128 a[244:247], v11 offset:33024\n"
    "v_cvt_pk_bf16_f32 v253, v186, v187\n"
    "v_mfma_f32_16x16x32_bf16 v[196:199], a[236:239], v[76:79], v[196:199]\n"
    "s_max_i32 s25, s25, -3\n"
    "ds_read_b128 a[248:251], v11 offset:33280\n"
    "v_add_f32 v26, v26, v187\n"
    "v_mfma_f32_16x16x32_bf16 v[184:187], a[236:239], v[92:95], v[168:171]\n"
    "s_min_i32 s25, s25, 0\n"
    "v_exp_f32 v188, v188\n"
    "v_mfma_f32_16x16x32_bf16 v[184:187], a[232:235], v[88:91], v[184:187]\n"
    "s_add_u32 s56, s56, s25\n"
    "v_exp_f32 v189, v189\n"
    "v_mfma_f32_16x16x32_bf16 v[184:187], a[228:231], v[84:87], v[184:187]\n"
    "s_sub_i32 s25, s56, s24\n"
    "v_exp_f32 v190, v190\n"
    "v_mfma_f32_16x16x32_bf16 v[184:187], a[224:227], v[80:83], v[184:187]\n"
    "s_max_i32 s25, s25, -10\n"
    "ds_read_b128 a[252:255], v11 offset:33536\n"
    "v_add_f32 v20, v20, v188\n"
    "v_mfma_f32_16x16x32_bf16 a[140:143], v[148:151], v[200:203], a[140:143]\n"
    "s_min_i32 s25, s25, 62\n"
    "v_exp_f32 v191, v191\n"
    "v_mfma_f32_16x16x32_bf16 a[144:147], v[148:151], v[204:207], a[144:147]\n"
    "s_add_u32 s56, s24, s25\n"
    "ds_read_b64_tr_b16 v[144:145], v14 offset:0\n"
    "v_add_f32 v20, v20, v189\n"
    "v_mfma_f32_16x16x32_bf16 a[148:151], v[148:151], v[208:211], a[148:151]\n"
    "s_add_u32 s56, s56, 125\n"
    "ds_read_b64_tr_b16 v[146:147], v14 offset:4096\n"
    "v_add_f32 v20, v20, v190\n"
    "v_mfma_f32_16x16x32_bf16 a[152:155], v[148:151], v[212:215], a[152:155]\n"
    "s_cmp_gt_i32 s25, 2\ns_cbranch_scc1 L_ta11_%=\nL_tb11_%=:\n"
    "v_cvt_pk_bf16_f32 v230, v188, v189\n"
    "v_cvt_pk_bf16_f32 v231, v190, v191\n"
    "v_mfma_f32_16x16x32_bf16 a[156:159], v[148:151], v[216:219], a[156:159]\n"
    "s_add_u32 s26, s26, 125\n"
    "v_exp_f32 v192, v192\n"
    "v_mfma_f32_16x16x32_bf16 a[160:163], v[148:151], v[220:223], a[160:163]\n"
    "s_waitcnt vmcnt(5)\n"
    "v_add_f32 v20, v20, v191\n"
    "v_add_f32 v21, v21, v192\n"
    "v_mfma_f32_16x16x32_bf16 a[164:167], v[148:151], v[224:227], a[164:167]\n"
    "buffer_load_dwordx4 v8, s[44:47], s58 offen nt lds\n"
    "v_mfma_f32_16x16x32_bf16 v[188:191], a[224:227], v[96:99], v[172:175]\n"
    "v_exp_f32 v193, v193\n"
    "v_mfma_f32_16x16x32_bf16 v[188:191], a[228:231], v[100:103], v[188:191]\n"
    "v_exp_f32 v194, v194\n"
    "v_mfma_f32_16x16x32_bf16 v[188:191], a[232:235], v[104:107], v[188:191]\n"
    "v_exp_f32 v195, v195\n"
    "v_add_f32 v21, v21, v193\n"
    "v_mfma_f32_16x16x32_bf16 v[188:191], a[236:239], v[108:111], v[188:191]\n"
    "ds_read_b64_tr_b16 v[148:149], v17 offset:0\n"
    "v_add_f32 v21, v21, v194\n"
    "v_cvt_pk_bf16_f32 v234, v192, v193\n"
    "v_cvt_pk_bf16_f32 v235, v194, v195\n"
    "v_add_f32 v21, v21, v195\n"
    "v_mfma_f32_16x16x32_bf16 v[192:195], a[236:239], v[124:127], v[176:179]\n"
    "v_exp_f32 v196, v196\n"
    "v_mfma_f32_16x16x32_bf16 v[192:195], a[232:235], v[120:123], v[192:195]\n"
    "s_waitcnt vmcnt(5)\n"
    "v_exp_f32 v197, v197\n"
    "v_mfma_f32_16x16x32_bf16 v[192:195], a[228:231], v[116:119], v[192:195]\n"
    "v_exp_f32 v198, v198\n"
    "v_mfma_f32_16x16x32_bf16 v[192:195], a[224:227], v[112:115], v[192:195]\n"
    "buffer_load_dwordx4 v9, s[44:47], s58 offen offset:2048 nt lds\n"
    "v_mfma_f32_16x16x32_bf16 a[168:171], v[152:155], v[200:203], a[168:171]\n"
    "s_add_u32 m0, s57, 102400\n"
    "ds_read_b64_tr_b16 v[150:151], v17 offset:4096\n"
    "v_add_f32 v22, v22, v196\n"
    "v_mfma_f32_16x16x32_bf16 a[172:175], v[152:155], v[204:207], a[172:175]\n"
    "s_add_u32 s58, s58, 4096\n"
    "v_exp_f32 v199, v199\n"
    "v_mfma_f32_16x16x32_bf16 a[176:179], v[152:155], v[208:211], a[176:179]\n"
    "v_add_f32 v22, v22, v197\n"
    "v_add_f32 v22, v22, v198\n"
    "v_mfma_f32_16x16x32_bf16 a[180:183], v[152:155], v[212:215], a[180:183]\n"
    "v_cvt_pk_bf16_f32 v238, v196, v197\n"
    "v_cvt_pk_bf16_f32 v239, v198, v199\n"
    "v_mfma_f32_16x16x32_bf16 a[184:187], v[152:155], v[216:219], a[184:187]\n"
    "v_exp_f32 v184, v184\n"
    "v_mfma_f32_16x16x32_bf16 a[188:191], v[152:155], v[220:223], a[188:191]\n"
    "v_add_f32 v22, v22, v199\n"
    "v_add_f32 v23, v23, v184\n"
    "v_mfma_f32_16x16x32_bf16 a[192:195], v[152:155], v[224:227], a[192:195]\n"
    "v_exp_f32 v185, v185\n"
    "v_mfma_f32_16x16x32_bf16 v[196:199], a[224:227], v[128:131], v[180:183]\n"
    "v_exp_f32 v186, v186\n"
    "v_mfma_f32_16x16x32_bf16 v[196:199], a[228:231], v[132:135], v[196:199]\n"
    "v_exp_f32 v187, v187\n"
    "v_mfma_f32_16x16x32_bf16 v[196:199], a[232:235], v[136:139], v[196:199]\n"
    "ds_read_b64_tr_b16 v[152:153], v14 offset:256\n"
    "v_add_f32 v23, v23, v185\n"
    "v_mfma_f32_16x16x32_bf16 v[196:199], a[236:239], v[140:143], v[196:199]\n"
    "ds_read_b64_tr_b16 v[154:155], v14 offset:4352\n"
    "v_add_f32 v23, v23, v186\n"
    "v_mfma_f32_16x16x32_bf16 a[196:199], v[28:31], v[200:203], a[196:199]\n"
    "ds_read_b128 a[224:227], v11 offset:36864\n"
    "v_cvt_pk_bf16_f32 v242, v184, v185\n"
    "v_mfma_f32_16x16x32_bf16 a[200:203], v[28:31], v[204:207], a[200:203]\n"
    "s_waitcnt vmcnt(5)\n"
    "ds_read_b128 a[228:231], v11 offset:37120\n"
    "v_cvt_pk_bf16_f32 v243, v186, v187\n"
    "v_mfma_f32_16x16x32_bf16 a[204:207], v[28:31], v[208:211], a[204:207]\n"
    "ds_read_b128 a[232:235], v11 offset:37376\n"
    "v_add_f32 v23, v23, v187\n"
    "v_mfma_f32_16x16x32_bf16 a[208:211], v[28:31], v[212:215], a[208:211]\n"
    "buffer_load_dwordx4 v8, s[44:47], s58 offen nt lds\n"
    "v_mfma_f32_16x16x32_bf16 a[212:215], v[28:31], v[216:219], a[212:215]\n"
    "v_exp_f32 v188, v188\n"
    "v_mfma_f32_16x16x32_bf16 a[216:219], v[28:31], v[220:223], a[216:219]\n"
    "v_exp_f32 v189, v189\n"
    "v_mfma_f32_16x16x32_bf16 a[220:223], v[28:31], v[224:227], a[220:223]\n"
    "s_waitcnt lgkmcnt(5)\n"
    "v_exp_f32 v190, v190\n"
    "v_mfma_f32_16x16x32_bf16 v[184:187], a[240:243], v[32:35], v[156:159]\n"
    "ds_read_b128 a[236:239], v11 offset:37632\n"
    "v_add_f32 v24, v24, v188\n"
    "v_mfma_f32_16x16x32_bf16 v[184:187], a[244:247], v[36:39], v[184:187]\n"
    "v_exp_f32 v191, v191\n"
    "v_mfma_f32_16x16x32_bf16 v[184:187], a[248:251], v[40:43], v[184:187]\n"
    "ds_read_b64_tr_b16 v[28:29], v17 offset:256\n"
    "v_add_f32 v24, v24, v189\n"
    "v_mfma_f32_16x16x32_bf16 v[184:187], a[252:255], v[44:47], v[184:187]\n"
    "ds_read_b64_tr_b16 v[30:31], v17 offset:4352\n"
    "v_add_f32 v24, v24, v190\n"
    "v_cvt_pk_bf16_f32 v246, v188, v189\n"
    "v_cvt_pk_bf16_f32 v247, v190, v191\n"
    "v_add_f32 v24, v24, v191\n"
    "v_mfma_f32_16x16x32_bf16 v[188:191], a[252:255], v[60:63], v[160:163]\n"
    "v_exp_f32 v192, v192\n"
    "v_mfma_f32_16x16x32_bf16 v[188:191], a[248:251], v[56:59], v[188:191]\n"
    "v_exp_f32 v193, v193\n"
    "v_mfma_f32_16x16x32_bf16 v[188:191], a[244:247], v[52:55], v[188:191]\n"
    "v_exp_f32 v194, v194\n"
    "v_mfma_f32_16x16x32_bf16 v[188:191], a[240:243], v[48:51], v[188:191]\n"
    "v_exp_f32 v195, v195\n"
    "v_mfma_f32_16x16x32_bf16 a[0:3], v[144:147], v[228:231], a[0:3]\n"
    "v_exp_f32 v196, v196\n"
    "v_mfma_f32_16x16x32_bf16 a[4:7], v[144:147], v[232:235], a[4:7]\n"
    "v_exp_f32 v197, v197\n"
    "v_mfma_f32_16x16x32_bf16 a[8:11], v[144:147], v[236:239], a[8:11]\n"
    "s_waitcnt vmcnt(5)\n"
    "v_exp_f32 v198, v198\n"
    "v_mfma_f32_16x16x32_bf16 a[12:15], v[144:147], v[240:243], a[12:15]\n"
    "v_exp_f32 v199, v199\n"
    "v_cvt_pk_bf16_f32 v250, v192, v193\n"
    "v_cvt_pk_bf16_f32 v251, v194, v195\n"
    "v_mfma_f32_16x16x32_bf16 a[16:19], v[144:147], v[244:247], a[16:19]\n"
    "buffer_load_dwordx4 v9, s[44:47], s58 offen offset:2048 nt lds\n"
    "v_cvt_pk_bf16_f32 v254, v196, v197\n"
    "v_cvt_pk_bf16_f32 v255, v198, v199\n"
    "v_mfma_f32_16x16x32_bf16 a[20:23], v[144:147], v[248:251], a[20:23]\n"
    "s_add_u32 m0, s57, 73728\n"
    "v_add_f32 v25, v25, v192\n"
    "v_add_f32 v25, v25, v193\n"
    "v_mfma_f32_16x16x32_bf16 a[24:27], v[144:147], v[252:255], a[24:27]\n"
    "s_add_u32 s58, s65, 106496\n"
    "v_add_f32 v25, v25, v194\n"
    "v_add_f32 v25, v25, v195\n"
    "v_mfma_f32_16x16x32_bf16 v[192:195], a[240:243], v[64:67], v[164:167]\n"
    "s_and_b32 s58, s58, 0x1ffff\n"
    "ds_read_b64_tr_b16 v[144:145], v14 offset:512\n"
    "v_add_f32 v26, v26, v196\n"
    "v_mfma_f32_16x16x32_bf16 v[192:195], a[244:247], v[68:71], v[192:195]\n"
    "ds_read_b64_tr_b16 v[146:147], v14 offset:4608\n"
    "v_add_f32 v26, v26, v197\n"
    "v_mfma_f32_16x16x32_bf16 v[192:195], a[248:251], v[72:75], v[192:195]\n"
    "v_exp_f32 v184, v184\n"
    "v_mfma_f32_16x16x32_bf16 v[192:195], a[252:255], v[76:79], v[192:195]\n"
    "v_add_f32 v26, v26, v198\n"
    "v_add_f32 v26, v26, v199\n"
    "v_mfma_f32_16x16x32_bf16 v[196:199], a[252:255], v[92:95], v[168:171]\n"
    "v_exp_f32 v185, v185\n"
    "v_mfma_f32_16x16x32_bf16 v[196:199], a[248:251], v[88:91], v[196:199]\n"
    "v_exp_f32 v186, v186\n"
    "v_mfma_f32_16x16x32_bf16 v[196:199], a[244:247], v[84:87], v[196:199]\n"
    "v_add_f32 v20, v20, v184\n"
    "v_add_f32 v20, v20, v185\n"
    "v_mfma_f32_16x16x32_bf16 v[196:199], a[240:243], v[80:83], v[196:199]\n"
    "v_exp_f32 v187, v187\n"
    "v_mfma_f32_16x16x32_bf16 a[28:31], v[148:151], v[228:231], a[28:31]\n"
    "v_add_f32 v20, v20, v186\n"
    "v_cvt_pk_bf16_f32 v200, v184, v185\n"
    "v_mfma_f32_16x16x32_bf16 a[32:35], v[148:151], v[232:235], a[32:35]\n"
    "v_cvt_pk_bf16_f32 v201, v186, v187\n"
    "v_add_f32 v20, v20, v187\n"
    "v_mfma_f32_16x16x32_bf16 a[36:39], v[148:151], v[236:239], a[36:39]\n"
    "s_waitcnt vmcnt(5)\n"
    "v_exp_f32 v188, v188\n"
    "v_mfma_f32_16x16x32_bf16 a[40:43], v[148:151], v[240:243], a[40:43]\n"
    "v_exp_f32 v189, v189\n"
    "v_mfma_f32_16x16x32_bf16 a[44:47], v[148:151], v[244:247], a[44:47]\n"
    "buffer_load_dwordx4 v10, s[48:51], s58 offen nt lds\n"
    "v_mfma_f32_16x16x32_bf16 a[48:51], v[148:151], v[248:251], a[48:51]\n"
    "v_exp_f32 v190, v190\n"
    "v_mfma_f32_16x16x32_bf16 a[52:55], v[148:151], v[252:255], a[52:55]\n"
    "ds_read_b64_tr_b16 v[148:149], v17 offset:512\n"
    "v_add_f32 v21, v21, v188\n"
    "v_mfma_f32_16x16x32_bf16 v[184:187], a[240:243], v[96:99], v[172:175]\n"
    "v_exp_f32 v191, v191\n"
    "v_mfma_f32_16x16x32_bf16 v[184:187], a[244:247], v[100:103], v[184:187]\n"
    "ds_read_b64_tr_b16 v[150:151], v17 offset:4608\n"
    "v_add_f32 v21, v21, v189\n"
    "v_mfma_f32_16x16x32_bf16 v[184:187], a[248:251], v[104:107], v[184:187]\n"
    "v_add_f32 v21, v21, v190\n"
    "v_cvt_pk_bf16_f32 v204, v188, v189\n"
    "v_mfma_f32_16x16x32_bf16 v[184:187], a[252:255], v[108:111], v[184:187]\n"
    "v_cvt_pk_bf16_f32 v205, v190, v191\n"
    "v_add_f32 v21, v21, v191\n"
    "v_mfma_f32_16x16x32_bf16 v[188:191], a[252:255], v[124:127], v[176:179]\n"
    "v_exp_f32 v192, v192\n"
    "v_mfma_f32_16x16x32_bf16 v[188:191], a[248:251], v[120:123], v[188:191]\n"
    "v_exp_f32 v193, v193\n"
    "v_mfma_f32_16x16x32_bf16 v[188:191], a[244:247], v[116:119], v[188:191]\n"
    "v_exp_f32 v194, v194\n"
    "v_mfma_f32_16x16x32_bf16 v[188:191], a[240:243], v[112:115], v[188:191]\n"
    "s_waitcnt lgkmcnt(2)\n"
    "v_add_f32 v22, v22, v192\n"
    "v_add_f32 v22, v22, v193\n"
    "v_mfma_f32_16x16x32_bf16 a[56:59], v[152:155], v[228:231], a[56:59]\n"
    "v_exp_f32 v195, v195\n"
    "v_mfma_f32_16x16x32_bf16 a[60:63], v[152:155], v[232:235], a[60:63]\n"
    "v_add_f32 v22, v22, v194\n"
    "v_cvt_pk_bf16_f32 v208, v192, v193\n"
    "v_mfma_f32_16x16x32_bf16 a[64:67], v[152:155], v[236:239], a[64:67]\n"
    "s_waitcnt vmcnt(5)\n"
    "v_cvt_pk_bf16_f32 v209, v194, v195\n"
    "v_add_f32 v22, v22, v195\n"
    "v_mfma_f32_16x16x32_bf16 a[68:71], v[152:155], v[240:243], a[68:71]\n"
    "v_exp_f32 v196, v196\n"
    "v_mfma_f32_16x16x32_bf16 a[72:75], v[152:155], v[244:247], a[72:75]\n"
    "buffer_load_dwordx4 v10, s[48:51], s58 offen offset:2048 nt lds\n"
    "v_mfma_f32_16x16x32_bf16 a[76:79], v[152:155], v[248:251], a[76:79]\n"
    "s_add_u32 m0, s57, 77824\n"
    "v_exp_f32 v197, v197\n"
    "v_mfma_f32_16x16x32_bf16 a[80:83], v[152:155], v[252:255], a[80:83]\n"
    "s_add_u32 s58, s58, 4096\n"
    "v_exp_f32 v198, v198\n"
    "v_mfma_f32_16x16x32_bf16 v[192:195], a[240:243], v[128:131], v[180:183]\n"
    "ds_read_b64_tr_b16 v[152:153], v14 offset:768\n"
    "v_add_f32 v23, v23, v196\n"
    "v_mfma_f32_16x16x32_bf16 v[192:195], a[244:247], v[132:135], v[192:195]\n"
    "v_exp_f32 v199, v199\n"
    "v_mfma_f32_16x16x32_bf16 v[192:195], a[248:251], v[136:139], v[192:195]\n"
    "ds_read_b64_tr_b16 v[154:155], v14 offset:4864\n"
    "v_add_f32 v23, v23, v197\n"
    "v_mfma_f32_16x16x32_bf16 v[192:195], a[252:255], v[140:143], v[192:195]\n"
    "v_add_f32 v23, v23, v198\n"
    "v_cvt_pk_bf16_f32 v212, v196, v197\n"
    "v_mfma_f32_16x16x32_bf16 a[84:87], v[28:31], v[228:231], a[84:87]\n"
    "v_cvt_pk_bf16_f32 v213, v198, v199\n"
    "v_add_f32 v23, v23, v199\n"
    "v_mfma_f32_16x16x32_bf16 a[88:91], v[28:31], v[232:235], a[88:91]\n"
    "v_exp_f32 v184, v184\n"
    "v_mfma_f32_16x16x32_bf16 a[92:95], v[28:31], v[236:239], a[92:95]\n"
    "v_exp_f32 v185, v185\n"
    "v_mfma_f32_16x16x32_bf16 a[96:99], v[28:31], v[240:243], a[96:99]\n"
    "v_exp_f32 v186, v186\n"
    "v_mfma_f32_16x16x32_bf16 a[100:103], v[28:31], v[244:247], a[100:103]\n"
    "v_add_f32 v24, v24, v184\n"
    "v_add_f32 v24, v24, v185\n"
    "v_mfma_f32_16x16x32_bf16 a[104:107], v[28:31], v[248:251], a[104:107]\n"
    "v_exp_f32 v187, v187\n"
    "v_mfma_f32_16x16x32_bf16 a[108:111], v[28:31], v[252:255], a[108:111]\n"
    "s_waitcnt vmcnt(5)\n"
    "ds_read_b64_tr_b16 v[28:29], v17 offset:768\n"
    "v_add_f32 v24, v24, v186\n"
    "v_mfma_f32_16x16x32_bf16 v[196:199], a[224:227], v[32:35], v[156:159]\n"
    "ds_read_b64_tr_b16 v[30:31], v17 offset:4864\n"
    "v_cvt_pk_bf16_f32 v216, v184, v185\n"
    "v_mfma_f32_16x16x32_bf16 v[196:199], a[228:231], v[36:39], v[196:199]\n"
    "buffer_load_dwordx4 v10, s[48:51], s58 offen nt lds\n"
    "v_mfma_f32_16x16x32_bf16 v[196:199], a[232:235], v[40:43], v[196:199]\n"
    "v_cvt_pk_bf16_f32 v217, v186, v187\n"
    "v_add_f32 v24, v24, v187\n"
    "v_mfma_f32_16x16x32_bf16 v[196:199], a[236:239], v[44:47], v[196:199]\n"
    "s_add_u32 s34, s13, 0\n"
    "v_exp_f32 v188, v188\n"
    "v_mfma_f32_16x16x32_bf16 v[184:187], a[236:239], v[60:63], v[160:163]\n"
    "s_and_b32 s34, s34, 0x1fff\n"
    "v_exp_f32 v189, v189\n"
    "v_mfma_f32_16x16x32_bf16 v[184:187], a[232:235], v[56:59], v[184:187]\n"
    "v_exp_f32 v190, v190\n"
    "v_mfma_f32_16x16x32_bf16 v[184:187], a[228:231], v[52:55], v[184:187]\n"
    "s_memrealtime s[24:25]\n"
    "v_add_f32 v25, v25, v188\n"
    "v_add_f32 v25, v25, v189\n"
    "v_mfma_f32_16x16x32_bf16 v[184:187], a[224:227], v[48:51], v[184:187]\n"
    "v_exp_f32 v191, v191\n"
    "v_mfma_f32_16x16x32_bf16 a[112:115], v[144:147], v[228:231], a[112:115]\n"
    "v_add_f32 v25, v25, v190\n"
    "v_cvt_pk_bf16_f32 v220, v188, v189\n"
    "v_mfma_f32_16x16x32_bf16 a[116:119], v[144:147], v[232:235], a[116:119]\n"
    "v_cvt_pk_bf16_f32 v221, v190, v191\n"
    "v_add_f32 v25, v25, v191\n"
    "v_mfma_f32_16x16x32_bf16 a[120:123], v[144:147], v[236:239], a[120:123]\n"
    "v_exp_f32 v192, v192\n"
    "v_mfma_f32_16x16x32_bf16 a[124:127], v[144:147], v[240:243], a[124:127]\n"
    "v_exp_f32 v193, v193\n"
    "v_mfma_f32_16x16x32_bf16 a[128:131], v[144:147], v[244:247], a[128:131]\n"
    "v_exp_f32 v194, v194\n"
    "v_mfma_f32_16x16x32_bf16 a[132:135], v[144:147], v[248:251], a[132:135]\n"
    "v_add_f32 v26, v26, v192\n"
    "v_add_f32 v26, v26, v193\n"
    "v_mfma_f32_16x16x32_bf16 a[136:139], v[144:147], v[252:255], a[136:139]\n"
    "s_waitcnt vmcnt(5)\n"
    "v_exp_f32 v195, v195\n"
    "v_mfma_f32_16x16x32_bf16 v[188:191], a[224:227], v[64:67], v[164:167]\n"
    "v_add_f32 v26, v26, v194\n"
    "v_cvt_pk_bf16_f32 v224, v192, v193\n"
    "v_mfma_f32_16x16x32_bf16 v[188:191], a[228:231], v[68:71], v[188:191]\n"
    "s_waitcnt vmcnt(7) lgkmcnt(0)\n"
    "s_barrier\n"
    "ds_read_b128 a[240:243], v12 offset:0\n"
    "v_mfma_f32_16x16x32_bf16 v[188:191], a[232:235], v[72:75], v[188:191]\n"
    "buffer_load_dwordx4 v10, s[48:51], s58 offen offset:2048 nt lds\n"
    "v_mfma_f32_16x16x32_bf16 v[188:191], a[236:239], v[76:79], v[188:191]\n"
    "s_sub_i32 s25, s26, s56\n"
    "ds_read_b128 a[244:247], v12 offset:256\n"
    "v_cvt_pk_bf16_f32 v225, v194, v195\n"
    "v_add_f32 v26, v26, v195\n"
    "v_mfma_f32_16x16x32_bf16 v[192:195], a[236:239], v[92:95], v[168:171]\n"
    "s_max_i32 s25, s25, -3\n"
    "v_exp_f32 v196, v196\n"
    "v_mfma_f32_16x16x32_bf16 v[192:195], a[232:235], v[88:91], v[192:195]\n"
    "s_min_i32 s25, s25, 0\n"
    "v_exp_f32 v197, v197\n"
    "v_mfma_f32_16x16x32_bf16 v[192:195], a[228:231], v[84:87], v[192:195]\n"
    "s_add_u32 s56, s56, s25\n"
    "v_exp_f32 v198, v198\n"
    "v_mfma_f32_16x16x32_bf16 v[192:195], a[224:227], v[80:83], v[192:195]\n"
    "s_sub_i32 s25, s56, s24\n"
    "ds_read_b128 a[248:251], v12 offset:512\n"
    "v_add_f32 v20, v20, v196\n"
    "v_mfma_f32_16x16x32_bf16 a[140:143], v[148:151], v[228:231], a[140:143]\n"
    "s_max_i32 s25, s25, -10\n"
    "v_exp_f32 v199, v199\n"
    "v_mfma_f32_16x16x32_bf16 a[144:147], v[148:151], v[232:235], a[144:147]\n"
    "s_min_i32 s25, s25, 62\n"
    "ds_read_b128 a[252:255], v12 offset:768\n"
    "v_add_f32 v20, v20, v197\n"
    "v_mfma_f32_16x16x32_bf16 a[148:151], v[148:151], v[236:239], a[148:151]\n"
    "s_add_u32 s56, s24, s25\n"
    "ds_read_b64_tr_b16 v[144:145], v14 offset:32768\n"
    "v_add_f32 v20, v20, v198\n"
    "v_mfma_f32_16x16x32_bf16 a[152:155], v[148:151], v[240:243], a[152:155]\n"
    "s_add_u32 s56, s56, 125\n"
    "ds_read_b64_tr_b16 v[146:147], v14 offset:36864\n"
    "v_cvt_pk_bf16_f32 v202, v196, v197\n"
    "v_mfma_f32_16x16x32_bf16 a[156:159], v[148:151], v[244:247], a[156:159]\n"
    "s_cmp_gt_i32 s25, 2\ns_cbranch_scc1 L_ta12_%=\nL_tb12_%=:\n"
    "v_cvt_pk_bf16_f32 v203, v198, v199\n"
    "v_add_f32 v20, v20, v199\n"
    "v_mfma_f32_16x16x32_bf16 a[160:163], v[148:151], v[248:251], a[160:163]\n"
    "s_add_u32 m0, s57, 131072\n"
    "v_exp_f32 v184, v184\n"
    "v_mfma_f32_16x16x32_bf16 a[164:167], v[148:151], v[252:255], a[164:167]\n"
    "s_add_u32 s58, s65, 122880\n"
    "v_exp_f32 v185, v185\n"
    "v_mfma_f32_16x16x32_bf16 v[196:199], a[224:227], v[96:99], v[172:175]\n"
    "s_waitcnt vmcnt(5)\n"
    "v_exp_f32 v186, v186\n"
    "v_mfma_f32_16x16x32_bf16 v[196:199], a[228:231], v[100:103], v[196:199]\n"
    "s_and_b32 s58, s58, 0x1ffff\n"
    "ds_read_b64_tr_b16 v[148:149], v17 offset:32768\n"
    "v_add_f32 v21, v21, v184\n"
    "v_mfma_f32_16x16x32_bf16 v[196:199], a[232:235], v[104:107], v[196:199]\n"
    "s_add_u32 s26, s26, 125\n"
    "v_exp_f32 v187, v187\n"
    "v_add_f32 v21, v21, v185\n"
    "v_mfma_f32_16x16x32_bf16 v[196:199], a[236:239], v[108:111], v[196:199]\n"
    "ds_read_b64_tr_b16 v[150:151], v17 offset:36864\n"
    "v_add_f32 v21, v21, v186\n"
    "v_cvt_pk_bf16_f32 v206, v184, v185\n"
    "v_cvt_pk_bf16_f32 v207, v186, v187\n"
    "v_add_f32 v21, v21, v187\n"
    "v_mfma_f32_16x16x32_bf16 v[184:187], a[236:239], v[124:127], v[176:179]\n"
    "buffer_load_dword v0, v10, s[28:31], s66 offen\n"
    "v_mfma_f32_16x16x32_bf16 v[184:187], a[232:235], v[120:123], v[184:187]\n"
    "v_exp_f32 v188, v188\n"
    "v_mfma_f32_16x16x32_bf16 v[184:187], a[228:231], v[116:119], v[184:187]\n"
    "v_exp_f32 v189, v189\n"
    "v_mfma_f32_16x16x32_bf16 v[184:187], a[224:227], v[112:115], v[184:187]\n"
    "v_exp_f32 v190, v190\n"
    "v_mfma_f32_16x16x32_bf16 a[168:171], v[152:155], v[228:231], a[168:171]\n"
    "v_add_f32 v22, v22, v188\n"
    "v_add_f32 v22, v22, v189\n"
    "v_mfma_f32_16x16x32_bf16 a[172:175], v[152:155], v[232:235], a[172:175]\n"
    "buffer_load_dwordx4 v8, s[44:47], s58 offen nt lds\n"
    "v_mfma_f32_16x16x32_bf16 a[176:179], v[152:155], v[236:239], a[176:179]\n"
    "v_exp_f32 v191, v191\n"
    "v_mfma_f32_16x16x32_bf16 a[180:183], v[152:155], v[240:243], a[180:183]\n"
    "v_add_f32 v22, v22, v190\n"
    "v_cvt_pk_bf16_f32 v210, v188, v189\n"
    "v_mfma_f32_16x16x32_bf16 a[184:187], v[152:155], v[244:247], a[184:187]\n"
    "v_cvt_pk_bf16_f32 v211, v190, v191\n"
    "v_add_f32 v22, v22, v191\n"
    "v_mfma_f32_16x16x32_bf16 a[188:191], v[152:155], v[248:251], a[188:191]\n"
    "v_exp_f32 v192, v192\n"
    "v_mfma_f32_16x16x32_bf16 a[192:195], v[152:155], v[252:255], a[192:195]\n"
    "v_exp_f32 v193, v193\n"
    "v_mfma_f32_16x16x32_bf16 v[188:191], a[224:227], v[128:131], v[180:183]\n"
    "s_waitcnt vmcnt(5)\n"
    "v_exp_f32 v194, v194\n"
    "v_mfma_f32_16x16x32_bf16 v[188:191], a[228:231], v[132:135], v[188:191]\n"
    "ds_read_b64_tr_b16 v[152:153], v14 offset:33024\n"
    "v_add_f32 v23, v23, v192\n"
    "v_mfma_f32_16x16x32_bf16 v[188:191], a[232:235], v[136:139], v[188:191]\n"
    "buffer_load_dwordx4 v9, s[44:47], s58 offen offset:2048 nt lds\n"
    "v_mfma_f32_16x16x32_bf16 v[188:191], a[236:239], v[140:143], v[188:191]\n"
    "s_add_u32 m0, s57, 135168\n"
    "v_exp_f32 v195, v195\n"
    "v_mfma_f32_16x16x32_bf16 a[196:199], v[28:31], v[228:231], a[196:199]\n"
    "s_add_u32 s58, s58, 4096\n"
    "ds_read_b64_tr_b16 v[154:155], v14 offset:37120\n"
    "v_add_f32 v23, v23, v193\n"
    "v_mfma_f32_16x16x32_bf16 a[200:203], v[28:31], v[232:235], a[200:203]\n"
    "ds_read_b128 a[224:227], v12 offset:4096\n"
    "v_add_f32 v23, v23, v194\n"
    "v_mfma_f32_16x16x32_bf16 a[204:207], v[28:31], v[236:239], a[204:207]\n"
    "ds_read_b128 a[228:231], v12 offset:4352\n"
    "v_cvt_pk_bf16_f32 v214, v192, v193\n"
    "v_mfma_f32_16x16x32_bf16 a[208:211], v[28:31], v[240:243], a[208:211]\n"
    "ds_read_b128 a[232:235], v12 offset:4608\n"
    "v_cvt_pk_bf16_f32 v215, v194, v195\n"
    "v_mfma_f32_16x16x32_bf16 a[212:215], v[28:31], v[244:247], a[212:215]\n"
    "ds_read_b128 a[236:239], v12 offset:4864\n"
    "v_add_f32 v23, v23, v195\n"
    "v_mfma_f32_16x16x32_bf16 a[216:219], v[28:31], v[248:251], a[216:219]\n"
    "v_exp_f32 v196, v196\n"
    "v_mfma_f32_16x16x32_bf16 a[220:223], v[28:31], v[252:255], a[220:223]\n"
    "s_waitcnt lgkmcnt(6)\n"
    "v_exp_f32 v197, v197\n"
    "v_mfma_f32_16x16x32_bf16 v[192:195], a[240:243], v[32:35], v[156:159]\n"
    "v_exp_f32 v198, v198\n"
    "v_mfma_f32_16x16x32_bf16 v[192:195], a[244:247], v[36:39], v[192:195]\n"
    "ds_read_b64_tr_b16 v[28:29], v17 offset:33024\n"
    "v_add_f32 v24, v24, v196\n"
    "v_mfma_f32_16x16x32_bf16 v[192:195], a[248:251], v[40:43], v[192:195]\n"
    "v_exp_f32 v199, v199\n"
    "v_add_f32 v24, v24, v197\n"
    "v_mfma_f32_16x16x32_bf16 v[192:195], a[252:255], v[44:47], v[192:195]\n"
    "ds_read_b64_tr_b16 v[30:31], v17 offset:37120\n"
    "v_add_f32 v24, v24, v198\n"
    "v_cvt_pk_bf16_f32 v218, v196, v197\n"
    "v_cvt_pk_bf16_f32 v219, v198, v199\n"
    "v_add_f32 v24, v24, v199\n"
    "v_mfma_f32_16x16x32_bf16 v[196:199], a[252:255], v[60:63], v[160:163]\n"
    "s_waitcnt vmcnt(5)\n"
    "v_exp_f32 v184, v184\n"
    "v_mfma_f32_16x16x32_bf16 v[196:199], a[248:251], v[56:59], v[196:199]\n"
    "v_exp_f32 v185, v185\n"
    "v_mfma_f32_16x16x32_bf16 v[196:199], a[244:247], v[52:55], v[196:199]\n"
    "buffer_load_dwordx4 v8, s[44:47], s58 offen nt lds\n"
    "v_mfma_f32_16x16x32_bf16 v[196:199], a[240:243], v[48:51], v[196:199]\n"
    "v_exp_f32 v186, v186\n"
    "v_mfma_f32_16x16x32_bf16 a[0:3], v[144:147], v[200:203], a[0:3]\n"
    "v_exp_f32 v187, v187\n"
    "v_mfma_f32_16x16x32_bf16 a[4:7], v[144:147], v[204:207], a[4:7]\n"
    "v_exp_f32 v188, v188\n"
    "v_mfma_f32_16x16x32_bf16 a[8:11], v[144:147], v[208:211], a[8:11]\n"
    "v_exp_f32 v189, v189\n"
    "v_mfma_f32_16x16x32_bf16 a[12:15], v[144:147], v[212:215], a[12:15]\n"
    "v_exp_f32 v190, v190\n"
    "v_cvt_pk_bf16_f32 v222, v184, v185\n"
    "v_cvt_pk_bf16_f32 v223, v186, v187\n"
    "v_exp_f32 v191, v191\n"
    "v_mfma_f32_16x16x32_bf16 a[16:19], v[144:147], v[216:219], a[16:19]\n"
    "v_cvt_pk_bf16_f32 v226, v188, v189\n"
    "v_cvt_pk_bf16_f32 v227, v190, v191\n"
    "v_mfma_f32_16x16x32_bf16 a[20:23], v[144:147], v[220:223], a[20:23]\n"
    "v_add_f32 v25, v25, v184\n"
    "v_add_f32 v25, v25, v185\n"
    "v_mfma_f32_16x16x32_bf16 a[24:27], v[144:147], v[224:227], a[24:27]\n"
    "v_add_f32 v25, v25, v186\n"
    "v_add_f32 v25, v25, v187\n"
    "v_mfma_f32_16x16x32_bf16 v[184:187], a[240:243], v[64:67], v[164:167]\n"
    "ds_read_b64_tr_b16 v[144:145], v14 offset:33280\n"
    "v_add_f32 v26, v26, v188\n"
    "v_mfma_f32_16x16x32_bf16 v[184:187], a[244:247], v[68:71], v[184:187]\n"
    "ds_read_b64_tr_b16 v[146:147], v14 offset:37376\n"
    "v_add_f32 v26, v26, v189\n"
    "v_mfma_f32_16x16x32_bf16 v[184:187], a[248:251], v[72:75], v[184:187]\n"
    "v_exp_f32 v192, v192\n"
    "v_mfma_f32_16x16x32_bf16 v[184:187], a[252:255], v[76:79], v[184:187]\n"
    "v_add_f32 v26, v26, v190\n"
    "v_add_f32 v26, v26, v191\n"
    "v_mfma_f32_16x16x32_bf16 v[188:191], a[252:255], v[92:95], v[168:171]\n"
    "s_waitcnt vmcnt(5)\n"
    "v_exp_f32 v193, v193\n"
    "v_mfma_f32_16x16x32_bf16 v[188:191], a[248:251], v[88:91], v[188:191]\n"
    "v_exp_f32 v194, v194\n"
    "v_mfma_f32_16x16x32_bf16 v[188:191], a[244:247], v[84:87], v[188:191]\n"
    "buffer_load_dwordx4 v9, s[44:47], s58 offen offset:2048 nt lds\n"
    "v_mfma_f32_16x16x32_bf16 v[188:191], a[240:243], v[80:83], v[188:191]\n"
    "s_add_u32 m0, s57, 106496\n"
    "v_add_f32 v20, v20, v192\n"
    "v_add_f32 v20, v20, v193\n"
    "v_mfma_f32_16x16x32_bf16 a[28:31], v[148:151], v[200:203], a[28:31]\n"
    "s_add_u32 s58, s65, 114688\n"
    "v_exp_f32 v195, v195\n"
    "v_mfma_f32_16x16x32_bf16 a[32:35], v[148:151], v[204:207], a[32:35]\n"
    "s_and_b32 s58, s58, 0x1ffff\n"
    "v_add_f32 v20, v20, v194\n"
    "v_cvt_pk_bf16_f32 v228, v192, v193\n"
    "v_mfma_f32_16x16x32_bf16 a[36:39], v[148:151], v[208:211], a[36:39]\n"
    "v_cvt_pk_bf16_f32 v229, v194, v195\n"
    "v_add_f32 v20, v20, v195\n"
    "v_mfma_f32_16x16x32_bf16 a[40:43], v[148:151], v[212:215], a[40:43]\n"
    "v_exp_f32 v196, v196\n"
    "v_mfma_f32_16x16x32_bf16 a[44:47], v[148:151], v[216:219], a[44:47]\n"
    "v_exp_f32 v197, v197\n"
    "v_mfma_f32_16x16x32_bf16 a[48:51], v[148:151], v[220:223], a[48:51]\n"
    "v_exp_f32 v198, v198\n"
    "v_mfma_f32_16x16x32_bf16 a[52:55], v[148:151], v[224:227], a[52:55]\n"
    "ds_read_b64_tr_b16 v[148:149], v17 offset:33280\n"
    "v_add_f32 v21, v21, v196\n"
    "v_mfma_f32_16x16x32_bf16 v[192:195], a[240:243], v[96:99], v[172:175]\n"
    "v_exp_f32 v199, v199\n"
    "v_mfma_f32_16x16x32_bf16 v[192:195], a[244:247], v[100:103], v[192:195]\n"
    "ds_read_b64_tr_b16 v[150:151], v17 offset:37376\n"
    "v_add_f32 v21, v21, v197\n"
    "v_mfma_f32_16x16x32_bf16 v[192:195], a[248:251], v[104:107], v[192:195]\n"
    "v_add_f32 v21, v21, v198\n"
    "v_cvt_pk_bf16_f32 v232, v196, v197\n"
    "v_mfma_f32_16x16x32_bf16 v[192:195], a[252:255], v[108:111], v[192:195]\n"
    "v_cvt_pk_bf16_f32 v233, v198, v199\n"
    "v_add_f32 v21, v21, v199\n"
    "v_mfma_f32_16x16x32_bf16 v[196:199], a[252:255], v[124:127], v[176:179]\n"
    "s_waitcnt vmcnt(5)\n"
    "v_exp_f32 v184, v184\n"
    "v_mfma_f32_16x16x32_bf16 v[196:199], a[248:251], v[120:123], v[196:199]\n"
    "v_exp_f32 v185, v185\n"
    "v_mfma_f32_16x16x32_bf16 v[196:199], a[244:247], v[116:119], v[196:199]\n"
    "buffer_load_dwordx4 v10, s[48:51], s58 offen nt lds\n"
    "v_mfma_f32_16x16x32_bf16 v[196:199], a[240:243], v[112:115], v[196:199]\n"
    "s_waitcnt lgkmcnt(2)\n"
    "v_exp_f32 v186, v186\n"
    "v_mfma_f32_16x16x32_bf16 a[56:59], v[152:155], v[200:203], a[56:59]\n"
    "v_add_f32 v22, v22, v184\n"
    "v_add_f32 v22, v22, v185\n"
    "v_mfma_f32_16x16x32_bf16 a[60:63], v[152:155], v[204:207], a[60:63]\n"
    "v_exp_f32 v187, v187\n"
    "v_mfma_f32_16x16x32_bf16 a[64:67], v[152:155], v[208:211], a[64:67]\n"
    "v_add_f32 v22, v22, v186\n"
    "v_cvt_pk_bf16_f32 v236, v184, v185\n"
    "v_mfma_f32_16x16x32_bf16 a[68:71], v[152:155], v[212:215], a[68:71]\n"
    "v_cvt_pk_bf16_f32 v237, v186, v187\n"
    "v_add_f32 v22, v22, v187\n"
    "v_mfma_f32_16x16x32_bf16 a[72:75], v[152:155], v[216:219], a[72:75]\n"
    "v_exp_f32 v188, v188\n"
    "v_mfma_f32_16x16x32_bf16 a[76:79], v[152:155], v[220:223], a[76:79]\n"
    "v_exp_f32 v189, v189\n"
    "v_mfma_f32_16x16x32_bf16 a[80:83], v[152:155], v[224:227], a[80:83]\n"
    "v_exp_f32 v190, v190\n"
    "v_mfma_f32_16x16x32_bf16 v[184:187], a[240:243], v[128:131], v[180:183]\n"
    "ds_read_b64_tr_b16 v[152:153], v14 offset:33536\n"
    "v_add_f32 v23, v23, v188\n"
    "v_mfma_f32_16x16x32_bf16 v[184:187], a[244:247], v[132:135], v[184:187]\n"
    "v_exp_f32 v191, v191\n"
    "v_mfma_f32_16x16x32_bf16 v[184:187], a[248:251], v[136:139], v[184:187]\n"
    "ds_read_b64_tr_b16 v[154:155], v14 offset:37632\n"
    "v_add_f32 v23, v23, v189\n"
    "v_mfma_f32_16x16x32_bf16 v[184:187], a[252:255], v[140:143], v[184:187]\n"
    "v_add_f32 v23, v23, v190\n"
    "v_cvt_pk_bf16_f32 v240, v188, v189\n"
    "v_mfma_f32_16x16x32_bf16 a[84:87], v[28:31], v[200:203], a[84:87]\n"
    "v_cvt_pk_bf16_f32 v241, v190, v191\n"
    "v_add_f32 v23, v23, v191\n"
    "v_mfma_f32_16x16x32_bf16 a[88:91], v[28:31], v[204:207], a[88:91]\n"
    "s_waitcnt vmcnt(5)\n"
    "v_exp_f32 v192, v192\n"
    "v_mfma_f32_16x16x32_bf16 a[92:95], v[28:31], v[208:211], a[92:95]\n"
    "v_exp_f32 v193, v193\n"
    "v_mfma_f32_16x16x32_bf16 a[96:99], v[28:31], v[212:215], a[96:99]\n"
    "buffer_load_dwordx4 v10, s[48:51], s58 offen offset:2048 nt lds\n"
    "v_mfma_f32_16x16x32_bf16 a[100:103], v[28:31], v[216:219], a[100:103]\n"
    "s_add_u32 m0, s57, 110592\n"
    "v_exp_f32 v194, v194\n"
    "v_mfma_f32_16x16x32_bf16 a[104:107], v[28:31], v[220:223], a[104:107]\n"
    "s_add_u32 s58, s58, 4096\n"
    "v_add_f32 v24, v24, v192\n"
    "v_add_f32 v24, v24, v193\n"
    "v_mfma_f32_16x16x32_bf16 a[108:111], v[28:31], v[224:227], a[108:111]\n"
    "v_exp_f32 v195, v195\n"
    "v_mfma_f32_16x16x32_bf16 v[188:191], a[224:227], v[32:35], v[156:159]\n"
    "ds_read_b64_tr_b16 v[28:29], v17 offset:33536\n"
    "v_add_f32 v24, v24, v194\n"
    "v_mfma_f32_16x16x32_bf16 v[188:191], a[228:231], v[36:39], v[188:191]\n"
    "ds_read_b64_tr_b16 v[30:31], v17 offset:37632\n"
    "v_cvt_pk_bf16_f32 v244, v192, v193\n"
    "v_mfma_f32_16x16x32_bf16 v[188:191], a[232:235], v[40:43], v[188:191]\n"
    "v_cvt_pk_bf16_f32 v245, v194, v195\n"
    "v_add_f32 v24, v24, v195\n"
    "v_mfma_f32_16x16x32_bf16 v[188:191], a[236:239], v[44:47], v[188:191]\n"
    "v_exp_f32 v196, v196\n"
    "v_mfma_f32_16x16x32_bf16 v[192:195], a[236:239], v[60:63], v[160:163]\n"
    "v_exp_f32 v197, v197\n"
    "v_mfma_f32_16x16x32_bf16 v[192:195], a[232:235], v[56:59], v[192:195]\n"
    "v_exp_f32 v198, v198\n"
    "v_mfma_f32_16x16x32_bf16 v[192:195], a[228:231], v[52:55], v[192:195]\n"
    "s_memrealtime s[24:25]\n"
    "v_add_f32 v25, v25, v196\n"
    "v_add_f32 v25, v25, v197\n"
    "v_mfma_f32_16x16x32_bf16 v[192:195], a[224:227], v[48:51], v[192:195]\n"
    "v_exp_f32 v199, v199\n"
    "v_mfma_f32_16x16x32_bf16 a[112:115], v[144:147], v[200:203], a[112:115]\n"
    "v_add_f32 v25, v25, v198\n"
    "v_cvt_pk_bf16_f32 v248, v196, v197\n"
    "v_mfma_f32_16x16x32_bf16 a[116:119], v[144:147], v[204:207], a[116:119]\n"
    "s_waitcnt vmcnt(5)\n"
    "v_cvt_pk_bf16_f32 v249, v198, v199\n"
    "v_add_f32 v25, v25, v199\n"
    "v_mfma_f32_16x16x32_bf16 a[120:123], v[144:147], v[208:211], a[120:123]\n"
    "v_exp_f32 v184, v184\n"
    "v_mfma_f32_16x16x32_bf16 a[124:127], v[144:147], v[212:215], a[124:127]\n"
    "buffer_load_dwordx4 v10, s[48:51], s58 offen nt lds\n"
    "v_mfma_f32_16x16x32_bf16 a[128:131], v[144:147], v[216:219], a[128:131]\n"
    "v_exp_f32 v185, v185\n"
    "v_mfma_f32_16x16x32_bf16 a[132:135], v[144:147], v[220:223], a[132:135]\n"
    "v_exp_f32 v186, v186\n"
    "v_mfma_f32_16x16x32_bf16 a[136:139], v[144:147], v[224:227], a[136:139]\n"
    "v_add_f32 v26, v26, v184\n"
    "v_add_f32 v26, v26, v185\n"
    "v_mfma_f32_16x16x32_bf16 v[196:199], a[224:227], v[64:67], v[164:167]\n"
    "v_exp_f32 v187, v187\n"
    "v_mfma_f32_16x16x32_bf16 v[196:199], a[228:231], v[68:71], v[196:199]\n"
    "s_waitcnt vmcnt(7) lgkmcnt(0)\n"
    "s_barrier\n"
    "ds_read_b128 a[240:243], v12 offset:32768\n"
    "v_mfma_f32_16x16x32_bf16 v[196:199], a[232:235], v[72:75], v[196:199]\n"
    "s_sub_i32 s25, s56, s24\n"
    "ds_read_b128 a[244:247], v12 offset:33024\n"
    "v_add_f32 v26, v26, v186\n"
    "v_mfma_f32_16x16x32_bf16 v[196:199], a[236:239], v[76:79], v[196:199]\n"
    "s_max_i32 s25, s25, -10\n"
    "ds_read_b128 a[248:251], v12 offset:33280\n"
    "v_cvt_pk_bf16_f32 v252, v184, v185\n"
    "v_cvt_pk_bf16_f32 v253, v186, v187\n"
    "v_add_f32 v26, v26, v187\n"
    "v_mfma_f32_16x16x32_bf16 v[184:187], a[236:239], v[92:95], v[168:171]\n"
    "s_min_i32 s25, s25, 62\n"
    "v_exp_f32 v188, v188\n"
    "v_mfma_f32_16x16x32_bf16 v[184:187], a[232:235], v[88:91], v[184:187]\n"
    "s_add_u32 s56, s24, s25\n"
    "v_exp_f32 v189, v189\n"
    "v_mfma_f32_16x16x32_bf16 v[184:187], a[228:231], v[84:87], v[184:187]\n"
    "s_add_u32 s56, s56, 125\n"
    "v_exp_f32 v190, v190\n"
    "v_mfma_f32_16x16x32_bf16 v[184:187], a[224:227], v[80:83], v[184:187]\n"
    "s_cmp_gt_i32 s25, 2\ns_cbranch_scc1 L_ta13_%=\nL_tb13_%=:\n"
    "ds_read_b128 a[252:255], v12 offset:33536\n"
    "v_add_f32 v20, v20, v188\n"
    "v_mfma_f32_16x16x32_bf16 a[140:143], v[148:151], v[200:203], a[140:143]\n"
    "v_exp_f32 v191, v191\n"
    "v_mfma_f32_16x16x32_bf16 a[144:147], v[148:151], v[204:207], a[144:147]\n"
    "s_waitcnt vmcnt(5)\n"
    "ds_read_b64_tr_b16 v[144:145], v15 offset:0\n"
    "v_add_f32 v20, v20, v189\n"
    "v_mfma_f32_16x16x32_bf16 a[148:151], v[148:151], v[208:211], a[148:151]\n"
    "ds_read_b64_tr_b16 v[146:147], v15 offset:4096\n"
    "v_add_f32 v20, v20, v190\n"
    "v_mfma_f32_16x16x32_bf16 a[152:155], v[148:151], v[212:215], a[152:155]\n"
    "buffer_load_dwordx4 v10, s[48:51], s58 offen offset:2048 nt lds\n"
    "v_mfma_f32_16x16x32_bf16 a[156:159], v[148:151], v[216:219], a[156:159]\n"
    "s_add_u32 m0, s57, 139264\n"
    "v_cvt_pk_bf16_f32 v230, v188, v189\n"
    "v_cvt_pk_bf16_f32 v231, v190, v191\n"
    "v_mfma_f32_16x16x32_bf16 a[160:163], v[148:151], v[220:223], a[160:163]\n"
    "s_add_u32 s58, s65, 122880\n"
    "v_exp_f32 v192, v192\n"
    "v_mfma_f32_16x16x32_bf16 a[164:167], v[148:151], v[224:227], a[164:167]\n"
    "s_and_b32 s58, s58, 0x1ffff\n"
    "ds_read_b64_tr_b16 v[148:149], v18 offset:0\n"
    "v_add_f32 v20, v20, v191\n"
    "v_mfma_f32_16x16x32_bf16 v[188:191], a[224:227], v[96:99], v[172:175]\n"
    "v_exp_f32 v193, v193\n"
    "v_mfma_f32_16x16x32_bf16 v[188:191], a[228:231], v[100:103], v[188:191]\n"
    "v_exp_f32 v194, v194\n"
    "v_mfma_f32_16x16x32_bf16 v[188:191], a[232:235], v[104:107], v[188:191]\n"
    "ds_read_b64_tr_b16 v[150:151], v18 offset:4096\n"
    "v_add_f32 v21, v21, v192\n"
    "v_exp_f32 v195, v195\n"
    "v_add_f32 v21, v21, v193\n"
    "v_mfma_f32_16x16x32_bf16 v[188:191], a[236:239], v[108:111], v[188:191]\n"
    "v_add_f32 v21, v21, v194\n"
    "v_cvt_pk_bf16_f32 v234, v192, v193\n"
    "v_cvt_pk_bf16_f32 v235, v194, v195\n"
    "v_add_f32 v21, v21, v195\n"
    "v_mfma_f32_16x16x32_bf16 v[192:195], a[236:239], v[124:127], v[176:179]\n"
    "v_exp_f32 v196, v196\n"
    "v_mfma_f32_16x16x32_bf16 v[192:195], a[232:235], v[120:123], v[192:195]\n"
    "v_exp_f32 v197, v197\n"
    "v_mfma_f32_16x16x32_bf16 v[192:195], a[228:231], v[116:119], v[192:195]\n"
    "v_exp_f32 v198, v198\n"
    "v_mfma_f32_16x16x32_bf16 v[192:195], a[224:227], v[112:115], v[192:195]\n"
    "v_add_f32 v22, v22, v196\n"
    "v_add_f32 v22, v22, v197\n"
    "v_mfma_f32_16x16x32_bf16 a[168:171], v[152:155], v[200:203], a[168:171]\n"
    "v_exp_f32 v199, v199\n"
    "v_mfma_f32_16x16x32_bf16 a[172:175], v[152:155], v[204:207], a[172:175]\n"
    "s_waitcnt vmcnt(5)\n"
    "v_add_f32 v22, v22, v198\n"
    "v_cvt_pk_bf16_f32 v238, v196, v197\n"
    "v_mfma_f32_16x16x32_bf16 a[176:179], v[152:155], v[208:211], a[176:179]\n"
    "v_cvt_pk_bf16_f32 v239, v198, v199\n"
    "v_add_f32 v22, v22, v199\n"
    "v_mfma_f32_16x16x32_bf16 a[180:183], v[152:155], v[212:215], a[180:183]\n"
    "buffer_load_dwordx4 v10, s[48:51], s58 offen nt lds\n"
    "v_mfma_f32_16x16x32_bf16 a[184:187], v[152:155], v[216:219], a[184:187]\n"
    "v_exp_f32 v184, v184\n"
    "v_mfma_f32_16x16x32_bf16 a[188:191], v[152:155], v[220:223], a[188:191]\n"
    "v_exp_f32 v185, v185\n"
    "v_mfma_f32_16x16x32_bf16 a[192:195], v[152:155], v[224:227], a[192:195]\n"
    "v_exp_f32 v186, v186\n"
    "v_mfma_f32_16x16x32_bf16 v[196:199], a[224:227], v[128:131], v[180:183]\n"
    "ds_read_b64_tr_b16 v[152:153], v15 offset:256\n"
    "v_add_f32 v23, v23, v184\n"
    "v_mfma_f32_16x16x32_bf16 v[196:199], a[228:231], v[132:135], v[196:199]\n"
    "v_exp_f32 v187, v187\n"
    "v_mfma_f32_16x16x32_bf16 v[196:199], a[232:235], v[136:139], v[196:199]\n"
    "ds_read_b64_tr_b16 v[154:155], v15 offset:4352\n"
    "v_add_f32 v23, v23, v185\n"
    "v_mfma_f32_16x16x32_bf16 v[196:199], a[236:239], v[140:143], v[196:199]\n"
    "ds_read_b128 a[224:227], v12 offset:36864\n"
    "v_add_f32 v23, v23, v186\n"
    "v_mfma_f32_16x16x32_bf16 a[196:199], v[28:31], v[200:203], a[196:199]\n"
    "ds_read_b128 a[228:231], v12 offset:37120\n"
    "v_cvt_pk_bf16_f32 v242, v184, v185\n"
    "v_mfma_f32_16x16x32_bf16 a[200:203], v[28:31], v[204:207], a[200:203]\n"
    "ds_read_b128 a[232:235], v12 offset:37376\n"
    "v_cvt_pk_bf16_f32 v243, v186, v187\n"
    "v_mfma_f32_16x16x32_bf16 a[204:207], v[28:31], v[208:211], a[204:207]\n"
    "ds_read_b128 a[236:239], v12 offset:37632\n"
    "v_add_f32 v23, v23, v187\n"
    "v_mfma_f32_16x16x32_bf16 a[208:211], v[28:31], v[212:215], a[208:211]\n"
    "v_exp_f32 v188, v188\n"
    "v_mfma_f32_16x16x32_bf16 a[212:215], v[28:31], v[216:219], a[212:215]\n"
    "v_exp_f32 v189, v189\n"
    "v_mfma_f32_16x16x32_bf16 a[216:219], v[28:31], v[220:223], a[216:219]\n"
    "s_waitcnt vmcnt(5)\n"
    "v_exp_f32 v190, v190\n"
    "v_mfma_f32_16x16x32_bf16 a[220:223], v[28:31], v[224:227], a[220:223]\n"
    "s_waitcnt lgkmcnt(6)\n"
    "ds_read_b64_tr_b16 v[28:29], v18 offset:256\n"
    "v_add_f32 v24, v24, v188\n"
    "v_mfma_f32_16x16x32_bf16 v[184:187], a[240:243], v[32:35], v[156:159]\n"
    "buffer_load_dwordx4 v10, s[48:51], s58 offen offset:2048 nt lds\n"
    "v_mfma_f32_16x16x32_bf16 v[184:187], a[244:247], v[36:39], v[184:187]\n"
    "s_add_u32 m0, s57, 143360\n"
    "v_exp_f32 v191, v191\n"
    "v_mfma_f32_16x16x32_bf16 v[184:187], a[248:251], v[40:43], v[184:187]\n"
    "s_add_u32 s58, s58, 4096\n"
    "ds_read_b64_tr_b16 v[30:31], v18 offset:4352\n"
    "v_add_f32 v24, v24, v189\n"
    "v_mfma_f32_16x16x32_bf16 v[184:187], a[252:255], v[44:47], v[184:187]\n"
    "v_add_f32 v24, v24, v190\n"
    "v_cvt_pk_bf16_f32 v246, v188, v189\n"
    "v_cvt_pk_bf16_f32 v247, v190, v191\n"
    "v_add_f32 v24, v24, v191\n"
    "v_mfma_f32_16x16x32_bf16 v[188:191], a[252:255], v[60:63], v[160:163]\n"
    "v_exp_f32 v192, v192\n"
    "v_mfma_f32_16x16x32_bf16 v[188:191], a[248:251], v[56:59], v[188:191]\n"
    "v_exp_f32 v193, v193\n"
    "v_mfma_f32_16x16x32_bf16 v[188:191], a[244:247], v[52:55], v[188:191]\n"
    "v_exp_f32 v194, v194\n"
    "v_mfma_f32_16x16x32_bf16 v[188:191], a[240:243], v[48:51], v[188:191]\n"
    "v_exp_f32 v195, v195\n"
    "v_mfma_f32_16x16x32_bf16 a[0:3], v[144:147], v[228:231], a[0:3]\n"
    "v_exp_f32 v196, v196\n"
    "v_mfma_f32_16x16x32_bf16 a[4:7], v[144:147], v[232:235], a[4:7]\n"
    "v_exp_f32 v197, v197\n"
    "v_mfma_f32_16x16x32_bf16 a[8:11], v[144:147], v[236:239], a[8:11]\n"
    "v_exp_f32 v198, v198\n"
    "v_mfma_f32_16x16x32_bf16 a[12:15], v[144:147], v[240:243], a[12:15]\n"
    "v_exp_f32 v199, v199\n"
    "v_cvt_pk_bf16_f32 v250, v192, v193\n"
    "v_cvt_pk_bf16_f32 v251, v194, v195\n"
    "v_mfma_f32_16x16x32_bf16 a[16:19], v[144:147], v[244:247], a[16:19]\n"
    "v_cvt_pk_bf16_f32 v254, v196, v197\n"
    "v_cvt_pk_bf16_f32 v255, v198, v199\n"
    "v_mfma_f32_16x16x32_bf16 a[20:23], v[144:147], v[248:251], a[20:23]\n"
    "s_waitcnt vmcnt(5)\n"
    "v_add_f32 v25, v25, v192\n"
    "v_add_f32 v25, v25, v193\n"
    "v_mfma_f32_16x16x32_bf16 a[24:27], v[144:147], v[252:255], a[24:27]\n"
    "v_add_f32 v25, v25, v194\n"
    "v_add_f32 v25, v25, v195\n"
    "v_mfma_f32_16x16x32_bf16 v[192:195], a[240:243], v[64:67], v[164:167]\n"
    "buffer_load_dwordx4 v10, s[48:51], s58 offen nt lds\n"
    "v_mfma_f32_16x16x32_bf16 v[192:195], a[244:247], v[68:71], v[192:195]\n"
    "ds_read_b64_tr_b16 v[144:145], v15 offset:512\n"
    "v_add_f32 v26, v26, v196\n"
    "v_mfma_f32_16x16x32_bf16 v[192:195], a[248:251], v[72:75], v[192:195]\n"
    "ds_read_b64_tr_b16 v[146:147], v15 offset:4608\n"
    "v_add_f32 v26, v26, v197\n"
    "v_mfma_f32_16x16x32_bf16 v[192:195], a[252:255], v[76:79], v[192:195]\n"
    "v_add_f32 v26, v26, v198\n"
    "v_add_f32 v26, v26, v199\n"
    "v_mfma_f32_16x16x32_bf16 v[196:199], a[252:255], v[92:95], v[168:171]\n"
    "v_exp_f32 v184, v184\n"
    "v_mfma_f32_16x16x32_bf16 v[196:199], a[248:251], v[88:91], v[196:199]\n"
    "v_exp_f32 v185, v185\n"
    "v_mfma_f32_16x16x32_bf16 v[196:199], a[244:247], v[84:87], v[196:199]\n"
    "v_exp_f32 v186, v186\n"
    "v_mfma_f32_16x16x32_bf16 v[196:199], a[240:243], v[80:83], v[196:199]\n"
    "v_add_f32 v20, v20, v184\n"
    "v_add_f32 v20, v20, v185\n"
    "v_mfma_f32_16x16x32_bf16 a[28:31], v[148:151], v[228:231], a[28:31]\n"
    "v_exp_f32 v187, v187\n"
    "v_mfma_f32_16x16x32_bf16 a[32:35], v[148:151], v[232:235], a[32:35]\n"
    "v_add_f32 v20, v20, v186\n"
    "v_cvt_pk_bf16_f32 v200, v184, v185\n"
    "v_mfma_f32_16x16x32_bf16 a[36:39], v[148:151], v[236:239], a[36:39]\n"
    "v_cvt_pk_bf16_f32 v201, v186, v187\n"
    "v_add_f32 v20, v20, v187\n"
    "v_mfma_f32_16x16x32_bf16 a[40:43], v[148:151], v[240:243], a[40:43]\n"
    "v_exp_f32 v188, v188\n"
    "v_mfma_f32_16x16x32_bf16 a[44:47], v[148:151], v[244:247], a[44:47]\n"
    "v_exp_f32 v189, v189\n"
    "v_mfma_f32_16x16x32_bf16 a[48:51], v[148:151], v[248:251], a[48:51]\n"
    "v_exp_f32 v190, v190\n"
    "v_mfma_f32_16x16x32_bf16 a[52:55], v[148:151], v[252:255], a[52:55]\n"
    "s_waitcnt vmcnt(5)\n"
    "ds_read_b64_tr_b16 v[148:149], v18 offset:512\n"
    "v_add_f32 v21, v21, v188\n"
    "v_mfma_f32_16x16x32_bf16 v[184:187], a[240:243], v[96:99], v[172:175]\n"
    "v_exp_f32 v191, v191\n"
    "v_mfma_f32_16x16x32_bf16 v[184:187], a[244:247], v[100:103], v[184:187]\n"
    "buffer_load_dwordx4 v10, s[48:51], s58 offen offset:2048 nt lds\n"
    "v_mfma_f32_16x16x32_bf16 v[184:187], a[248:251], v[104:107], v[184:187]\n"
    "s_lshl2_add_u32 s58, s34, s35\n"
    "ds_read_b64_tr_b16 v[150:151], v18 offset:4608\n"
    "v_add_f32 v21, v21, v189\n"
    "v_mfma_f32_16x16x32_bf16 v[184:187], a[252:255], v[108:111], v[184:187]\n"
    "s_add_u32 s58, s58, 0\n"
    "v_add_f32 v21, v21, v190\n"
    "v_cvt_pk_bf16_f32 v204, v188, v189\n"
    "v_cvt_pk_bf16_f32 v205, v190, v191\n"
    "v_add_f32 v21, v21, v191\n"
    "v_mfma_f32_16x16x32_bf16 v[188:191], a[252:255], v[124:127], v[176:179]\n"
    "s_add_u32 s34, s34, s57\n"
    "v_exp_f32 v192, v192\n"
    "v_mfma_f32_16x16x32_bf16 v[188:191], a[248:251], v[120:123], v[188:191]\n"
    "s_add_u32 m0, s34, 0\n"
    "v_exp_f32 v193, v193\n"
    "v_mfma_f32_16x16x32_bf16 v[188:191], a[244:247], v[116:119], v[188:191]\n"
    "s_add_u32 s34, s13, 2048\n"
    "v_exp_f32 v194, v194\n"
    "v_mfma_f32_16x16x32_bf16 v[188:191], a[240:243], v[112:115], v[188:191]\n"
    "s_waitcnt lgkmcnt(2)\n"
    "v_add_f32 v22, v22, v192\n"
    "v_add_f32 v22, v22, v193\n"
    "v_mfma_f32_16x16x32_bf16 a[56:59], v[152:155], v[228:231], a[56:59]\n"
    "s_and_b32 s34, s34, 0x1fff\n"
    "v_exp_f32 v195, v195\n"
    "v_mfma_f32_16x16x32_bf16 a[60:63], v[152:155], v[232:235], a[60:63]\n"
    "v_add_f32 v22, v22, v194\n"
    "v_cvt_pk_bf16_f32 v208, v192, v193\n"
    "v_mfma_f32_16x16x32_bf16 a[64:67], v[152:155], v[236:239], a[64:67]\n"
    "v_cvt_pk_bf16_f32 v209, v194, v195\n"
    "v_add_f32 v22, v22, v195\n"
    "v_mfma_f32_16x16x32_bf16 a[68:71], v[152:155], v[240:243], a[68:71]\n"
    "v_exp_f32 v196, v196\n"
    "v_mfma_f32_16x16x32_bf16 a[72:75], v[152:155], v[244:247], a[72:75]\n"
    "v_exp_f32 v197, v197\n"
    "v_mfma_f32_16x16x32_bf16 a[76:79], v[152:155], v[248:251], a[76:79]\n"
    "v_exp_f32 v198, v198\n"
    "v_mfma_f32_16x16x32_bf16 a[80:83], v[152:155], v[252:255], a[80:83]\n"
    "ds_read_b64_tr_b16 v[152:153], v15 offset:768\n"
    "v_add_f32 v23, v23, v196\n"
    "v_mfma_f32_16x16x32_bf16 v[192:195], a[240:243], v[128:131], v[180:183]\n"
    "v_exp_f32 v199, v199\n"
    "v_mfma_f32_16x16x32_bf16 v[192:195], a[244:247], v[132:135], v[192:195]\n"
    "buffer_load_dwordx4 v7, s[40:43], s58 offen lds\n"
    "v_mfma_f32_16x16x32_bf16 v[192:195], a[248:251], v[136:139], v[192:195]\n"
    "s_lshl2_add_u32 s58, s34, s35\n"
    "ds_read_b64_tr_b16 v[154:155], v15 offset:4864\n"
    "v_add_f32 v23, v23, v197\n"
    "v_mfma_f32_16x16x32_bf16 v[192:195], a[252:255], v[140:143], v[192:195]\n"
    "s_add_u32 s58, s58, 0\n"
    "v_add_f32 v23, v23, v198\n"
    "v_cvt_pk_bf16_f32 v212, v196, v197\n"
    "v_mfma_f32_16x16x32_bf16 a[84:87], v[28:31], v[228:231], a[84:87]\n"
    "s_add_u32 s34, s34, s57\n"
    "v_cvt_pk_bf16_f32 v213, v198, v199\n"
    "v_add_f32 v23, v23, v199\n"
    "v_mfma_f32_16x16x32_bf16 a[88:91], v[28:31], v[232:235], a[88:91]\n"
    "s_add_u32 m0, s34, 0\n"
    "v_exp_f32 v184, v184\n"
    "v_mfma_f32_16x16x32_bf16 a[92:95], v[28:31], v[236:239], a[92:95]\n"
    "s_add_u32 s34, s13, 4096\n"
    "v_exp_f32 v185, v185\n"
    "v_mfma_f32_16x16x32_bf16 a[96:99], v[28:31], v[240:243], a[96:99]\n"
    "s_and_b32 s34, s34, 0x1fff\n"
    "v_exp_f32 v186, v186\n"
    "v_mfma_f32_16x16x32_bf16 a[100:103], v[28:31], v[244:247], a[100:103]\n"
    "v_add_f32 v24, v24, v184\n"
    "v_add_f32 v24, v24, v185\n"
    "v_mfma_f32_16x16x32_bf16 a[104:107], v[28:31], v[248:251], a[104:107]\n"
    "v_exp_f32 v187, v187\n"
    "v_mfma_f32_16x16x32_bf16 a[108:111], v[28:31], v[252:255], a[108:111]\n"
    "ds_read_b64_tr_b16 v[28:29], v18 offset:768\n"
    "v_add_f32 v24, v24, v186\n"
    "v_mfma_f32_16x16x32_bf16 v[196:199], a[224:227], v[32:35], v[156:159]\n"
    "buffer_load_dwordx4 v7, s[40:43], s58 offen lds\n"
    "v_mfma_f32_16x16x32_bf16 v[196:199], a[228:231], v[36:39], v[196:199]\n"
    "s_lshl2_add_u32 s58, s34, s35\n"
    "ds_read_b64_tr_b16 v[30:31], v18 offset:4864\n"
    "v_cvt_pk_bf16_f32 v216, v184, v185\n"
    "v_mfma_f32_16x16x32_bf16 v[196:199], a[232:235], v[40:43], v[196:199]\n"
    "s_add_u32 s58, s58, 0\n"
    "v_cvt_pk_bf16_f32 v217, v186, v187\n"
    "v_add_f32 v24, v24, v187\n"
    "v_mfma_f32_16x16x32_bf16 v[196:199], a[236:239], v[44:47], v[196:199]\n"
    "s_add_u32 s34, s34, s57\n"
    "v_exp_f32 v188, v188\n"
    "v_mfma_f32_16x16x32_bf16 v[184:187], a[236:239], v[60:63], v[160:163]\n"
    "s_add_u32 m0, s34, 0\n"
    "v_exp_f32 v189, v189\n"
    "v_mfma_f32_16x16x32_bf16 v[184:187], a[232:235], v[56:59], v[184:187]\n"
    "s_add_u32 s34, s13, 6144\n"
    "v_exp_f32 v190, v190\n"
    "v_mfma_f32_16x16x32_bf16 v[184:187], a[228:231], v[52:55], v[184:187]\n"
    "s_memrealtime s[24:25]\n"
    "v_add_f32 v25, v25, v188\n"
    "v_add_f32 v25, v25, v189\n"
    "v_mfma_f32_16x16x32_bf16 v[184:187], a[224:227], v[48:51], v[184:187]\n"
    "s_and_b32 s34, s34, 0x1fff\n"
    "v_exp_f32 v191, v191\n"
    "v_mfma_f32_16x16x32_bf16 a[112:115], v[144:147], v[228:231], a[112:115]\n"
    "v_add_f32 v25, v25, v190\n"
    "v_cvt_pk_bf16_f32 v220, v188, v189\n"
    "v_mfma_f32_16x16x32_bf16 a[116:119], v[144:147], v[232:235], a[116:119]\n"
    "v_cvt_pk_bf16_f32 v221, v190, v191\n"
    "v_add_f32 v25, v25, v191\n"
    "v_mfma_f32_16x16x32_bf16 a[120:123], v[144:147], v[236:239], a[120:123]\n"
    "buffer_load_dwordx4 v7, s[40:43], s58 offen lds\n"
    "v_mfma_f32_16x16x32_bf16 a[124:127], v[144:147], v[240:243], a[124:127]\n"
    "s_lshl2_add_u32 s58, s34, s35\n"
    "v_exp_f32 v192, v192\n"
    "v_mfma_f32_16x16x32_bf16 a[128:131], v[144:147], v[244:247], a[128:131]\n"
    "s_add_u32 s58, s58, 0\n"
    "v_exp_f32 v193, v193\n"
    "v_mfma_f32_16x16x32_bf16 a[132:135], v[144:147], v[248:251], a[132:135]\n"
    "s_add_u32 s34, s34, s57\n"
    "v_exp_f32 v194, v194\n"
    "v_mfma_f32_16x16x32_bf16 a[136:139], v[144:147], v[252:255], a[136:139]\n"
    "s_add_u32 m0, s34, 0\n"
    "v_add_f32 v26, v26, v192\n"
    "v_add_f32 v26, v26, v193\n"
    "v_mfma_f32_16x16x32_bf16 v[188:191], a[224:227], v[64:67], v[164:167]\n"
    "s_add_u32 s34, s13, 0\n"
    "v_exp_f32 v195, v195\n"
    "v_mfma_f32_16x16x32_bf16 v[188:191], a[228:231], v[68:71], v[188:191]\n"
    "s_waitcnt vmcnt(7) lgkmcnt(0)\n"
    "s_barrier\n"
    "ds_read_b128 a[240:243], v13 offset:0\n"
    "v_mfma_f32_16x16x32_bf16 v[188:191], a[232:235], v[72:75], v[188:191]\n"
    "s_sub_i32 s25, s56, s24\n"
    "ds_read_b128 a[244:247], v13 offset:256\n"
    "v_add_f32 v26, v26, v194\n"
    "v_mfma_f32_16x16x32_bf16 v[188:191], a[236:239], v[76:79], v[188:191]\n"
    "s_max_i32 s25, s25, -10\n"
    "ds_read_b128 a[248:251], v13 offset:512\n"
    "v_cvt_pk_bf16_f32 v224, v192, v193\n"
    "v_cvt_pk_bf16_f32 v225, v194, v195\n"
    "v_add_f32 v26, v26, v195\n"
    "v_mfma_f32_16x16x32_bf16 v[192:195], a[236:239], v[92:95], v[168:171]\n"
    "s_min_i32 s25, s25, 62\n"
    "v_exp_f32 v196, v196\n"
    "v_mfma_f32_16x16x32_bf16 v[192:195], a[232:235], v[88:91], v[192:195]\n"
    "buffer_load_dwordx4 v7, s[40:43], s58 offen lds\n"
    "v_mfma_f32_16x16x32_bf16 v[192:195], a[228:231], v[84:87], v[192:195]\n"
    "s_add_u32 s56, s24, s25\n"
    "v_exp_f32 v197, v197\n"
    "v_mfma_f32_16x16x32_bf16 v[192:195], a[224:227], v[80:83], v[192:195]\n"
    "s_add_u32 s56, s56, 125\n"
    "v_exp_f32 v198, v198\n"
    "v_mfma_f32_16x16x32_bf16 a[140:143], v[148:151], v[228:231], a[140:143]\n"
    "s_cmp_gt_i32 s25, 2\ns_cbranch_scc1 L_ta14_%=\nL_tb14_%=:\n"
    "ds_read_b128 a[252:255], v13 offset:768\n"
    "v_add_f32 v20, v20, v196\n"
    "v_mfma_f32_16x16x32_bf16 a[144:147], v[148:151], v[232:235], a[144:147]\n"
    "s_and_b32 s34, s34, 0x1fff\n"
    "v_exp_f32 v199, v199\n"
    "v_mfma_f32_16x16x32_bf16 a[148:151], v[148:151], v[236:239], a[148:151]\n"
    "s_lshl2_add_u32 s58, s34, s35\n"
    "ds_read_b64_tr_b16 v[144:145], v15 offset:32768\n"
    "v_add_f32 v20, v20, v197\n"
    "v_mfma_f32_16x16x32_bf16 a[152:155], v[148:151], v[240:243], a[152:155]\n"
    "s_add_u32 s58, s58, 32768\n"
    "ds_read_b64_tr_b16 v[146:147], v15 offset:36864\n"
    "v_add_f32 v20, v20, v198\n"
    "v_mfma_f32_16x16x32_bf16 a[156:159], v[148:151], v[244:247], a[156:159]\n"
    "s_add_u32 s34, s34, s57\n"
    "v_cvt_pk_bf16_f32 v202, v196, v197\n"
    "v_cvt_pk_bf16_f32 v203, v198, v199\n"
    "v_mfma_f32_16x16x32_bf16 a[160:163], v[148:151], v[248:251], a[160:163]\n"
    "s_add_u32 m0, s34, 8192\n"
    "v_add_f32 v20, v20, v199\n"
    "v_lshrrev_b32 v12, 9, v1\n"
    "v_mfma_f32_16x16x32_bf16 a[164:167], v[148:151], v[252:255], a[164:167]\n"
    "s_add_u32 s34, s13, 2048\n"
    "v_exp_f32 v184, v184\n"
    "v_mfma_f32_16x16x32_bf16 v[196:199], a[224:227], v[96:99], v[172:175]\n"
    "buffer_load_dwordx4 v7, s[40:43], s58 offen lds\n"
    "v_mfma_f32_16x16x32_bf16 v[196:199], a[228:231], v[100:103], v[196:199]\n"
    "s_and_b32 s34, s34, 0x1fff\n"
    "v_exp_f32 v185, v185\n"
    "v_mfma_f32_16x16x32_bf16 v[196:199], a[232:235], v[104:107], v[196:199]\n"
    "s_lshl2_add_u32 s58, s34, s35\n"
    "v_exp_f32 v186, v186\n"
    "v_add_f32 v21, v21, v184\n"
    "v_exp_f32 v187, v187\n"
    "v_add_f32 v21, v21, v185\n"
    "v_mfma_f32_16x16x32_bf16 v[196:199], a[236:239], v[108:111], v[196:199]\n"
    "s_add_u32 s58, s58, 32768\n"
    "ds_read_b64_tr_b16 v[148:149], v18 offset:32768\n"
    "v_add_f32 v21, v21, v186\n"
    "v_cvt_pk_bf16_f32 v206, v184, v185\n"
    "v_cvt_pk_bf16_f32 v207, v186, v187\n"
    "v_add_f32 v21, v21, v187\n"
    "v_mfma_f32_16x16x32_bf16 v[184:187], a[236:239], v[124:127], v[176:179]\n"
    "s_add_u32 s34, s34, s57\n"
    "v_exp_f32 v188, v188\n"
    "v_mfma_f32_16x16x32_bf16 v[184:187], a[232:235], v[120:123], v[184:187]\n"
    "s_add_u32 m0, s34, 8192\n"
    "v_exp_f32 v189, v189\n"
    "v_mfma_f32_16x16x32_bf16 v[184:187], a[228:231], v[116:119], v[184:187]\n"
    "s_add_u32 s34, s13, 4096\n"
    "v_exp_f32 v190, v190\n"
    "v_mfma_f32_16x16x32_bf16 v[184:187], a[224:227], v[112:115], v[184:187]\n"
    "s_and_b32 s34, s34, 0x1fff\n"
    "ds_read_b64_tr_b16 v[150:151], v18 offset:36864\n"
    "v_add_f32 v22, v22, v188\n"
    "v_mfma_f32_16x16x32_bf16 a[168:171], v[152:155], v[228:231], a[168:171]\n"
    "v_exp_f32 v191, v191\n"
    "v_mfma_f32_16x16x32_bf16 a[172:175], v[152:155], v[232:235], a[172:175]\n"
    "v_add_f32 v22, v22, v189\n"
    "v_add_f32 v22, v22, v190\n"
    "v_mfma_f32_16x16x32_bf16 a[176:179], v[152:155], v[236:239], a[176:179]\n"
    "buffer_load_dwordx4 v7, s[40:43], s58 offen lds\n"
    "v_mfma_f32_16x16x32_bf16 a[180:183], v[152:155], v[240:243], a[180:183]\n"
    "s_lshl2_add_u32 s58, s34, s35\n"
    "v_cvt_pk_bf16_f32 v210, v188, v189\n"
    "v_cvt_pk_bf16_f32 v211, v190, v191\n"
    "v_mfma_f32_16x16x32_bf16 a[184:187], v[152:155], v[244:247], a[184:187]\n"
    "s_add_u32 s58, s58, 32768\n"
    "v_add_f32 v22, v22, v191\n"
    "v_lshrrev_b32 v9, 1, v12\n"
    "v_mfma_f32_16x16x32_bf16 a[188:191], v[152:155], v[248:251], a[188:191]\n"
    "s_add_u32 s34, s34, s57\n"
    "v_exp_f32 v192, v192\n"
    "v_mfma_f32_16x16x32_bf16 a[192:195], v[152:155], v[252:255], a[192:195]\n"
    "s_add_u32 m0, s34, 8192\n"
    "v_exp_f32 v193, v193\n"
    "v_mfma_f32_16x16x32_bf16 v[188:191], a[224:227], v[128:131], v[180:183]\n"
    "s_add_u32 s34, s13, 6144\n"
    "v_exp_f32 v194, v194\n"
    "v_mfma_f32_16x16x32_bf16 v[188:191], a[228:231], v[132:135], v[188:191]\n"
    "s_and_b32 s34, s34, 0x1fff\n"
    "ds_read_b64_tr_b16 v[152:153], v15 offset:33024\n"
    "v_add_f32 v23, v23, v192\n"
    "v_mfma_f32_16x16x32_bf16 v[188:191], a[232:235], v[136:139], v[188:191]\n"
    "v_exp_f32 v195, v195\n"
    "v_mfma_f32_16x16x32_bf16 v[188:191], a[236:239], v[140:143], v[188:191]\n"
    "ds_read_b64_tr_b16 v[154:155], v15 offset:37120\n"
    "v_add_f32 v23, v23, v193\n"
    "v_mfma_f32_16x16x32_bf16 a[196:199], v[28:31], v[228:231], a[196:199]\n"
    "ds_read_b128 a[224:227], v13 offset:4096\n"
    "v_add_f32 v23, v23, v194\n"
    "v_mfma_f32_16x16x32_bf16 a[200:203], v[28:31], v[232:235], a[200:203]\n"
    "buffer_load_dwordx4 v7, s[40:43], s58 offen lds\n"
    "v_mfma_f32_16x16x32_bf16 a[204:207], v[28:31], v[236:239], a[204:207]\n"
    "s_lshl2_add_u32 s58, s34, s35\n"
    "ds_read_b128 a[228:231], v13 offset:4352\n"
    "v_cvt_pk_bf16_f32 v214, v192, v193\n"
    "v_mfma_f32_16x16x32_bf16 a[208:211], v[28:31], v[240:243], a[208:211]\n"
    "s_add_u32 s58, s58, 32768\n"
    "ds_read_b128 a[232:235], v13 offset:4608\n"
    "v_cvt_pk_bf16_f32 v215, v194, v195\n"
    "v_mfma_f32_16x16x32_bf16 a[212:215], v[28:31], v[244:247], a[212:215]\n"
    "s_add_u32 s34, s34, s57\n"
    "ds_read_b128 a[236:239], v13 offset:4864\n"
    "v_add_f32 v23, v23, v195\n"
    "v_mfma_f32_16x16x32_bf16 a[216:219], v[28:31], v[248:251], a[216:219]\n"
    "s_add_u32 m0, s34, 8192\n"
    "v_exp_f32 v196, v196\n"
    "v_mfma_f32_16x16x32_bf16 a[220:223], v[28:31], v[252:255], a[220:223]\n"
    "s_waitcnt lgkmcnt(6)\n"
    "v_exp_f32 v197, v197\n"
    "v_mfma_f32_16x16x32_bf16 v[192:195], a[240:243], v[32:35], v[156:159]\n"
    "s_add_u32 s34, s13, 0\n"
    "v_exp_f32 v198, v198\n"
    "v_mfma_f32_16x16x32_bf16 v[192:195], a[244:247], v[36:39], v[192:195]\n"
    "s_and_b32 s34, s34, 0x1fff\n"
    "ds_read_b64_tr_b16 v[28:29], v18 offset:33024\n"
    "v_add_f32 v24, v24, v196\n"
    "v_mfma_f32_16x16x32_bf16 v[192:195], a[248:251], v[40:43], v[192:195]\n"
    "v_exp_f32 v199, v199\n"
    "v_add_f32 v24, v24, v197\n"
    "v_mfma_f32_16x16x32_bf16 v[192:195], a[252:255], v[44:47], v[192:195]\n"
    "ds_read_b64_tr_b16 v[30:31], v18 offset:37120\n"
    "v_add_f32 v24, v24, v198\n"
    "v_cvt_pk_bf16_f32 v218, v196, v197\n"
    "v_cvt_pk_bf16_f32 v219, v198, v199\n"
    "v_add_f32 v24, v24, v199\n"
    "v_mfma_f32_16x16x32_bf16 v[196:199], a[252:255], v[60:63], v[160:163]\n"
    "buffer_load_dwordx4 v7, s[40:43], s58 offen lds\n"
    "v_mfma_f32_16x16x32_bf16 v[196:199], a[248:251], v[56:59], v[196:199]\n"
    "s_lshl2_add_u32 s58, s34, s35\n"
    "v_exp_f32 v184, v184\n"
    "v_mfma_f32_16x16x32_bf16 v[196:199], a[244:247], v[52:55], v[196:199]\n"
    "s_add_u32 s58, s58, 65536\n"
    "v_exp_f32 v185, v185\n"
    "v_mfma_f32_16x16x32_bf16 v[196:199], a[240:243], v[48:51], v[196:199]\n"
    "s_add_u32 s34, s34, s57\n"
    "v_exp_f32 v186, v186\n"
    "v_mfma_f32_16x16x32_bf16 a[0:3], v[144:147], v[200:203], a[0:3]\n"
    "s_add_u32 m0, s34, 32768\n"
    "v_exp_f32 v187, v187\n"
    "v_mfma_f32_16x16x32_bf16 a[4:7], v[144:147], v[204:207], a[4:7]\n"
    "s_add_u32 s34, s13, 2048\n"
    "v_exp_f32 v188, v188\n"
    "v_mfma_f32_16x16x32_bf16 a[8:11], v[144:147], v[208:211], a[8:11]\n"
    "s_and_b32 s34, s34, 0x1fff\n"
    "v_exp_f32 v189, v189\n"
    "v_mfma_f32_16x16x32_bf16 a[12:15], v[144:147], v[212:215], a[12:15]\n"
    "v_exp_f32 v190, v190\n"
    "v_cvt_pk_bf16_f32 v222, v184, v185\n"
    "v_cvt_pk_bf16_f32 v223, v186, v187\n"
    "v_exp_f32 v191, v191\n"
    "v_mfma_f32_16x16x32_bf16 a[16:19], v[144:147], v[216:219], a[16:19]\n"
    "v_cvt_pk_bf16_f32 v226, v188, v189\n"
    "v_cvt_pk_bf16_f32 v227, v190, v191\n"
    "v_mfma_f32_16x16x32_bf16 a[20:23], v[144:147], v[220:223], a[20:23]\n"
    "v_add_f32 v25, v25, v184\n"
    "v_add_f32 v25, v25, v185\n"
    "v_mfma_f32_16x16x32_bf16 a[24:27], v[144:147], v[224:227], a[24:27]\n"
    "v_add_f32 v25, v25, v186\n"
    "v_add_f32 v25, v25, v187\n"
    "v_mfma_f32_16x16x32_bf16 v[184:187], a[240:243], v[64:67], v[164:167]\n"
    "buffer_load_dwordx4 v7, s[40:43], s58 offen lds\n"
    "v_mfma_f32_16x16x32_bf16 v[184:187], a[244:247], v[68:71], v[184:187]\n"
    "s_lshl2_add_u32 s58, s34, s35\n"
    "ds_read_b64_tr_b16 v[144:145], v15 offset:33280\n"
    "v_add_f32 v26, v26, v188\n"
    "v_mfma_f32_16x16x32_bf16 v[184:187], a[248:251], v[72:75], v[184:187]\n"
    "s_add_u32 s58, s58, 65536\n"
    "ds_read_b64_tr_b16 v[146:147], v15 offset:37376\n"
    "v_add_f32 v26, v26, v189\n"
    "v_mfma_f32_16x16x32_bf16 v[184:187], a[252:255], v[76:79], v[184:187]\n"
    "s_add_u32 s34, s34, s57\n"
    "v_add_f32 v26, v26, v190\n"
    "v_add_f32 v26, v26, v191\n"
    "v_mfma_f32_16x16x32_bf16 v[188:191], a[252:255], v[92:95], v[168:171]\n"
    "s_add_u32 m0, s34, 32768\n"
    "v_exp_f32 v192, v192\n"
    "v_mfma_f32_16x16x32_bf16 v[188:191], a[248:251], v[88:91], v[188:191]\n"
    "s_add_u32 s34, s13, 4096\n"
    "v_exp_f32 v193, v193\n"
    "v_mfma_f32_16x16x32_bf16 v[188:191], a[244:247], v[84:87], v[188:191]\n"
    "s_and_b32 s34, s34, 0x1fff\n"
    "v_exp_f32 v194, v194\n"
    "v_mfma_f32_16x16x32_bf16 v[188:191], a[240:243], v[80:83], v[188:191]\n"
    "v_add_f32 v20, v20, v192\n"
    "v_add_f32 v20, v20, v193\n"
    "v_mfma_f32_16x16x32_bf16 a[28:31], v[148:151], v[200:203], a[28:31]\n"
    "v_exp_f32 v195, v195\n"
    "v_mfma_f32_16x16x32_bf16 a[32:35], v[148:151], v[204:207], a[32:35]\n"
    "buffer_load_dwordx4 v7, s[40:43], s58 offen lds\n"
    "v_mfma_f32_16x16x32_bf16 a[36:39], v[148:151], v[208:211], a[36:39]\n"
    "s_lshl2_add_u32 s58, s34, s35\n"
    "v_add_f32 v20, v20, v194\n"
    "v_cvt_pk_bf16_f32 v228, v192, v193\n"
    "v_mfma_f32_16x16x32_bf16 a[40:43], v[148:151], v[212:215], a[40:43]\n"
    "s_add_u32 s58, s58, 65536\n"
    "v_cvt_pk_bf16_f32 v229, v194, v195\n"
    "v_add_f32 v20, v20, v195\n"
    "v_mfma_f32_16x16x32_bf16 a[44:47], v[148:151], v[216:219], a[44:47]\n"
    "s_add_u32 s34, s34, s57\n"
    "v_exp_f32 v196, v196\n"
    "v_mfma_f32_16x16x32_bf16 a[48:51], v[148:151], v[220:223], a[48:51]\n"
    "s_add_u32 m0, s34, 32768\n"
    "v_exp_f32 v197, v197\n"
    "v_mfma_f32_16x16x32_bf16 a[52:55], v[148:151], v[224:227], a[52:55]\n"
    "s_add_u32 s34, s13, 6144\n"
    "v_exp_f32 v198, v198\n"
    "v_mfma_f32_16x16x32_bf16 v[192:195], a[240:243], v[96:99], v[172:175]\n"
    "s_and_b32 s34, s34, 0x1fff\n"
    "ds_read_b64_tr_b16 v[148:149], v18 offset:33280\n"
    "v_add_f32 v21, v21, v196\n"
    "v_mfma_f32_16x16x32_bf16 v[192:195], a[244:247], v[100:103], v[192:195]\n"
    "v_exp_f32 v199, v199\n"
    "v_mfma_f32_16x16x32_bf16 v[192:195], a[248:251], v[104:107], v[192:195]\n"
    "ds_read_b64_tr_b16 v[150:151], v18 offset:37376\n"
    "v_add_f32 v21, v21, v197\n"
    "v_mfma_f32_16x16x32_bf16 v[192:195], a[252:255], v[108:111], v[192:195]\n"
    "v_add_f32 v21, v21, v198\n"
    "v_cvt_pk_bf16_f32 v232, v196, v197\n"
    "v_cvt_pk_bf16_f32 v233, v198, v199\n"
    "v_add_f32 v21, v21, v199\n"
    "v_mfma_f32_16x16x32_bf16 v[196:199], a[252:255], v[124:127], v[176:179]\n"
    "buffer_load_dwordx4 v7, s[40:43], s58 offen lds\n"
    "v_mfma_f32_16x16x32_bf16 v[196:199], a[248:251], v[120:123], v[196:199]\n"
    "s_lshl2_add_u32 s58, s34, s35\n"
    "v_exp_f32 v184, v184\n"
    "v_mfma_f32_16x16x32_bf16 v[196:199], a[244:247], v[116:119], v[196:199]\n"
    "s_add_u32 s58, s58, 65536\n"
    "v_exp_f32 v185, v185\n"
    "v_mfma_f32_16x16x32_bf16 v[196:199], a[240:243], v[112:115], v[196:199]\n"
    "s_waitcnt lgkmcnt(2)\n"
    "v_exp_f32 v186, v186\n"
    "v_mfma_f32_16x16x32_bf16 a[56:59], v[152:155], v[200:203], a[56:59]\n"
    "s_add_u32 s34, s34, s57\n"
    "v_add_f32 v22, v22, v184\n"
    "v_add_f32 v22, v22, v185\n"
    "v_mfma_f32_16x16x32_bf16 a[60:63], v[152:155], v[204:207], a[60:63]\n"
    "s_add_u32 m0, s34, 32768\n"
    "v_exp_f32 v187, v187\n"
    "v_mfma_f32_16x16x32_bf16 a[64:67], v[152:155], v[208:211], a[64:67]\n"
    "s_add_u32 s34, s13, 0\n"
    "v_add_f32 v22, v22, v186\n"
    "v_cvt_pk_bf16_f32 v236, v184, v185\n"
    "v_mfma_f32_16x16x32_bf16 a[68:71], v[152:155], v[212:215], a[68:71]\n"
    "s_and_b32 s34, s34, 0x1fff\n"
    "v_cvt_pk_bf16_f32 v237, v186, v187\n"
    "v_add_f32 v22, v22, v187\n"
    "v_mfma_f32_16x16x32_bf16 a[72:75], v[152:155], v[216:219], a[72:75]\n"
    "v_exp_f32 v188, v188\n"
    "v_mfma_f32_16x16x32_bf16 a[76:79], v[152:155], v[220:223], a[76:79]\n"
    "v_exp_f32 v189, v189\n"
    "v_mfma_f32_16x16x32_bf16 a[80:83], v[152:155], v[224:227], a[80:83]\n"
    "buffer_load_dwordx4 v7, s[40:43], s58 offen lds\n"
    "v_mfma_f32_16x16x32_bf16 v[184:187], a[240:243], v[128:131], v[180:183]\n"
    "s_lshl2_add_u32 s58, s34, s35\n"
    "v_exp_f32 v190, v190\n"
    "v_mfma_f32_16x16x32_bf16 v[184:187], a[244:247], v[132:135], v[184:187]\n"
    "s_add_u32 s58, s58, 98304\n"
    "ds_read_b64_tr_b16 v[152:153], v15 offset:33536\n"
    "v_add_f32 v23, v23, v188\n"
    "v_mfma_f32_16x16x32_bf16 v[184:187], a[248:251], v[136:139], v[184:187]\n"
    "s_add_u32 s34, s34, s57\n"
    "v_exp_f32 v191, v191\n"
    "v_mfma_f32_16x16x32_bf16 v[184:187], a[252:255], v[140:143], v[184:187]\n"
    "s_add_u32 m0, s34, 40960\n"
    "ds_read_b64_tr_b16 v[154:155], v15 offset:37632\n"
    "v_add_f32 v23, v23, v189\n"
    "v_mfma_f32_16x16x32_bf16 a[84:87], v[28:31], v[200:203], a[84:87]\n"
    "s_add_u32 s34, s13, 2048\n"
    "v_add_f32 v23, v23, v190\n"
    "v_cvt_pk_bf16_f32 v240, v188, v189\n"
    "v_mfma_f32_16x16x32_bf16 a[88:91], v[28:31], v[204:207], a[88:91]\n"
    "s_and_b32 s34, s34, 0x1fff\n"
    "v_cvt_pk_bf16_f32 v241, v190, v191\n"
    "v_add_f32 v23, v23, v191\n"
    "v_mfma_f32_16x16x32_bf16 a[92:95], v[28:31], v[208:211], a[92:95]\n"
    "v_exp_f32 v192, v192\n"
    "v_mfma_f32_16x16x32_bf16 a[96:99], v[28:31], v[212:215], a[96:99]\n"
    "v_exp_f32 v193, v193\n"
    "v_mfma_f32_16x16x32_bf16 a[100:103], v[28:31], v[216:219], a[100:103]\n"
    "v_exp_f32 v194, v194\n"
    "v_mfma_f32_16x16x32_bf16 a[104:107], v[28:31], v[220:223], a[104:107]\n"
    "buffer_load_dwordx4 v7, s[40:43], s58 offen lds\n"
    "v_mfma_f32_16x16x32_bf16 a[108:111], v[28:31], v[224:227], a[108:111]\n"
    "s_lshl2_add_u32 s58, s34, s35\n"
    "ds_read_b64_tr_b16 v[28:29], v18 offset:33536\n"
    "v_add_f32 v24, v24, v192\n"
    "v_mfma_f32_16x16x32_bf16 v[188:191], a[224:227], v[32:35], v[156:159]\n"
    "s_add_u32 s58, s58, 98304\n"
    "v_exp_f32 v195, v195\n"
    "v_mfma_f32_16x16x32_bf16 v[188:191], a[228:231], v[36:39], v[188:191]\n"
    "s_add_u32 s34, s34, s57\n"
    "ds_read_b64_tr_b16 v[30:31], v18 offset:37632\n"
    "v_add_f32 v24, v24, v193\n"
    "v_mfma_f32_16x16x32_bf16 v[188:191], a[232:235], v[40:43], v[188:191]\n"
    "s_add_u32 m0, s34, 40960\n"
    "v_add_f32 v24, v24, v194\n"
    "v_cvt_pk_bf16_f32 v244, v192, v193\n"
    "v_mfma_f32_16x16x32_bf16 v[188:191], a[236:239], v[44:47], v[188:191]\n"
    "buffer_load_dword v4, v2, s[68:71], 0 offen offset:128 sc1\n"
    "v_cvt_pk_bf16_f32 v245, v194, v195\n"
    "v_add_f32 v24, v24, v195\n"
    "v_mfma_f32_16x16x32_bf16 v[192:195], a[236:239], v[60:63], v[160:163]\n"
    "s_add_u32 s34, s13, 4096\n"
    "v_exp_f32 v196, v196\n"
    "v_mfma_f32_16x16x32_bf16 v[192:195], a[232:235], v[56:59], v[192:195]\n"
    "s_and_b32 s34, s34, 0x1fff\n"
    "v_exp_f32 v197, v197\n"
    "v_mfma_f32_16x16x32_bf16 v[192:195], a[228:231], v[52:55], v[192:195]\n"
    "s_memrealtime s[24:25]\n"
    "v_exp_f32 v198, v198\n"
    "v_mfma_f32_16x16x32_bf16 v[192:195], a[224:227], v[48:51], v[192:195]\n"
    "v_add_f32 v25, v25, v196\n"
    "v_add_f32 v25, v25, v197\n"
    "v_mfma_f32_16x16x32_bf16 a[112:115], v[144:147], v[200:203], a[112:115]\n"
    "buffer_load_dwordx4 v7, s[40:43], s58 offen lds\n"
    "v_mfma_f32_16x16x32_bf16 a[116:119], v[144:147], v[204:207], a[116:119]\n"
    "s_lshl2_add_u32 s58, s34, s35\n"
    "v_exp_f32 v199, v199\n"
    "v_mfma_f32_16x16x32_bf16 a[120:123], v[144:147], v[208:211], a[120:123]\n"
    "s_add_u32 s58, s58, 98304\n"
    "v_add_f32 v25, v25, v198\n"
    "v_cvt_pk_bf16_f32 v248, v196, v197\n"
    "v_mfma_f32_16x16x32_bf16 a[124:127], v[144:147], v[212:215], a[124:127]\n"
    "s_add_u32 s34, s34, s57\n"
    "v_cvt_pk_bf16_f32 v249, v198, v199\n"
    "v_add_f32 v25, v25, v199\n"
    "v_mfma_f32_16x16x32_bf16 a[128:131], v[144:147], v[216:219], a[128:131]\n"
    "s_add_u32 m0, s34, 40960\n"
    "v_exp_f32 v184, v184\n"
    "v_mfma_f32_16x16x32_bf16 a[132:135], v[144:147], v[220:223], a[132:135]\n"
    "s_add_u32 s34, s13, 6144\n"
    "v_exp_f32 v185, v185\n"
    "v_mfma_f32_16x16x32_bf16 a[136:139], v[144:147], v[224:227], a[136:139]\n"
    "s_and_b32 s34, s34, 0x1fff\n"
    "v_exp_f32 v186, v186\n"
    "v_mfma_f32_16x16x32_bf16 v[196:199], a[224:227], v[64:67], v[164:167]\n"
    "v_add_f32 v26, v26, v184\n"
    "v_add_f32 v26, v26, v185\n"
    "v_mfma_f32_16x16x32_bf16 v[196:199], a[228:231], v[68:71], v[196:199]\n"
    "s_waitcnt vmcnt(15) lgkmcnt(0)\n"
    "s_barrier\n"
    "ds_read_b64_tr_b16 v[144:145], v16 offset:0\n"
    "v_mfma_f32_16x16x32_bf16 v[196:199], a[232:235], v[72:75], v[196:199]\n"
    "s_sub_i32 s25, s56, s24\n"
    "v_exp_f32 v187, v187\n"
    "v_mfma_f32_16x16x32_bf16 v[196:199], a[236:239], v[76:79], v[196:199]\n"
    "s_max_i32 s25, s25, -10\n"
    "ds_read_b64_tr_b16 v[146:147], v16 offset:4096\n"
    "v_add_f32 v26, v26, v186\n"
    "v_cvt_pk_bf16_f32 v252, v184, v185\n"
    "v_cvt_pk_bf16_f32 v253, v186, v187\n"
    "v_add_f32 v26, v26, v187\n"
    "v_mfma_f32_16x16x32_bf16 v[184:187], a[236:239], v[92:95], v[168:171]\n"
    "buffer_load_dwordx4 v7, s[40:43], s58 offen lds\n"
    "v_mfma_f32_16x16x32_bf16 v[184:187], a[232:235], v[88:91], v[184:187]\n"
    "s_min_i32 s25, s25, 62\n"
    "v_exp_f32 v188, v188\n"
    "v_mfma_f32_16x16x32_bf16 v[184:187], a[228:231], v[84:87], v[184:187]\n"
    "s_add_u32 s56, s24, s25\n"
    "v_exp_f32 v189, v189\n"
    "v_mfma_f32_16x16x32_bf16 v[184:187], a[224:227], v[80:83], v[184:187]\n"
    "s_add_u32 s56, s56, 125\n"
    "v_exp_f32 v190, v190\n"
    "v_mfma_f32_16x16x32_bf16 a[140:143], v[148:151], v[200:203], a[140:143]\n"
    "s_cmp_gt_i32 s25, 2\ns_cbranch_scc1 L_ta15_%=\nL_tb15_%=:\n"
    "v_add_f32 v20, v20, v188\n"
    "v_add_f32 v20, v20, v189\n"
    "v_mfma_f32_16x16x32_bf16 a[144:147], v[148:151], v[204:207], a[144:147]\n"
    "s_lshl2_add_u32 s58, s34, s35\n"
    "v_exp_f32 v191, v191\n"
    "v_mfma_f32_16x16x32_bf16 a[148:151], v[148:151], v[208:211], a[148:151]\n"
    "s_add_u32 s58, s58, 98304\n"
    "v_add_f32 v20, v20, v190\n"
    "v_cvt_pk_bf16_f32 v230, v188, v189\n"
    "v_mfma_f32_16x16x32_bf16 a[152:155], v[148:151], v[212:215], a[152:155]\n"
    "s_add_u32 s34, s34, s57\n"
    "v_cvt_pk_bf16_f32 v231, v190, v191\n"
    "v_add_f32 v20, v20, v191\n"
    "v_mfma_f32_16x16x32_bf16 a[156:159], v[148:151], v[216:219], a[156:159]\n"
    "s_add_u32 m0, s34, 40960\n"
    "v_exp_f32 v192, v192\n"
    "v_mfma_f32_16x16x32_bf16 a[160:163], v[148:151], v[220:223], a[160:163]\n"
    "buffer_load_dwordx4 v7, s[40:43], s58 offen lds\n"
    "v_mfma_f32_16x16x32_bf16 a[164:167], v[148:151], v[224:227], a[164:167]\n"
    "s_add_u32 m0, s57, 65536\n"
    "v_exp_f32 v193, v193\n"
    "v_mfma_f32_16x16x32_bf16 v[188:191], a[224:227], v[96:99], v[172:175]\n"
    "s_add_u32 s58, s10, 0\n"
    "v_exp_f32 v194, v194\n"
    "v_mfma_f32_16x16x32_bf16 v[188:191], a[228:231], v[100:103], v[188:191]\n"
    "ds_read_b64_tr_b16 v[148:149], v19 offset:0\n"
    "v_add_f32 v21, v21, v192\n"
    "v_mfma_f32_16x16x32_bf16 v[188:191], a[232:235], v[104:107], v[188:191]\n"
    "v_exp_f32 v195, v195\n"
    "v_add_f32 v21, v21, v193\n"
    "v_mfma_f32_16x16x32_bf16 v[188:191], a[236:239], v[108:111], v[188:191]\n"
    "ds_read_b64_tr_b16 v[150:151], v19 offset:4096\n"
    "v_add_f32 v21, v21, v194\n"
    "v_cvt_pk_bf16_f32 v234, v192, v193\n"
    "v_cvt_pk_bf16_f32 v235, v194, v195\n"
    "v_add_f32 v21, v21, v195\n"
    "v_mfma_f32_16x16x32_bf16 v[192:195], a[236:239], v[124:127], v[176:179]\n"
    "v_exp_f32 v196, v196\n"
    "v_mfma_f32_16x16x32_bf16 v[192:195], a[232:235], v[120:123], v[192:195]\n"
    "v_exp_f32 v197, v197\n"
    "v_mfma_f32_16x16x32_bf16 v[192:195], a[228:231], v[116:119], v[192:195]\n"
    "v_exp_f32 v198, v198\n"
    "v_mfma_f32_16x16x32_bf16 v[192:195], a[224:227], v[112:115], v[192:195]\n"
    "v_add_f32 v22, v22, v196\n"
    "v_add_f32 v22, v22, v197\n"
    "v_mfma_f32_16x16x32_bf16 a[168:171], v[152:155], v[200:203], a[168:171]\n"
    "buffer_load_dwordx4 v7, s[40:43], s58 offen lds\n"
    "v_mfma_f32_16x16x32_bf16 a[172:175], v[152:155], v[204:207], a[172:175]\n"
    "s_add_u32 m0, s57, 67584\n"
    "v_exp_f32 v199, v199\n"
    "v_mfma_f32_16x16x32_bf16 a[176:179], v[152:155], v[208:211], a[176:179]\n"
    "s_add_u32 s58, s10, 8192\n"
    "v_add_f32 v22, v22, v198\n"
    "v_cvt_pk_bf16_f32 v238, v196, v197\n"
    "v_mfma_f32_16x16x32_bf16 a[180:183], v[152:155], v[212:215], a[180:183]\n"
    "v_cvt_pk_bf16_f32 v239, v198, v199\n"
    "v_add_f32 v22, v22, v199\n"
    "v_mfma_f32_16x16x32_bf16 a[184:187], v[152:155], v[216:219], a[184:187]\n"
    "v_exp_f32 v184, v184\n"
    "v_mfma_f32_16x16x32_bf16 a[188:191], v[152:155], v[220:223], a[188:191]\n"
    "buffer_load_dwordx4 v7, s[40:43], s58 offen lds\n"
    "v_mfma_f32_16x16x32_bf16 a[192:195], v[152:155], v[224:227], a[192:195]\n"
    "s_add_u32 m0, s57, 69632\n"
    "v_exp_f32 v185, v185\n"
    "v_mfma_f32_16x16x32_bf16 v[196:199], a[224:227], v[128:131], v[180:183]\n"
    "s_add_u32 s58, s10, 16384\n"
    "v_exp_f32 v186, v186\n"
    "v_mfma_f32_16x16x32_bf16 v[196:199], a[228:231], v[132:135], v[196:199]\n"
    "v_exp_f32 v187, v187\n"
    "v_mfma_f32_16x16x32_bf16 v[196:199], a[232:235], v[136:139], v[196:199]\n"
    "v_exp_f32 v188, v188\n"
    "v_mfma_f32_16x16x32_bf16 v[196:199], a[236:239], v[140:143], v[196:199]\n"
    "buffer_load_dwordx4 v7, s[40:43], s58 offen lds\n"
    "v_mfma_f32_16x16x32_bf16 a[196:199], v[28:31], v[200:203], a[196:199]\n"
    "s_add_u32 m0, s57, 71680\n"
    "v_exp_f32 v189, v189\n"
    "v_mfma_f32_16x16x32_bf16 a[200:203], v[28:31], v[204:207], a[200:203]\n"
    "s_add_u32 s58, s10, 24576\n"
    "v_exp_f32 v190, v190\n"
    "v_mfma_f32_16x16x32_bf16 a[204:207], v[28:31], v[208:211], a[204:207]\n"
    "v_exp_f32 v191, v191\n"
    "v_mfma_f32_16x16x32_bf16 a[208:211], v[28:31], v[212:215], a[208:211]\n"
    "ds_read_b64_tr_b16 v[152:153], v16 offset:256\n"
    "v_cvt_pk_bf16_f32 v242, v184, v185\n"
    "v_mfma_f32_16x16x32_bf16 a[212:215], v[28:31], v[216:219], a[212:215]\n"
    "buffer_load_dwordx4 v7, s[40:43], s58 offen lds\n"
    "v_mfma_f32_16x16x32_bf16 a[216:219], v[28:31], v[220:223], a[216:219]\n"
    "s_add_u32 m0, s57, 73728\n"
    "ds_read_b64_tr_b16 v[154:155], v16 offset:4352\n"
    "v_cvt_pk_bf16_f32 v243, v186, v187\n"
    "v_mfma_f32_16x16x32_bf16 a[220:223], v[28:31], v[224:227], a[220:223]\n"
    "s_waitcnt lgkmcnt(2)\n"
    "v_exp_f32 v192, v192\n"
    "v_mfma_f32_16x16x32_bf16 a[0:3], v[144:147], v[228:231], a[0:3]\n"
    "s_add_u32 s58, s10, 32768\n"
    "v_exp_f32 v193, v193\n"
    "v_mfma_f32_16x16x32_bf16 a[4:7], v[144:147], v[232:235], a[4:7]\n"
    "v_exp_f32 v194, v194\n"
    "v_mfma_f32_16x16x32_bf16 a[8:11], v[144:147], v[236:239], a[8:11]\n"
    "v_exp_f32 v195, v195\n"
    "v_cvt_pk_bf16_f32 v246, v188, v189\n"
    "v_cvt_pk_bf16_f32 v247, v190, v191\n"
    "v_mfma_f32_16x16x32_bf16 a[12:15], v[144:147], v[240:243], a[12:15]\n"
    "v_exp_f32 v196, v196\n"
    "v_cvt_pk_bf16_f32 v250, v192, v193\n"
    "v_cvt_pk_bf16_f32 v251, v194, v195\n"
    "v_exp_f32 v197, v197\n"
    "v_exp_f32 v198, v198\n"
    "v_exp_f32 v199, v199\n"
    "v_mfma_f32_16x16x32_bf16 a[16:19], v[144:147], v[244:247], a[16:19]\n"
    "ds_read_b64_tr_b16 v[28:29], v19 offset:256\n"
    "v_cvt_pk_bf16_f32 v254, v196, v197\n"
    "v_cvt_pk_bf16_f32 v255, v198, v199\n"
    "v_mfma_f32_16x16x32_bf16 a[20:23], v[144:147], v[248:251], a[20:23]\n"
    "buffer_load_dwordx4 v7, s[40:43], s58 offen lds\n"
    "v_mfma_f32_16x16x32_bf16 a[24:27], v[144:147], v[252:255], a[24:27]\n"
    "s_add_u32 m0, s57, 75776\n"
    "ds_read_b64_tr_b16 v[30:31], v19 offset:4352\n"
    "v_add_f32 v23, v23, v184\n"
    "v_mfma_f32_16x16x32_bf16 a[28:31], v[148:151], v[228:231], a[28:31]\n"
    "s_add_u32 s58, s10, 40960\n"
    "ds_read_b64_tr_b16 v[144:145], v16 offset:512\n"
    "v_add_f32 v24, v24, v188\n"
    "v_mfma_f32_16x16x32_bf16 a[32:35], v[148:151], v[232:235], a[32:35]\n"
    "ds_read_b64_tr_b16 v[146:147], v16 offset:4608\n"
    "v_add_f32 v23, v23, v185\n"
    "v_mfma_f32_16x16x32_bf16 a[36:39], v[148:151], v[236:239], a[36:39]\n"
    "v_add_f32 v24, v24, v189\n"
    "v_add_f32 v25, v25, v192\n"
    "v_mfma_f32_16x16x32_bf16 a[40:43], v[148:151], v[240:243], a[40:43]\n"
    "buffer_load_dwordx4 v7, s[40:43], s58 offen lds\n"
    "v_mfma_f32_16x16x32_bf16 a[44:47], v[148:151], v[244:247], a[44:47]\n"
    "s_add_u32 m0, s57, 77824\n"
    "v_add_f32 v26, v26, v196\n"
    "v_add_f32 v23, v23, v186\n"
    "v_mfma_f32_16x16x32_bf16 a[48:51], v[148:151], v[248:251], a[48:51]\n"
    "s_add_u32 s58, s10, 49152\n"
    "v_add_f32 v24, v24, v190\n"
    "v_add_f32 v25, v25, v193\n"
    "v_mfma_f32_16x16x32_bf16 a[52:55], v[148:151], v[252:255], a[52:55]\n"
    "s_waitcnt lgkmcnt(4)\n"
    "ds_read_b64_tr_b16 v[148:149], v19 offset:512\n"
    "v_add_f32 v26, v26, v197\n"
    "v_mfma_f32_16x16x32_bf16 a[56:59], v[152:155], v[228:231], a[56:59]\n"
    "ds_read_b64_tr_b16 v[150:151], v19 offset:4608\n"
    "v_add_f32 v23, v23, v187\n"
    "v_mfma_f32_16x16x32_bf16 a[60:63], v[152:155], v[232:235], a[60:63]\n"
    "buffer_load_dwordx4 v7, s[40:43], s58 offen lds\n"
    "v_mfma_f32_16x16x32_bf16 a[64:67], v[152:155], v[236:239], a[64:67]\n"
    "s_add_u32 m0, s57, 79872\n"
    "v_add_f32 v24, v24, v191\n"
    "v_add_f32 v25, v25, v194\n"
    "v_mfma_f32_16x16x32_bf16 a[68:71], v[152:155], v[240:243], a[68:71]\n"
    "s_add_u32 s58, s10, 57344\n"
    "v_add_f32 v26, v26, v198\n"
    "v_mov_b32 v156, v20\n"
    "v_mfma_f32_16x16x32_bf16 a[72:75], v[152:155], v[244:247], a[72:75]\n"
    "v_mov_b32 v160, v21\n"
    "v_mov_b32 v164, v22\n"
    "v_mfma_f32_16x16x32_bf16 a[76:79], v[152:155], v[248:251], a[76:79]\n"
    "v_add_f32 v25, v25, v195\n"
    "v_add_f32 v26, v26, v199\n"
    "v_mfma_f32_16x16x32_bf16 a[80:83], v[152:155], v[252:255], a[80:83]\n"
    "s_waitcnt lgkmcnt(2)\n"
    "ds_read_b64_tr_b16 v[152:153], v16 offset:768\n"
    "v_permlane32_swap_b32 v156, v20\n"
    "v_mfma_f32_16x16x32_bf16 a[84:87], v[28:31], v[228:231], a[84:87]\n"
    "buffer_load_dwordx4 v7, s[40:43], s58 offen lds\n"
    "v_mfma_f32_16x16x32_bf16 a[88:91], v[28:31], v[232:235], a[88:91]\n"
    "s_add_u32 m0, s57, 131072\n"
    "ds_read_b64_tr_b16 v[154:155], v16 offset:4864\n"
    "v_permlane32_swap_b32 v160, v21\n"
    "v_mfma_f32_16x16x32_bf16 a[92:95], v[28:31], v[236:239], a[92:95]\n"
    "s_add_u32 s58, s11, 0\n"
    "v_permlane32_swap_b32 v164, v22\n"
    "v_mov_b32 v168, v23\n"
    "v_mfma_f32_16x16x32_bf16 a[96:99], v[28:31], v[240:243], a[96:99]\n"
    "v_mov_b32 v172, v24\n"
    "v_add_f32 v20, v20, v156\n"
    "v_mfma_f32_16x16x32_bf16 a[100:103], v[28:31], v[244:247], a[100:103]\n"
    "v_add_f32 v21, v21, v160\n"
    "v_add_f32 v22, v22, v164\n"
    "v_mfma_f32_16x16x32_bf16 a[104:107], v[28:31], v[248:251], a[104:107]\n"
    "buffer_load_dwordx4 v7, s[40:43], s58 offen lds\n"
    "v_mfma_f32_16x16x32_bf16 a[108:111], v[28:31], v[252:255], a[108:111]\n"
    "s_add_u32 m0, s57, 133120\n"
    "ds_read_b64_tr_b16 v[28:29], v19 offset:768\n"
    "v_permlane32_swap_b32 v168, v23\n"
    "v_mfma_f32_16x16x32_bf16 a[112:115], v[144:147], v[228:231], a[112:115]\n"
    "s_add_u32 s58, s11, 8192\n"
    "ds_read_b64_tr_b16 v[30:31], v19 offset:4864\n"
    "v_permlane32_swap_b32 v172, v24\n"
    "v_mfma_f32_16x16x32_bf16 a[116:119], v[144:147], v[232:235], a[116:119]\n"
    "v_mov_b32 v176, v25\n"
    "v_mov_b32 v180, v26\n"
    "v_mfma_f32_16x16x32_bf16 a[120:123], v[144:147], v[236:239], a[120:123]\n"
    "v_mov_b32 v156, v20\n"
    "v_mov_b32 v160, v21\n"
    "v_mfma_f32_16x16x32_bf16 a[124:127], v[144:147], v[240:243], a[124:127]\n"
    "buffer_load_dwordx4 v7, s[40:43], s58 offen lds\n"
    "v_mfma_f32_16x16x32_bf16 a[128:131], v[144:147], v[244:247], a[128:131]\n"
    "s_add_u32 m0, s57, 135168\n"
    "v_mov_b32 v164, v22\n"
    "v_add_f32 v23, v23, v168\n"
    "v_mfma_f32_16x16x32_bf16 a[132:135], v[144:147], v[248:251], a[132:135]\n"
    "s_add_u32 s58, s11, 16384\n"
    "v_add_f32 v24, v24, v172\n"
    "v_permlane32_swap_b32 v176, v25\n"
    "v_mfma_f32_16x16x32_bf16 a[136:139], v[144:147], v[252:255], a[136:139]\n"
    "s_waitcnt lgkmcnt(2)\n"
    "v_permlane32_swap_b32 v180, v26\n"
    "v_permlane16_swap_b32 v156, v20\n"
    "v_mfma_f32_16x16x32_bf16 a[140:143], v[148:151], v[228:231], a[140:143]\n"
    "v_permlane16_swap_b32 v160, v21\n"
    "v_permlane16_swap_b32 v164, v22\n"
    "v_mfma_f32_16x16x32_bf16 a[144:147], v[148:151], v[232:235], a[144:147]\n"
    "buffer_load_dwordx4 v7, s[40:43], s58 offen lds\n"
    "v_mfma_f32_16x16x32_bf16 a[148:151], v[148:151], v[236:239], a[148:151]\n"
    "s_add_u32 m0, s57, 137216\n"
    "v_mov_b32 v168, v23\n"
    "v_mov_b32 v172, v24\n"
    "v_mfma_f32_16x16x32_bf16 a[152:155], v[148:151], v[240:243], a[152:155]\n"
    "s_add_u32 s58, s11, 24576\n"
    "v_add_f32 v25, v25, v176\n"
    "v_add_f32 v26, v26, v180\n"
    "v_mfma_f32_16x16x32_bf16 a[156:159], v[148:151], v[244:247], a[156:159]\n"
    "v_add_f32 v20, v20, v156\n"
    "v_add_f32 v21, v21, v160\n"
    "v_mfma_f32_16x16x32_bf16 a[160:163], v[148:151], v[248:251], a[160:163]\n"
    "v_add_f32 v22, v22, v164\n"
    "v_permlane16_swap_b32 v168, v23\n"
    "v_mfma_f32_16x16x32_bf16 a[164:167], v[148:151], v[252:255], a[164:167]\n"
    "buffer_load_dwordx4 v7, s[40:43], s58 offen lds\n"
    "v_mfma_f32_16x16x32_bf16 a[168:171], v[152:155], v[228:231], a[168:171]\n"
    "v_permlane16_swap_b32 v172, v24\n"
    "v_mov_b32 v176, v25\n"
    "v_mfma_f32_16x16x32_bf16 a[172:175], v[152:155], v[232:235], a[172:175]\n"
    "v_mov_b32 v180, v26\n"
    "v_add_f32 v23, v23, v168\n"
    "v_mfma_f32_16x16x32_bf16 a[176:179], v[152:155], v[236:239], a[176:179]\n"
    "v_add_f32 v24, v24, v172\n"
    "v_permlane16_swap_b32 v176, v25\n"
    "v_mfma_f32_16x16x32_bf16 a[180:183], v[152:155], v[240:243], a[180:183]\n"
    "v_permlane16_swap_b32 v180, v26\n"
    "v_max3_f32 v157, v20, v21, v22\n"
    "v_mfma_f32_16x16x32_bf16 a[184:187], v[152:155], v[244:247], a[184:187]\n"
    "v_min3_f32 v158, v20, v21, v22\n"
    "v_add_f32 v25, v25, v176\n"
    "v_mfma_f32_16x16x32_bf16 a[188:191], v[152:155], v[248:251], a[188:191]\n"
    "v_add_f32 v26, v26, v180\n"
    "v_max3_f32 v157, v157, v23, v24\n"
    "v_mfma_f32_16x16x32_bf16 a[192:195], v[152:155], v[252:255], a[192:195]\n"
    "s_waitcnt lgkmcnt(0)\n"
    "v_min3_f32 v158, v158, v23, v24\n"
    "v_max3_f32 v157, v157, v25, v26\n"
    "v_mfma_f32_16x16x32_bf16 a[196:199], v[28:31], v[228:231], a[196:199]\n"
    "v_min3_f32 v158, v158, v25, v26\n"
    "v_min_u32 v8, 1, v12\n"
    "v_mfma_f32_16x16x32_bf16 a[200:203], v[28:31], v[232:235], a[200:203]\n"
    "v_xor_b32 v10, 1, v9\n"
    "v_cmp_ngt_f32 s[66:67], s63, v157\n"
    "v_mfma_f32_16x16x32_bf16 a[204:207], v[28:31], v[236:239], a[204:207]\n"
    "v_rcp_f32 v20, v20\n"
    "v_mfma_f32_16x16x32_bf16 a[208:211], v[28:31], v[240:243], a[208:211]\n"
    "v_rcp_f32 v21, v21\n"
    "v_mfma_f32_16x16x32_bf16 a[212:215], v[28:31], v[244:247], a[212:215]\n"
    "v_rcp_f32 v22, v22\n"
    "v_mfma_f32_16x16x32_bf16 a[216:219], v[28:31], v[248:251], a[216:219]\n"
    "v_rcp_f32 v23, v23\n"
    "v_mfma_f32_16x16x32_bf16 a[220:223], v[28:31], v[252:255], a[220:223]\n"
    "v_rcp_f32 v24, v24\n"
    "v_cmp_nlt_f32 vcc, 0x27800000, v158\n"
    "s_nop 4\n"
    "s_or_b64 s[66:67], s[66:67], vcc\n"
    "v_rcp_f32 v25, v25\n"
    "v_rcp_f32 v26, v26\n"
    "v_lshrrev_b32 v27, 3, v1\n"
    "v_lshl_add_u32 v8, v8, 23, v3\n"
    "v_lshl_add_u32 v9, v9, 23, v3\n"
    "v_lshl_add_u32 v10, v10, 23, v3\n"
    "s_cmp_lg_u64 s[66:67], 0\n"
    "s_cbranch_scc1 L_slow_%=\n"
    "L_epi_%=:\n"
    "v_accvgpr_read_b32 v200, a0\n"
    "v_accvgpr_read_b32 v201, a1\n"
    "v_accvgpr_read_b32 v202, a2\n"
    "v_accvgpr_read_b32 v203, a3\n"
    "v_accvgpr_read_b32 v204, a28\n"
    "v_accvgpr_read_b32 v205, a29\n"
    "v_accvgpr_read_b32 v206, a30\n"
    "v_accvgpr_read_b32 v207, a31\n"
    "v_mul_f32 v200, v20, v200\n"
    "v_mul_f32 v201, v20, v201\n"
    "v_mul_f32 v202, v20, v202\n"
    "v_mul_f32 v203, v20, v203\n"
    "v_mul_f32 v204, v20, v204\n"
    "v_mul_f32 v205, v20, v205\n"
    "v_mul_f32 v206, v20, v206\n"
    "v_mul_f32 v207, v20, v207\n"
    "v_cvt_pk_bf16_f32 v32, v200, v201\n"
    "v_cvt_pk_bf16_f32 v33, v202, v203\n"
    "v_cvt_pk_bf16_f32 v34, v204, v205\n"
    "v_cvt_pk_bf16_f32 v35, v206, v207\n"
    "s_mov_b32 s64, 0\n"
    "buffer_store_dwordx4 v[32:35], v27, s[52:55], s64 offen sc1\n"
    "v_accvgpr_read_b32 v208, a56\n"
    "v_accvgpr_read_b32 v209, a57\n"
    "v_accvgpr_read_b32 v210, a58\n"
    "v_accvgpr_read_b32 v211, a59\n"
    "v_accvgpr_read_b32 v212, a84\n"
    "v_accvgpr_read_b32 v213, a85\n"
    "v_accvgpr_read_b32 v214, a86\n"
    "v_accvgpr_read_b32 v215, a87\n"
    "v_mul_f32 v208, v20, v208\n"
    "v_mul_f32 v209, v20, v209\n"
    "v_mul_f32 v210, v20, v210\n"
    "v_mul_f32 v211, v20, v211\n"
    "v_mul_f32 v212, v20, v212\n"
    "v_mul_f32 v213, v20, v213\n"
    "v_mul_f32 v214, v20, v214\n"
    "v_mul_f32 v215, v20, v215\n"
    "v_cvt_pk_bf16_f32 v60, v208, v209\n"
    "v_cvt_pk_bf16_f32 v61, v210, v211\n"
    "v_cvt_pk_bf16_f32 v62, v212, v213\n"
    "v_cvt_pk_bf16_f32 v63, v214, v215\n"
    "s_mov_b32 s64, 0\n"
    "buffer_store_dwordx4 v[60:63], v27, s[52:55], s64 offen offset:1024 sc1\n"
    "v_accvgpr_read_b32 v216, a4\n"
    "v_accvgpr_read_b32 v217, a5\n"
    "v_accvgpr_read_b32 v218, a6\n"
    "v_accvgpr_read_b32 v219, a7\n"
    "v_accvgpr_read_b32 v220, a32\n"
    "v_accvgpr_read_b32 v221, a33\n"
    "v_accvgpr_read_b32 v222, a34\n"
    "v_accvgpr_read_b32 v223, a35\n"
    "v_mul_f32 v216, v21, v216\n"
    "v_mul_f32 v217, v21, v217\n"
    "v_mul_f32 v218, v21, v218\n"
    "v_mul_f32 v219, v21, v219\n"
    "v_mul_f32 v220, v21, v220\n"
    "v_mul_f32 v221, v21, v221\n"
    "v_mul_f32 v222, v21, v222\n"
    "v_mul_f32 v223, v21, v223\n"
    "v_cvt_pk_bf16_f32 v36, v216, v217\n"
    "v_cvt_pk_bf16_f32 v37, v218, v219\n"
    "v_cvt_pk_bf16_f32 v38, v220, v221\n"
    "v_cvt_pk_bf16_f32 v39, v222, v223\n"
    "s_mov_b32 s64, 0\n"
    "buffer_store_dwordx4 v[36:39], v27, s[52:55], s64 offen offset:2048 sc1\n"
    "v_accvgpr_read_b32 v200, a60\n"
    "v_accvgpr_read_b32 v201, a61\n"
    "v_accvgpr_read_b32 v202, a62\n"
    "v_accvgpr_read_b32 v203, a63\n"
    "v_accvgpr_read_b32 v204, a88\n"
    "v_accvgpr_read_b32 v205, a89\n"
    "v_accvgpr_read_b32 v206, a90\n"
    "v_accvgpr_read_b32 v207, a91\n"
    "v_mul_f32 v200, v21, v200\n"
    "v_mul_f32 v201, v21, v201\n"
    "v_mul_f32 v202, v21, v202\n"
    "v_mul_f32 v203, v21, v203\n"
    "v_mul_f32 v204, v21, v204\n"
    "v_mul_f32 v205, v21, v205\n"
    "v_mul_f32 v206, v21, v206\n"
    "v_mul_f32 v207, v21, v207\n"
    "v_cvt_pk_bf16_f32 v64, v200, v201\n"
    "v_cvt_pk_bf16_f32 v65, v202, v203\n"
    "v_cvt_pk_bf16_f32 v66, v204, v205\n"
    "v_cvt_pk_bf16_f32 v67, v206, v207\n"
    "s_mov_b32 s64, 0\n"
    "buffer_store_dwordx4 v[64:67], v27, s[52:55], s64 offen offset:3072 sc1\n"
    "v_accvgpr_read_b32 v208, a8\n"
    "v_accvgpr_read_b32 v209, a9\n"
    "v_accvgpr_read_b32 v210, a10\n"
    "v_accvgpr_read_b32 v211, a11\n"
    "v_accvgpr_read_b32 v212, a36\n"
    "v_accvgpr_read_b32 v213, a37\n"
    "v_accvgpr_read_b32 v214, a38\n"
    "v_accvgpr_read_b32 v215, a39\n"
    "v_mul_f32 v208, v22, v208\n"
    "v_mul_f32 v209, v22, v209\n"
    "v_mul_f32 v210, v22, v210\n"
    "v_mul_f32 v211, v22, v211\n"
    "v_mul_f32 v212, v22, v212\n"
    "v_mul_f32 v213, v22, v213\n"
    "v_mul_f32 v214, v22, v214\n"
    "v_mul_f32 v215, v22, v215\n"
    "v_cvt_pk_bf16_f32 v40, v208, v209\n"
    "v_cvt_pk_bf16_f32 v41, v210, v211\n"
    "v_cvt_pk_bf16_f32 v42, v212, v213\n"
    "v_cvt_pk_bf16_f32 v43, v214, v215\n"
    "s_mov_b32 s64, 4096\n"
    "buffer_store_dwordx4 v[40:43], v27, s[52:55], s64 offen sc1\n"
    "v_accvgpr_read_b32 v216, a64\n"
    "v_accvgpr_read_b32 v217, a65\n"
    "v_accvgpr_read_b32 v218, a66\n"
    "v_accvgpr_read_b32 v219, a67\n"
    "v_accvgpr_read_b32 v220, a92\n"
    "v_accvgpr_read_b32 v221, a93\n"
    "v_accvgpr_read_b32 v222, a94\n"
    "v_accvgpr_read_b32 v223, a95\n"
    "v_mul_f32 v216, v22, v216\n"
    "v_mul_f32 v217, v22, v217\n"
    "v_mul_f32 v218, v22, v218\n"
    "v_mul_f32 v219, v22, v219\n"
    "v_mul_f32 v220, v22, v220\n"
    "v_mul_f32 v221, v22, v221\n"
    "v_mul_f32 v222, v22, v222\n"
    "v_mul_f32 v223, v22, v223\n"
    "v_cvt_pk_bf16_f32 v68, v216, v217\n"
    "v_cvt_pk_bf16_f32 v69, v218, v219\n"
    "v_cvt_pk_bf16_f32 v70, v220, v221\n"
    "v_cvt_pk_bf16_f32 v71, v222, v223\n"
    "s_mov_b32 s64, 4096\n"
    "buffer_store_dwordx4 v[68:71], v27, s[52:55], s64 offen offset:1024 sc1\n"
    "v_accvgpr_read_b32 v200, a12\n"
    "v_accvgpr_read_b32 v201, a13\n"
    "v_accvgpr_read_b32 v202, a14\n"
    "v_accvgpr_read_b32 v203, a15\n"
    "v_accvgpr_read_b32 v204, a40\n"
    "v_accvgpr_read_b32 v205, a41\n"
    "v_accvgpr_read_b32 v206, a42\n"
    "v_accvgpr_read_b32 v207, a43\n"
    "v_mul_f32 v200, v23, v200\n"
    "v_mul_f32 v201, v23, v201\n"
    "v_mul_f32 v202, v23, v202\n"
    "v_mul_f32 v203, v23, v203\n"
    "v_mul_f32 v204, v23, v204\n"
    "v_mul_f32 v205, v23, v205\n"
    "v_mul_f32 v206, v23, v206\n"
    "v_mul_f32 v207, v23, v207\n"
    "v_cvt_pk_bf16_f32 v44, v200, v201\n"
    "v_cvt_pk_bf16_f32 v45, v202, v203\n"
    "v_cvt_pk_bf16_f32 v46, v204, v205\n"
    "v_cvt_pk_bf16_f32 v47, v206, v207\n"
    "s_mov_b32 s64, 4096\n"
    "buffer_store_dwordx4 v[44:47], v27, s[52:55], s64 offen offset:2048 sc1\n"
    "s_waitcnt vmcnt(7)\n"
    "s_barrier\n"
    "v_subrev_u32 v14, 8192, v14\n"
    "v_subrev_u32 v15, 8192, v15\n"
    "v_subrev_u32 v16, 8192, v16\n"
    "v_subrev_u32 v17, 8192, v17\n"
    "v_subrev_u32 v18, 8192, v18\n"
    "v_subrev_u32 v19, 8192, v19\n"
    "ds_read_b64_tr_b16 a[224:225], v14 offset:0\n"
    "ds_read_b64_tr_b16 a[226:227], v14 offset:4096\n"
    "ds_read_b64_tr_b16 a[228:229], v14 offset:8192\n"
    "ds_read_b64_tr_b16 a[230:231], v14 offset:12288\n"
    "ds_read_b64_tr_b16 a[232:233], v14 offset:256\n"
    "ds_read_b64_tr_b16 a[234:235], v14 offset:4352\n"
    "ds_read_b64_tr_b16 a[236:237], v14 offset:8448\n"
    "ds_read_b64_tr_b16 a[238:239], v14 offset:12544\n"
    "ds_read_b64_tr_b16 a[240:241], v14 offset:512\n"
    "ds_read_b64_tr_b16 a[242:243], v14 offset:4608\n"
    "ds_read_b64_tr_b16 a[244:245], v14 offset:8704\n"
    "ds_read_b64_tr_b16 a[246:247], v14 offset:12800\n"
    "v_accvgpr_read_b32 v208, a68\n"
    "v_accvgpr_read_b32 v209, a69\n"
    "v_accvgpr_read_b32 v210, a70\n"
    "v_accvgpr_read_b32 v211, a71\n"
    "v_accvgpr_read_b32 v212, a96\n"
    "v_accvgpr_read_b32 v213, a97\n"
    "v_accvgpr_read_b32 v214, a98\n"
    "v_accvgpr_read_b32 v215, a99\n"
    "v_mul_f32 v208, v23, v208\n"
    "v_mul_f32 v209, v23, v209\n"
    "v_mul_f32 v210, v23, v210\n"
    "v_mul_f32 v211, v23, v211\n"
    "v_mul_f32 v212, v23, v212\n"
    "v_mul_f32 v213, v23, v213\n"
    "v_mul_f32 v214, v23, v214\n"
    "v_mul_f32 v215, v23, v215\n"
    "v_cvt_pk_bf16_f32 v72, v208, v209\n"
    "v_cvt_pk_bf16_f32 v73, v210, v211\n"
    "v_cvt_pk_bf16_f32 v74, v212, v213\n"
    "v_cvt_pk_bf16_f32 v75, v214, v215\n"
    "s_mov_b32 s64, 4096\n"
    "buffer_store_dwordx4 v[72:75], v27, s[52:55], s64 offen offset:3072 sc1\n"
    "v_accvgpr_read_b32 v216, a16\n"
    "v_accvgpr_read_b32 v217, a17\n"
    "v_accvgpr_read_b32 v218, a18\n"
    "v_accvgpr_read_b32 v219, a19\n"
    "v_accvgpr_read_b32 v220, a44\n"
    "v_accvgpr_read_b32 v221, a45\n"
    "v_accvgpr_read_b32 v222, a46\n"
    "v_accvgpr_read_b32 v223, a47\n"
    "v_mul_f32 v216, v24, v216\n"
    "v_mul_f32 v217, v24, v217\n"
    "v_mul_f32 v218, v24, v218\n"
    "v_mul_f32 v219, v24, v219\n"
    "v_mul_f32 v220, v24, v220\n"
    "v_mul_f32 v221, v24, v221\n"
    "v_mul_f32 v222, v24, v222\n"
    "v_mul_f32 v223, v24, v223\n"
    "v_cvt_pk_bf16_f32 v48, v216, v217\n"
    "v_cvt_pk_bf16_f32 v49, v218, v219\n"
    "v_cvt_pk_bf16_f32 v50, v220, v221\n"
    "v_cvt_pk_bf16_f32 v51, v222, v223\n"
    "s_mov_b32 s64, 8192\n"
    "buffer_store_dwordx4 v[48:51], v27, s[52:55], s64 offen sc1\n"
    "v_accvgpr_read_b32 v200, a72\n"
    "v_accvgpr_read_b32 v201, a73\n"
    "v_accvgpr_read_b32 v202, a74\n"
    "v_accvgpr_read_b32 v203, a75\n"
    "v_accvgpr_read_b32 v204, a100\n"
    "v_accvgpr_read_b32 v205, a101\n"
    "v_accvgpr_read_b32 v206, a102\n"
    "v_accvgpr_read_b32 v207, a103\n"
    "v_mul_f32 v200, v24, v200\n"
    "v_mul_f32 v201, v24, v201\n"
    "v_mul_f32 v202, v24, v202\n"
    "v_mul_f32 v203, v24, v203\n"
    "v_mul_f32 v204, v24, v204\n"
    "v_mul_f32 v205, v24, v205\n"
    "v_mul_f32 v206, v24, v206\n"
    "v_mul_f32 v207, v24, v207\n"
    "v_cvt_pk_bf16_f32 v76, v200, v201\n"
    "v_cvt_pk_bf16_f32 v77, v202, v203\n"
    "v_cvt_pk_bf16_f32 v78, v204, v205\n"
    "v_cvt_pk_bf16_f32 v79, v206, v207\n"
    "s_mov_b32 s64, 8192\n"
    "buffer_store_dwordx4 v[76:79], v27, s[52:55], s64 offen offset:1024 sc1\n"
    "v_accvgpr_read_b32 v208, a20\n"
    "v_accvgpr_read_b32 v209, a21\n"
    "v_accvgpr_read_b32 v210, a22\n"
    "v_accvgpr_read_b32 v211, a23\n"
    "v_accvgpr_read_b32 v212, a48\n"
    "v_accvgpr_read_b32 v213, a49\n"
    "v_accvgpr_read_b32 v214, a50\n"
    "v_accvgpr_read_b32 v215, a51\n"
    "v_mul_f32 v208, v25, v208\n"
    "v_mul_f32 v209, v25, v209\n"
    "v_mul_f32 v210, v25, v210\n"
    "v_mul_f32 v211, v25, v211\n"
    "v_mul_f32 v212, v25, v212\n"
    "v_mul_f32 v213, v25, v213\n"
    "v_mul_f32 v214, v25, v214\n"
    "v_mul_f32 v215, v25, v215\n"
    "v_cvt_pk_bf16_f32 v52, v208, v209\n"
    "v_cvt_pk_bf16_f32 v53, v210, v211\n"
    "v_cvt_pk_bf16_f32 v54, v212, v213\n"
    "v_cvt_pk_bf16_f32 v55, v214, v215\n"
    "s_mov_b32 s64, 8192\n"
    "buffer_store_dwordx4 v[52:55], v27, s[52:55], s64 offen offset:2048 sc1\n"
    "v_accvgpr_read_b32 v216, a76\n"
    "v_accvgpr_read_b32 v217, a77\n"
    "v_accvgpr_read_b32 v218, a78\n"
    "v_accvgpr_read_b32 v219, a79\n"
    "v_accvgpr_read_b32 v220, a104\n"
    "v_accvgpr_read_b32 v221, a105\n"
    "v_accvgpr_read_b32 v222, a106\n"
    "v_accvgpr_read_b32 v223, a107\n"
    "v_mul_f32 v216, v25, v216\n"
    "v_mul_f32 v217, v25, v217\n"
    "v_mul_f32 v218, v25, v218\n"
    "v_mul_f32 v219, v25, v219\n"
    "v_mul_f32 v220, v25, v220\n"
    "v_mul_f32 v221, v25, v221\n"
    "v_mul_f32 v222, v25, v222\n"
    "v_mul_f32 v223, v25, v223\n"
    "v_cvt_pk_bf16_f32 v80, v216, v217\n"
    "v_cvt_pk_bf16_f32 v81, v218, v219\n"
    "v_cvt_pk_bf16_f32 v82, v220, v221\n"
    "v_cvt_pk_bf16_f32 v83, v222, v223\n"
    "s_mov_b32 s64, 8192\n"
    "buffer_store_dwordx4 v[80:83], v27, s[52:55], s64 offen offset:3072 sc1\n"
    "v_accvgpr_read_b32 v200, a24\n"
    "v_accvgpr_read_b32 v201, a25\n"
    "v_accvgpr_read_b32 v202, a26\n"
    "v_accvgpr_read_b32 v203, a27\n"
    "v_accvgpr_read_b32 v204, a52\n"
    "v_accvgpr_read_b32 v205, a53\n"
    "v_accvgpr_read_b32 v206, a54\n"
    "v_accvgpr_read_b32 v207, a55\n"
    "v_mul_f32 v200, v26, v200\n"
    "v_mul_f32 v201, v26, v201\n"
    "v_mul_f32 v202, v26, v202\n"
    "v_mul_f32 v203, v26, v203\n"
    "v_mul_f32 v204, v26, v204\n"
    "v_mul_f32 v205, v26, v205\n"
    "v_mul_f32 v206, v26, v206\n"
    "v_mul_f32 v207, v26, v207\n"
    "v_cvt_pk_bf16_f32 v56, v200, v201\n"
    "v_cvt_pk_bf16_f32 v57, v202, v203\n"
    "v_cvt_pk_bf16_f32 v58, v204, v205\n"
    "v_cvt_pk_bf16_f32 v59, v206, v207\n"
    "s_mov_b32 s64, 12288\n"
    "buffer_store_dwordx4 v[56:59], v27, s[52:55], s64 offen sc1\n"
    "v_accvgpr_read_b32 v208, a80\n"
    "v_accvgpr_read_b32 v209, a81\n"
    "v_accvgpr_read_b32 v210, a82\n"
    "v_accvgpr_read_b32 v211, a83\n"
    "v_accvgpr_read_b32 v212, a108\n"
    "v_accvgpr_read_b32 v213, a109\n"
    "v_accvgpr_read_b32 v214, a110\n"
    "v_accvgpr_read_b32 v215, a111\n"
    "v_mul_f32 v208, v26, v208\n"
    "v_mul_f32 v209, v26, v209\n"
    "v_mul_f32 v210, v26, v210\n"
    "v_mul_f32 v211, v26, v211\n"
    "v_mul_f32 v212, v26, v212\n"
    "v_mul_f32 v213, v26, v213\n"
    "v_mul_f32 v214, v26, v214\n"
    "v_mul_f32 v215, v26, v215\n"
    "v_cvt_pk_bf16_f32 v84, v208, v209\n"
    "v_cvt_pk_bf16_f32 v85, v210, v211\n"
    "v_cvt_pk_bf16_f32 v86, v212, v213\n"
    "v_cvt_pk_bf16_f32 v87, v214, v215\n"
    "s_mov_b32 s64, 12288\n"
    "buffer_store_dwordx4 v[84:87], v27, s[52:55], s64 offen offset:1024 sc1\n"
    "s_waitcnt lgkmcnt(10)\n"
    "v_mfma_f32_16x16x32_bf16 a[0:3], a[224:227], v[32:35], 0\n"
    "s_waitcnt lgkmcnt(8)\n"
    "ds_read_b64_tr_b16 a[248:249], v14 offset:768\n"
    "v_accvgpr_read_b32 v216, a112\n"
    "v_mfma_f32_16x16x32_bf16 a[0:3], a[228:231], v[60:63], a[0:3]\n"
    "s_add_u32 m0, s57, 98304\n"
    "ds_read_b64_tr_b16 a[250:251], v14 offset:4864\n"
    "v_accvgpr_read_b32 v217, a113\n"
    "v_mfma_f32_16x16x32_bf16 a[4:7], a[224:227], v[36:39], 0\n"
    "s_add_u32 s58, s11, 32768\n"
    "ds_read_b64_tr_b16 a[252:253], v14 offset:8960\n"
    "v_accvgpr_read_b32 v218, a114\n"
    "v_mfma_f32_16x16x32_bf16 a[4:7], a[228:231], v[64:67], a[4:7]\n"
    "s_mul_i32 s34, s60, 24576\n"
    "ds_read_b64_tr_b16 a[254:255], v14 offset:13056\n"
    "v_accvgpr_read_b32 v219, a115\n"
    "v_mfma_f32_16x16x32_bf16 a[8:11], a[224:227], v[40:43], 0\n"
    "s_add_u32 s34, s34, 65536\n"
    "v_accvgpr_read_b32 v220, a140\n"
    "v_accvgpr_read_b32 v221, a141\n"
    "v_mfma_f32_16x16x32_bf16 a[8:11], a[228:231], v[68:71], a[8:11]\n"
    "s_mov_b64 s[24:25], s[22:23]\n"
    "v_accvgpr_read_b32 v222, a142\n"
    "v_accvgpr_read_b32 v223, a143\n"
    "v_mfma_f32_16x16x32_bf16 a[12:15], a[224:227], v[44:47], 0\n"
    "s_mov_b32 s30, 172032\n"
    "v_mul_f32 v216, v20, v216\n"
    "v_mul_f32 v217, v20, v217\n"
    "v_mfma_f32_16x16x32_bf16 a[12:15], a[228:231], v[72:75], a[12:15]\n"
    "s_mov_b32 s26, 227360\n"
    "v_mul_f32 v218, v20, v218\n"
    "v_mul_f32 v219, v20, v219\n"
    "v_mul_f32 v220, v20, v220\n"
    "v_mul_f32 v221, v20, v221\n"
    "v_mul_f32 v222, v20, v222\n"
    "v_mul_f32 v223, v20, v223\n"
    "v_cvt_pk_bf16_f32 v88, v216, v217\n"
    "v_cvt_pk_bf16_f32 v89, v218, v219\n"
    "v_cvt_pk_bf16_f32 v90, v220, v221\n"
    "v_cvt_pk_bf16_f32 v91, v222, v223\n"
    "v_mfma_f32_16x16x32_bf16 a[16:19], a[224:227], v[48:51], 0\n"
    "s_add_u32 s65, s8, 0\n"
    "v_accvgpr_read_b32 v200, a116\n"
    "v_accvgpr_read_b32 v201, a117\n"
    "v_mfma_f32_16x16x32_bf16 a[16:19], a[228:231], v[76:79], a[16:19]\n"
    "s_mov_b32 s59, 0xffff0000\n"
    "v_accvgpr_read_b32 v202, a118\n"
    "v_accvgpr_read_b32 v203, a119\n"
    "v_mfma_f32_16x16x32_bf16 a[20:23], a[224:227], v[52:55], 0\n"
    "v_accvgpr_read_b32 v204, a144\n"
    "v_accvgpr_read_b32 v205, a145\n"
    "v_mfma_f32_16x16x32_bf16 a[20:23], a[228:231], v[80:83], a[20:23]\n"
    "v_accvgpr_read_b32 v206, a146\n"
    "v_accvgpr_read_b32 v207, a147\n"
    "v_mfma_f32_16x16x32_bf16 a[24:27], a[224:227], v[56:59], 0\n"
    "ds_read_b64_tr_b16 a[224:225], v17 offset:0\n"
    "v_mul_f32 v200, v21, v200\n"
    "v_mfma_f32_16x16x32_bf16 a[24:27], a[228:231], v[84:87], a[24:27]\n"
    "s_waitcnt lgkmcnt(3)\n"
    "ds_read_b64_tr_b16 a[226:227], v17 offset:4096\n"
    "v_mul_f32 v201, v21, v201\n"
    "v_mfma_f32_16x16x32_bf16 a[28:31], a[232:235], v[32:35], 0\n"
    "s_mov_b32 s64, 12288\n"
    "ds_read_b64_tr_b16 a[228:229], v17 offset:8192\n"
    "v_mul_f32 v202, v21, v202\n"
    "v_mfma_f32_16x16x32_bf16 a[28:31], a[236:239], v[60:63], a[28:31]\n"
    "ds_read_b64_tr_b16 a[230:231], v17 offset:12288\n"
    "v_mul_f32 v203, v21, v203\n"
    "v_mul_f32 v204, v21, v204\n"
    "v_mul_f32 v205, v21, v205\n"
    "v_mul_f32 v206, v21, v206\n"
    "v_mul_f32 v207, v21, v207\n"
    "v_cvt_pk_bf16_f32 v92, v200, v201\n"
    "v_cvt_pk_bf16_f32 v93, v202, v203\n"
    "v_cvt_pk_bf16_f32 v94, v204, v205\n"
    "v_cvt_pk_bf16_f32 v95, v206, v207\n"
    "v_mfma_f32_16x16x32_bf16 a[32:35], a[232:235], v[36:39], 0\n"
    "v_accvgpr_read_b32 v208, a120\n"
    "v_accvgpr_read_b32 v209, a121\n"
    "v_mfma_f32_16x16x32_bf16 a[32:35], a[236:239], v[64:67], a[32:35]\n"
    "v_accvgpr_read_b32 v210, a122\n"
    "v_accvgpr_read_b32 v211, a123\n"
    "v_mfma_f32_16x16x32_bf16 a[36:39], a[232:235], v[40:43], 0\n"
    "v_accvgpr_read_b32 v212, a148\n"
    "v_accvgpr_read_b32 v213, a149\n"
    "v_mfma_f32_16x16x32_bf16 a[36:39], a[236:239], v[68:71], a[36:39]\n"
    "v_accvgpr_read_b32 v214, a150\n"
    "v_accvgpr_read_b32 v215, a151\n"
    "v_mfma_f32_16x16x32_bf16 a[40:43], a[232:235], v[44:47], 0\n"
    "v_mul_f32 v208, v22, v208\n"
    "v_mul_f32 v209, v22, v209\n"
    "v_mfma_f32_16x16x32_bf16 a[40:43], a[236:239], v[72:75], a[40:43]\n"
    "v_mul_f32 v210, v22, v210\n"
    "v_mul_f32 v211, v22, v211\n"
    "v_mfma_f32_16x16x32_bf16 a[44:47], a[232:235], v[48:51], 0\n"
    "v_mul_f32 v212, v22, v212\n"
    "v_mul_f32 v213, v22, v213\n"
    "v_mfma_f32_16x16x32_bf16 a[44:47], a[236:239], v[76:79], a[44:47]\n"
    "v_mul_f32 v214, v22, v214\n"
    "v_mul_f32 v215, v22, v215\n"
    "v_cvt_pk_bf16_f32 v96, v208, v209\n"
    "v_cvt_pk_bf16_f32 v97, v210, v211\n"
    "v_cvt_pk_bf16_f32 v98, v212, v213\n"
    "v_cvt_pk_bf16_f32 v99, v214, v215\n"
    "v_mfma_f32_16x16x32_bf16 a[48:51], a[232:235], v[52:55], 0\n"
    "v_accvgpr_read_b32 v216, a124\n"
    "v_accvgpr_read_b32 v217, a125\n"
    "v_mfma_f32_16x16x32_bf16 a[48:51], a[236:239], v[80:83], a[48:51]\n"
    "v_accvgpr_read_b32 v218, a126\n"
    "v_accvgpr_read_b32 v219, a127\n"
    "v_mfma_f32_16x16x32_bf16 a[52:55], a[232:235], v[56:59], 0\n"
    "ds_read_b64_tr_b16 a[232:233], v17 offset:256\n"
    "v_accvgpr_read_b32 v220, a152\n"
    "v_mfma_f32_16x16x32_bf16 a[52:55], a[236:239], v[84:87], a[52:55]\n"
    "ds_read_b64_tr_b16 a[234:235], v17 offset:4352\n"
    "v_accvgpr_read_b32 v221, a153\n"
    "v_mfma_f32_16x16x32_bf16 a[56:59], a[240:243], v[32:35], 0\n"
    "ds_read_b64_tr_b16 a[236:237], v17 offset:8448\n"
    "v_accvgpr_read_b32 v222, a154\n"
    "v_mfma_f32_16x16x32_bf16 a[56:59], a[244:247], v[60:63], a[56:59]\n"
    "ds_read_b64_tr_b16 a[238:239], v17 offset:12544\n"
    "v_accvgpr_read_b32 v223, a155\n"
    "v_mfma_f32_16x16x32_bf16 a[60:63], a[240:243], v[36:39], 0\n"
    "v_mul_f32 v216, v23, v216\n"
    "v_mul_f32 v217, v23, v217\n"
    "v_mfma_f32_16x16x32_bf16 a[60:63], a[244:247], v[64:67], a[60:63]\n"
    "v_mul_f32 v218, v23, v218\n"
    "v_mul_f32 v219, v23, v219\n"
    "v_mul_f32 v220, v23, v220\n"
    "v_mul_f32 v221, v23, v221\n"
    "v_mul_f32 v222, v23, v222\n"
    "v_mul_f32 v223, v23, v223\n"
    "v_cvt_pk_bf16_f32 v100, v216, v217\n"
    "v_cvt_pk_bf16_f32 v101, v218, v219\n"
    "v_cvt_pk_bf16_f32 v102, v220, v221\n"
    "v_cvt_pk_bf16_f32 v103, v222, v223\n"
    "v_mfma_f32_16x16x32_bf16 a[64:67], a[240:243], v[40:43], 0\n"
    "v_accvgpr_read_b32 v200, a128\n"
    "v_accvgpr_read_b32 v201, a129\n"
    "v_mfma_f32_16x16x32_bf16 a[64:67], a[244:247], v[68:71], a[64:67]\n"
    "v_accvgpr_read_b32 v202, a130\n"
    "v_accvgpr_read_b32 v203, a131\n"
    "v_mfma_f32_16x16x32_bf16 a[68:71], a[240:243], v[44:47], 0\n"
    "v_accvgpr_read_b32 v204, a156\n"
    "v_accvgpr_read_b32 v205, a157\n"
    "v_mfma_f32_16x16x32_bf16 a[68:71], a[244:247], v[72:75], a[68:71]\n"
    "v_accvgpr_read_b32 v206, a158\n"
    "v_accvgpr_read_b32 v207, a159\n"
    "v_mfma_f32_16x16x32_bf16 a[72:75], a[240:243], v[48:51], 0\n"
    "v_mul_f32 v200, v24, v200\n"
    "v_mul_f32 v201, v24, v201\n"
    "v_mfma_f32_16x16x32_bf16 a[72:75], a[244:247], v[76:79], a[72:75]\n"
    "v_mul_f32 v202, v24, v202\n"
    "v_mul_f32 v203, v24, v203\n"
    "v_mfma_f32_16x16x32_bf16 a[76:79], a[240:243], v[52:55], 0\n"
    "v_mul_f32 v204, v24, v204\n"
    "v_mul_f32 v205, v24, v205\n"
    "v_mfma_f32_16x16x32_bf16 a[76:79], a[244:247], v[80:83], a[76:79]\n"
    "v_mul_f32 v206, v24, v206\n"
    "v_mul_f32 v207, v24, v207\n"
    "v_cvt_pk_bf16_f32 v104, v200, v201\n"
    "v_cvt_pk_bf16_f32 v105, v202, v203\n"
    "v_cvt_pk_bf16_f32 v106, v204, v205\n"
    "v_cvt_pk_bf16_f32 v107, v206, v207\n"
    "v_mfma_f32_16x16x32_bf16 a[80:83], a[240:243], v[56:59], 0\n"
    "ds_read_b64_tr_b16 a[240:241], v17 offset:512\n"
    "v_accvgpr_read_b32 v208, a132\n"
    "v_mfma_f32_16x16x32_bf16 a[80:83], a[244:247], v[84:87], a[80:83]\n"
    "ds_read_b64_tr_b16 a[242:243], v17 offset:4608\n"
    "v_accvgpr_read_b32 v209, a133\n"
    "v_mfma_f32_16x16x32_bf16 a[84:87], a[248:251], v[32:35], 0\n"
    "s_waitcnt lgkmcnt(2)\n"
    "ds_read_b64_tr_b16 a[244:245], v17 offset:8704\n"
    "v_accvgpr_read_b32 v210, a134\n"
    "v_mfma_f32_16x16x32_bf16 a[84:87], a[252:255], v[60:63], a[84:87]\n"
    "ds_read_b64_tr_b16 a[246:247], v17 offset:12800\n"
    "v_accvgpr_read_b32 v211, a135\n"
    "v_mfma_f32_16x16x32_bf16 a[88:91], a[248:251], v[36:39], 0\n"
    "v_accvgpr_read_b32 v212, a160\n"
    "v_accvgpr_read_b32 v213, a161\n"
    "v_mfma_f32_16x16x32_bf16 a[88:91], a[252:255], v[64:67], a[88:91]\n"
    "v_accvgpr_read_b32 v214, a162\n"
    "v_accvgpr_read_b32 v215, a163\n"
    "v_mfma_f32_16x16x32_bf16 a[92:95], a[248:251], v[40:43], 0\n"
    "v_mul_f32 v208, v25, v208\n"
    "v_mul_f32 v209, v25, v209\n"
    "v_mfma_f32_16x16x32_bf16 a[92:95], a[252:255], v[68:71], a[92:95]\n"
    "v_mul_f32 v210, v25, v210\n"
    "v_mul_f32 v211, v25, v211\n"
    "v_mul_f32 v212, v25, v212\n"
    "v_mul_f32 v213, v25, v213\n"
    "v_mul_f32 v214, v25, v214\n"
    "v_mul_f32 v215, v25, v215\n"
    "v_cvt_pk_bf16_f32 v108, v208, v209\n"
    "v_cvt_pk_bf16_f32 v109, v210, v211\n"
    "v_cvt_pk_bf16_f32 v110, v212, v213\n"
    "v_cvt_pk_bf16_f32 v111, v214, v215\n"
    "v_mfma_f32_16x16x32_bf16 a[96:99], a[248:251], v[44:47], 0\n"
    "v_accvgpr_read_b32 v216, a136\n"
    "v_accvgpr_read_b32 v217, a137\n"
    "v_mfma_f32_16x16x32_bf16 a[96:99], a[252:255], v[72:75], a[96:99]\n"
    "v_accvgpr_read_b32 v218, a138\n"
    "v_accvgpr_read_b32 v219, a139\n"
    "v_mfma_f32_16x16x32_bf16 a[100:103], a[248:251], v[48:51], 0\n"
    "v_accvgpr_read_b32 v220, a164\n"
    "v_accvgpr_read_b32 v221, a165\n"
    "v_mfma_f32_16x16x32_bf16 a[100:103], a[252:255], v[76:79], a[100:103]\n"
    "v_accvgpr_read_b32 v222, a166\n"
    "v_accvgpr_read_b32 v223, a167\n"
    "v_mfma_f32_16x16x32_bf16 a[104:107], a[248:251], v[52:55], 0\n"
    "v_mul_f32 v216, v26, v216\n"
    "v_mul_f32 v217, v26, v217\n"
    "v_mfma_f32_16x16x32_bf16 a[104:107], a[252:255], v[80:83], a[104:107]\n"
    "v_mul_f32 v218, v26, v218\n"
    "v_mul_f32 v219, v26, v219\n"
    "v_mfma_f32_16x16x32_bf16 a[108:111], a[248:251], v[56:59], 0\n"
    "ds_read_b64_tr_b16 a[248:249], v17 offset:768\n"
    "v_mul_f32 v220, v26, v220\n"
    "v_mfma_f32_16x16x32_bf16 a[108:111], a[252:255], v[84:87], a[108:111]\n"
    "ds_read_b64_tr_b16 a[250:251], v17 offset:4864\n"
    "v_mul_f32 v221, v26, v221\n"
    "v_mul_f32 v222, v26, v222\n"
    "v_mul_f32 v223, v26, v223\n"
    "v_cvt_pk_bf16_f32 v112, v216, v217\n"
    "v_cvt_pk_bf16_f32 v113, v218, v219\n"
    "v_cvt_pk_bf16_f32 v114, v220, v221\n"
    "v_cvt_pk_bf16_f32 v115, v222, v223\n"
    "v_mfma_f32_16x16x32_bf16 a[112:115], a[224:227], v[32:35], 0\n"
    "s_waitcnt vmcnt(0)\n"
    "s_barrier\n"
    "ds_read_b64_tr_b16 a[252:253], v17 offset:8960\n"
    "v_mfma_f32_16x16x32_bf16 a[112:115], a[228:231], v[60:63], a[112:115]\n"
    "buffer_store_dword v4, v8, s[72:75], 0 offen sc1\n"
    "v_mfma_f32_16x16x32_bf16 a[116:119], a[224:227], v[36:39], 0\n"
    "buffer_load_dword v5, v3, s[68:71], 0 offen offset:128 sc1\n"
    "v_mfma_f32_16x16x32_bf16 a[116:119], a[228:231], v[64:67], a[116:119]\n"
    "buffer_load_dwordx4 v7, s[40:43], s58 offen lds\n"
    "v_mfma_f32_16x16x32_bf16 a[120:123], a[224:227], v[40:43], 0\n"
    "s_add_u32 m0, s57, 100352\n"
    "ds_read_b64_tr_b16 a[254:255], v17 offset:13056\n"
    "v_accvgpr_read_b32 v200, a168\n"
    "v_mfma_f32_16x16x32_bf16 a[120:123], a[228:231], v[68:71], a[120:123]\n"
    "s_add_u32 s58, s11, 40960\n"
    "v_accvgpr_read_b32 v201, a169\n"
    "v_accvgpr_read_b32 v202, a170\n"
    "v_mfma_f32_16x16x32_bf16 a[124:127], a[224:227], v[44:47], 0\n"
    "v_accvgpr_read_b32 v203, a171\n"
    "v_accvgpr_read_b32 v204, a196\n"
    "v_accvgpr_read_b32 v205, a197\n"
    "v_accvgpr_read_b32 v206, a198\n"
    "v_accvgpr_read_b32 v207, a199\n"
    "v_mfma_f32_16x16x32_bf16 a[124:127], a[228:231], v[72:75], a[124:127]\n"
    "v_mul_f32 v200, v20, v200\n"
    "v_mul_f32 v201, v20, v201\n"
    "v_mul_f32 v202, v20, v202\n"
    "v_mul_f32 v203, v20, v203\n"
    "v_mul_f32 v204, v20, v204\n"
    "v_mul_f32 v205, v20, v205\n"
    "v_mul_f32 v206, v20, v206\n"
    "v_mul_f32 v207, v20, v207\n"
    "v_cvt_pk_bf16_f32 v116, v200, v201\n"
    "v_cvt_pk_bf16_f32 v117, v202, v203\n"
    "v_cvt_pk_bf16_f32 v118, v204, v205\n"
    "v_cvt_pk_bf16_f32 v119, v206, v207\n"
    "v_mfma_f32_16x16x32_bf16 a[128:131], a[224:227], v[48:51], 0\n"
    "buffer_load_dwordx4 v7, s[40:43], s58 offen lds\n"
    "v_mfma_f32_16x16x32_bf16 a[128:131], a[228:231], v[76:79], a[128:131]\n"
    "s_add_u32 m0, s57, 102400\n"
    "v_accvgpr_read_b32 v208, a172\n"
    "v_accvgpr_read_b32 v209, a173\n"
    "v_mfma_f32_16x16x32_bf16 a[132:135], a[224:227], v[52:55], 0\n"
    "s_add_u32 s58, s11, 49152\n"
    "v_accvgpr_read_b32 v210, a174\n"
    "v_accvgpr_read_b32 v211, a175\n"
    "v_mfma_f32_16x16x32_bf16 a[132:135], a[228:231], v[80:83], a[132:135]\n"
    "buffer_load_dwordx4 v7, s[40:43], s58 offen lds\n"
    "v_mfma_f32_16x16x32_bf16 a[136:139], a[224:227], v[56:59], 0\n"
    "s_add_u32 m0, s57, 104448\n"
    "v_accvgpr_read_b32 v212, a200\n"
    "v_accvgpr_read_b32 v213, a201\n"
    "v_mfma_f32_16x16x32_bf16 a[136:139], a[228:231], v[84:87], a[136:139]\n"
    "s_add_u32 s58, s11, 57344\n"
    "v_accvgpr_read_b32 v214, a202\n"
    "v_accvgpr_read_b32 v215, a203\n"
    "v_mfma_f32_16x16x32_bf16 a[140:143], a[232:235], v[32:35], 0\n"
    "v_mul_f32 v208, v21, v208\n"
    "v_mul_f32 v209, v21, v209\n"
    "v_mfma_f32_16x16x32_bf16 a[140:143], a[236:239], v[60:63], a[140:143]\n"
    "v_mul_f32 v210, v21, v210\n"
    "v_mul_f32 v211, v21, v211\n"
    "v_mul_f32 v212, v21, v212\n"
    "v_mul_f32 v213, v21, v213\n"
    "v_mul_f32 v214, v21, v214\n"
    "v_mul_f32 v215, v21, v215\n"
    "v_cvt_pk_bf16_f32 v120, v208, v209\n"
    "v_cvt_pk_bf16_f32 v121, v210, v211\n"
    "v_cvt_pk_bf16_f32 v122, v212, v213\n"
    "v_cvt_pk_bf16_f32 v123, v214, v215\n"
    "v_mfma_f32_16x16x32_bf16 a[144:147], a[232:235], v[36:39], 0\n"
    "buffer_load_dwordx4 v7, s[40:43], s58 offen lds\n"
    "v_mfma_f32_16x16x32_bf16 a[144:147], a[236:239], v[64:67], a[144:147]\n"
    "s_add_u32 m0, s57, 106496\n"
    "v_accvgpr_read_b32 v216, a176\n"
    "v_accvgpr_read_b32 v217, a177\n"
    "v_mfma_f32_16x16x32_bf16 a[148:151], a[232:235], v[40:43], 0\n"
    "s_add_u32 s58, s12, 0\n"
    "v_accvgpr_read_b32 v218, a178\n"
    "v_accvgpr_read_b32 v219, a179\n"
    "v_mfma_f32_16x16x32_bf16 a[148:151], a[236:239], v[68:71], a[148:151]\n"
    "buffer_load_dwordx4 v7, s[40:43], s58 offen lds\n"
    "v_mfma_f32_16x16x32_bf16 a[152:155], a[232:235], v[44:47], 0\n"
    "s_add_u32 m0, s57, 108544\n"
    "v_accvgpr_read_b32 v220, a204\n"
    "v_accvgpr_read_b32 v221, a205\n"
    "v_mfma_f32_16x16x32_bf16 a[152:155], a[236:239], v[72:75], a[152:155]\n"
    "s_add_u32 s58, s12, 8192\n"
    "v_accvgpr_read_b32 v222, a206\n"
    "v_accvgpr_read_b32 v223, a207\n"
    "v_mfma_f32_16x16x32_bf16 a[156:159], a[232:235], v[48:51], 0\n"
    "s_waitcnt lgkmcnt(0)\n"
    "s_barrier\n"
    "ds_read_b64_tr_b16 a[224:225], v14 offset:32768\n"
    "v_mfma_f32_16x16x32_bf16 a[156:159], a[236:239], v[76:79], a[156:159]\n"
    "ds_read_b64_tr_b16 a[226:227], v14 offset:36864\n"
    "v_mul_f32 v216, v22, v216\n"
    "v_mul_f32 v217, v22, v217\n"
    "v_mul_f32 v218, v22, v218\n"
    "v_mul_f32 v219, v22, v219\n"
    "v_mul_f32 v220, v22, v220\n"
    "v_mul_f32 v221, v22, v221\n"
    "v_mul_f32 v222, v22, v222\n"
    "v_mul_f32 v223, v22, v223\n"
    "v_cvt_pk_bf16_f32 v124, v216, v217\n"
    "v_cvt_pk_bf16_f32 v125, v218, v219\n"
    "v_cvt_pk_bf16_f32 v126, v220, v221\n"
    "v_cvt_pk_bf16_f32 v127, v222, v223\n"
    "v_mfma_f32_16x16x32_bf16 a[160:163], a[232:235], v[52:55], 0\n"
    "buffer_load_dwordx4 v7, s[40:43], s58 offen lds\n"
    "v_mfma_f32_16x16x32_bf16 a[160:163], a[236:239], v[80:83], a[160:163]\n"
    "s_add_u32 m0, s57, 110592\n"
    "ds_read_b64_tr_b16 a[228:229], v14 offset:40960\n"
    "v_accvgpr_read_b32 v200, a180\n"
    "v_mfma_f32_16x16x32_bf16 a[164:167], a[232:235], v[56:59], 0\n"
    "s_add_u32 s58, s12, 16384\n"
    "ds_read_b64_tr_b16 a[230:231], v14 offset:45056\n"
    "v_accvgpr_read_b32 v201, a181\n"
    "v_mfma_f32_16x16x32_bf16 a[164:167], a[236:239], v[84:87], a[164:167]\n"
    "buffer_load_dwordx4 v7, s[40:43], s58 offen lds\n"
    "v_mfma_f32_16x16x32_bf16 a[168:171], a[240:243], v[32:35], 0\n"
    "s_add_u32 m0, s57, 112640\n"
    "ds_read_b64_tr_b16 a[232:233], v14 offset:33024\n"
    "v_accvgpr_read_b32 v202, a182\n"
    "v_mfma_f32_16x16x32_bf16 a[168:171], a[244:247], v[60:63], a[168:171]\n"
    "s_add_u32 s58, s12, 24576\n"
    "ds_read_b64_tr_b16 a[234:235], v14 offset:37120\n"
    "v_accvgpr_read_b32 v203, a183\n"
    "v_mfma_f32_16x16x32_bf16 a[172:175], a[240:243], v[36:39], 0\n"
    "ds_read_b64_tr_b16 a[236:237], v14 offset:41216\n"
    "v_accvgpr_read_b32 v204, a208\n"
    "v_accvgpr_read_b32 v205, a209\n"
    "v_accvgpr_read_b32 v206, a210\n"
    "v_accvgpr_read_b32 v207, a211\n"
    "v_mfma_f32_16x16x32_bf16 a[172:175], a[244:247], v[64:67], a[172:175]\n"
    "ds_read_b64_tr_b16 a[238:239], v14 offset:45312\n"
    "v_mul_f32 v200, v23, v200\n"
    "v_mul_f32 v201, v23, v201\n"
    "v_mul_f32 v202, v23, v202\n"
    "v_mul_f32 v203, v23, v203\n"
    "v_mul_f32 v204, v23, v204\n"
    "v_mul_f32 v205, v23, v205\n"
    "v_mul_f32 v206, v23, v206\n"
    "v_mul_f32 v207, v23, v207\n"
    "v_cvt_pk_bf16_f32 v128, v200, v201\n"
    "v_cvt_pk_bf16_f32 v129, v202, v203\n"
    "v_cvt_pk_bf16_f32 v130, v204, v205\n"
    "v_cvt_pk_bf16_f32 v131, v206, v207\n"
    "v_mfma_f32_16x16x32_bf16 a[176:179], a[240:243], v[40:43], 0\n"
    "buffer_load_dwordx4 v7, s[40:43], s58 offen lds\n"
    "v_mfma_f32_16x16x32_bf16 a[176:179], a[244:247], v[68:71], a[176:179]\n"
    "s_add_u32 m0, s57, 139264\n"
    "v_accvgpr_read_b32 v208, a184\n"
    "v_accvgpr_read_b32 v209, a185\n"
    "v_mfma_f32_16x16x32_bf16 a[180:183], a[240:243], v[44:47], 0\n"
    "s_add_u32 s58, s12, 32768\n"
    "v_accvgpr_read_b32 v210, a186\n"
    "v_accvgpr_read_b32 v211, a187\n"
    "v_mfma_f32_16x16x32_bf16 a[180:183], a[244:247], v[72:75], a[180:183]\n"
    "buffer_load_dwordx4 v7, s[40:43], s58 offen lds\n"
    "v_mfma_f32_16x16x32_bf16 a[184:187], a[240:243], v[48:51], 0\n"
    "s_add_u32 m0, s57, 141312\n"
    "v_accvgpr_read_b32 v216, a188\n"
    "v_accvgpr_read_b32 v217, a189\n"
    "v_mfma_f32_16x16x32_bf16 a[184:187], a[244:247], v[76:79], a[184:187]\n"
    "s_add_u32 s58, s12, 40960\n"
    "v_accvgpr_read_b32 v218, a190\n"
    "v_accvgpr_read_b32 v219, a191\n"
    "v_mfma_f32_16x16x32_bf16 a[188:191], a[240:243], v[52:55], 0\n"
    "v_accvgpr_read_b32 v212, a212\n"
    "v_accvgpr_read_b32 v213, a213\n"
    "v_accvgpr_read_b32 v214, a214\n"
    "v_accvgpr_read_b32 v215, a215\n"
    "v_mfma_f32_16x16x32_bf16 a[188:191], a[244:247], v[80:83], a[188:191]\n"
    "v_mul_f32 v208, v24, v208\n"
    "v_mul_f32 v209, v24, v209\n"
    "v_mul_f32 v210, v24, v210\n"
    "v_mul_f32 v211, v24, v211\n"
    "v_mul_f32 v212, v24, v212\n"
    "v_mul_f32 v213, v24, v213\n"
    "v_mul_f32 v214, v24, v214\n"
    "v_mul_f32 v215, v24, v215\n"
    "v_cvt_pk_bf16_f32 v132, v208, v209\n"
    "v_cvt_pk_bf16_f32 v133, v210, v211\n"
    "v_cvt_pk_bf16_f32 v134, v212, v213\n"
    "v_cvt_pk_bf16_f32 v135, v214, v215\n"
    "v_accvgpr_read_b32 v200, a192\n"
    "v_accvgpr_read_b32 v201, a193\n"
    "v_accvgpr_read_b32 v202, a194\n"
    "v_accvgpr_read_b32 v203, a195\n"
    "v_mfma_f32_16x16x32_bf16 a[192:195], a[240:243], v[56:59], 0\n"
    "buffer_load_dwordx4 v7, s[40:43], s58 offen lds\n"
    "v_mfma_f32_16x16x32_bf16 a[192:195], a[244:247], v[84:87], a[192:195]\n"
    "s_add_u32 m0, s57, 143360\n"
    "ds_read_b64_tr_b16 a[240:241], v14 offset:33280\n"
    "v_accvgpr_read_b32 v220, a216\n"
    "v_mfma_f32_16x16x32_bf16 a[196:199], a[248:251], v[32:35], 0\n"
    "s_add_u32 s58, s12, 49152\n"
    "ds_read_b64_tr_b16 a[242:243], v14 offset:37376\n"
    "v_accvgpr_read_b32 v221, a217\n"
    "v_mfma_f32_16x16x32_bf16 a[196:199], a[252:255], v[60:63], a[196:199]\n"
    "buffer_load_dwordx4 v7, s[40:43], s58 offen lds\n"
    "v_mfma_f32_16x16x32_bf16 a[200:203], a[248:251], v[36:39], 0\n"
    "s_add_u32 m0, s57, 145408\n"
    "ds_read_b64_tr_b16 a[244:245], v14 offset:41472\n"
    "v_accvgpr_read_b32 v222, a218\n"
    "v_mfma_f32_16x16x32_bf16 a[200:203], a[252:255], v[64:67], a[200:203]\n"
    "s_add_u32 s58, s12, 57344\n"
    "ds_read_b64_tr_b16 a[246:247], v14 offset:45568\n"
    "v_accvgpr_read_b32 v223, a219\n"
    "v_mfma_f32_16x16x32_bf16 a[204:207], a[248:251], v[40:43], 0\n"
    "v_mul_f32 v216, v25, v216\n"
    "v_mul_f32 v217, v25, v217\n"
    "v_mfma_f32_16x16x32_bf16 a[204:207], a[252:255], v[68:71], a[204:207]\n"
    "v_mul_f32 v218, v25, v218\n"
    "v_mul_f32 v219, v25, v219\n"
    "v_mul_f32 v220, v25, v220\n"
    "v_mul_f32 v221, v25, v221\n"
    "v_mul_f32 v222, v25, v222\n"
    "v_mul_f32 v223, v25, v223\n"
    "v_cvt_pk_bf16_f32 v136, v216, v217\n"
    "v_cvt_pk_bf16_f32 v137, v218, v219\n"
    "v_cvt_pk_bf16_f32 v138, v220, v221\n"
    "v_cvt_pk_bf16_f32 v139, v222, v223\n"
    "v_mfma_f32_16x16x32_bf16 a[208:211], a[248:251], v[44:47], 0\n"
    "buffer_load_dwordx4 v7, s[40:43], s58 offen lds\n"
    "v_mfma_f32_16x16x32_bf16 a[208:211], a[252:255], v[72:75], a[208:211]\n"
    "s_add_u32 m0, s57, 0\n"
    "v_accvgpr_read_b32 v204, a220\n"
    "v_accvgpr_read_b32 v205, a221\n"
    "v_mfma_f32_16x16x32_bf16 a[212:215], a[248:251], v[48:51], 0\n"
    "s_add_u32 s58, s11, 65536\n"
    "v_accvgpr_read_b32 v206, a222\n"
    "v_accvgpr_read_b32 v207, a223\n"
    "v_mfma_f32_16x16x32_bf16 a[212:215], a[252:255], v[76:79], a[212:215]\n"
    "buffer_load_dwordx4 v7, s[40:43], s58 offen lds\n"
    "v_mfma_f32_16x16x32_bf16 a[216:219], a[248:251], v[52:55], 0\n"
    "s_add_u32 m0, s57, 2048\n"
    "v_mul_f32 v200, v26, v200\n"
    "v_mul_f32 v201, v26, v201\n"
    "v_mfma_f32_16x16x32_bf16 a[216:219], a[252:255], v[80:83], a[216:219]\n"
    "s_add_u32 s58, s11, 73728\n"
    "v_mul_f32 v202, v26, v202\n"
    "v_mul_f32 v203, v26, v203\n"
    "v_mfma_f32_16x16x32_bf16 a[220:223], a[248:251], v[56:59], 0\n"
    "ds_read_b64_tr_b16 a[248:249], v14 offset:33536\n"
    "v_mul_f32 v204, v26, v204\n"
    "v_mfma_f32_16x16x32_bf16 a[220:223], a[252:255], v[84:87], a[220:223]\n"
    "s_waitcnt lgkmcnt(3)\n"
    "ds_read_b64_tr_b16 a[250:251], v14 offset:37632\n"
    "v_mul_f32 v205, v26, v205\n"
    "v_mul_f32 v206, v26, v206\n"
    "v_mul_f32 v207, v26, v207\n"
    "v_cvt_pk_bf16_f32 v140, v200, v201\n"
    "v_cvt_pk_bf16_f32 v141, v202, v203\n"
    "v_cvt_pk_bf16_f32 v142, v204, v205\n"
    "v_cvt_pk_bf16_f32 v143, v206, v207\n"
    "v_mfma_f32_16x16x32_bf16 a[0:3], a[224:227], v[88:91], a[0:3]\n"
    "buffer_load_dwordx4 v7, s[40:43], s58 offen lds\n"
    "v_mfma_f32_16x16x32_bf16 a[0:3], a[228:231], v[116:119], a[0:3]\n"
    "s_add_u32 m0, s57, 4096\n"
    "ds_read_b64_tr_b16 a[252:253], v14 offset:41728\n"
    "v_sub_u32 v29, v8, v3\n"
    "v_mfma_f32_16x16x32_bf16 a[4:7], a[224:227], v[92:95], a[4:7]\n"
    "s_add_u32 s58, s11, 81920\n"
    "ds_read_b64_tr_b16 a[254:255], v14 offset:45824\n"
    "v_add_u32 v29, v29, v2\n"
    "v_mfma_f32_16x16x32_bf16 a[4:7], a[228:231], v[120:123], a[4:7]\n"
    "buffer_load_dwordx4 v7, s[40:43], s58 offen lds\n"
    "v_mfma_f32_16x16x32_bf16 a[8:11], a[224:227], v[96:99], a[8:11]\n"
    "s_add_u32 m0, s57, 6144\n"
    "v_lshrrev_b32 v31, 9, v1\n"
    "v_xor_b32 v31, 1, v31\n"
    "s_add_u32 s58, s11, 90112\n"
    "v_mfma_f32_16x16x32_bf16 a[8:11], a[228:231], v[124:127], a[8:11]\n"
    "buffer_load_dwordx4 v7, s[40:43], s58 offen lds\n"
    "v_mfma_f32_16x16x32_bf16 a[12:15], a[224:227], v[100:103], a[12:15]\n"
    "s_add_u32 m0, s57, 8192\n"
    "v_lshl_add_u32 v31, v31, 23, v2\n"
    "v_add_u32 v12, s34, v27\n"
    "s_add_u32 s58, s11, 98304\n"
    "v_mfma_f32_16x16x32_bf16 a[12:15], a[228:231], v[128:131], a[12:15]\n"
    "buffer_load_dwordx4 v7, s[40:43], s58 offen lds\n"
    "v_mfma_f32_16x16x32_bf16 a[16:19], a[224:227], v[104:107], a[16:19]\n"
    "s_add_u32 m0, s57, 10240\n"
    "v_lshrrev_b32 v22, 4, v1\n"
    "v_bfe_u32 v21, v1, 7, 4\n"
    "s_add_u32 s58, s11, 106496\n"
    "v_mfma_f32_16x16x32_bf16 a[16:19], a[228:231], v[132:135], a[16:19]\n"
    "buffer_load_dwordx4 v7, s[40:43], s58 offen lds\n"
    "v_mfma_f32_16x16x32_bf16 a[20:23], a[224:227], v[108:111], a[20:23]\n"
    "s_add_u32 m0, s57, 12288\n"
    "v_lshrrev_b32 v20, 11, v1\n"
    "v_lshlrev_b32 v21, 5, v21\n"
    "s_add_u32 s58, s11, 114688\n"
    "v_mfma_f32_16x16x32_bf16 a[20:23], a[228:231], v[136:139], a[20:23]\n"
    "buffer_load_dwordx4 v7, s[40:43], s58 offen lds\n"
    "v_mfma_f32_16x16x32_bf16 a[24:27], a[224:227], v[112:115], a[24:27]\n"
    "s_add_u32 m0, s57, 14336\n"
    "ds_read_b64_tr_b16 a[224:225], v17 offset:32768\n"
    "v_lshl_add_u32 v21, v20, 3, v21\n"
    "s_add_u32 s58, s11, 122880\n"
    "v_mfma_f32_16x16x32_bf16 a[24:27], a[228:231], v[140:143], a[24:27]\n"
    "buffer_load_dwordx4 v7, s[40:43], s58 offen lds\n"
    "v_mfma_f32_16x16x32_bf16 a[28:31], a[232:235], v[88:91], a[28:31]\n"
    "buffer_load_dword v6, v29, s[68:71], 0 offen sc1\n"
    "v_mfma_f32_16x16x32_bf16 a[28:31], a[236:239], v[116:119], a[28:31]\n"
    "s_add_u32 m0, s57, 65536\n"
    "ds_read_b64_tr_b16 a[226:227], v17 offset:36864\n"
    "v_lshrrev_b32 v11, 8, v1\n"
    "v_mfma_f32_16x16x32_bf16 a[32:35], a[232:235], v[92:95], a[32:35]\n"
    "s_add_u32 s58, s10, 65536\n"
    "ds_read_b64_tr_b16 a[228:229], v17 offset:40960\n"
    "v_sub_u32 v30, v10, v3\n"
    "v_mfma_f32_16x16x32_bf16 a[32:35], a[236:239], v[120:123], a[32:35]\n"
    "s_xor_b32 s34, s60, 2\n"
    "ds_read_b64_tr_b16 a[230:231], v17 offset:45056\n"
    "v_add_u32 v21, v21, v12\n"
    "v_mfma_f32_16x16x32_bf16 a[36:39], a[232:235], v[96:99], a[36:39]\n"
    "s_mul_i32 s34, s34, 24576\n"
    "v_bfe_u32 v20, v1, 7, 1\n"
    "v_lshlrev_b32 v11, 11, v11\n"
    "v_mfma_f32_16x16x32_bf16 a[36:39], a[236:239], v[124:127], a[36:39]\n"
    "s_add_u32 s34, s34, 65536\n"
    "v_add_u32 v13, s34, v27\n"
    "v_add_u32 v30, v30, v2\n"
    "v_mfma_f32_16x16x32_bf16 a[40:43], a[232:235], v[100:103], a[40:43]\n"
    "s_and_b32 s34, s7, 3\n"
    "v_sub_u32 v21, v21, v27\n"
    "v_lshl_add_u32 v11, v20, 4, v11\n"
    "v_mfma_f32_16x16x32_bf16 a[40:43], a[236:239], v[128:131], a[40:43]\n"
    "s_lshl2_add_u32 s34, s6, s34\n"
    "s_lshl4_add_u32 s34, s34, s60\n"
    "s_mul_i32 s34, s34, 13312\n"
    "v_mfma_f32_16x16x32_bf16 a[44:47], a[232:235], v[104:107], a[44:47]\n"
    "s_add_u32 s34, s34, 41943040\n"
    "s_add_u32 s28, s20, s34\ns_addc_u32 s29, s21, 0\n"
    "v_mfma_f32_16x16x32_bf16 a[44:47], a[236:239], v[132:135], a[44:47]\n"
    "s_waitcnt vmcnt(0)\n"
    "v_cmp_ne_u32 vcc, v6, v5\n"
    "s_nop 4\n"
    "s_and_b32 s67, vcc_lo, 0xe\n"
    "s_cbranch_scc1 L_retryA_%=\nL_okA_%=:\n"
    "v_mfma_f32_16x16x32_bf16 a[48:51], a[232:235], v[108:111], a[48:51]\n"
    "buffer_load_dword v4, v2, s[68:71], 0 offen offset:128 sc1\n"
    "v_mfma_f32_16x16x32_bf16 a[48:51], a[236:239], v[136:139], a[48:51]\n"
    "v_mfma_f32_16x16x32_bf16 a[52:55], a[232:235], v[112:115], a[52:55]\n"
    "buffer_load_dwordx4 v[144:147], v27, s[36:39], s65 offen sc1\n"
    "v_mfma_f32_16x16x32_bf16 a[52:55], a[236:239], v[140:143], a[52:55]\n"
    "buffer_load_dwordx4 v[172:175], v27, s[36:39], s65 offen offset:1024 sc1\n"
    "v_mfma_f32_16x16x32_bf16 a[56:59], a[240:243], v[88:91], a[56:59]\n"
    "s_waitcnt lgkmcnt(1)\n"
    "ds_read_b64_tr_b16 a[232:233], v17 offset:33024\n"
    "v_mfma_f32_16x16x32_bf16 a[56:59], a[244:247], v[116:119], a[56:59]\n"
    "buffer_load_dwordx4 v[148:151], v27, s[36:39], s65 offen offset:2048 sc1\n"
    "v_mfma_f32_16x16x32_bf16 a[60:63], a[240:243], v[92:95], a[60:63]\n"
    "buffer_load_dwordx4 v[176:179], v27, s[36:39], s65 offen offset:3072 sc1\n"
    "v_mfma_f32_16x16x32_bf16 a[60:63], a[244:247], v[120:123], a[60:63]\n"
    "s_add_u32 s65, s8, 4096\n"
    "ds_read_b64_tr_b16 a[234:235], v17 offset:37120\n"
    "v_mfma_f32_16x16x32_bf16 a[64:67], a[240:243], v[96:99], a[64:67]\n"
    "buffer_load_dwordx4 v[152:155], v27, s[36:39], s65 offen sc1\n"
    "v_mfma_f32_16x16x32_bf16 a[64:67], a[244:247], v[124:127], a[64:67]\n"
    "buffer_load_dwordx4 v[180:183], v27, s[36:39], s65 offen offset:1024 sc1\n"
    "v_mfma_f32_16x16x32_bf16 a[68:71], a[240:243], v[100:103], a[68:71]\n"
    "buffer_load_dwordx4 v[156:159], v27, s[36:39], s65 offen offset:2048 sc1\n"
    "v_mfma_f32_16x16x32_bf16 a[68:71], a[244:247], v[128:131], a[68:71]\n"
    "buffer_load_dwordx4 v[184:187], v27, s[36:39], s65 offen offset:3072 sc1\n"
    "v_mfma_f32_16x16x32_bf16 a[72:75], a[240:243], v[104:107], a[72:75]\n"
    "s_add_u32 s65, s8, 8192\n"
    "ds_read_b64_tr_b16 a[236:237], v17 offset:41216\n"
    "v_mfma_f32_16x16x32_bf16 a[72:75], a[244:247], v[132:135], a[72:75]\n"
    "buffer_load_dwordx4 v[160:163], v27, s[36:39], s65 offen sc1\n"
    "v_mfma_f32_16x16x32_bf16 a[76:79], a[240:243], v[108:111], a[76:79]\n"
    "buffer_load_dwordx4 v[188:191], v27, s[36:39], s65 offen offset:1024 sc1\n"
    "v_mfma_f32_16x16x32_bf16 a[76:79], a[244:247], v[136:139], a[76:79]\n"
    "buffer_load_dwordx4 v[164:167], v27, s[36:39], s65 offen offset:2048 sc1\n"
    "v_mfma_f32_16x16x32_bf16 a[80:83], a[240:243], v[112:115], a[80:83]\n"
    "buffer_load_dwordx4 v[192:195], v27, s[36:39], s65 offen offset:3072 sc1\n"
    "v_mfma_f32_16x16x32_bf16 a[80:83], a[244:247], v[140:143], a[80:83]\n"
    "s_add_u32 s65, s8, 12288\n"
    "ds_read_b64_tr_b16 a[238:239], v17 offset:45312\n"
    "v_mfma_f32_16x16x32_bf16 a[84:87], a[248:251], v[88:91], a[84:87]\n"
    "buffer_load_dwordx4 v[168:171], v27, s[36:39], s65 offen sc1\n"
    "v_mfma_f32_16x16x32_bf16 a[84:87], a[252:255], v[116:119], a[84:87]\n"
    "buffer_load_dwordx4 v[196:199], v27, s[36:39], s65 offen offset:1024 sc1\n"
    "v_mfma_f32_16x16x32_bf16 a[88:91], a[248:251], v[92:95], a[88:91]\n"
    "buffer_store_dwordx4 v[88:91], v27, s[52:55], s64 offen offset:2048 sc1\n"
    "v_mfma_f32_16x16x32_bf16 a[88:91], a[252:255], v[120:123], a[88:91]\n"
    "s_mov_b32 s64, 12288\n"
    "ds_read_b64_tr_b16 a[240:241], v17 offset:33280\n"
    "s_add_u32 s65, s9, 0\n"
    "v_mfma_f32_16x16x32_bf16 a[92:95], a[248:251], v[96:99], a[92:95]\n"
    "ds_read_b64_tr_b16 a[242:243], v17 offset:37376\n"
    "v_mfma_f32_16x16x32_bf16 a[92:95], a[252:255], v[124:127], a[92:95]\n"
    "ds_read_b64_tr_b16 a[244:245], v17 offset:41472\n"
    "v_mfma_f32_16x16x32_bf16 a[96:99], a[248:251], v[100:103], a[96:99]\n"
    "ds_read_b64_tr_b16 a[246:247], v17 offset:45568\n"
    "v_mfma_f32_16x16x32_bf16 a[96:99], a[252:255], v[128:131], a[96:99]\n"
    "buffer_store_dwordx4 v[116:119], v27, s[52:55], s64 offen offset:3072 sc1\n"
    "v_mfma_f32_16x16x32_bf16 a[100:103], a[248:251], v[104:107], a[100:103]\n"
    "s_mov_b32 s64, 16384\n"
    "v_mfma_f32_16x16x32_bf16 a[100:103], a[252:255], v[132:135], a[100:103]\n"
    "v_mfma_f32_16x16x32_bf16 a[104:107], a[248:251], v[108:111], a[104:107]\n"
    "v_mfma_f32_16x16x32_bf16 a[104:107], a[252:255], v[136:139], a[104:107]\n"
    "v_mfma_f32_16x16x32_bf16 a[108:111], a[248:251], v[112:115], a[108:111]\n"
    "ds_read_b64_tr_b16 a[248:249], v17 offset:33536\n"
    "v_mfma_f32_16x16x32_bf16 a[108:111], a[252:255], v[140:143], a[108:111]\n"
    "ds_read_b64_tr_b16 a[250:251], v17 offset:37632\n"
    "v_mfma_f32_16x16x32_bf16 a[112:115], a[224:227], v[88:91], a[112:115]\n"
    "s_waitcnt lgkmcnt(6)\n"
    "ds_read_b64_tr_b16 a[252:253], v17 offset:41728\n"
    "v_mfma_f32_16x16x32_bf16 a[112:115], a[228:231], v[116:119], a[112:115]\n"
    "ds_read_b64_tr_b16 a[254:255], v17 offset:45824\n"
    "v_mfma_f32_16x16x32_bf16 a[116:119], a[224:227], v[92:95], a[116:119]\n"
    "buffer_store_dwordx4 v[92:95], v27, s[52:55], s64 offen sc1\n"
    "v_mfma_f32_16x16x32_bf16 a[116:119], a[228:231], v[120:123], a[116:119]\n"
    "s_mov_b32 s64, 16384\n"
    "v_mfma_f32_16x16x32_bf16 a[120:123], a[224:227], v[96:99], a[120:123]\n"
    "v_mfma_f32_16x16x32_bf16 a[120:123], a[228:231], v[124:127], a[120:123]\n"
    "v_mfma_f32_16x16x32_bf16 a[124:127], a[224:227], v[100:103], a[124:127]\n"
    "v_mfma_f32_16x16x32_bf16 a[124:127], a[228:231], v[128:131], a[124:127]\n"
    "v_mfma_f32_16x16x32_bf16 a[128:131], a[224:227], v[104:107], a[128:131]\n"
    "v_mfma_f32_16x16x32_bf16 a[128:131], a[228:231], v[132:135], a[128:131]\n"
    "v_mfma_f32_16x16x32_bf16 a[132:135], a[224:227], v[108:111], a[132:135]\n"
    "v_mfma_f32_16x16x32_bf16 a[132:135], a[228:231], v[136:139], a[132:135]\n"
    "buffer_store_dwordx4 v[120:123], v27, s[52:55], s64 offen offset:1024 sc1\n"
    "v_mfma_f32_16x16x32_bf16 a[136:139], a[224:227], v[112:115], a[136:139]\n"
    "s_mov_b32 s64, 16384\n"
    "v_mfma_f32_16x16x32_bf16 a[136:139], a[228:231], v[140:143], a[136:139]\n"
    "v_mfma_f32_16x16x32_bf16 a[140:143], a[232:235], v[88:91], a[140:143]\n"
    "v_mfma_f32_16x16x32_bf16 a[140:143], a[236:239], v[116:119], a[140:143]\n"
    "v_mfma_f32_16x16x32_bf16 a[144:147], a[232:235], v[92:95], a[144:147]\n"
    "v_mfma_f32_16x16x32_bf16 a[144:147], a[236:239], v[120:123], a[144:147]\n"
    "v_mfma_f32_16x16x32_bf16 a[148:151], a[232:235], v[96:99], a[148:151]\n"
    "v_mfma_f32_16x16x32_bf16 a[148:151], a[236:239], v[124:127], a[148:151]\n"
    "v_mfma_f32_16x16x32_bf16 a[152:155], a[232:235], v[100:103], a[152:155]\n"
    "buffer_store_dwordx4 v[96:99], v27, s[52:55], s64 offen offset:2048 sc1\n"
    "v_mfma_f32_16x16x32_bf16 a[152:155], a[236:239], v[128:131], a[152:155]\n"
    "s_mov_b32 s64, 16384\n"
    "v_mfma_f32_16x16x32_bf16 a[156:159], a[232:235], v[104:107], a[156:159]\n"
    "s_waitcnt lgkmcnt(0)\n"
    "s_barrier\n"
    "ds_read_b64_tr_b16 a[224:225], v15 offset:0\n"
    "v_mfma_f32_16x16x32_bf16 a[156:159], a[236:239], v[132:135], a[156:159]\n"
    "ds_read_b64_tr_b16 a[226:227], v15 offset:4096\n"
    "v_mfma_f32_16x16x32_bf16 a[160:163], a[232:235], v[108:111], a[160:163]\n"
    "ds_read_b64_tr_b16 a[228:229], v15 offset:8192\n"
    "v_mfma_f32_16x16x32_bf16 a[160:163], a[236:239], v[136:139], a[160:163]\n"
    "ds_read_b64_tr_b16 a[230:231], v15 offset:12288\n"
    "v_mfma_f32_16x16x32_bf16 a[164:167], a[232:235], v[112:115], a[164:167]\n"
    "ds_read_b64_tr_b16 a[232:233], v15 offset:256\n"
    "v_mfma_f32_16x16x32_bf16 a[164:167], a[236:239], v[140:143], a[164:167]\n"
    "ds_read_b64_tr_b16 a[234:235], v15 offset:4352\n"
    "v_mfma_f32_16x16x32_bf16 a[168:171], a[240:243], v[88:91], a[168:171]\n"
    "ds_read_b64_tr_b16 a[236:237], v15 offset:8448\n"
    "v_mfma_f32_16x16x32_bf16 a[168:171], a[244:247], v[116:119], a[168:171]\n"
    "buffer_store_dwordx4 v[124:127], v27, s[52:55], s64 offen offset:3072 sc1\n"
    "v_mfma_f32_16x16x32_bf16 a[172:175], a[240:243], v[92:95], a[172:175]\n"
    "s_mov_b32 s64, 20480\n"
    "ds_read_b64_tr_b16 a[238:239], v15 offset:12544\n"
    "v_mfma_f32_16x16x32_bf16 a[172:175], a[244:247], v[120:123], a[172:175]\n"
    "v_mfma_f32_16x16x32_bf16 a[176:179], a[240:243], v[96:99], a[176:179]\n"
    "v_mfma_f32_16x16x32_bf16 a[176:179], a[244:247], v[124:127], a[176:179]\n"
    "v_mfma_f32_16x16x32_bf16 a[180:183], a[240:243], v[100:103], a[180:183]\n"
    "v_mfma_f32_16x16x32_bf16 a[180:183], a[244:247], v[128:131], a[180:183]\n"
    "v_mfma_f32_16x16x32_bf16 a[184:187], a[240:243], v[104:107], a[184:187]\n"
    "v_mfma_f32_16x16x32_bf16 a[184:187], a[244:247], v[132:135], a[184:187]\n"
    "v_mfma_f32_16x16x32_bf16 a[188:191], a[240:243], v[108:111], a[188:191]\n"
    "v_mfma_f32_16x16x32_bf16 a[188:191], a[244:247], v[136:139], a[188:191]\n"
    "buffer_store_dwordx4 v[100:103], v27, s[52:55], s64 offen sc1\n"
    "v_mfma_f32_16x16x32_bf16 a[192:195], a[240:243], v[112:115], a[192:195]\n"
    "s_mov_b32 s64, 20480\n"
    "ds_read_b64_tr_b16 a[240:241], v15 offset:512\n"
    "v_mfma_f32_16x16x32_bf16 a[192:195], a[244:247], v[140:143], a[192:195]\n"
    "ds_read_b64_tr_b16 a[242:243], v15 offset:4608\n"
    "v_mfma_f32_16x16x32_bf16 a[196:199], a[248:251], v[88:91], a[196:199]\n"
    "ds_read_b64_tr_b16 a[244:245], v15 offset:8704\n"
    "v_mfma_f32_16x16x32_bf16 a[196:199], a[252:255], v[116:119], a[196:199]\n"
    "ds_read_b64_tr_b16 a[246:247], v15 offset:12800\n"
    "v_mfma_f32_16x16x32_bf16 a[200:203], a[248:251], v[92:95], a[200:203]\n"
    "v_mfma_f32_16x16x32_bf16 a[200:203], a[252:255], v[120:123], a[200:203]\n"
    "v_mfma_f32_16x16x32_bf16 a[204:207], a[248:251], v[96:99], a[204:207]\n"
    "v_mfma_f32_16x16x32_bf16 a[204:207], a[252:255], v[124:127], a[204:207]\n"
    "v_mfma_f32_16x16x32_bf16 a[208:211], a[248:251], v[100:103], a[208:211]\n"
    "buffer_store_dwordx4 v[128:131], v27, s[52:55], s64 offen offset:1024 sc1\n"
    "v_mfma_f32_16x16x32_bf16 a[208:211], a[252:255], v[128:131], a[208:211]\n"
    "s_mov_b32 s64, 20480\n"
    "v_mfma_f32_16x16x32_bf16 a[212:215], a[248:251], v[104:107], a[212:215]\n"
    "v_mfma_f32_16x16x32_bf16 a[212:215], a[252:255], v[132:135], a[212:215]\n"
    "v_mfma_f32_16x16x32_bf16 a[216:219], a[248:251], v[108:111], a[216:219]\n"
    "v_mfma_f32_16x16x32_bf16 a[216:219], a[252:255], v[136:139], a[216:219]\n"
    "v_mfma_f32_16x16x32_bf16 a[220:223], a[248:251], v[112:115], a[220:223]\n"
    "ds_read_b64_tr_b16 a[248:249], v15 offset:768\n"
    "v_mfma_f32_16x16x32_bf16 a[220:223], a[252:255], v[140:143], a[220:223]\n"
    "s_waitcnt vmcnt(21) lgkmcnt(1)\n"
    "ds_read_b64_tr_b16 a[250:251], v15 offset:4864\n"
    "v_mfma_f32_16x16x32_bf16 a[0:3], a[224:227], v[144:147], a[0:3]\n"
    "s_waitcnt vmcnt(20)\n"
    "ds_read_b64_tr_b16 a[252:253], v15 offset:8960\n"
    "v_mfma_f32_16x16x32_bf16 a[0:3], a[228:231], v[172:175], a[0:3]\n"
    "s_waitcnt vmcnt(19)\n"
    "ds_read_b64_tr_b16 a[254:255], v15 offset:13056\n"
    "v_mfma_f32_16x16x32_bf16 a[4:7], a[224:227], v[148:151], a[4:7]\n"
    "s_waitcnt vmcnt(18)\n"
    "v_mfma_f32_16x16x32_bf16 a[4:7], a[228:231], v[176:179], a[4:7]\n"
    "s_waitcnt vmcnt(17)\n"
    "v_mfma_f32_16x16x32_bf16 a[8:11], a[224:227], v[152:155], a[8:11]\n"
    "s_waitcnt vmcnt(16)\n"
    "v_mfma_f32_16x16x32_bf16 a[8:11], a[228:231], v[180:183], a[8:11]\n"
    "s_waitcnt vmcnt(15)\n"
    "v_mfma_f32_16x16x32_bf16 a[12:15], a[224:227], v[156:159], a[12:15]\n"
    "s_waitcnt vmcnt(14)\n"
    "v_mfma_f32_16x16x32_bf16 a[12:15], a[228:231], v[184:187], a[12:15]\n"
    "s_waitcnt vmcnt(13)\n"
    "v_mfma_f32_16x16x32_bf16 a[16:19], a[224:227], v[160:163], a[16:19]\n"
    "s_waitcnt vmcnt(12)\n"
    "v_mfma_f32_16x16x32_bf16 a[16:19], a[228:231], v[188:191], a[16:19]\n"
    "s_waitcnt vmcnt(11)\n"
    "v_mfma_f32_16x16x32_bf16 a[20:23], a[224:227], v[164:167], a[20:23]\n"
    "s_waitcnt vmcnt(10)\n"
    "v_mfma_f32_16x16x32_bf16 a[20:23], a[228:231], v[192:195], a[20:23]\n"
    "s_waitcnt vmcnt(9)\n"
    "v_mfma_f32_16x16x32_bf16 a[24:27], a[224:227], v[168:171], a[24:27]\n"
    "s_waitcnt vmcnt(8)\n"
    "ds_read_b64_tr_b16 a[224:225], v18 offset:0\n"
    "v_mfma_f32_16x16x32_bf16 a[24:27], a[228:231], v[196:199], a[24:27]\n"
    "buffer_store_dwordx4 v[104:107], v27, s[52:55], s64 offen offset:2048 sc1\n"
    "v_mfma_f32_16x16x32_bf16 a[28:31], a[232:235], v[144:147], a[28:31]\n"
    "buffer_load_dwordx4 v[200:203], v27, s[36:39], s65 offen sc1\n"
    "v_mfma_f32_16x16x32_bf16 a[28:31], a[236:239], v[172:175], a[28:31]\n"
    "buffer_load_dwordx4 v[228:231], v27, s[36:39], s65 offen offset:1024 sc1\n"
    "v_mfma_f32_16x16x32_bf16 a[32:35], a[232:235], v[148:151], a[32:35]\n"
    "s_mov_b32 s64, 20480\n"
    "ds_read_b64_tr_b16 a[226:227], v18 offset:4096\n"
    "v_mfma_f32_16x16x32_bf16 a[32:35], a[236:239], v[176:179], a[32:35]\n"
    "ds_read_b64_tr_b16 a[228:229], v18 offset:8192\n"
    "v_mfma_f32_16x16x32_bf16 a[36:39], a[232:235], v[152:155], a[36:39]\n"
    "buffer_load_dwordx4 v[204:207], v27, s[36:39], s65 offen offset:2048 sc1\n"
    "v_mfma_f32_16x16x32_bf16 a[36:39], a[236:239], v[180:183], a[36:39]\n"
    "ds_read_b64_tr_b16 a[230:231], v18 offset:12288\n"
    "v_mfma_f32_16x16x32_bf16 a[40:43], a[232:235], v[156:159], a[40:43]\n"
    "v_mfma_f32_16x16x32_bf16 a[40:43], a[236:239], v[184:187], a[40:43]\n"
    "v_mfma_f32_16x16x32_bf16 a[44:47], a[232:235], v[160:163], a[44:47]\n"
    "buffer_load_dwordx4 v[232:235], v27, s[36:39], s65 offen offset:3072 sc1\n"
    "v_mfma_f32_16x16x32_bf16 a[44:47], a[236:239], v[188:191], a[44:47]\n"
    "s_add_u32 s65, s9, 4096\n"
    "v_mfma_f32_16x16x32_bf16 a[48:51], a[232:235], v[164:167], a[48:51]\n"
    "v_mfma_f32_16x16x32_bf16 a[48:51], a[236:239], v[192:195], a[48:51]\n"
    "v_mfma_f32_16x16x32_bf16 a[52:55], a[232:235], v[168:171], a[52:55]\n"
    "buffer_load_dwordx4 v[208:211], v27, s[36:39], s65 offen sc1\n"
    "v_mfma_f32_16x16x32_bf16 a[52:55], a[236:239], v[196:199], a[52:55]\n"
    "ds_read_b64_tr_b16 a[232:233], v18 offset:256\n"
    "v_mfma_f32_16x16x32_bf16 a[56:59], a[240:243], v[144:147], a[56:59]\n"
    "ds_read_b64_tr_b16 a[234:235], v18 offset:4352\n"
    "v_mfma_f32_16x16x32_bf16 a[56:59], a[244:247], v[172:175], a[56:59]\n"
    "ds_read_b64_tr_b16 a[236:237], v18 offset:8448\n"
    "v_mfma_f32_16x16x32_bf16 a[60:63], a[240:243], v[148:151], a[60:63]\n"
    "buffer_load_dwordx4 v[236:239], v27, s[36:39], s65 offen offset:1024 sc1\n"
    "v_mfma_f32_16x16x32_bf16 a[60:63], a[244:247], v[176:179], a[60:63]\n"
    "ds_read_b64_tr_b16 a[238:239], v18 offset:12544\n"
    "v_mfma_f32_16x16x32_bf16 a[64:67], a[240:243], v[152:155], a[64:67]\n"
    "v_mfma_f32_16x16x32_bf16 a[64:67], a[244:247], v[180:183], a[64:67]\n"
    "v_mfma_f32_16x16x32_bf16 a[68:71], a[240:243], v[156:159], a[68:71]\n"
    "buffer_load_dwordx4 v[212:215], v27, s[36:39], s65 offen offset:2048 sc1\n"
    "v_mfma_f32_16x16x32_bf16 a[68:71], a[244:247], v[184:187], a[68:71]\n"
    "v_mfma_f32_16x16x32_bf16 a[72:75], a[240:243], v[160:163], a[72:75]\n"
    "v_mfma_f32_16x16x32_bf16 a[72:75], a[244:247], v[188:191], a[72:75]\n"
    "v_mfma_f32_16x16x32_bf16 a[76:79], a[240:243], v[164:167], a[76:79]\n"
    "buffer_load_dwordx4 v[240:243], v27, s[36:39], s65 offen offset:3072 sc1\n"
    "v_mfma_f32_16x16x32_bf16 a[76:79], a[244:247], v[192:195], a[76:79]\n"
    "s_add_u32 s65, s9, 8192\n"
    "v_mfma_f32_16x16x32_bf16 a[80:83], a[240:243], v[168:171], a[80:83]\n"
    "ds_read_b64_tr_b16 a[240:241], v18 offset:512\n"
    "v_mfma_f32_16x16x32_bf16 a[80:83], a[244:247], v[196:199], a[80:83]\n"
    "s_waitcnt lgkmcnt(2)\n"
    "ds_read_b64_tr_b16 a[242:243], v18 offset:4608\n"
    "v_mfma_f32_16x16x32_bf16 a[84:87], a[248:251], v[144:147], a[84:87]\n"
    "buffer_load_dwordx4 v[216:219], v27, s[36:39], s65 offen sc1\n"
    "v_mfma_f32_16x16x32_bf16 a[84:87], a[252:255], v[172:175], a[84:87]\n"
    "ds_read_b64_tr_b16 a[244:245], v18 offset:8704\n"
    "v_mfma_f32_16x16x32_bf16 a[88:91], a[248:251], v[148:151], a[88:91]\n"
    "ds_read_b64_tr_b16 a[246:247], v18 offset:12800\n"
    "v_mfma_f32_16x16x32_bf16 a[88:91], a[252:255], v[176:179], a[88:91]\n"
    "v_mfma_f32_16x16x32_bf16 a[92:95], a[248:251], v[152:155], a[92:95]\n"
    "buffer_load_dwordx4 v[244:247], v27, s[36:39], s65 offen offset:1024 sc1\n"
    "v_mfma_f32_16x16x32_bf16 a[92:95], a[252:255], v[180:183], a[92:95]\n"
    "v_mfma_f32_16x16x32_bf16 a[96:99], a[248:251], v[156:159], a[96:99]\n"
    "v_mfma_f32_16x16x32_bf16 a[96:99], a[252:255], v[184:187], a[96:99]\n"
    "v_mfma_f32_16x16x32_bf16 a[100:103], a[248:251], v[160:163], a[100:103]\n"
    "buffer_load_dwordx4 v[220:223], v27, s[36:39], s65 offen offset:2048 sc1\n"
    "v_mfma_f32_16x16x32_bf16 a[100:103], a[252:255], v[188:191], a[100:103]\n"
    "v_mfma_f32_16x16x32_bf16 a[104:107], a[248:251], v[164:167], a[104:107]\n"
    "v_mfma_f32_16x16x32_bf16 a[104:107], a[252:255], v[192:195], a[104:107]\n"
    "v_mfma_f32_16x16x32_bf16 a[108:111], a[248:251], v[168:171], a[108:111]\n"
    "buffer_load_dwordx4 v[248:251], v27, s[36:39], s65 offen offset:3072 sc1\n"
    "v_mfma_f32_16x16x32_bf16 a[108:111], a[252:255], v[196:199], a[108:111]\n"
    "s_add_u32 s65, s9, 12288\n"
    "ds_read_b64_tr_b16 a[248:249], v18 offset:768\n"
    "v_mfma_f32_16x16x32_bf16 a[112:115], a[224:227], v[144:147], a[112:115]\n"
    "ds_read_b64_tr_b16 a[250:251], v18 offset:4864\n"
    "v_mfma_f32_16x16x32_bf16 a[112:115], a[228:231], v[172:175], a[112:115]\n"
    "ds_read_b64_tr_b16 a[252:253], v18 offset:8960\n"
    "v_mfma_f32_16x16x32_bf16 a[116:119], a[224:227], v[148:151], a[116:119]\n"
    "buffer_load_dwordx4 v[224:227], v27, s[36:39], s65 offen sc1\n"
    "v_mfma_f32_16x16x32_bf16 a[116:119], a[228:231], v[176:179], a[116:119]\n"
    "ds_read_b64_tr_b16 a[254:255], v18 offset:13056\n"
    "v_mfma_f32_16x16x32_bf16 a[120:123], a[224:227], v[152:155], a[120:123]\n"
    "v_mfma_f32_16x16x32_bf16 a[120:123], a[228:231], v[180:183], a[120:123]\n"
    "v_mfma_f32_16x16x32_bf16 a[124:127], a[224:227], v[156:159], a[124:127]\n"
    "buffer_load_dwordx4 v[252:255], v27, s[36:39], s65 offen offset:1024 sc1\n"
    "v_mfma_f32_16x16x32_bf16 a[124:127], a[228:231], v[184:187], a[124:127]\n"
    "s_add_u32 s65, s14, 0\n"
    "v_mfma_f32_16x16x32_bf16 a[128:131], a[224:227], v[160:163], a[128:131]\n"
    "v_mfma_f32_16x16x32_bf16 a[128:131], a[228:231], v[188:191], a[128:131]\n"
    "v_mfma_f32_16x16x32_bf16 a[132:135], a[224:227], v[164:167], a[132:135]\n"
    "buffer_store_dwordx4 v[132:135], v27, s[52:55], s64 offen offset:3072 sc1\n"
    "v_mfma_f32_16x16x32_bf16 a[132:135], a[228:231], v[192:195], a[132:135]\n"
    "s_mov_b32 s64, 24576\n"
    "v_mfma_f32_16x16x32_bf16 a[136:139], a[224:227], v[168:171], a[136:139]\n"
    "v_mfma_f32_16x16x32_bf16 a[136:139], a[228:231], v[196:199], a[136:139]\n"
    "v_mfma_f32_16x16x32_bf16 a[140:143], a[232:235], v[144:147], a[140:143]\n"
    "s_waitcnt lgkmcnt(1)\n"
    "v_mfma_f32_16x16x32_bf16 a[140:143], a[236:239], v[172:175], a[140:143]\n"
    "v_mfma_f32_16x16x32_bf16 a[144:147], a[232:235], v[148:151], a[144:147]\n"
    "v_mfma_f32_16x16x32_bf16 a[144:147], a[236:239], v[176:179], a[144:147]\n"
    "v_mfma_f32_16x16x32_bf16 a[148:151], a[232:235], v[152:155], a[148:151]\n"
    "v_mfma_f32_16x16x32_bf16 a[148:151], a[236:239], v[180:183], a[148:151]\n"
    "buffer_store_dwordx4 v[108:111], v27, s[52:55], s64 offen sc1\n"
    "v_mfma_f32_16x16x32_bf16 a[152:155], a[232:235], v[156:159], a[152:155]\n"
    "s_mov_b32 s64, 24576\n"
    "v_mfma_f32_16x16x32_bf16 a[152:155], a[236:239], v[184:187], a[152:155]\n"
    "v_mfma_f32_16x16x32_bf16 a[156:159], a[232:235], v[160:163], a[156:159]\n"
    "s_waitcnt lgkmcnt(0)\n"
    "s_barrier\n"
    "ds_read_b64_tr_b16 a[224:225], v16 offset:0\n"
    "v_mfma_f32_16x16x32_bf16 a[156:159], a[236:239], v[188:191], a[156:159]\n"
    "ds_read_b64_tr_b16 a[226:227], v16 offset:4096\n"
    "v_mfma_f32_16x16x32_bf16 a[160:163], a[232:235], v[164:167], a[160:163]\n"
    "ds_read_b64_tr_b16 a[228:229], v15 offset:32768\n"
    "v_mfma_f32_16x16x32_bf16 a[160:163], a[236:239], v[192:195], a[160:163]\n"
    "ds_read_b64_tr_b16 a[230:231], v15 offset:36864\n"
    "v_mfma_f32_16x16x32_bf16 a[164:167], a[232:235], v[168:171], a[164:167]\n"
    "ds_read_b64_tr_b16 a[232:233], v16 offset:256\n"
    "v_mfma_f32_16x16x32_bf16 a[164:167], a[236:239], v[196:199], a[164:167]\n"
    "ds_read_b64_tr_b16 a[234:235], v16 offset:4352\n"
    "v_mfma_f32_16x16x32_bf16 a[168:171], a[240:243], v[144:147], a[168:171]\n"
    "ds_read_b64_tr_b16 a[236:237], v15 offset:33024\n"
    "v_mfma_f32_16x16x32_bf16 a[168:171], a[244:247], v[172:175], a[168:171]\n"
    "buffer_store_dwordx4 v[136:139], v27, s[52:55], s64 offen offset:1024 sc1\n"
    "v_mfma_f32_16x16x32_bf16 a[172:175], a[240:243], v[148:151], a[172:175]\n"
    "s_mov_b32 s64, 24576\n"
    "ds_read_b64_tr_b16 a[238:239], v15 offset:37120\n"
    "v_mfma_f32_16x16x32_bf16 a[172:175], a[244:247], v[176:179], a[172:175]\n"
    "v_mfma_f32_16x16x32_bf16 a[176:179], a[240:243], v[152:155], a[176:179]\n"
    "v_mfma_f32_16x16x32_bf16 a[176:179], a[244:247], v[180:183], a[176:179]\n"
    "v_mfma_f32_16x16x32_bf16 a[180:183], a[240:243], v[156:159], a[180:183]\n"
    "v_mfma_f32_16x16x32_bf16 a[180:183], a[244:247], v[184:187], a[180:183]\n"
    "v_mfma_f32_16x16x32_bf16 a[184:187], a[240:243], v[160:163], a[184:187]\n"
    "v_mfma_f32_16x16x32_bf16 a[184:187], a[244:247], v[188:191], a[184:187]\n"
    "v_mfma_f32_16x16x32_bf16 a[188:191], a[240:243], v[164:167], a[188:191]\n"
    "buffer_store_dwordx4 v[112:115], v27, s[52:55], s64 offen offset:2048 sc1\n"
    "v_mfma_f32_16x16x32_bf16 a[188:191], a[244:247], v[192:195], a[188:191]\n"
    "s_mov_b32 s64, 24576\n"
    "v_mfma_f32_16x16x32_bf16 a[192:195], a[240:243], v[168:171], a[192:195]\n"
    "ds_read_b64_tr_b16 a[240:241], v16 offset:512\n"
    "v_mfma_f32_16x16x32_bf16 a[192:195], a[244:247], v[196:199], a[192:195]\n"
    "ds_read_b64_tr_b16 a[242:243], v16 offset:4608\n"
    "v_mfma_f32_16x16x32_bf16 a[196:199], a[248:251], v[144:147], a[196:199]\n"
    "ds_read_b64_tr_b16 a[244:245], v15 offset:33280\n"
    "v_mfma_f32_16x16x32_bf16 a[196:199], a[252:255], v[172:175], a[196:199]\n"
    "ds_read_b64_tr_b16 a[246:247], v15 offset:37376\n"
    "v_mfma_f32_16x16x32_bf16 a[200:203], a[248:251], v[148:151], a[200:203]\n"
    "v_mfma_f32_16x16x32_bf16 a[200:203], a[252:255], v[176:179], a[200:203]\n"
    "v_mfma_f32_16x16x32_bf16 a[204:207], a[248:251], v[152:155], a[204:207]\n"
    "v_mfma_f32_16x16x32_bf16 a[204:207], a[252:255], v[180:183], a[204:207]\n"
    "buffer_store_dwordx4 v[140:143], v27, s[52:55], s64 offen offset:3072 sc1\n"
    "v_mfma_f32_16x16x32_bf16 a[208:211], a[248:251], v[156:159], a[208:211]\n"
    "s_xor_b32 s64, s19, 1\n"
    "s_mul_i32 s64, s64, 53248\n"
    "s_add_u32 s64, s64, 0\n"
    "v_mfma_f32_16x16x32_bf16 a[208:211], a[252:255], v[184:187], a[208:211]\n"
    "v_mfma_f32_16x16x32_bf16 a[212:215], a[248:251], v[160:163], a[212:215]\n"
    "v_mfma_f32_16x16x32_bf16 a[212:215], a[252:255], v[188:191], a[212:215]\n"
    "v_mfma_f32_16x16x32_bf16 a[216:219], a[248:251], v[164:167], a[216:219]\n"
    "v_mfma_f32_16x16x32_bf16 a[216:219], a[252:255], v[192:195], a[216:219]\n"
    "v_mfma_f32_16x16x32_bf16 a[220:223], a[248:251], v[168:171], a[220:223]\n"
    "ds_read_b64_tr_b16 a[248:249], v16 offset:768\n"
    "v_mfma_f32_16x16x32_bf16 a[220:223], a[252:255], v[196:199], a[220:223]\n"
    "s_waitcnt vmcnt(11) lgkmcnt(1)\n"
    "ds_read_b64_tr_b16 a[250:251], v16 offset:4864\n"
    "v_mfma_f32_16x16x32_bf16 a[0:3], a[224:227], v[200:203], a[0:3]\n"
    "buffer_load_dwordx4 v[32:35], v27, s[36:39], s65 offen sc1\n"
    "v_mfma_f32_16x16x32_bf16 a[0:3], a[228:231], v[228:231], a[0:3]\n"
    "ds_read_b64_tr_b16 a[252:253], v15 offset:33536\n"
    "v_mfma_f32_16x16x32_bf16 a[4:7], a[224:227], v[204:207], a[4:7]\n"
    "ds_read_b64_tr_b16 a[254:255], v15 offset:37632\n"
    "v_mfma_f32_16x16x32_bf16 a[4:7], a[228:231], v[232:235], a[4:7]\n"
    "v_mfma_f32_16x16x32_bf16 a[8:11], a[224:227], v[208:211], a[8:11]\n"
    "buffer_load_dwordx4 v[60:63], v27, s[36:39], s65 offen offset:1024 sc1\n"
    "v_mfma_f32_16x16x32_bf16 a[8:11], a[228:231], v[236:239], a[8:11]\n"
    "v_mfma_f32_16x16x32_bf16 a[12:15], a[224:227], v[212:215], a[12:15]\n"
    "v_mfma_f32_16x16x32_bf16 a[12:15], a[228:231], v[240:243], a[12:15]\n"
    "s_waitcnt vmcnt(12)\n"
    "v_mfma_f32_16x16x32_bf16 a[16:19], a[224:227], v[216:219], a[16:19]\n"
    "s_waitcnt vmcnt(11)\n"
    "v_mfma_f32_16x16x32_bf16 a[16:19], a[228:231], v[244:247], a[16:19]\n"
    "s_waitcnt vmcnt(10)\n"
    "v_mfma_f32_16x16x32_bf16 a[20:23], a[224:227], v[220:223], a[20:23]\n"
    "s_waitcnt vmcnt(9)\n"
    "v_mfma_f32_16x16x32_bf16 a[20:23], a[228:231], v[248:251], a[20:23]\n"
    "s_waitcnt vmcnt(8)\n"
    "v_mfma_f32_16x16x32_bf16 a[24:27], a[224:227], v[224:227], a[24:27]\n"
    "s_waitcnt vmcnt(7)\n"
    "ds_read_b64_tr_b16 a[224:225], v19 offset:0\n"
    "v_mfma_f32_16x16x32_bf16 a[24:27], a[228:231], v[252:255], a[24:27]\n"
    "buffer_load_dwordx4 v[36:39], v27, s[36:39], s65 offen offset:2048 sc1\n"
    "v_mfma_f32_16x16x32_bf16 a[28:31], a[232:235], v[200:203], a[28:31]\n"
    "buffer_load_dwordx4 v[64:67], v27, s[36:39], s65 offen offset:3072 sc1\n"
    "v_mfma_f32_16x16x32_bf16 a[28:31], a[236:239], v[228:231], a[28:31]\n"
    "s_add_u32 s65, s14, 4096\n"
    "ds_read_b64_tr_b16 a[226:227], v19 offset:4096\n"
    "v_mfma_f32_16x16x32_bf16 a[32:35], a[232:235], v[204:207], a[32:35]\n"
    "ds_read_b64_tr_b16 a[228:229], v18 offset:32768\n"
    "v_mfma_f32_16x16x32_bf16 a[32:35], a[236:239], v[232:235], a[32:35]\n"
    "ds_read_b64_tr_b16 a[230:231], v18 offset:36864\n"
    "v_mfma_f32_16x16x32_bf16 a[36:39], a[232:235], v[208:211], a[36:39]\n"
    "v_mfma_f32_16x16x32_bf16 a[36:39], a[236:239], v[236:239], a[36:39]\n"
    "v_mfma_f32_16x16x32_bf16 a[40:43], a[232:235], v[212:215], a[40:43]\n"
    "v_mfma_f32_16x16x32_bf16 a[40:43], a[236:239], v[240:243], a[40:43]\n"
    "v_mfma_f32_16x16x32_bf16 a[44:47], a[232:235], v[216:219], a[44:47]\n"
    "v_mfma_f32_16x16x32_bf16 a[44:47], a[236:239], v[244:247], a[44:47]\n"
    "v_mfma_f32_16x16x32_bf16 a[48:51], a[232:235], v[220:223], a[48:51]\n"
    "s_waitcnt vmcnt(0)\n"
    "s_barrier\n"
    "v_mfma_f32_16x16x32_bf16 a[48:51], a[236:239], v[248:251], a[48:51]\n"
    "buffer_store_dword v4, v9, s[72:75], 0 offen sc1\n"
    "v_mfma_f32_16x16x32_bf16 a[52:55], a[232:235], v[224:227], a[52:55]\n"
    "ds_read_b64_tr_b16 a[232:233], v19 offset:256\n"
    "v_mfma_f32_16x16x32_bf16 a[52:55], a[236:239], v[252:255], a[52:55]\n"
    "ds_read_b64_tr_b16 a[234:235], v19 offset:4352\n"
    "v_mfma_f32_16x16x32_bf16 a[56:59], a[240:243], v[200:203], a[56:59]\n"
    "ds_read_b64_tr_b16 a[236:237], v18 offset:33024\n"
    "v_mfma_f32_16x16x32_bf16 a[56:59], a[244:247], v[228:231], a[56:59]\n"
    "buffer_load_dwordx4 v[40:43], v27, s[36:39], s65 offen sc1\n"
    "v_mfma_f32_16x16x32_bf16 a[60:63], a[240:243], v[204:207], a[60:63]\n"
    "ds_read_b64_tr_b16 a[238:239], v18 offset:37120\n"
    "v_mfma_f32_16x16x32_bf16 a[60:63], a[244:247], v[232:235], a[60:63]\n"
    "v_mfma_f32_16x16x32_bf16 a[64:67], a[240:243], v[208:211], a[64:67]\n"
    "v_mfma_f32_16x16x32_bf16 a[64:67], a[244:247], v[236:239], a[64:67]\n"
    "buffer_load_dwordx4 v[68:71], v27, s[36:39], s65 offen offset:1024 sc1\n"
    "v_mfma_f32_16x16x32_bf16 a[68:71], a[240:243], v[212:215], a[68:71]\n"
    "v_mfma_f32_16x16x32_bf16 a[68:71], a[244:247], v[240:243], a[68:71]\n"
    "v_mfma_f32_16x16x32_bf16 a[72:75], a[240:243], v[216:219], a[72:75]\n"
    "v_mfma_f32_16x16x32_bf16 a[72:75], a[244:247], v[244:247], a[72:75]\n"
    "buffer_load_dwordx4 v[44:47], v27, s[36:39], s65 offen offset:2048 sc1\n"
    "v_mfma_f32_16x16x32_bf16 a[76:79], a[240:243], v[220:223], a[76:79]\n"
    "v_mfma_f32_16x16x32_bf16 a[76:79], a[244:247], v[248:251], a[76:79]\n"
    "v_mfma_f32_16x16x32_bf16 a[80:83], a[240:243], v[224:227], a[80:83]\n"
    "ds_read_b64_tr_b16 a[240:241], v19 offset:512\n"
    "v_mfma_f32_16x16x32_bf16 a[80:83], a[244:247], v[252:255], a[80:83]\n"
    "s_waitcnt lgkmcnt(2)\n"
    "ds_read_b64_tr_b16 a[242:243], v19 offset:4608\n"
    "v_mfma_f32_16x16x32_bf16 a[84:87], a[248:251], v[200:203], a[84:87]\n"
    "buffer_load_dwordx4 v[72:75], v27, s[36:39], s65 offen offset:3072 sc1\n"
    "v_mfma_f32_16x16x32_bf16 a[84:87], a[252:255], v[228:231], a[84:87]\n"
    "s_add_u32 s65, s14, 8192\n"
    "ds_read_b64_tr_b16 a[244:245], v18 offset:33280\n"
    "v_mfma_f32_16x16x32_bf16 a[88:91], a[248:251], v[204:207], a[88:91]\n"
    "ds_read_b64_tr_b16 a[246:247], v18 offset:37376\n"
    "v_mfma_f32_16x16x32_bf16 a[88:91], a[252:255], v[232:235], a[88:91]\n"
    "buffer_load_dwordx4 v[48:51], v27, s[36:39], s65 offen sc1\n"
    "v_mfma_f32_16x16x32_bf16 a[92:95], a[248:251], v[208:211], a[92:95]\n"
    "v_mfma_f32_16x16x32_bf16 a[92:95], a[252:255], v[236:239], a[92:95]\n"
    "v_mfma_f32_16x16x32_bf16 a[96:99], a[248:251], v[212:215], a[96:99]\n"
    "v_mfma_f32_16x16x32_bf16 a[96:99], a[252:255], v[240:243], a[96:99]\n"
    "buffer_load_dwordx4 v[76:79], v27, s[36:39], s65 offen offset:1024 sc1\n"
    "v_mfma_f32_16x16x32_bf16 a[100:103], a[248:251], v[216:219], a[100:103]\n"
    "v_mfma_f32_16x16x32_bf16 a[100:103], a[252:255], v[244:247], a[100:103]\n"
    "v_mfma_f32_16x16x32_bf16 a[104:107], a[248:251], v[220:223], a[104:107]\n"
    "v_mfma_f32_16x16x32_bf16 a[104:107], a[252:255], v[248:251], a[104:107]\n"
    "v_mfma_f32_16x16x32_bf16 a[108:111], a[248:251], v[224:227], a[108:111]\n"
    "buffer_load_dwordx4 v[52:55], v27, s[36:39], s65 offen offset:2048 sc1\n"
    "v_mfma_f32_16x16x32_bf16 a[108:111], a[252:255], v[252:255], a[108:111]\n"
    "ds_read_b64_tr_b16 a[248:249], v19 offset:768\n"
    "v_mfma_f32_16x16x32_bf16 a[112:115], a[224:227], v[200:203], a[112:115]\n"
    "ds_read_b64_tr_b16 a[250:251], v19 offset:4864\n"
    "v_mfma_f32_16x16x32_bf16 a[112:115], a[228:231], v[228:231], a[112:115]\n"
    "ds_read_b64_tr_b16 a[252:253], v18 offset:33536\n"
    "v_mfma_f32_16x16x32_bf16 a[116:119], a[224:227], v[204:207], a[116:119]\n"
    "buffer_load_dwordx4 v[80:83], v27, s[36:39], s65 offen offset:3072 sc1\n"
    "v_mfma_f32_16x16x32_bf16 a[116:119], a[228:231], v[232:235], a[116:119]\n"
    "s_add_u32 s65, s14, 12288\n"
    "ds_read_b64_tr_b16 a[254:255], v18 offset:37632\n"
    "v_mfma_f32_16x16x32_bf16 a[120:123], a[224:227], v[208:211], a[120:123]\n"
    "v_mfma_f32_16x16x32_bf16 a[120:123], a[228:231], v[236:239], a[120:123]\n"
    "v_mfma_f32_16x16x32_bf16 a[124:127], a[224:227], v[212:215], a[124:127]\n"
    "buffer_load_dwordx4 v[56:59], v27, s[36:39], s65 offen sc1\n"
    "v_mfma_f32_16x16x32_bf16 a[124:127], a[228:231], v[240:243], a[124:127]\n"
    "v_mfma_f32_16x16x32_bf16 a[128:131], a[224:227], v[216:219], a[128:131]\n"
    "v_mfma_f32_16x16x32_bf16 a[128:131], a[228:231], v[244:247], a[128:131]\n"
    "v_mfma_f32_16x16x32_bf16 a[132:135], a[224:227], v[220:223], a[132:135]\n"
    "buffer_load_dwordx4 v[84:87], v27, s[36:39], s65 offen offset:1024 sc1\n"
    "v_mfma_f32_16x16x32_bf16 a[132:135], a[228:231], v[248:251], a[132:135]\n"
    "s_add_u32 s65, s8, 12288\n"
    "v_mfma_f32_16x16x32_bf16 a[136:139], a[224:227], v[224:227], a[136:139]\n"
    "v_mfma_f32_16x16x32_bf16 a[136:139], a[228:231], v[252:255], a[136:139]\n"
    "v_mfma_f32_16x16x32_bf16 a[140:143], a[232:235], v[200:203], a[140:143]\n"
    "s_waitcnt lgkmcnt(1)\n"
    "v_mfma_f32_16x16x32_bf16 a[140:143], a[236:239], v[228:231], a[140:143]\n"
    "buffer_load_dwordx4 v7, s[40:43], s58 offen lds\n"
    "v_mfma_f32_16x16x32_bf16 a[144:147], a[232:235], v[204:207], a[144:147]\n"
    "s_add_u32 m0, s57, 67584\n"
    "s_add_u32 s58, s10, 73728\n"
    "v_mfma_f32_16x16x32_bf16 a[144:147], a[236:239], v[232:235], a[144:147]\n"
    "v_mfma_f32_16x16x32_bf16 a[148:151], a[232:235], v[208:211], a[148:151]\n"
    "v_mfma_f32_16x16x32_bf16 a[148:151], a[236:239], v[236:239], a[148:151]\n"
    "buffer_load_dwordx4 v7, s[40:43], s58 offen lds\n"
    "v_mfma_f32_16x16x32_bf16 a[152:155], a[232:235], v[212:215], a[152:155]\n"
    "s_add_u32 m0, s57, 69632\n"
    "s_add_u32 s58, s10, 81920\n"
    "v_mfma_f32_16x16x32_bf16 a[152:155], a[236:239], v[240:243], a[152:155]\n"
    "v_mfma_f32_16x16x32_bf16 a[156:159], a[232:235], v[216:219], a[156:159]\n"
    "s_waitcnt lgkmcnt(0)\n"
    "s_barrier\n"
    "ds_read_b64_tr_b16 a[224:225], v15 offset:40960\n"
    "v_mfma_f32_16x16x32_bf16 a[156:159], a[236:239], v[244:247], a[156:159]\n"
    "buffer_load_dwordx4 v7, s[40:43], s58 offen lds\n"
    "v_mfma_f32_16x16x32_bf16 a[160:163], a[232:235], v[220:223], a[160:163]\n"
    "s_add_u32 m0, s57, 71680\n"
    "ds_read_b64_tr_b16 a[226:227], v15 offset:45056\n"
    "s_add_u32 s58, s10, 90112\n"
    "v_mfma_f32_16x16x32_bf16 a[160:163], a[236:239], v[248:251], a[160:163]\n"
    "ds_read_b64_tr_b16 a[228:229], v16 offset:8192\n"
    "v_mfma_f32_16x16x32_bf16 a[164:167], a[232:235], v[224:227], a[164:167]\n"
    "ds_read_b64_tr_b16 a[230:231], v16 offset:12288\n"
    "v_mfma_f32_16x16x32_bf16 a[164:167], a[236:239], v[252:255], a[164:167]\n"
    "buffer_load_dwordx4 v7, s[40:43], s58 offen lds\n"
    "v_mfma_f32_16x16x32_bf16 a[168:171], a[240:243], v[200:203], a[168:171]\n"
    "s_add_u32 m0, s57, 73728\n"
    "ds_read_b64_tr_b16 a[232:233], v15 offset:41216\n"
    "s_add_u32 s58, s10, 98304\n"
    "v_mfma_f32_16x16x32_bf16 a[168:171], a[244:247], v[228:231], a[168:171]\n"
    "ds_read_b64_tr_b16 a[234:235], v15 offset:45312\n"
    "v_mfma_f32_16x16x32_bf16 a[172:175], a[240:243], v[204:207], a[172:175]\n"
    "ds_read_b64_tr_b16 a[236:237], v16 offset:8448\n"
    "v_mfma_f32_16x16x32_bf16 a[172:175], a[244:247], v[232:235], a[172:175]\n"
    "buffer_load_dwordx4 v7, s[40:43], s58 offen lds\n"
    "v_mfma_f32_16x16x32_bf16 a[176:179], a[240:243], v[208:211], a[176:179]\n"
    "s_add_u32 m0, s57, 75776\n"
    "ds_read_b64_tr_b16 a[238:239], v16 offset:12544\n"
    "s_add_u32 s58, s10, 106496\n"
    "v_mfma_f32_16x16x32_bf16 a[176:179], a[244:247], v[236:239], a[176:179]\n"
    "v_mfma_f32_16x16x32_bf16 a[180:183], a[240:243], v[212:215], a[180:183]\n"
    "v_mfma_f32_16x16x32_bf16 a[180:183], a[244:247], v[240:243], a[180:183]\n"
    "buffer_load_dwordx4 v7, s[40:43], s58 offen lds\n"
    "v_mfma_f32_16x16x32_bf16 a[184:187], a[240:243], v[216:219], a[184:187]\n"
    "s_add_u32 m0, s57, 77824\n"
    "s_add_u32 s58, s10, 114688\n"
    "v_mfma_f32_16x16x32_bf16 a[184:187], a[244:247], v[244:247], a[184:187]\n"
    "v_mfma_f32_16x16x32_bf16 a[188:191], a[240:243], v[220:223], a[188:191]\n"
    "v_mfma_f32_16x16x32_bf16 a[188:191], a[244:247], v[248:251], a[188:191]\n"
    "v_mfma_f32_16x16x32_bf16 a[192:195], a[240:243], v[224:227], a[192:195]\n"
    "buffer_load_dwordx4 v7, s[40:43], s58 offen lds\n"
    "v_mfma_f32_16x16x32_bf16 a[192:195], a[244:247], v[252:255], a[192:195]\n"
    "s_add_u32 m0, s57, 79872\n"
    "ds_read_b64_tr_b16 a[240:241], v15 offset:41472\n"
    "s_add_u32 s58, s10, 122880\n"
    "v_mfma_f32_16x16x32_bf16 a[196:199], a[248:251], v[200:203], a[196:199]\n"
    "ds_read_b64_tr_b16 a[242:243], v15 offset:45568\n"
    "v_mfma_f32_16x16x32_bf16 a[196:199], a[252:255], v[228:231], a[196:199]\n"
    "ds_read_b64_tr_b16 a[244:245], v16 offset:8704\n"
    "v_mfma_f32_16x16x32_bf16 a[200:203], a[248:251], v[204:207], a[200:203]\n"
    "buffer_load_dwordx4 v7, s[40:43], s58 offen lds\n"
    "v_mfma_f32_16x16x32_bf16 a[200:203], a[252:255], v[232:235], a[200:203]\n"
    "s_add_u32 m0, s57, 32768\n"
    "ds_read_b64_tr_b16 a[246:247], v16 offset:12800\n"
    "s_add_u32 s58, s12, 65536\n"
    "v_mfma_f32_16x16x32_bf16 a[204:207], a[248:251], v[208:211], a[204:207]\n"
    "v_mfma_f32_16x16x32_bf16 a[204:207], a[252:255], v[236:239], a[204:207]\n"
    "v_mfma_f32_16x16x32_bf16 a[208:211], a[248:251], v[212:215], a[208:211]\n"
    "buffer_load_dword v6, v31, s[68:71], 0 offen sc1\n"
    "v_mfma_f32_16x16x32_bf16 a[208:211], a[252:255], v[240:243], a[208:211]\n"
    "v_mfma_f32_16x16x32_bf16 a[212:215], a[248:251], v[216:219], a[212:215]\n"
    "v_mfma_f32_16x16x32_bf16 a[212:215], a[252:255], v[244:247], a[212:215]\n"
    "v_mfma_f32_16x16x32_bf16 a[216:219], a[248:251], v[220:223], a[216:219]\n"
    "buffer_load_dwordx4 v7, s[40:43], s58 offen lds\n"
    "v_mfma_f32_16x16x32_bf16 a[216:219], a[252:255], v[248:251], a[216:219]\n"
    "s_add_u32 m0, s57, 34816\n"
    "s_add_u32 s58, s12, 73728\n"
    "v_mfma_f32_16x16x32_bf16 a[220:223], a[248:251], v[224:227], a[220:223]\n"
    "ds_read_b64_tr_b16 a[248:249], v15 offset:41728\n"
    "v_mfma_f32_16x16x32_bf16 a[220:223], a[252:255], v[252:255], a[220:223]\n"
    "s_waitcnt lgkmcnt(2)\n"
    "ds_read_b64_tr_b16 a[250:251], v15 offset:45824\n"
    "v_mfma_f32_16x16x32_bf16 a[0:3], a[224:227], v[32:35], a[0:3]\n"
    "buffer_load_dwordx4 v7, s[40:43], s58 offen lds\n"
    "v_mfma_f32_16x16x32_bf16 a[0:3], a[228:231], v[60:63], a[0:3]\n"
    "s_add_u32 m0, s57, 36864\n"
    "ds_read_b64_tr_b16 a[252:253], v16 offset:8960\n"
    "s_add_u32 s58, s12, 81920\n"
    "v_mfma_f32_16x16x32_bf16 a[4:7], a[224:227], v[36:39], a[4:7]\n"
    "ds_read_b64_tr_b16 a[254:255], v16 offset:13056\n"
    "v_mfma_f32_16x16x32_bf16 a[4:7], a[228:231], v[64:67], a[4:7]\n"
    "s_waitcnt vmcnt(20)\n"
    "v_mfma_f32_16x16x32_bf16 a[8:11], a[224:227], v[40:43], a[8:11]\n"
    "s_waitcnt vmcnt(19)\n"
    "v_mfma_f32_16x16x32_bf16 a[8:11], a[228:231], v[68:71], a[8:11]\n"
    "s_waitcnt vmcnt(18)\n"
    "v_mfma_f32_16x16x32_bf16 a[12:15], a[224:227], v[44:47], a[12:15]\n"
    "s_waitcnt vmcnt(17)\n"
    "v_mfma_f32_16x16x32_bf16 a[12:15], a[228:231], v[72:75], a[12:15]\n"
    "s_waitcnt vmcnt(16)\n"
    "v_mfma_f32_16x16x32_bf16 a[16:19], a[224:227], v[48:51], a[16:19]\n"
    "s_waitcnt vmcnt(15)\n"
    "v_mfma_f32_16x16x32_bf16 a[16:19], a[228:231], v[76:79], a[16:19]\n"
    "s_waitcnt vmcnt(14)\n"
    "v_mfma_f32_16x16x32_bf16 a[20:23], a[224:227], v[52:55], a[20:23]\n"
    "s_waitcnt vmcnt(13)\n"
    "v_mfma_f32_16x16x32_bf16 a[20:23], a[228:231], v[80:83], a[20:23]\n"
    "s_waitcnt vmcnt(12)\n"
    "v_mfma_f32_16x16x32_bf16 a[24:27], a[224:227], v[56:59], a[24:27]\n"
    "s_waitcnt vmcnt(11)\n"
    "ds_read_b64_tr_b16 a[224:225], v18 offset:40960\n"
    "v_mfma_f32_16x16x32_bf16 a[24:27], a[228:231], v[84:87], a[24:27]\n"
    "buffer_load_dwordx4 v7, s[40:43], s58 offen lds\n"
    "v_mfma_f32_16x16x32_bf16 a[28:31], a[232:235], v[32:35], a[28:31]\n"
    "s_add_u32 m0, s57, 38912\n"
    "ds_read_b64_tr_b16 a[226:227], v18 offset:45056\n"
    "s_add_u32 s58, s12, 90112\n"
    "v_mfma_f32_16x16x32_bf16 a[28:31], a[236:239], v[60:63], a[28:31]\n"
    "buffer_load_dwordx4 v7, s[40:43], s58 offen lds\n"
    "v_mfma_f32_16x16x32_bf16 a[32:35], a[232:235], v[36:39], a[32:35]\n"
    "s_add_u32 m0, s57, 40960\n"
    "ds_read_b64_tr_b16 a[228:229], v19 offset:8192\n"
    "s_add_u32 s58, s12, 98304\n"
    "v_mfma_f32_16x16x32_bf16 a[32:35], a[236:239], v[64:67], a[32:35]\n"
    "buffer_load_dwordx4 v7, s[40:43], s58 offen lds\n"
    "v_mfma_f32_16x16x32_bf16 a[36:39], a[232:235], v[40:43], a[36:39]\n"
    "s_add_u32 m0, s57, 43008\n"
    "ds_read_b64_tr_b16 a[230:231], v19 offset:12288\n"
    "s_add_u32 s58, s12, 106496\n"
    "v_mfma_f32_16x16x32_bf16 a[36:39], a[236:239], v[68:71], a[36:39]\n"
    "buffer_load_dwordx4 v7, s[40:43], s58 offen lds\n"
    "v_mfma_f32_16x16x32_bf16 a[40:43], a[232:235], v[44:47], a[40:43]\n"
    "s_add_u32 m0, s57, 45056\n"
    "s_add_u32 s58, s12, 114688\n"
    "v_mfma_f32_16x16x32_bf16 a[40:43], a[236:239], v[72:75], a[40:43]\n"
    "buffer_load_dwordx4 v7, s[40:43], s58 offen lds\n"
    "v_mfma_f32_16x16x32_bf16 a[44:47], a[232:235], v[48:51], a[44:47]\n"
    "s_add_u32 m0, s57, 47104\n"
    "s_add_u32 s58, s12, 122880\n"
    "v_mfma_f32_16x16x32_bf16 a[44:47], a[236:239], v[76:79], a[44:47]\n"
    "v_mfma_f32_16x16x32_bf16 a[48:51], a[232:235], v[52:55], a[48:51]\n"
    "v_mfma_f32_16x16x32_bf16 a[48:51], a[236:239], v[80:83], a[48:51]\n"
    "v_mfma_f32_16x16x32_bf16 a[52:55], a[232:235], v[56:59], a[52:55]\n"
    "buffer_load_dwordx4 v7, s[40:43], s58 offen lds\n"
    "v_mfma_f32_16x16x32_bf16 a[52:55], a[236:239], v[84:87], a[52:55]\n"
    "ds_read_b64_tr_b16 a[232:233], v18 offset:41216\n"
    "v_mfma_f32_16x16x32_bf16 a[56:59], a[240:243], v[32:35], a[56:59]\n"
    "s_waitcnt lgkmcnt(2)\n"
    "ds_read_b64_tr_b16 a[234:235], v18 offset:45312\n"
    "v_mfma_f32_16x16x32_bf16 a[56:59], a[244:247], v[60:63], a[56:59]\n"
    "ds_read_b64_tr_b16 a[236:237], v19 offset:8448\n"
    "v_mfma_f32_16x16x32_bf16 a[60:63], a[240:243], v[36:39], a[60:63]\n"
    "s_waitcnt vmcnt(8)\n"
    "v_cmp_ne_u32 vcc, v6, v5\n"
    "s_nop 4\n"
    "s_and_b32 s67, vcc_lo, 0xe0\n"
    "s_cbranch_scc1 L_retryB_%=\nL_okB_%=:\n"
    "v_mfma_f32_16x16x32_bf16 a[60:63], a[244:247], v[64:67], a[60:63]\n"
    "buffer_load_dwordx4 v[88:91], v27, s[36:39], s65 offen offset:2048 sc1\n"
    "v_mfma_f32_16x16x32_bf16 a[64:67], a[240:243], v[40:43], a[64:67]\n"
    "ds_read_b64_tr_b16 a[238:239], v19 offset:12544\n"
    "v_mfma_f32_16x16x32_bf16 a[64:67], a[244:247], v[68:71], a[64:67]\n"
    "v_mfma_f32_16x16x32_bf16 a[68:71], a[240:243], v[44:47], a[68:71]\n"
    "v_mfma_f32_16x16x32_bf16 a[68:71], a[244:247], v[72:75], a[68:71]\n"
    "buffer_load_dwordx4 v[116:119], v27, s[36:39], s65 offen offset:3072 sc1\n"
    "v_mfma_f32_16x16x32_bf16 a[72:75], a[240:243], v[48:51], a[72:75]\n"
    "s_add_u32 s65, s8, 16384\n"
    "v_mfma_f32_16x16x32_bf16 a[72:75], a[244:247], v[76:79], a[72:75]\n"
    "v_mfma_f32_16x16x32_bf16 a[76:79], a[240:243], v[52:55], a[76:79]\n"
    "v_mfma_f32_16x16x32_bf16 a[76:79], a[244:247], v[80:83], a[76:79]\n"
    "buffer_load_dwordx4 v[92:95], v27, s[36:39], s65 offen sc1\n"
    "v_mfma_f32_16x16x32_bf16 a[80:83], a[240:243], v[56:59], a[80:83]\n"
    "ds_read_b64_tr_b16 a[240:241], v18 offset:41472\n"
    "v_mfma_f32_16x16x32_bf16 a[80:83], a[244:247], v[84:87], a[80:83]\n"
    "ds_read_b64_tr_b16 a[242:243], v18 offset:45568\n"
    "v_mfma_f32_16x16x32_bf16 a[84:87], a[248:251], v[32:35], a[84:87]\n"
    "ds_read_b64_tr_b16 a[244:245], v19 offset:8704\n"
    "v_mfma_f32_16x16x32_bf16 a[84:87], a[252:255], v[60:63], a[84:87]\n"
    "buffer_load_dwordx4 v[120:123], v27, s[36:39], s65 offen offset:1024 sc1\n"
    "v_mfma_f32_16x16x32_bf16 a[88:91], a[248:251], v[36:39], a[88:91]\n"
    "ds_read_b64_tr_b16 a[246:247], v19 offset:12800\n"
    "v_mfma_f32_16x16x32_bf16 a[88:91], a[252:255], v[64:67], a[88:91]\n"
    "v_mfma_f32_16x16x32_bf16 a[92:95], a[248:251], v[40:43], a[92:95]\n"
    "v_mfma_f32_16x16x32_bf16 a[92:95], a[252:255], v[68:71], a[92:95]\n"
    "buffer_load_dwordx4 v[96:99], v27, s[36:39], s65 offen offset:2048 sc1\n"
    "v_mfma_f32_16x16x32_bf16 a[96:99], a[248:251], v[44:47], a[96:99]\n"
    "v_mfma_f32_16x16x32_bf16 a[96:99], a[252:255], v[72:75], a[96:99]\n"
    "v_mfma_f32_16x16x32_bf16 a[100:103], a[248:251], v[48:51], a[100:103]\n"
    "v_mfma_f32_16x16x32_bf16 a[100:103], a[252:255], v[76:79], a[100:103]\n"
    "buffer_load_dwordx4 v[124:127], v27, s[36:39], s65 offen offset:3072 sc1\n"
    "v_mfma_f32_16x16x32_bf16 a[104:107], a[248:251], v[52:55], a[104:107]\n"
    "s_add_u32 s65, s8, 20480\n"
    "v_mfma_f32_16x16x32_bf16 a[104:107], a[252:255], v[80:83], a[104:107]\n"
    "v_mfma_f32_16x16x32_bf16 a[108:111], a[248:251], v[56:59], a[108:111]\n"
    "ds_read_b64_tr_b16 a[248:249], v18 offset:41728\n"
    "v_mfma_f32_16x16x32_bf16 a[108:111], a[252:255], v[84:87], a[108:111]\n"
    "ds_read_b64_tr_b16 a[250:251], v18 offset:45824\n"
    "v_mfma_f32_16x16x32_bf16 a[112:115], a[224:227], v[32:35], a[112:115]\n"
    "s_waitcnt lgkmcnt(2)\n"
    "ds_read_b64_tr_b16 a[252:253], v19 offset:8960\n"
    "v_mfma_f32_16x16x32_bf16 a[112:115], a[228:231], v[60:63], a[112:115]\n"
    "buffer_load_dwordx4 v[100:103], v27, s[36:39], s65 offen sc1\n"
    "v_mfma_f32_16x16x32_bf16 a[116:119], a[224:227], v[36:39], a[116:119]\n"
    "ds_read_b64_tr_b16 a[254:255], v19 offset:13056\n"
    "v_mfma_f32_16x16x32_bf16 a[116:119], a[228:231], v[64:67], a[116:119]\n"
    "v_mfma_f32_16x16x32_bf16 a[120:123], a[224:227], v[40:43], a[120:123]\n"
    "buffer_load_dwordx4 v[128:131], v27, s[36:39], s65 offen offset:1024 sc1\n"
    "v_mfma_f32_16x16x32_bf16 a[120:123], a[228:231], v[68:71], a[120:123]\n"
    "v_mfma_f32_16x16x32_bf16 a[124:127], a[224:227], v[44:47], a[124:127]\n"
    "v_mfma_f32_16x16x32_bf16 a[124:127], a[228:231], v[72:75], a[124:127]\n"
    "v_mfma_f32_16x16x32_bf16 a[128:131], a[224:227], v[48:51], a[128:131]\n"
    "buffer_load_dwordx4 v[104:107], v27, s[36:39], s65 offen offset:2048 sc1\n"
    "v_mfma_f32_16x16x32_bf16 a[128:131], a[228:231], v[76:79], a[128:131]\n"
    "v_mfma_f32_16x16x32_bf16 a[132:135], a[224:227], v[52:55], a[132:135]\n"
    "v_mfma_f32_16x16x32_bf16 a[132:135], a[228:231], v[80:83], a[132:135]\n"
    "v_mfma_f32_16x16x32_bf16 a[136:139], a[224:227], v[56:59], a[136:139]\n"
    "buffer_load_dwordx4 v[132:135], v27, s[36:39], s65 offen offset:3072 sc1\n"
    "v_mfma_f32_16x16x32_bf16 a[136:139], a[228:231], v[84:87], a[136:139]\n"
    "s_add_u32 s65, s8, 24576\n"
    "v_mfma_f32_16x16x32_bf16 a[140:143], a[232:235], v[32:35], a[140:143]\n"
    "v_mfma_f32_16x16x32_bf16 a[140:143], a[236:239], v[60:63], a[140:143]\n"
    "v_mfma_f32_16x16x32_bf16 a[144:147], a[232:235], v[36:39], a[144:147]\n"
    "v_mfma_f32_16x16x32_bf16 a[144:147], a[236:239], v[64:67], a[144:147]\n"
    "buffer_load_dwordx4 v[108:111], v27, s[36:39], s65 offen sc1\n"
    "v_mfma_f32_16x16x32_bf16 a[148:151], a[232:235], v[40:43], a[148:151]\n"
    "v_mfma_f32_16x16x32_bf16 a[148:151], a[236:239], v[68:71], a[148:151]\n"
    "v_mfma_f32_16x16x32_bf16 a[152:155], a[232:235], v[44:47], a[152:155]\n"
    "v_mfma_f32_16x16x32_bf16 a[152:155], a[236:239], v[72:75], a[152:155]\n"
    "buffer_load_dwordx4 v[136:139], v27, s[36:39], s65 offen offset:1024 sc1\n"
    "v_mfma_f32_16x16x32_bf16 a[156:159], a[232:235], v[48:51], a[156:159]\n"
    "s_waitcnt lgkmcnt(0)\n"
    "s_barrier\n"
    "ds_read_b64_tr_b16 a[224:225], v15 offset:0\n"
    "v_mfma_f32_16x16x32_bf16 a[156:159], a[236:239], v[76:79], a[156:159]\n"
    "ds_read_b64_tr_b16 a[226:227], v15 offset:4096\n"
    "v_mfma_f32_16x16x32_bf16 a[160:163], a[232:235], v[52:55], a[160:163]\n"
    "ds_read_b64_tr_b16 a[228:229], v15 offset:8192\n"
    "v_mfma_f32_16x16x32_bf16 a[160:163], a[236:239], v[80:83], a[160:163]\n"
    "buffer_load_dwordx4 v[112:115], v27, s[36:39], s65 offen offset:2048 sc1\n"
    "v_mfma_f32_16x16x32_bf16 a[164:167], a[232:235], v[56:59], a[164:167]\n"
    "ds_read_b64_tr_b16 a[230:231], v15 offset:12288\n"
    "v_mfma_f32_16x16x32_bf16 a[164:167], a[236:239], v[84:87], a[164:167]\n"
    "ds_read_b64_tr_b16 a[232:233], v15 offset:256\n"
    "v_mfma_f32_16x16x32_bf16 a[168:171], a[240:243], v[32:35], a[168:171]\n"
    "ds_read_b64_tr_b16 a[234:235], v15 offset:4352\n"
    "v_mfma_f32_16x16x32_bf16 a[168:171], a[244:247], v[60:63], a[168:171]\n"
    "buffer_load_dwordx4 v[140:143], v27, s[36:39], s65 offen offset:3072 sc1\n"
    "v_mfma_f32_16x16x32_bf16 a[172:175], a[240:243], v[36:39], a[172:175]\n"
    "s_add_u32 s65, s9, 12288\n"
    "ds_read_b64_tr_b16 a[236:237], v15 offset:8448\n"
    "v_mfma_f32_16x16x32_bf16 a[172:175], a[244:247], v[64:67], a[172:175]\n"
    "ds_read_b64_tr_b16 a[238:239], v15 offset:12544\n"
    "v_mfma_f32_16x16x32_bf16 a[176:179], a[240:243], v[40:43], a[176:179]\n"
    "v_mfma_f32_16x16x32_bf16 a[176:179], a[244:247], v[68:71], a[176:179]\n"
    "buffer_load_dwordx4 v[144:147], v27, s[36:39], s65 offen offset:2048 sc1\n"
    "v_mfma_f32_16x16x32_bf16 a[180:183], a[240:243], v[44:47], a[180:183]\n"
    "v_mfma_f32_16x16x32_bf16 a[180:183], a[244:247], v[72:75], a[180:183]\n"
    "v_mfma_f32_16x16x32_bf16 a[184:187], a[240:243], v[48:51], a[184:187]\n"
    "v_mfma_f32_16x16x32_bf16 a[184:187], a[244:247], v[76:79], a[184:187]\n"
    "v_mfma_f32_16x16x32_bf16 a[188:191], a[240:243], v[52:55], a[188:191]\n"
    "buffer_load_dwordx4 v[172:175], v27, s[36:39], s65 offen offset:3072 sc1\n"
    "v_mfma_f32_16x16x32_bf16 a[188:191], a[244:247], v[80:83], a[188:191]\n"
    "s_add_u32 s65, s9, 16384\n"
    "v_mfma_f32_16x16x32_bf16 a[192:195], a[240:243], v[56:59], a[192:195]\n"
    "ds_read_b64_tr_b16 a[240:241], v15 offset:512\n"
    "v_mfma_f32_16x16x32_bf16 a[192:195], a[244:247], v[84:87], a[192:195]\n"
    "ds_read_b64_tr_b16 a[242:243], v15 offset:4608\n"
    "v_mfma_f32_16x16x32_bf16 a[196:199], a[248:251], v[32:35], a[196:199]\n"
    "buffer_load_dwordx4 v[148:151], v27, s[36:39], s65 offen sc1\n"
    "v_mfma_f32_16x16x32_bf16 a[196:199], a[252:255], v[60:63], a[196:199]\n"
    "ds_read_b64_tr_b16 a[244:245], v15 offset:8704\n"
    "v_mfma_f32_16x16x32_bf16 a[200:203], a[248:251], v[36:39], a[200:203]\n"
    "ds_read_b64_tr_b16 a[246:247], v15 offset:12800\n"
    "v_mfma_f32_16x16x32_bf16 a[200:203], a[252:255], v[64:67], a[200:203]\n"
    "v_mfma_f32_16x16x32_bf16 a[204:207], a[248:251], v[40:43], a[204:207]\n"
    "buffer_load_dwordx4 v[176:179], v27, s[36:39], s65 offen offset:1024 sc1\n"
    "v_mfma_f32_16x16x32_bf16 a[204:207], a[252:255], v[68:71], a[204:207]\n"
    "v_mfma_f32_16x16x32_bf16 a[208:211], a[248:251], v[44:47], a[208:211]\n"
    "v_mfma_f32_16x16x32_bf16 a[208:211], a[252:255], v[72:75], a[208:211]\n"
    "v_mfma_f32_16x16x32_bf16 a[212:215], a[248:251], v[48:51], a[212:215]\n"
    "buffer_load_dwordx4 v[152:155], v27, s[36:39], s65 offen offset:2048 sc1\n"
    "v_mfma_f32_16x16x32_bf16 a[212:215], a[252:255], v[76:79], a[212:215]\n"
    "v_mfma_f32_16x16x32_bf16 a[216:219], a[248:251], v[52:55], a[216:219]\n"
    "v_mfma_f32_16x16x32_bf16 a[216:219], a[252:255], v[80:83], a[216:219]\n"
    "v_mfma_f32_16x16x32_bf16 a[220:223], a[248:251], v[56:59], a[220:223]\n"
    "buffer_load_dwordx4 v[180:183], v27, s[36:39], s65 offen offset:3072 sc1\n"
    "v_mfma_f32_16x16x32_bf16 a[220:223], a[252:255], v[84:87], a[220:223]\n"
    "s_waitcnt vmcnt(19) lgkmcnt(1)\n"
    "ds_read_b64_tr_b16 a[248:249], v15 offset:768\n"
    "s_add_u32 s65, s9, 20480\n"
    "v_mfma_f32_16x16x32_bf16 a[0:3], a[224:227], v[88:91], a[0:3]\n"
    "s_waitcnt vmcnt(18)\n"
    "ds_read_b64_tr_b16 a[250:251], v15 offset:4864\n"
    "v_mfma_f32_16x16x32_bf16 a[0:3], a[228:231], v[116:119], a[0:3]\n"
    "s_waitcnt vmcnt(17)\n"
    "ds_read_b64_tr_b16 a[252:253], v15 offset:8960\n"
    "v_mfma_f32_16x16x32_bf16 a[4:7], a[224:227], v[92:95], a[4:7]\n"
    "s_waitcnt vmcnt(16)\n"
    "ds_read_b64_tr_b16 a[254:255], v15 offset:13056\n"
    "v_mfma_f32_16x16x32_bf16 a[4:7], a[228:231], v[120:123], a[4:7]\n"
    "s_waitcnt vmcnt(15)\n"
    "v_mfma_f32_16x16x32_bf16 a[8:11], a[224:227], v[96:99], a[8:11]\n"
    "s_waitcnt vmcnt(14)\n"
    "v_mfma_f32_16x16x32_bf16 a[8:11], a[228:231], v[124:127], a[8:11]\n"
    "s_waitcnt vmcnt(13)\n"
    "v_mfma_f32_16x16x32_bf16 a[12:15], a[224:227], v[100:103], a[12:15]\n"
    "s_waitcnt vmcnt(12)\n"
    "v_mfma_f32_16x16x32_bf16 a[12:15], a[228:231], v[128:131], a[12:15]\n"
    "s_waitcnt vmcnt(11)\n"
    "v_mfma_f32_16x16x32_bf16 a[16:19], a[224:227], v[104:107], a[16:19]\n"
    "s_waitcnt vmcnt(10)\n"
    "v_mfma_f32_16x16x32_bf16 a[16:19], a[228:231], v[132:135], a[16:19]\n"
    "s_waitcnt vmcnt(9)\n"
    "v_mfma_f32_16x16x32_bf16 a[20:23], a[224:227], v[108:111], a[20:23]\n"
    "s_waitcnt vmcnt(8)\n"
    "v_mfma_f32_16x16x32_bf16 a[20:23], a[228:231], v[136:139], a[20:23]\n"
    "s_waitcnt vmcnt(7)\n"
    "v_mfma_f32_16x16x32_bf16 a[24:27], a[224:227], v[112:115], a[24:27]\n"
    "s_waitcnt vmcnt(6)\n"
    "ds_read_b64_tr_b16 a[224:225], v18 offset:0\n"
    "v_mfma_f32_16x16x32_bf16 a[24:27], a[228:231], v[140:143], a[24:27]\n"
    "buffer_load_dwordx4 v[156:159], v27, s[36:39], s65 offen sc1\n"
    "v_mfma_f32_16x16x32_bf16 a[28:31], a[232:235], v[88:91], a[28:31]\n"
    "buffer_load_dwordx4 v[184:187], v27, s[36:39], s65 offen offset:1024 sc1\n"
    "v_mfma_f32_16x16x32_bf16 a[28:31], a[236:239], v[116:119], a[28:31]\n"
    "buffer_load_dwordx4 v[160:163], v27, s[36:39], s65 offen offset:2048 sc1\n"
    "v_mfma_f32_16x16x32_bf16 a[32:35], a[232:235], v[92:95], a[32:35]\n"
    "buffer_load_dwordx4 v[188:191], v27, s[36:39], s65 offen offset:3072 sc1\n"
    "v_mfma_f32_16x16x32_bf16 a[32:35], a[236:239], v[120:123], a[32:35]\n"
    "s_add_u32 s65, s9, 24576\n"
    "ds_read_b64_tr_b16 a[226:227], v18 offset:4096\n"
    "v_mfma_f32_16x16x32_bf16 a[36:39], a[232:235], v[96:99], a[36:39]\n"
    "ds_read_b64_tr_b16 a[228:229], v18 offset:8192\n"
    "v_mfma_f32_16x16x32_bf16 a[36:39], a[236:239], v[124:127], a[36:39]\n"
    "buffer_load_dwordx4 v[164:167], v27, s[36:39], s65 offen sc1\n"
    "v_mfma_f32_16x16x32_bf16 a[40:43], a[232:235], v[100:103], a[40:43]\n"
    "ds_read_b64_tr_b16 a[230:231], v18 offset:12288\n"
    "v_mfma_f32_16x16x32_bf16 a[40:43], a[236:239], v[128:131], a[40:43]\n"
    "v_mfma_f32_16x16x32_bf16 a[44:47], a[232:235], v[104:107], a[44:47]\n"
    "v_mfma_f32_16x16x32_bf16 a[44:47], a[236:239], v[132:135], a[44:47]\n"
    "v_mfma_f32_16x16x32_bf16 a[48:51], a[232:235], v[108:111], a[48:51]\n"
    "buffer_load_dwordx4 v[192:195], v27, s[36:39], s65 offen offset:1024 sc1\n"
    "v_mfma_f32_16x16x32_bf16 a[48:51], a[236:239], v[136:139], a[48:51]\n"
    "v_mfma_f32_16x16x32_bf16 a[52:55], a[232:235], v[112:115], a[52:55]\n"
    "ds_read_b64_tr_b16 a[232:233], v18 offset:256\n"
    "v_mfma_f32_16x16x32_bf16 a[52:55], a[236:239], v[140:143], a[52:55]\n"
    "ds_read_b64_tr_b16 a[234:235], v18 offset:4352\n"
    "v_mfma_f32_16x16x32_bf16 a[56:59], a[240:243], v[88:91], a[56:59]\n"
    "s_waitcnt lgkmcnt(5)\n"
    "ds_read_b64_tr_b16 a[236:237], v18 offset:8448\n"
    "v_mfma_f32_16x16x32_bf16 a[56:59], a[244:247], v[116:119], a[56:59]\n"
    "buffer_load_dwordx4 v[168:171], v27, s[36:39], s65 offen offset:2048 sc1\n"
    "v_mfma_f32_16x16x32_bf16 a[60:63], a[240:243], v[92:95], a[60:63]\n"
    "ds_read_b64_tr_b16 a[238:239], v18 offset:12544\n"
    "v_mfma_f32_16x16x32_bf16 a[60:63], a[244:247], v[120:123], a[60:63]\n"
    "v_mfma_f32_16x16x32_bf16 a[64:67], a[240:243], v[96:99], a[64:67]\n"
    "buffer_load_dwordx4 v[196:199], v27, s[36:39], s65 offen offset:3072 sc1\n"
    "v_mfma_f32_16x16x32_bf16 a[64:67], a[244:247], v[124:127], a[64:67]\n"
    "s_add_u32 s65, s14, 12288\n"
    "v_mfma_f32_16x16x32_bf16 a[68:71], a[240:243], v[100:103], a[68:71]\n"
    "v_mfma_f32_16x16x32_bf16 a[68:71], a[244:247], v[128:131], a[68:71]\n"
    "v_mfma_f32_16x16x32_bf16 a[72:75], a[240:243], v[104:107], a[72:75]\n"
    "buffer_load_dwordx4 v[200:203], v27, s[36:39], s65 offen offset:2048 sc1\n"
    "v_mfma_f32_16x16x32_bf16 a[72:75], a[244:247], v[132:135], a[72:75]\n"
    "v_mfma_f32_16x16x32_bf16 a[76:79], a[240:243], v[108:111], a[76:79]\n"
    "v_mfma_f32_16x16x32_bf16 a[76:79], a[244:247], v[136:139], a[76:79]\n"
    "v_mfma_f32_16x16x32_bf16 a[80:83], a[240:243], v[112:115], a[80:83]\n"
    "buffer_load_dwordx4 v[228:231], v27, s[36:39], s65 offen offset:3072 sc1\n"
    "v_mfma_f32_16x16x32_bf16 a[80:83], a[244:247], v[140:143], a[80:83]\n"
    "s_add_u32 s65, s14, 16384\n"
    "ds_read_b64_tr_b16 a[240:241], v18 offset:512\n"
    "v_mfma_f32_16x16x32_bf16 a[84:87], a[248:251], v[88:91], a[84:87]\n"
    "ds_read_b64_tr_b16 a[242:243], v18 offset:4608\n"
    "v_mfma_f32_16x16x32_bf16 a[84:87], a[252:255], v[116:119], a[84:87]\n"
    "ds_read_b64_tr_b16 a[244:245], v18 offset:8704\n"
    "v_mfma_f32_16x16x32_bf16 a[88:91], a[248:251], v[92:95], a[88:91]\n"
    "ds_read_b64_tr_b16 a[246:247], v18 offset:12800\n"
    "v_mfma_f32_16x16x32_bf16 a[88:91], a[252:255], v[120:123], a[88:91]\n"
    "buffer_load_dwordx4 v[204:207], v27, s[36:39], s65 offen sc1\n"
    "v_mfma_f32_16x16x32_bf16 a[92:95], a[248:251], v[96:99], a[92:95]\n"
    "v_mfma_f32_16x16x32_bf16 a[92:95], a[252:255], v[124:127], a[92:95]\n"
    "v_mfma_f32_16x16x32_bf16 a[96:99], a[248:251], v[100:103], a[96:99]\n"
    "v_mfma_f32_16x16x32_bf16 a[96:99], a[252:255], v[128:131], a[96:99]\n"
    "buffer_load_dwordx4 v[232:235], v27, s[36:39], s65 offen offset:1024 sc1\n"
    "v_mfma_f32_16x16x32_bf16 a[100:103], a[248:251], v[104:107], a[100:103]\n"
    "v_mfma_f32_16x16x32_bf16 a[100:103], a[252:255], v[132:135], a[100:103]\n"
    "v_mfma_f32_16x16x32_bf16 a[104:107], a[248:251], v[108:111], a[104:107]\n"
    "v_mfma_f32_16x16x32_bf16 a[104:107], a[252:255], v[136:139], a[104:107]\n"
    "buffer_load_dwordx4 v[208:211], v27, s[36:39], s65 offen offset:2048 sc1\n"
    "v_mfma_f32_16x16x32_bf16 a[108:111], a[248:251], v[112:115], a[108:111]\n"
    "ds_read_b64_tr_b16 a[248:249], v18 offset:768\n"
    "v_mfma_f32_16x16x32_bf16 a[108:111], a[252:255], v[140:143], a[108:111]\n"
    "s_waitcnt lgkmcnt(2)\n"
    "ds_read_b64_tr_b16 a[250:251], v18 offset:4864\n"
    "v_mfma_f32_16x16x32_bf16 a[112:115], a[224:227], v[88:91], a[112:115]\n"
    "ds_read_b64_tr_b16 a[252:253], v18 offset:8960\n"
    "v_mfma_f32_16x16x32_bf16 a[112:115], a[228:231], v[116:119], a[112:115]\n"
    "buffer_load_dwordx4 v[236:239], v27, s[36:39], s65 offen offset:3072 sc1\n"
    "v_mfma_f32_16x16x32_bf16 a[116:119], a[224:227], v[92:95], a[116:119]\n"
    "s_add_u32 s65, s14, 20480\n"
    "ds_read_b64_tr_b16 a[254:255], v18 offset:13056\n"
    "v_mfma_f32_16x16x32_bf16 a[116:119], a[228:231], v[120:123], a[116:119]\n"
    "v_mfma_f32_16x16x32_bf16 a[120:123], a[224:227], v[96:99], a[120:123]\n"
    "v_mfma_f32_16x16x32_bf16 a[120:123], a[228:231], v[124:127], a[120:123]\n"
    "buffer_load_dwordx4 v[212:215], v27, s[36:39], s65 offen sc1\n"
    "v_mfma_f32_16x16x32_bf16 a[124:127], a[224:227], v[100:103], a[124:127]\n"
    "v_mfma_f32_16x16x32_bf16 a[124:127], a[228:231], v[128:131], a[124:127]\n"
    "v_mfma_f32_16x16x32_bf16 a[128:131], a[224:227], v[104:107], a[128:131]\n"
    "v_mfma_f32_16x16x32_bf16 a[128:131], a[228:231], v[132:135], a[128:131]\n"
    "v_mfma_f32_16x16x32_bf16 a[132:135], a[224:227], v[108:111], a[132:135]\n"
    "buffer_load_dwordx4 v[240:243], v27, s[36:39], s65 offen offset:1024 sc1\n"
    "v_mfma_f32_16x16x32_bf16 a[132:135], a[228:231], v[136:139], a[132:135]\n"
    "v_mfma_f32_16x16x32_bf16 a[136:139], a[224:227], v[112:115], a[136:139]\n"
    "v_mfma_f32_16x16x32_bf16 a[136:139], a[228:231], v[140:143], a[136:139]\n"
    "v_mfma_f32_16x16x32_bf16 a[140:143], a[232:235], v[88:91], a[140:143]\n"
    "buffer_load_dwordx4 v[216:219], v27, s[36:39], s65 offen offset:2048 sc1\n"
    "v_mfma_f32_16x16x32_bf16 a[140:143], a[236:239], v[116:119], a[140:143]\n"
    "v_mfma_f32_16x16x32_bf16 a[144:147], a[232:235], v[92:95], a[144:147]\n"
    "v_mfma_f32_16x16x32_bf16 a[144:147], a[236:239], v[120:123], a[144:147]\n"
    "v_mfma_f32_16x16x32_bf16 a[148:151], a[232:235], v[96:99], a[148:151]\n"
    "buffer_load_dwordx4 v[244:247], v27, s[36:39], s65 offen offset:3072 sc1\n"
    "v_mfma_f32_16x16x32_bf16 a[148:151], a[236:239], v[124:127], a[148:151]\n"
    "s_add_u32 s65, s14, 24576\n"
    "v_mfma_f32_16x16x32_bf16 a[152:155], a[232:235], v[100:103], a[152:155]\n"
    "v_mfma_f32_16x16x32_bf16 a[152:155], a[236:239], v[128:131], a[152:155]\n"
    "v_mfma_f32_16x16x32_bf16 a[156:159], a[232:235], v[104:107], a[156:159]\n"
    "buffer_load_dwordx4 v[220:223], v27, s[36:39], s65 offen sc1\n"
    "v_mfma_f32_16x16x32_bf16 a[156:159], a[236:239], v[132:135], a[156:159]\n"
    "v_mfma_f32_16x16x32_bf16 a[160:163], a[232:235], v[108:111], a[160:163]\n"
    "v_mfma_f32_16x16x32_bf16 a[160:163], a[236:239], v[136:139], a[160:163]\n"
    "v_mfma_f32_16x16x32_bf16 a[164:167], a[232:235], v[112:115], a[164:167]\n"
    "buffer_load_dwordx4 v[248:251], v27, s[36:39], s65 offen offset:1024 sc1\n"
    "v_mfma_f32_16x16x32_bf16 a[164:167], a[236:239], v[140:143], a[164:167]\n"
    "v_mfma_f32_16x16x32_bf16 a[168:171], a[240:243], v[88:91], a[168:171]\n"
    "s_waitcnt lgkmcnt(0)\n"
    "v_mfma_f32_16x16x32_bf16 a[168:171], a[244:247], v[116:119], a[168:171]\n"
    "v_mfma_f32_16x16x32_bf16 a[172:175], a[240:243], v[92:95], a[172:175]\n"
    "v_mfma_f32_16x16x32_bf16 a[172:175], a[244:247], v[120:123], a[172:175]\n"
    "buffer_load_dwordx4 v[224:227], v27, s[36:39], s65 offen offset:2048 sc1\n"
    "v_mfma_f32_16x16x32_bf16 a[176:179], a[240:243], v[96:99], a[176:179]\n"
    "v_mfma_f32_16x16x32_bf16 a[176:179], a[244:247], v[124:127], a[176:179]\n"
    "v_mfma_f32_16x16x32_bf16 a[180:183], a[240:243], v[100:103], a[180:183]\n"
    "v_mfma_f32_16x16x32_bf16 a[180:183], a[244:247], v[128:131], a[180:183]\n"
    "buffer_load_dwordx4 v[252:255], v27, s[36:39], s65 offen offset:3072 sc1\n"
    "v_mfma_f32_16x16x32_bf16 a[184:187], a[240:243], v[104:107], a[184:187]\n"
    "s_barrier\n"
    "ds_read_b64_tr_b16 a[224:225], v17 offset:256\n"
    "v_mfma_f32_16x16x32_bf16 a[184:187], a[244:247], v[132:135], a[184:187]\n"
    "ds_read_b64_tr_b16 a[226:227], v17 offset:4352\n"
    "v_mfma_f32_16x16x32_bf16 a[188:191], a[240:243], v[108:111], a[188:191]\n"
    "ds_read_b64_tr_b16 a[228:229], v17 offset:8448\n"
    "v_mfma_f32_16x16x32_bf16 a[188:191], a[244:247], v[136:139], a[188:191]\n"
    "ds_read_b64_tr_b16 a[230:231], v17 offset:12544\n"
    "v_mfma_f32_16x16x32_bf16 a[192:195], a[240:243], v[112:115], a[192:195]\n"
    "ds_read_b64_tr_b16 a[232:233], v17 offset:33024\n"
    "v_mfma_f32_16x16x32_bf16 a[192:195], a[244:247], v[140:143], a[192:195]\n"
    "ds_read_b64_tr_b16 a[234:235], v17 offset:37120\n"
    "v_mfma_f32_16x16x32_bf16 a[196:199], a[248:251], v[88:91], a[196:199]\n"
    "ds_read_b64_tr_b16 a[236:237], v17 offset:41216\n"
    "v_mfma_f32_16x16x32_bf16 a[196:199], a[252:255], v[116:119], a[196:199]\n"
    "ds_read_b64_tr_b16 a[238:239], v17 offset:45312\n"
    "v_mfma_f32_16x16x32_bf16 a[200:203], a[248:251], v[92:95], a[200:203]\n"
    "ds_read_b64_tr_b16 a[240:241], v14 offset:256\n"
    "v_mfma_f32_16x16x32_bf16 a[200:203], a[252:255], v[120:123], a[200:203]\n"
    "ds_read_b64_tr_b16 a[242:243], v14 offset:4352\n"
    "v_mfma_f32_16x16x32_bf16 a[204:207], a[248:251], v[96:99], a[204:207]\n"
    "ds_read_b64_tr_b16 a[244:245], v14 offset:8448\n"
    "v_mfma_f32_16x16x32_bf16 a[204:207], a[252:255], v[124:127], a[204:207]\n"
    "ds_read_b64_tr_b16 a[246:247], v14 offset:12544\n"
    "v_mfma_f32_16x16x32_bf16 a[208:211], a[248:251], v[100:103], a[208:211]\n"
    "v_mfma_f32_16x16x32_bf16 a[208:211], a[252:255], v[128:131], a[208:211]\n"
    "v_mfma_f32_16x16x32_bf16 a[212:215], a[248:251], v[104:107], a[212:215]\n"
    "v_mfma_f32_16x16x32_bf16 a[212:215], a[252:255], v[132:135], a[212:215]\n"
    "v_mfma_f32_16x16x32_bf16 a[216:219], a[248:251], v[108:111], a[216:219]\n"
    "v_mfma_f32_16x16x32_bf16 a[216:219], a[252:255], v[136:139], a[216:219]\n"
    "v_mfma_f32_16x16x32_bf16 a[220:223], a[248:251], v[112:115], a[220:223]\n"
    "ds_read_b64_tr_b16 a[248:249], v14 offset:33024\n"
    "v_mfma_f32_16x16x32_bf16 a[220:223], a[252:255], v[140:143], a[220:223]\n"
    "s_waitcnt vmcnt(17) lgkmcnt(5)\n"
    "ds_read_b64_tr_b16 a[250:251], v14 offset:37120\n"
    "v_mfma_f32_16x16x32_bf16 a[140:143], a[224:227], v[144:147], a[140:143]\n"
    "ds_read_b64_tr_b16 a[252:253], v14 offset:41216\n"
    "v_mfma_f32_16x16x32_bf16 a[140:143], a[228:231], v[172:175], a[140:143]\n"
    "s_waitcnt vmcnt(13)\n"
    "ds_read_b64_tr_b16 a[254:255], v14 offset:45312\n"
    "v_mfma_f32_16x16x32_bf16 a[140:143], a[232:235], v[200:203], a[140:143]\n"
    "s_waitcnt vmcnt(12)\n"
    "v_mfma_f32_16x16x32_bf16 a[140:143], a[236:239], v[228:231], a[140:143]\n"
    "v_mfma_f32_16x16x32_bf16 a[144:147], a[224:227], v[148:151], a[144:147]\n"
    "v_mfma_f32_16x16x32_bf16 a[144:147], a[228:231], v[176:179], a[144:147]\n"
    "s_waitcnt vmcnt(11)\n"
    "v_mfma_f32_16x16x32_bf16 a[144:147], a[232:235], v[204:207], a[144:147]\n"
    "s_waitcnt vmcnt(10)\n"
    "v_mfma_f32_16x16x32_bf16 a[144:147], a[236:239], v[232:235], a[144:147]\n"
    "v_mfma_f32_16x16x32_bf16 a[148:151], a[224:227], v[152:155], a[148:151]\n"
    "v_mfma_f32_16x16x32_bf16 a[148:151], a[228:231], v[180:183], a[148:151]\n"
    "s_waitcnt vmcnt(9)\n"
    "ds_write_b128 v12, a[140:143] offset:0\n"
    "v_mfma_f32_16x16x32_bf16 a[148:151], a[232:235], v[208:211], a[148:151]\n"
    "s_waitcnt vmcnt(8)\n"
    "v_mfma_f32_16x16x32_bf16 a[148:151], a[236:239], v[236:239], a[148:151]\n"
    "v_mfma_f32_16x16x32_bf16 a[152:155], a[224:227], v[156:159], a[152:155]\n"
    "ds_write_b128 v12, a[144:147] offset:1024\n"
    "v_mfma_f32_16x16x32_bf16 a[152:155], a[228:231], v[184:187], a[152:155]\n"
    "s_waitcnt vmcnt(7)\n"
    "v_mfma_f32_16x16x32_bf16 a[152:155], a[232:235], v[212:215], a[152:155]\n"
    "s_waitcnt vmcnt(6)\n"
    "v_mfma_f32_16x16x32_bf16 a[152:155], a[236:239], v[240:243], a[152:155]\n"
    "v_mfma_f32_16x16x32_bf16 a[156:159], a[224:227], v[160:163], a[156:159]\n"
    "ds_write_b128 v12, a[148:151] offset:2048\n"
    "v_mfma_f32_16x16x32_bf16 a[156:159], a[228:231], v[188:191], a[156:159]\n"
    "s_waitcnt vmcnt(5)\n"
    "v_mfma_f32_16x16x32_bf16 a[156:159], a[232:235], v[216:219], a[156:159]\n"
    "s_waitcnt vmcnt(4)\n"
    "v_mfma_f32_16x16x32_bf16 a[156:159], a[236:239], v[244:247], a[156:159]\n"
    "v_mfma_f32_16x16x32_bf16 a[160:163], a[224:227], v[164:167], a[160:163]\n"
    "ds_write_b128 v12, a[152:155] offset:3072\n"
    "v_mfma_f32_16x16x32_bf16 a[160:163], a[228:231], v[192:195], a[160:163]\n"
    "s_waitcnt vmcnt(3)\n"
    "v_mfma_f32_16x16x32_bf16 a[160:163], a[232:235], v[220:223], a[160:163]\n"
    "s_waitcnt vmcnt(2)\n"
    "v_mfma_f32_16x16x32_bf16 a[160:163], a[236:239], v[248:251], a[160:163]\n"
    "v_mfma_f32_16x16x32_bf16 a[164:167], a[224:227], v[168:171], a[164:167]\n"
    "ds_write_b128 v12, a[156:159] offset:4096\n"
    "v_mfma_f32_16x16x32_bf16 a[164:167], a[228:231], v[196:199], a[164:167]\n"
    "s_waitcnt vmcnt(1)\n"
    "v_mfma_f32_16x16x32_bf16 a[164:167], a[232:235], v[224:227], a[164:167]\n"
    "s_waitcnt vmcnt(0)\n"
    "v_mfma_f32_16x16x32_bf16 a[164:167], a[236:239], v[252:255], a[164:167]\n"
    "s_waitcnt lgkmcnt(3)\n"
    "ds_write_b128 v12, a[160:163] offset:5120\n"
    "ds_read_b64_tr_b16 a[224:225], v17 offset:512\n"
    "v_mfma_f32_16x16x32_bf16 a[28:31], a[240:243], v[144:147], a[28:31]\n"
    "ds_read_b64_tr_b16 a[226:227], v17 offset:4608\n"
    "ds_read_b64_tr_b16 a[228:229], v17 offset:8704\n"
    "v_mfma_f32_16x16x32_bf16 a[28:31], a[244:247], v[172:175], a[28:31]\n"
    "ds_read_b64_tr_b16 a[230:231], v17 offset:12800\n"
    "ds_read_b64_tr_b16 a[232:233], v17 offset:33280\n"
    "v_mfma_f32_16x16x32_bf16 a[28:31], a[248:251], v[200:203], a[28:31]\n"
    "ds_read_b64_tr_b16 a[234:235], v17 offset:37376\n"
    "ds_read_b64_tr_b16 a[236:237], v17 offset:41472\n"
    "v_mfma_f32_16x16x32_bf16 a[28:31], a[252:255], v[228:231], a[28:31]\n"
    "ds_read_b64_tr_b16 a[238:239], v17 offset:45568\n"
    "v_mfma_f32_16x16x32_bf16 a[32:35], a[240:243], v[148:151], a[32:35]\n"
    "ds_write_b128 v12, a[164:167] offset:6144\n"
    "v_mfma_f32_16x16x32_bf16 a[32:35], a[244:247], v[176:179], a[32:35]\n"
    "v_mfma_f32_16x16x32_bf16 a[32:35], a[248:251], v[204:207], a[32:35]\n"
    "v_mfma_f32_16x16x32_bf16 a[32:35], a[252:255], v[232:235], a[32:35]\n"
    "v_mfma_f32_16x16x32_bf16 a[36:39], a[240:243], v[152:155], a[36:39]\n"
    "v_mfma_f32_16x16x32_bf16 a[36:39], a[244:247], v[180:183], a[36:39]\n"
    "v_accvgpr_read_b32 v116, a28\n"
    "v_accvgpr_read_b32 v117, a29\n"
    "v_mfma_f32_16x16x32_bf16 a[36:39], a[248:251], v[208:211], a[36:39]\n"
    "v_accvgpr_read_b32 v118, a30\n"
    "v_accvgpr_read_b32 v119, a31\n"
    "v_mfma_f32_16x16x32_bf16 a[36:39], a[252:255], v[236:239], a[36:39]\n"
    "s_waitcnt lgkmcnt(0)\n"
    "s_barrier\n"
    "ds_read_b128 v[32:35], v13 offset:0\n"
    "v_mfma_f32_16x16x32_bf16 a[40:43], a[240:243], v[156:159], a[40:43]\n"
    "ds_read_b128 v[36:39], v13 offset:1024\n"
    "v_accvgpr_read_b32 v120, a32\n"
    "v_mfma_f32_16x16x32_bf16 a[40:43], a[244:247], v[184:187], a[40:43]\n"
    "ds_read_b128 v[40:43], v13 offset:2048\n"
    "v_accvgpr_read_b32 v121, a33\n"
    "v_mfma_f32_16x16x32_bf16 a[40:43], a[248:251], v[212:215], a[40:43]\n"
    "ds_read_b128 v[44:47], v13 offset:3072\n"
    "v_accvgpr_read_b32 v122, a34\n"
    "v_mfma_f32_16x16x32_bf16 a[40:43], a[252:255], v[240:243], a[40:43]\n"
    "ds_read_b128 v[48:51], v13 offset:4096\n"
    "v_accvgpr_read_b32 v123, a35\n"
    "v_mfma_f32_16x16x32_bf16 a[44:47], a[240:243], v[160:163], a[44:47]\n"
    "ds_read_b128 v[52:55], v13 offset:5120\n"
    "v_accvgpr_read_b32 v124, a36\n"
    "v_mfma_f32_16x16x32_bf16 a[44:47], a[244:247], v[188:191], a[44:47]\n"
    "ds_read_b128 v[56:59], v13 offset:6144\n"
    "v_accvgpr_read_b32 v125, a37\n"
    "v_mfma_f32_16x16x32_bf16 a[44:47], a[248:251], v[216:219], a[44:47]\n"
    "v_accvgpr_read_b32 v126, a38\n"
    "v_accvgpr_read_b32 v127, a39\n"
    "v_mfma_f32_16x16x32_bf16 a[44:47], a[252:255], v[244:247], a[44:47]\n"
    "s_waitcnt lgkmcnt(6)\n"
    "v_add_f32 v116, v32, v116\n"
    "v_add_f32 v117, v33, v117\n"
    "v_mfma_f32_16x16x32_bf16 a[48:51], a[240:243], v[164:167], a[48:51]\n"
    "s_waitcnt lgkmcnt(5)\n"
    "v_add_f32 v118, v34, v118\n"
    "v_add_f32 v119, v35, v119\n"
    "v_mfma_f32_16x16x32_bf16 a[48:51], a[244:247], v[192:195], a[48:51]\n"
    "s_waitcnt lgkmcnt(4)\n"
    "v_add_f32 v120, v36, v120\n"
    "v_add_f32 v121, v37, v121\n"
    "v_mfma_f32_16x16x32_bf16 a[48:51], a[248:251], v[220:223], a[48:51]\n"
    "v_add_f32 v122, v38, v122\n"
    "v_add_f32 v123, v39, v123\n"
    "v_mfma_f32_16x16x32_bf16 a[48:51], a[252:255], v[248:251], a[48:51]\n"
    "v_add_f32 v124, v40, v124\n"
    "v_add_f32 v125, v41, v125\n"
    "v_mfma_f32_16x16x32_bf16 a[52:55], a[240:243], v[168:171], a[52:55]\n"
    "ds_read_b64_tr_b16 a[240:241], v14 offset:512\n"
    "v_add_f32 v126, v42, v126\n"
    "v_mfma_f32_16x16x32_bf16 a[52:55], a[244:247], v[196:199], a[52:55]\n"
    "ds_read_b64_tr_b16 a[242:243], v14 offset:4608\n"
    "v_add_f32 v127, v43, v127\n"
    "v_mfma_f32_16x16x32_bf16 a[52:55], a[248:251], v[224:227], a[52:55]\n"
    "ds_read_b64_tr_b16 a[244:245], v14 offset:8704\n"
    "v_cvt_pk_bf16_f32 v32, v116, v117\n"
    "v_mfma_f32_16x16x32_bf16 a[52:55], a[252:255], v[252:255], a[52:55]\n"
    "ds_read_b64_tr_b16 a[246:247], v14 offset:12800\n"
    "v_cvt_pk_bf16_f32 v33, v118, v119\n"
    "v_mfma_f32_16x16x32_bf16 a[168:171], a[224:227], v[144:147], a[168:171]\n"
    "ds_read_b64_tr_b16 a[248:249], v14 offset:33280\n"
    "v_cvt_pk_bf16_f32 v34, v120, v121\n"
    "v_mfma_f32_16x16x32_bf16 a[168:171], a[228:231], v[172:175], a[168:171]\n"
    "ds_read_b64_tr_b16 a[250:251], v14 offset:37376\n"
    "v_cvt_pk_bf16_f32 v35, v122, v123\n"
    "v_mfma_f32_16x16x32_bf16 a[168:171], a[232:235], v[200:203], a[168:171]\n"
    "buffer_store_dwordx4 v[32:35], v27, s[28:31], s64 offen sc1\n"
    "v_mfma_f32_16x16x32_bf16 a[168:171], a[236:239], v[228:231], a[168:171]\n"
    "ds_read_b64_tr_b16 a[252:253], v14 offset:41472\n"
    "v_cvt_pk_bf16_f32 v40, v124, v125\n"
    "v_mfma_f32_16x16x32_bf16 a[172:175], a[224:227], v[148:151], a[172:175]\n"
    "ds_read_b64_tr_b16 a[254:255], v14 offset:45568\n"
    "v_cvt_pk_bf16_f32 v41, v126, v127\n"
    "v_mfma_f32_16x16x32_bf16 a[172:175], a[228:231], v[176:179], a[172:175]\n"
    "v_accvgpr_read_b32 v128, a40\n"
    "v_accvgpr_read_b32 v129, a41\n"
    "v_mfma_f32_16x16x32_bf16 a[172:175], a[232:235], v[204:207], a[172:175]\n"
    "s_waitcnt lgkmcnt(5)\n"
    "v_accvgpr_read_b32 v130, a42\n"
    "v_accvgpr_read_b32 v131, a43\n"
    "v_mfma_f32_16x16x32_bf16 a[172:175], a[236:239], v[232:235], a[172:175]\n"
    "ds_write_b128 v12, a[168:171] offset:7168\n"
    "v_mfma_f32_16x16x32_bf16 a[176:179], a[224:227], v[152:155], a[176:179]\n"
    "v_add_f32 v128, v44, v128\n"
    "v_add_f32 v129, v45, v129\n"
    "v_mfma_f32_16x16x32_bf16 a[176:179], a[228:231], v[180:183], a[176:179]\n"
    "v_add_f32 v130, v46, v130\n"
    "v_add_f32 v131, v47, v131\n"
    "v_mfma_f32_16x16x32_bf16 a[176:179], a[232:235], v[208:211], a[176:179]\n"
    "v_cvt_pk_bf16_f32 v42, v128, v129\n"
    "v_cvt_pk_bf16_f32 v43, v130, v131\n"
    "v_mfma_f32_16x16x32_bf16 a[176:179], a[236:239], v[236:239], a[176:179]\n"
    "buffer_store_dwordx4 v[40:43], v27, s[28:31], s64 offen offset:1024 sc1\n"
    "v_mfma_f32_16x16x32_bf16 a[180:183], a[224:227], v[156:159], a[180:183]\n"
    "ds_write_b128 v12, a[172:175] offset:8192\n"
    "v_mfma_f32_16x16x32_bf16 a[180:183], a[228:231], v[184:187], a[180:183]\n"
    "v_accvgpr_read_b32 v132, a44\n"
    "v_accvgpr_read_b32 v133, a45\n"
    "v_mfma_f32_16x16x32_bf16 a[180:183], a[232:235], v[212:215], a[180:183]\n"
    "v_accvgpr_read_b32 v134, a46\n"
    "v_accvgpr_read_b32 v135, a47\n"
    "v_mfma_f32_16x16x32_bf16 a[180:183], a[236:239], v[240:243], a[180:183]\n"
    "ds_write_b128 v12, a[176:179] offset:9216\n"
    "v_mfma_f32_16x16x32_bf16 a[184:187], a[224:227], v[160:163], a[184:187]\n"
    "v_add_f32 v132, v48, v132\n"
    "v_add_f32 v133, v49, v133\n"
    "v_mfma_f32_16x16x32_bf16 a[184:187], a[228:231], v[188:191], a[184:187]\n"
    "v_add_f32 v134, v50, v134\n"
    "v_add_f32 v135, v51, v135\n"
    "v_mfma_f32_16x16x32_bf16 a[184:187], a[232:235], v[216:219], a[184:187]\n"
    "v_cvt_pk_bf16_f32 v48, v132, v133\n"
    "v_cvt_pk_bf16_f32 v49, v134, v135\n"
    "v_mfma_f32_16x16x32_bf16 a[184:187], a[236:239], v[244:247], a[184:187]\n"
    "ds_write_b128 v12, a[180:183] offset:10240\n"
    "v_mfma_f32_16x16x32_bf16 a[188:191], a[224:227], v[164:167], a[188:191]\n"
    "v_accvgpr_read_b32 v136, a48\n"
    "v_accvgpr_read_b32 v137, a49\n"
    "v_mfma_f32_16x16x32_bf16 a[188:191], a[228:231], v[192:195], a[188:191]\n"
    "v_accvgpr_read_b32 v138, a50\n"
    "v_accvgpr_read_b32 v139, a51\n"
    "v_mfma_f32_16x16x32_bf16 a[188:191], a[232:235], v[220:223], a[188:191]\n"
    "ds_write_b128 v12, a[184:187] offset:11264\n"
    "v_mfma_f32_16x16x32_bf16 a[188:191], a[236:239], v[248:251], a[188:191]\n"
    "v_add_f32 v136, v52, v136\n"
    "v_add_f32 v137, v53, v137\n"
    "v_mfma_f32_16x16x32_bf16 a[192:195], a[224:227], v[168:171], a[192:195]\n"
    "v_add_f32 v138, v54, v138\n"
    "v_add_f32 v139, v55, v139\n"
    "v_mfma_f32_16x16x32_bf16 a[192:195], a[228:231], v[196:199], a[192:195]\n"
    "v_cvt_pk_bf16_f32 v50, v136, v137\n"
    "v_cvt_pk_bf16_f32 v51, v138, v139\n"
    "ds_write_b128 v12, a[188:191] offset:12288\n"
    "v_mfma_f32_16x16x32_bf16 a[192:195], a[232:235], v[224:227], a[192:195]\n"
    "buffer_store_dwordx4 v[48:51], v27, s[28:31], s64 offen offset:2048 sc1\n"
    "ds_read_b64_tr_b16 a[224:225], v17 offset:768\n"
    "v_mfma_f32_16x16x32_bf16 a[192:195], a[236:239], v[252:255], a[192:195]\n"
    "ds_read_b64_tr_b16 a[226:227], v17 offset:4864\n"
    "v_accvgpr_read_b32 v140, a52\n"
    "ds_read_b64_tr_b16 a[228:229], v17 offset:8960\n"
    "v_mfma_f32_16x16x32_bf16 a[56:59], a[240:243], v[144:147], a[56:59]\n"
    "s_waitcnt lgkmcnt(6)\n"
    "ds_read_b64_tr_b16 a[230:231], v17 offset:13056\n"
    "v_accvgpr_read_b32 v141, a53\n"
    "ds_read_b64_tr_b16 a[232:233], v17 offset:33536\n"
    "v_mfma_f32_16x16x32_bf16 a[56:59], a[244:247], v[172:175], a[56:59]\n"
    "ds_read_b64_tr_b16 a[234:235], v17 offset:37632\n"
    "v_accvgpr_read_b32 v142, a54\n"
    "v_mfma_f32_16x16x32_bf16 a[56:59], a[248:251], v[200:203], a[56:59]\n"
    "ds_read_b64_tr_b16 a[236:237], v17 offset:41728\n"
    "v_accvgpr_read_b32 v143, a55\n"
    "v_mfma_f32_16x16x32_bf16 a[56:59], a[252:255], v[228:231], a[56:59]\n"
    "ds_read_b64_tr_b16 a[238:239], v17 offset:45824\n"
    "v_add_f32 v140, v56, v140\n"
    "v_mfma_f32_16x16x32_bf16 a[60:63], a[240:243], v[148:151], a[60:63]\n"
    "ds_write_b128 v12, a[192:195] offset:13312\n"
    "v_mfma_f32_16x16x32_bf16 a[60:63], a[244:247], v[176:179], a[60:63]\n"
    "v_add_f32 v141, v57, v141\n"
    "v_add_f32 v142, v58, v142\n"
    "v_mfma_f32_16x16x32_bf16 a[60:63], a[248:251], v[204:207], a[60:63]\n"
    "v_accvgpr_read_b32 v116, a56\n"
    "v_accvgpr_read_b32 v117, a57\n"
    "v_mfma_f32_16x16x32_bf16 a[60:63], a[252:255], v[232:235], a[60:63]\n"
    "v_accvgpr_read_b32 v118, a58\n"
    "v_accvgpr_read_b32 v119, a59\n"
    "v_mfma_f32_16x16x32_bf16 a[64:67], a[240:243], v[152:155], a[64:67]\n"
    "v_add_f32 v143, v59, v143\n"
    "v_cvt_pk_bf16_f32 v56, v140, v141\n"
    "v_mfma_f32_16x16x32_bf16 a[64:67], a[244:247], v[180:183], a[64:67]\n"
    "v_cvt_pk_bf16_f32 v57, v142, v143\n"
    "v_mfma_f32_16x16x32_bf16 a[64:67], a[248:251], v[208:211], a[64:67]\n"
    "buffer_store_dwordx2 v[56:57], v22, s[28:31], s64 offen offset:3072 sc1\n"
    "v_mfma_f32_16x16x32_bf16 a[64:67], a[252:255], v[236:239], a[64:67]\n"
    "s_waitcnt lgkmcnt(0)\n"
    "s_barrier\n"
    "ds_read_b128 v[60:63], v13 offset:7168\n"
    "v_mfma_f32_16x16x32_bf16 a[68:71], a[240:243], v[156:159], a[68:71]\n"
    "s_xor_b32 s64, s19, 2\n"
    "ds_read_b128 v[64:67], v13 offset:8192\n"
    "v_accvgpr_read_b32 v120, a60\n"
    "v_mfma_f32_16x16x32_bf16 a[68:71], a[244:247], v[184:187], a[68:71]\n"
    "s_mul_i32 s64, s64, 53248\n"
    "ds_read_b128 v[68:71], v13 offset:9216\n"
    "v_accvgpr_read_b32 v121, a61\n"
    "v_mfma_f32_16x16x32_bf16 a[68:71], a[248:251], v[212:215], a[68:71]\n"
    "s_add_u32 s64, s64, 4096\n"
    "ds_read_b128 v[72:75], v13 offset:10240\n"
    "v_accvgpr_read_b32 v122, a62\n"
    "v_mfma_f32_16x16x32_bf16 a[68:71], a[252:255], v[240:243], a[68:71]\n"
    "ds_read_b128 v[76:79], v13 offset:11264\n"
    "v_accvgpr_read_b32 v123, a63\n"
    "v_mfma_f32_16x16x32_bf16 a[72:75], a[240:243], v[160:163], a[72:75]\n"
    "ds_read_b128 v[80:83], v13 offset:12288\n"
    "v_accvgpr_read_b32 v124, a64\n"
    "v_mfma_f32_16x16x32_bf16 a[72:75], a[244:247], v[188:191], a[72:75]\n"
    "ds_read_b128 v[84:87], v13 offset:13312\n"
    "v_accvgpr_read_b32 v125, a65\n"
    "v_mfma_f32_16x16x32_bf16 a[72:75], a[248:251], v[216:219], a[72:75]\n"
    "v_accvgpr_read_b32 v126, a66\n"
    "v_accvgpr_read_b32 v127, a67\n"
    "v_mfma_f32_16x16x32_bf16 a[72:75], a[252:255], v[244:247], a[72:75]\n"
    "s_waitcnt lgkmcnt(6)\n"
    "v_add_f32 v116, v60, v116\n"
    "v_add_f32 v117, v61, v117\n"
    "v_mfma_f32_16x16x32_bf16 a[76:79], a[240:243], v[164:167], a[76:79]\n"
    "s_waitcnt lgkmcnt(5)\n"
    "v_add_f32 v118, v62, v118\n"
    "v_add_f32 v119, v63, v119\n"
    "v_mfma_f32_16x16x32_bf16 a[76:79], a[244:247], v[192:195], a[76:79]\n"
    "s_waitcnt lgkmcnt(4)\n"
    "v_add_f32 v120, v64, v120\n"
    "v_add_f32 v121, v65, v121\n"
    "v_mfma_f32_16x16x32_bf16 a[76:79], a[248:251], v[220:223], a[76:79]\n"
    "v_add_f32 v122, v66, v122\n"
    "v_add_f32 v123, v67, v123\n"
    "v_mfma_f32_16x16x32_bf16 a[76:79], a[252:255], v[248:251], a[76:79]\n"
    "v_add_f32 v124, v68, v124\n"
    "v_add_f32 v125, v69, v125\n"
    "v_mfma_f32_16x16x32_bf16 a[80:83], a[240:243], v[168:171], a[80:83]\n"
    "ds_read_b64_tr_b16 a[240:241], v14 offset:768\n"
    "v_add_f32 v126, v70, v126\n"
    "v_mfma_f32_16x16x32_bf16 a[80:83], a[244:247], v[196:199], a[80:83]\n"
    "ds_read_b64_tr_b16 a[242:243], v14 offset:4864\n"
    "v_add_f32 v127, v71, v127\n"
    "v_mfma_f32_16x16x32_bf16 a[80:83], a[248:251], v[224:227], a[80:83]\n"
    "ds_read_b64_tr_b16 a[244:245], v14 offset:8960\n"
    "v_cvt_pk_bf16_f32 v60, v116, v117\n"
    "v_mfma_f32_16x16x32_bf16 a[80:83], a[252:255], v[252:255], a[80:83]\n"
    "ds_read_b64_tr_b16 a[246:247], v14 offset:13056\n"
    "v_cvt_pk_bf16_f32 v61, v118, v119\n"
    "v_mfma_f32_16x16x32_bf16 a[196:199], a[224:227], v[144:147], a[196:199]\n"
    "ds_read_b64_tr_b16 a[248:249], v14 offset:33536\n"
    "v_cvt_pk_bf16_f32 v62, v120, v121\n"
    "v_mfma_f32_16x16x32_bf16 a[196:199], a[228:231], v[172:175], a[196:199]\n"
    "ds_read_b64_tr_b16 a[250:251], v14 offset:37632\n"
    "v_cvt_pk_bf16_f32 v63, v122, v123\n"
    "v_mfma_f32_16x16x32_bf16 a[196:199], a[232:235], v[200:203], a[196:199]\n"
    "buffer_store_dwordx4 v[60:63], v27, s[28:31], s64 offen sc1\n"
    "v_mfma_f32_16x16x32_bf16 a[196:199], a[236:239], v[228:231], a[196:199]\n"
    "ds_read_b64_tr_b16 a[252:253], v14 offset:41728\n"
    "v_cvt_pk_bf16_f32 v68, v124, v125\n"
    "v_mfma_f32_16x16x32_bf16 a[200:203], a[224:227], v[148:151], a[200:203]\n"
    "ds_read_b64_tr_b16 a[254:255], v14 offset:45824\n"
    "v_cvt_pk_bf16_f32 v69, v126, v127\n"
    "v_mfma_f32_16x16x32_bf16 a[200:203], a[228:231], v[176:179], a[200:203]\n"
    "v_accvgpr_read_b32 v128, a68\n"
    "v_accvgpr_read_b32 v129, a69\n"
    "v_mfma_f32_16x16x32_bf16 a[200:203], a[232:235], v[204:207], a[200:203]\n"
    "s_waitcnt lgkmcnt(5)\n"
    "v_accvgpr_read_b32 v130, a70\n"
    "v_accvgpr_read_b32 v131, a71\n"
    "v_mfma_f32_16x16x32_bf16 a[200:203], a[236:239], v[232:235], a[200:203]\n"
    "ds_write_b128 v12, a[196:199] offset:14336\n"
    "v_mfma_f32_16x16x32_bf16 a[204:207], a[224:227], v[152:155], a[204:207]\n"
    "v_add_f32 v128, v72, v128\n"
    "v_add_f32 v129, v73, v129\n"
    "v_mfma_f32_16x16x32_bf16 a[204:207], a[228:231], v[180:183], a[204:207]\n"
    "v_add_f32 v130, v74, v130\n"
    "v_add_f32 v131, v75, v131\n"
    "v_mfma_f32_16x16x32_bf16 a[204:207], a[232:235], v[208:211], a[204:207]\n"
    "v_cvt_pk_bf16_f32 v70, v128, v129\n"
    "v_cvt_pk_bf16_f32 v71, v130, v131\n"
    "v_mfma_f32_16x16x32_bf16 a[204:207], a[236:239], v[236:239], a[204:207]\n"
    "buffer_store_dwordx4 v[68:71], v27, s[28:31], s64 offen offset:1024 sc1\n"
    "v_mfma_f32_16x16x32_bf16 a[208:211], a[224:227], v[156:159], a[208:211]\n"
    "ds_write_b128 v12, a[200:203] offset:15360\n"
    "v_mfma_f32_16x16x32_bf16 a[208:211], a[228:231], v[184:187], a[208:211]\n"
    "v_accvgpr_read_b32 v132, a72\n"
    "v_accvgpr_read_b32 v133, a73\n"
    "v_mfma_f32_16x16x32_bf16 a[208:211], a[232:235], v[212:215], a[208:211]\n"
    "v_accvgpr_read_b32 v134, a74\n"
    "v_accvgpr_read_b32 v135, a75\n"
    "v_mfma_f32_16x16x32_bf16 a[208:211], a[236:239], v[240:243], a[208:211]\n"
    "ds_write_b128 v12, a[204:207] offset:16384\n"
    "v_mfma_f32_16x16x32_bf16 a[212:215], a[224:227], v[160:163], a[212:215]\n"
    "v_add_f32 v132, v76, v132\n"
    "v_add_f32 v133, v77, v133\n"
    "v_mfma_f32_16x16x32_bf16 a[212:215], a[228:231], v[188:191], a[212:215]\n"
    "v_add_f32 v134, v78, v134\n"
    "v_add_f32 v135, v79, v135\n"
    "v_mfma_f32_16x16x32_bf16 a[212:215], a[232:235], v[216:219], a[212:215]\n"
    "v_cvt_pk_bf16_f32 v76, v132, v133\n"
    "v_cvt_pk_bf16_f32 v77, v134, v135\n"
    "v_mfma_f32_16x16x32_bf16 a[212:215], a[236:239], v[244:247], a[212:215]\n"
    "ds_write_b128 v12, a[208:211] offset:17408\n"
    "v_mfma_f32_16x16x32_bf16 a[216:219], a[224:227], v[164:167], a[216:219]\n"
    "v_accvgpr_read_b32 v136, a76\n"
    "v_accvgpr_read_b32 v137, a77\n"
    "v_mfma_f32_16x16x32_bf16 a[216:219], a[228:231], v[192:195], a[216:219]\n"
    "v_accvgpr_read_b32 v138, a78\n"
    "v_accvgpr_read_b32 v139, a79\n"
    "v_mfma_f32_16x16x32_bf16 a[216:219], a[232:235], v[220:223], a[216:219]\n"
    "ds_write_b128 v12, a[212:215] offset:18432\n"
    "v_mfma_f32_16x16x32_bf16 a[216:219], a[236:239], v[248:251], a[216:219]\n"
    "v_add_f32 v136, v80, v136\n"
    "v_add_f32 v137, v81, v137\n"
    "v_mfma_f32_16x16x32_bf16 a[220:223], a[224:227], v[168:171], a[220:223]\n"
    "v_add_f32 v138, v82, v138\n"
    "v_add_f32 v139, v83, v139\n"
    "v_mfma_f32_16x16x32_bf16 a[220:223], a[228:231], v[196:199], a[220:223]\n"
    "v_cvt_pk_bf16_f32 v78, v136, v137\n"
    "v_cvt_pk_bf16_f32 v79, v138, v139\n"
    "ds_write_b128 v12, a[216:219] offset:19456\n"
    "v_mfma_f32_16x16x32_bf16 a[220:223], a[232:235], v[224:227], a[220:223]\n"
    "buffer_store_dwordx4 v[76:79], v27, s[28:31], s64 offen offset:2048 sc1\n"
    "ds_read_b64_tr_b16 a[224:225], v17 offset:0\n"
    "v_mfma_f32_16x16x32_bf16 a[220:223], a[236:239], v[252:255], a[220:223]\n"
    "ds_read_b64_tr_b16 a[226:227], v17 offset:4096\n"
    "v_accvgpr_read_b32 v140, a80\n"
    "ds_read_b64_tr_b16 a[228:229], v17 offset:8192\n"
    "v_mfma_f32_16x16x32_bf16 a[84:87], a[240:243], v[144:147], a[84:87]\n"
    "s_waitcnt lgkmcnt(6)\n"
    "ds_read_b64_tr_b16 a[230:231], v17 offset:12288\n"
    "v_accvgpr_read_b32 v141, a81\n"
    "ds_read_b64_tr_b16 a[232:233], v17 offset:32768\n"
    "v_mfma_f32_16x16x32_bf16 a[84:87], a[244:247], v[172:175], a[84:87]\n"
    "ds_read_b64_tr_b16 a[234:235], v17 offset:36864\n"
    "v_accvgpr_read_b32 v142, a82\n"
    "v_mfma_f32_16x16x32_bf16 a[84:87], a[248:251], v[200:203], a[84:87]\n"
    "ds_read_b64_tr_b16 a[236:237], v17 offset:40960\n"
    "v_accvgpr_read_b32 v143, a83\n"
    "v_mfma_f32_16x16x32_bf16 a[84:87], a[252:255], v[228:231], a[84:87]\n"
    "ds_read_b64_tr_b16 a[238:239], v17 offset:45056\n"
    "v_add_f32 v140, v84, v140\n"
    "v_mfma_f32_16x16x32_bf16 a[88:91], a[240:243], v[148:151], a[88:91]\n"
    "ds_write_b128 v12, a[220:223] offset:20480\n"
    "v_mfma_f32_16x16x32_bf16 a[88:91], a[244:247], v[176:179], a[88:91]\n"
    "v_add_f32 v141, v85, v141\n"
    "v_add_f32 v142, v86, v142\n"
    "v_mfma_f32_16x16x32_bf16 a[88:91], a[248:251], v[204:207], a[88:91]\n"
    "v_accvgpr_read_b32 v116, a84\n"
    "v_accvgpr_read_b32 v117, a85\n"
    "v_mfma_f32_16x16x32_bf16 a[88:91], a[252:255], v[232:235], a[88:91]\n"
    "v_accvgpr_read_b32 v118, a86\n"
    "v_accvgpr_read_b32 v119, a87\n"
    "v_mfma_f32_16x16x32_bf16 a[92:95], a[240:243], v[152:155], a[92:95]\n"
    "v_add_f32 v143, v87, v143\n"
    "v_cvt_pk_bf16_f32 v84, v140, v141\n"
    "v_mfma_f32_16x16x32_bf16 a[92:95], a[244:247], v[180:183], a[92:95]\n"
    "v_cvt_pk_bf16_f32 v85, v142, v143\n"
    "v_mfma_f32_16x16x32_bf16 a[92:95], a[248:251], v[208:211], a[92:95]\n"
    "buffer_store_dwordx2 v[84:85], v22, s[28:31], s64 offen offset:3072 sc1\n"
    "v_mfma_f32_16x16x32_bf16 a[92:95], a[252:255], v[236:239], a[92:95]\n"
    "s_waitcnt lgkmcnt(0)\n"
    "s_barrier\n"
    "ds_read_b128 v[88:91], v13 offset:14336\n"
    "v_mfma_f32_16x16x32_bf16 a[96:99], a[240:243], v[156:159], a[96:99]\n"
    "s_xor_b32 s64, s19, 3\n"
    "ds_read_b128 v[92:95], v13 offset:15360\n"
    "v_accvgpr_read_b32 v120, a88\n"
    "v_mfma_f32_16x16x32_bf16 a[96:99], a[244:247], v[184:187], a[96:99]\n"
    "s_mul_i32 s64, s64, 53248\n"
    "ds_read_b128 v[96:99], v13 offset:16384\n"
    "v_accvgpr_read_b32 v121, a89\n"
    "v_mfma_f32_16x16x32_bf16 a[96:99], a[248:251], v[212:215], a[96:99]\n"
    "s_add_u32 s64, s64, 8192\n"
    "ds_read_b128 v[100:103], v13 offset:17408\n"
    "v_accvgpr_read_b32 v122, a90\n"
    "v_mfma_f32_16x16x32_bf16 a[96:99], a[252:255], v[240:243], a[96:99]\n"
    "ds_read_b128 v[104:107], v13 offset:18432\n"
    "v_accvgpr_read_b32 v123, a91\n"
    "v_mfma_f32_16x16x32_bf16 a[100:103], a[240:243], v[160:163], a[100:103]\n"
    "ds_read_b128 v[108:111], v13 offset:19456\n"
    "v_accvgpr_read_b32 v124, a92\n"
    "v_mfma_f32_16x16x32_bf16 a[100:103], a[244:247], v[188:191], a[100:103]\n"
    "ds_read_b128 v[112:115], v13 offset:20480\n"
    "v_accvgpr_read_b32 v125, a93\n"
    "v_mfma_f32_16x16x32_bf16 a[100:103], a[248:251], v[216:219], a[100:103]\n"
    "v_accvgpr_read_b32 v126, a94\n"
    "v_accvgpr_read_b32 v127, a95\n"
    "v_mfma_f32_16x16x32_bf16 a[100:103], a[252:255], v[244:247], a[100:103]\n"
    "s_waitcnt lgkmcnt(6)\n"
    "v_add_f32 v116, v88, v116\n"
    "v_add_f32 v117, v89, v117\n"
    "v_mfma_f32_16x16x32_bf16 a[104:107], a[240:243], v[164:167], a[104:107]\n"
    "s_waitcnt lgkmcnt(5)\n"
    "v_add_f32 v118, v90, v118\n"
    "v_add_f32 v119, v91, v119\n"
    "v_mfma_f32_16x16x32_bf16 a[104:107], a[244:247], v[192:195], a[104:107]\n"
    "s_waitcnt lgkmcnt(4)\n"
    "v_add_f32 v120, v92, v120\n"
    "v_add_f32 v121, v93, v121\n"
    "v_mfma_f32_16x16x32_bf16 a[104:107], a[248:251], v[220:223], a[104:107]\n"
    "v_add_f32 v122, v94, v122\n"
    "v_add_f32 v123, v95, v123\n"
    "v_mfma_f32_16x16x32_bf16 a[104:107], a[252:255], v[248:251], a[104:107]\n"
    "v_add_f32 v124, v96, v124\n"
    "v_add_f32 v125, v97, v125\n"
    "v_mfma_f32_16x16x32_bf16 a[108:111], a[240:243], v[168:171], a[108:111]\n"
    "ds_read_b64_tr_b16 a[240:241], v14 offset:0\n"
    "v_add_f32 v126, v98, v126\n"
    "v_mfma_f32_16x16x32_bf16 a[108:111], a[244:247], v[196:199], a[108:111]\n"
    "ds_read_b64_tr_b16 a[242:243], v14 offset:4096\n"
    "v_add_f32 v127, v99, v127\n"
    "v_mfma_f32_16x16x32_bf16 a[108:111], a[248:251], v[224:227], a[108:111]\n"
    "ds_read_b64_tr_b16 a[244:245], v14 offset:8192\n"
    "v_cvt_pk_bf16_f32 v88, v116, v117\n"
    "v_mfma_f32_16x16x32_bf16 a[108:111], a[252:255], v[252:255], a[108:111]\n"
    "ds_read_b64_tr_b16 a[246:247], v14 offset:12288\n"
    "v_cvt_pk_bf16_f32 v89, v118, v119\n"
    "v_cvt_pk_bf16_f32 v90, v120, v121\n"
    "v_cvt_pk_bf16_f32 v91, v122, v123\n"
    "v_cvt_pk_bf16_f32 v96, v124, v125\n"
    "v_cvt_pk_bf16_f32 v97, v126, v127\n"
    "v_mfma_f32_16x16x32_bf16 a[112:115], a[224:227], v[144:147], a[112:115]\n"
    "buffer_store_dwordx4 v[88:91], v27, s[28:31], s64 offen sc1\n"
    "v_mfma_f32_16x16x32_bf16 a[112:115], a[228:231], v[172:175], a[112:115]\n"
    "ds_read_b64_tr_b16 a[248:249], v14 offset:32768\n"
    "v_accvgpr_read_b32 v128, a96\n"
    "v_mfma_f32_16x16x32_bf16 a[112:115], a[232:235], v[200:203], a[112:115]\n"
    "s_waitcnt lgkmcnt(5)\n"
    "ds_read_b64_tr_b16 a[250:251], v14 offset:36864\n"
    "v_accvgpr_read_b32 v129, a97\n"
    "v_accvgpr_read_b32 v130, a98\n"
    "v_accvgpr_read_b32 v131, a99\n"
    "v_mfma_f32_16x16x32_bf16 a[112:115], a[236:239], v[228:231], a[112:115]\n"
    "ds_read_b64_tr_b16 a[252:253], v14 offset:40960\n"
    "v_add_f32 v128, v100, v128\n"
    "v_add_f32 v129, v101, v129\n"
    "v_add_f32 v130, v102, v130\n"
    "v_add_f32 v131, v103, v131\n"
    "v_cvt_pk_bf16_f32 v98, v128, v129\n"
    "v_cvt_pk_bf16_f32 v99, v130, v131\n"
    "v_mfma_f32_16x16x32_bf16 a[116:119], a[224:227], v[148:151], a[116:119]\n"
    "buffer_store_dwordx4 v[96:99], v27, s[28:31], s64 offen offset:1024 sc1\n"
    "v_mfma_f32_16x16x32_bf16 a[116:119], a[228:231], v[176:179], a[116:119]\n"
    "ds_read_b64_tr_b16 a[254:255], v14 offset:45056\n"
    "v_accvgpr_read_b32 v132, a100\n"
    "v_mfma_f32_16x16x32_bf16 a[116:119], a[232:235], v[204:207], a[116:119]\n"
    "v_accvgpr_read_b32 v133, a101\n"
    "v_accvgpr_read_b32 v134, a102\n"
    "v_accvgpr_read_b32 v135, a103\n"
    "v_mfma_f32_16x16x32_bf16 a[116:119], a[236:239], v[232:235], a[116:119]\n"
    "ds_write_b128 v12, a[112:115] offset:0\n"
    "v_add_f32 v132, v104, v132\n"
    "v_add_f32 v133, v105, v133\n"
    "v_add_f32 v134, v106, v134\n"
    "v_add_f32 v135, v107, v135\n"
    "v_cvt_pk_bf16_f32 v104, v132, v133\n"
    "v_cvt_pk_bf16_f32 v105, v134, v135\n"
    "v_mfma_f32_16x16x32_bf16 a[120:123], a[224:227], v[152:155], a[120:123]\n"
    "v_accvgpr_read_b32 v136, a104\n"
    "v_accvgpr_read_b32 v137, a105\n"
    "v_mfma_f32_16x16x32_bf16 a[120:123], a[228:231], v[180:183], a[120:123]\n"
    "v_accvgpr_read_b32 v138, a106\n"
    "v_accvgpr_read_b32 v139, a107\n"
    "v_mfma_f32_16x16x32_bf16 a[120:123], a[232:235], v[208:211], a[120:123]\n"
    "v_add_f32 v136, v108, v136\n"
    "v_add_f32 v137, v109, v137\n"
    "v_mfma_f32_16x16x32_bf16 a[120:123], a[236:239], v[236:239], a[120:123]\n"
    "ds_write_b128 v12, a[116:119] offset:1024\n"
    "v_add_f32 v138, v110, v138\n"
    "v_add_f32 v139, v111, v139\n"
    "v_cvt_pk_bf16_f32 v106, v136, v137\n"
    "v_cvt_pk_bf16_f32 v107, v138, v139\n"
    "v_mfma_f32_16x16x32_bf16 a[124:127], a[224:227], v[156:159], a[124:127]\n"
    "buffer_store_dwordx4 v[104:107], v27, s[28:31], s64 offen offset:2048 sc1\n"
    "v_mfma_f32_16x16x32_bf16 a[124:127], a[228:231], v[184:187], a[124:127]\n"
    "v_accvgpr_read_b32 v140, a108\n"
    "v_accvgpr_read_b32 v141, a109\n"
    "v_mfma_f32_16x16x32_bf16 a[124:127], a[232:235], v[212:215], a[124:127]\n"
    "v_accvgpr_read_b32 v142, a110\n"
    "v_accvgpr_read_b32 v143, a111\n"
    "v_mfma_f32_16x16x32_bf16 a[124:127], a[236:239], v[240:243], a[124:127]\n"
    "ds_write_b128 v12, a[120:123] offset:2048\n"
    "v_add_f32 v140, v112, v140\n"
    "v_add_f32 v141, v113, v141\n"
    "v_add_f32 v142, v114, v142\n"
    "v_add_f32 v143, v115, v143\n"
    "v_cvt_pk_bf16_f32 v112, v140, v141\n"
    "v_cvt_pk_bf16_f32 v113, v142, v143\n"
    "v_mfma_f32_16x16x32_bf16 a[128:131], a[224:227], v[160:163], a[128:131]\n"
    "buffer_store_dwordx2 v[112:113], v22, s[28:31], s64 offen offset:3072 sc1\n"
    "v_mfma_f32_16x16x32_bf16 a[128:131], a[228:231], v[188:191], a[128:131]\n"
    "v_mfma_f32_16x16x32_bf16 a[128:131], a[232:235], v[216:219], a[128:131]\n"
    "v_mfma_f32_16x16x32_bf16 a[128:131], a[236:239], v[244:247], a[128:131]\n"
    "ds_write_b128 v12, a[124:127] offset:3072\n"
    "v_mfma_f32_16x16x32_bf16 a[132:135], a[224:227], v[164:167], a[132:135]\n"
    "v_mfma_f32_16x16x32_bf16 a[132:135], a[228:231], v[192:195], a[132:135]\n"
    "v_mfma_f32_16x16x32_bf16 a[132:135], a[232:235], v[220:223], a[132:135]\n"
    "v_mfma_f32_16x16x32_bf16 a[132:135], a[236:239], v[248:251], a[132:135]\n"
    "v_mfma_f32_16x16x32_bf16 a[136:139], a[224:227], v[168:171], a[136:139]\n"
    "v_mfma_f32_16x16x32_bf16 a[136:139], a[228:231], v[196:199], a[136:139]\n"
    "v_mfma_f32_16x16x32_bf16 a[136:139], a[232:235], v[224:227], a[136:139]\n"
    "ds_write_b128 v12, a[128:131] offset:4096\n"
    "v_mfma_f32_16x16x32_bf16 a[136:139], a[236:239], v[252:255], a[136:139]\n"
    "s_waitcnt lgkmcnt(2)\n"
    "v_mfma_f32_16x16x32_bf16 a[0:3], a[240:243], v[144:147], a[0:3]\n"
    "v_mfma_f32_16x16x32_bf16 a[0:3], a[244:247], v[172:175], a[0:3]\n"
    "ds_write_b128 v12, a[132:135] offset:5120\n"
    "v_mfma_f32_16x16x32_bf16 a[0:3], a[248:251], v[200:203], a[0:3]\n"
    "v_mfma_f32_16x16x32_bf16 a[0:3], a[252:255], v[228:231], a[0:3]\n"
    "v_mfma_f32_16x16x32_bf16 a[4:7], a[240:243], v[148:151], a[4:7]\n"
    "v_mfma_f32_16x16x32_bf16 a[4:7], a[244:247], v[176:179], a[4:7]\n"
    "ds_write_b128 v12, a[136:139] offset:6144\n"
    "v_mfma_f32_16x16x32_bf16 a[4:7], a[248:251], v[204:207], a[4:7]\n"
    "v_mfma_f32_16x16x32_bf16 a[4:7], a[252:255], v[232:235], a[4:7]\n"
    "v_mfma_f32_16x16x32_bf16 a[8:11], a[240:243], v[152:155], a[8:11]\n"
    "v_mfma_f32_16x16x32_bf16 a[8:11], a[244:247], v[180:183], a[8:11]\n"
    "v_mfma_f32_16x16x32_bf16 a[8:11], a[248:251], v[208:211], a[8:11]\n"
    "v_mfma_f32_16x16x32_bf16 a[8:11], a[252:255], v[236:239], a[8:11]\n"
    "v_mfma_f32_16x16x32_bf16 a[12:15], a[240:243], v[156:159], a[12:15]\n"
    "s_waitcnt vmcnt(0) lgkmcnt(0)\n"
    "s_barrier\n"
    "v_mfma_f32_16x16x32_bf16 a[12:15], a[244:247], v[184:187], a[12:15]\n"
    "buffer_store_dword v4, v10, s[72:75], 0 offen sc1\n"
    "v_mfma_f32_16x16x32_bf16 a[12:15], a[248:251], v[212:215], a[12:15]\n"
    "v_mfma_f32_16x16x32_bf16 a[12:15], a[252:255], v[240:243], a[12:15]\n"
    "v_mfma_f32_16x16x32_bf16 a[16:19], a[240:243], v[160:163], a[16:19]\n"
    "v_mfma_f32_16x16x32_bf16 a[16:19], a[244:247], v[188:191], a[16:19]\n"
    "v_mfma_f32_16x16x32_bf16 a[16:19], a[248:251], v[216:219], a[16:19]\n"
    "v_mfma_f32_16x16x32_bf16 a[16:19], a[252:255], v[244:247], a[16:19]\n"
    "v_mfma_f32_16x16x32_bf16 a[20:23], a[240:243], v[164:167], a[20:23]\n"
    "v_mfma_f32_16x16x32_bf16 a[20:23], a[244:247], v[192:195], a[20:23]\n"
    "v_mfma_f32_16x16x32_bf16 a[20:23], a[248:251], v[220:223], a[20:23]\n"
    "v_mfma_f32_16x16x32_bf16 a[20:23], a[252:255], v[248:251], a[20:23]\n"
    "v_mfma_f32_16x16x32_bf16 a[24:27], a[240:243], v[168:171], a[24:27]\n"
    "v_mfma_f32_16x16x32_bf16 a[24:27], a[244:247], v[196:199], a[24:27]\n"
    "v_mfma_f32_16x16x32_bf16 a[24:27], a[248:251], v[224:227], a[24:27]\n"
    "v_mfma_f32_16x16x32_bf16 a[24:27], a[252:255], v[252:255], a[24:27]\n"
    "s_mul_i32 s34, s19, 53248\n"
    "ds_read_b128 v[144:147], v13 offset:0\n"
    "ds_read_b128 v[148:151], v13 offset:1024\n"
    "ds_read_b128 v[152:155], v13 offset:2048\n"
    "ds_read_b128 v[156:159], v13 offset:3072\n"
    "ds_read_b128 v[160:163], v13 offset:4096\n"
    "ds_read_b128 v[164:167], v13 offset:5120\n"
    "ds_read_b128 v[168:171], v13 offset:6144\n"
    "v_accvgpr_read_b32 v116, a0\n"
    "v_accvgpr_read_b32 v117, a1\n"
    "v_accvgpr_read_b32 v118, a2\n"
    "v_accvgpr_read_b32 v119, a3\n"
    "v_accvgpr_read_b32 v120, a4\n"
    "v_accvgpr_read_b32 v121, a5\n"
    "v_accvgpr_read_b32 v122, a6\n"
    "v_accvgpr_read_b32 v123, a7\n"
    "v_accvgpr_read_b32 v124, a8\n"
    "v_accvgpr_read_b32 v125, a9\n"
    "v_accvgpr_read_b32 v126, a10\n"
    "v_accvgpr_read_b32 v127, a11\n"
    "v_accvgpr_read_b32 v128, a12\n"
    "v_accvgpr_read_b32 v129, a13\n"
    "v_accvgpr_read_b32 v130, a14\n"
    "v_accvgpr_read_b32 v131, a15\n"
    "v_accvgpr_read_b32 v132, a16\n"
    "v_accvgpr_read_b32 v133, a17\n"
    "v_accvgpr_read_b32 v134, a18\n"
    "v_accvgpr_read_b32 v135, a19\n"
    "v_accvgpr_read_b32 v136, a20\n"
    "v_accvgpr_read_b32 v137, a21\n"
    "v_accvgpr_read_b32 v138, a22\n"
    "v_accvgpr_read_b32 v139, a23\n"
    "v_accvgpr_read_b32 v140, a24\n"
    "v_accvgpr_read_b32 v141, a25\n"
    "v_accvgpr_read_b32 v142, a26\n"
    "v_accvgpr_read_b32 v143, a27\n"
    "s_waitcnt lgkmcnt(6)\n"
    "v_pk_add_f32 v[144:145], v[144:145], v[116:117]\n"
    "v_pk_add_f32 v[146:147], v[146:147], v[118:119]\n"
    "s_waitcnt lgkmcnt(5)\n"
    "v_pk_add_f32 v[148:149], v[148:149], v[120:121]\n"
    "v_pk_add_f32 v[150:151], v[150:151], v[122:123]\n"
    "s_waitcnt lgkmcnt(4)\n"
    "v_pk_add_f32 v[152:153], v[152:153], v[124:125]\n"
    "v_pk_add_f32 v[154:155], v[154:155], v[126:127]\n"
    "s_waitcnt lgkmcnt(3)\n"
    "v_pk_add_f32 v[156:157], v[156:157], v[128:129]\n"
    "v_pk_add_f32 v[158:159], v[158:159], v[130:131]\n"
    "s_waitcnt lgkmcnt(2)\n"
    "v_pk_add_f32 v[160:161], v[160:161], v[132:133]\n"
    "v_pk_add_f32 v[162:163], v[162:163], v[134:135]\n"
    "s_waitcnt lgkmcnt(1)\n"
    "v_pk_add_f32 v[164:165], v[164:165], v[136:137]\n"
    "v_pk_add_f32 v[166:167], v[166:167], v[138:139]\n"
    "s_waitcnt lgkmcnt(0)\n"
    "v_pk_add_f32 v[168:169], v[168:169], v[140:141]\n"
    "v_pk_add_f32 v[170:171], v[170:171], v[142:143]\n"
    "buffer_load_dword v6, v30, s[68:71], 0 offen sc1\n"
    "s_waitcnt vmcnt(0)\n"
    "v_cmp_ne_u32 vcc, v6, v5\n"
    "s_nop 4\n"
    "s_and_b32 s67, vcc_lo, 0xe00\n"
    "s_cbranch_scc0 L_okC_%=\ns_mov_b32 s63, 0\nL_inC_%=:\ns_sleep 1\nbuffer_load_dword v28, v30, s[68:71], 0 offen sc1\ns_add_u32 s63, s63, 1\ns_cmp_gt_u32 s63, 40\ns_cbranch_scc1 L_retryC_%=\ns_waitcnt vmcnt(0)\nv_cmp_ne_u32 vcc, v28, v5\ns_nop 4\ns_and_b32 s67, vcc_lo, 0xe00\ns_cbranch_scc1 L_inC_%=\nL_okC_%=:\n"
    "s_add_u32 s65, s34, 0\n"
    "buffer_load_dwordx4 v[192:195], v27, s[28:31], s65 offen sc1\n"
    "s_add_u32 s65, s34, 4096\n"
    "buffer_load_dwordx4 v[208:211], v27, s[28:31], s65 offen sc1\n"
    "s_add_u32 s65, s34, 8192\n"
    "buffer_load_dwordx4 v[224:227], v27, s[28:31], s65 offen sc1\n"
    "s_add_u32 s65, s34, 0\n"
    "buffer_load_dwordx4 v[196:199], v27, s[28:31], s65 offen offset:1024 sc1\n"
    "s_add_u32 s65, s34, 4096\n"
    "buffer_load_dwordx4 v[212:215], v27, s[28:31], s65 offen offset:1024 sc1\n"
    "s_add_u32 s65, s34, 8192\n"
    "buffer_load_dwordx4 v[228:231], v27, s[28:31], s65 offen offset:1024 sc1\n"
    "s_add_u32 s65, s34, 0\n"
    "buffer_load_dwordx4 v[200:203], v27, s[28:31], s65 offen offset:2048 sc1\n"
    "s_add_u32 s65, s34, 4096\n"
    "buffer_load_dwordx4 v[216:219], v27, s[28:31], s65 offen offset:2048 sc1\n"
    "s_add_u32 s65, s34, 8192\n"
    "buffer_load_dwordx4 v[232:235], v27, s[28:31], s65 offen offset:2048 sc1\n"
    "s_add_u32 s65, s34, 0\n"
    "buffer_load_dwordx2 v[204:205], v22, s[28:31], s65 offen offset:3072 sc1\n"
    "s_add_u32 s65, s34, 4096\n"
    "buffer_load_dwordx2 v[220:221], v22, s[28:31], s65 offen offset:3072 sc1\n"
    "s_add_u32 s65, s34, 8192\n"
    "buffer_load_dwordx2 v[236:237], v22, s[28:31], s65 offen offset:3072 sc1\n"
    "buffer_load_dword v4, v30, s[68:71], 0 offen offset:128 sc1\n"
    "s_waitcnt vmcnt(12)\n"
    "v_lshlrev_b32 v120, 16, v192\n"
    "v_and_b32 v121, s59, v192\n"
    "v_lshlrev_b32 v122, 16, v193\n"
    "v_and_b32 v123, s59, v193\n"
    "v_pk_add_f32 v[144:145], v[144:145], v[120:121]\n"
    "v_pk_add_f32 v[146:147], v[146:147], v[122:123]\n"
    "s_waitcnt vmcnt(11)\n"
    "v_lshlrev_b32 v124, 16, v208\n"
    "v_and_b32 v125, s59, v208\n"
    "v_lshlrev_b32 v126, 16, v209\n"
    "v_and_b32 v127, s59, v209\n"
    "v_pk_add_f32 v[144:145], v[144:145], v[124:125]\n"
    "v_pk_add_f32 v[146:147], v[146:147], v[126:127]\n"
    "s_waitcnt vmcnt(10)\n"
    "v_lshlrev_b32 v128, 16, v224\n"
    "v_and_b32 v129, s59, v224\n"
    "v_lshlrev_b32 v130, 16, v225\n"
    "v_and_b32 v131, s59, v225\n"
    "v_pk_add_f32 v[144:145], v[144:145], v[128:129]\n"
    "v_pk_add_f32 v[146:147], v[146:147], v[130:131]\n"
    "v_cvt_pk_bf16_f32 v144, v144, v145\n"
    "v_cvt_pk_bf16_f32 v145, v146, v147\n"
    "ds_write_b64 v21, v[144:145] offset:8192\n"
    "v_lshlrev_b32 v132, 16, v194\n"
    "v_and_b32 v133, s59, v194\n"
    "v_lshlrev_b32 v134, 16, v195\n"
    "v_and_b32 v135, s59, v195\n"
    "v_pk_add_f32 v[148:149], v[148:149], v[132:133]\n"
    "v_pk_add_f32 v[150:151], v[150:151], v[134:135]\n"
    "v_lshlrev_b32 v136, 16, v210\n"
    "v_and_b32 v137, s59, v210\n"
    "v_lshlrev_b32 v138, 16, v211\n"
    "v_and_b32 v139, s59, v211\n"
    "v_pk_add_f32 v[148:149], v[148:149], v[136:137]\n"
    "v_pk_add_f32 v[150:151], v[150:151], v[138:139]\n"
    "v_lshlrev_b32 v140, 16, v226\n"
    "v_and_b32 v141, s59, v226\n"
    "v_lshlrev_b32 v142, 16, v227\n"
    "v_and_b32 v143, s59, v227\n"
    "v_pk_add_f32 v[148:149], v[148:149], v[140:141]\n"
    "v_pk_add_f32 v[150:151], v[150:151], v[142:143]\n"
    "v_cvt_pk_bf16_f32 v148, v148, v149\n"
    "v_cvt_pk_bf16_f32 v149, v150, v151\n"
    "ds_write_b64 v21, v[148:149] offset:8704\n"
    "ds_read_b128 v[32:35], v12 offset:8192\n"
    "s_waitcnt vmcnt(9)\n"
    "v_lshlrev_b32 v116, 16, v196\n"
    "v_and_b32 v117, s59, v196\n"
    "v_lshlrev_b32 v118, 16, v197\n"
    "v_and_b32 v119, s59, v197\n"
    "v_pk_add_f32 v[152:153], v[152:153], v[116:117]\n"
    "v_pk_add_f32 v[154:155], v[154:155], v[118:119]\n"
    "s_waitcnt vmcnt(8)\n"
    "v_lshlrev_b32 v120, 16, v212\n"
    "v_and_b32 v121, s59, v212\n"
    "v_lshlrev_b32 v122, 16, v213\n"
    "v_and_b32 v123, s59, v213\n"
    "v_pk_add_f32 v[152:153], v[152:153], v[120:121]\n"
    "v_pk_add_f32 v[154:155], v[154:155], v[122:123]\n"
    "s_waitcnt vmcnt(7)\n"
    "v_lshlrev_b32 v124, 16, v228\n"
    "v_and_b32 v125, s59, v228\n"
    "v_lshlrev_b32 v126, 16, v229\n"
    "v_and_b32 v127, s59, v229\n"
    "v_pk_add_f32 v[152:153], v[152:153], v[124:125]\n"
    "v_pk_add_f32 v[154:155], v[154:155], v[126:127]\n"
    "v_cvt_pk_bf16_f32 v152, v152, v153\n"
    "v_cvt_pk_bf16_f32 v153, v154, v155\n"
    "ds_write_b64 v21, v[152:153] offset:9216\n"
    "s_mov_b32 s64, 0\n"
    "s_waitcnt lgkmcnt(1)\n"
    "buffer_store_dwordx4 v[32:35], v11, s[24:27], s64 offen\n"
    "v_lshlrev_b32 v128, 16, v198\n"
    "v_and_b32 v129, s59, v198\n"
    "v_lshlrev_b32 v130, 16, v199\n"
    "v_and_b32 v131, s59, v199\n"
    "v_pk_add_f32 v[156:157], v[156:157], v[128:129]\n"
    "v_pk_add_f32 v[158:159], v[158:159], v[130:131]\n"
    "v_lshlrev_b32 v132, 16, v214\n"
    "v_and_b32 v133, s59, v214\n"
    "v_lshlrev_b32 v134, 16, v215\n"
    "v_and_b32 v135, s59, v215\n"
    "v_pk_add_f32 v[156:157], v[156:157], v[132:133]\n"
    "v_pk_add_f32 v[158:159], v[158:159], v[134:135]\n"
    "v_lshlrev_b32 v136, 16, v230\n"
    "v_and_b32 v137, s59, v230\n"
    "v_lshlrev_b32 v138, 16, v231\n"
    "v_and_b32 v139, s59, v231\n"
    "v_pk_add_f32 v[156:157], v[156:157], v[136:137]\n"
    "v_pk_add_f32 v[158:159], v[158:159], v[138:139]\n"
    "v_cvt_pk_bf16_f32 v156, v156, v157\n"
    "v_cvt_pk_bf16_f32 v157, v158, v159\n"
    "ds_write_b64 v21, v[156:157] offset:9728\n"
    "ds_read_b128 v[36:39], v12 offset:9216\n"
    "s_waitcnt vmcnt(7)\n"
    "v_lshlrev_b32 v140, 16, v200\n"
    "v_and_b32 v141, s59, v200\n"
    "v_lshlrev_b32 v142, 16, v201\n"
    "v_and_b32 v143, s59, v201\n"
    "v_pk_add_f32 v[160:161], v[160:161], v[140:141]\n"
    "v_pk_add_f32 v[162:163], v[162:163], v[142:143]\n"
    "s_waitcnt vmcnt(6)\n"
    "v_lshlrev_b32 v116, 16, v216\n"
    "v_and_b32 v117, s59, v216\n"
    "v_lshlrev_b32 v118, 16, v217\n"
    "v_and_b32 v119, s59, v217\n"
    "v_pk_add_f32 v[160:161], v[160:161], v[116:117]\n"
    "v_pk_add_f32 v[162:163], v[162:163], v[118:119]\n"
    "s_waitcnt vmcnt(5)\n"
    "v_lshlrev_b32 v120, 16, v232\n"
    "v_and_b32 v121, s59, v232\n"
    "v_lshlrev_b32 v122, 16, v233\n"
    "v_and_b32 v123, s59, v233\n"
    "v_pk_add_f32 v[160:161], v[160:161], v[120:121]\n"
    "v_pk_add_f32 v[162:163], v[162:163], v[122:123]\n"
    "v_cvt_pk_bf16_f32 v160, v160, v161\n"
    "v_cvt_pk_bf16_f32 v161, v162, v163\n"
    "ds_write_b64 v21, v[160:161] offset:10240\n"
    "s_mov_b32 s64, 65536\n"
    "s_waitcnt lgkmcnt(1)\n"
    "buffer_store_dwordx4 v[36:39], v11, s[24:27], s64 offen\n"
    "v_lshlrev_b32 v124, 16, v202\n"
    "v_and_b32 v125, s59, v202\n"
    "v_lshlrev_b32 v126, 16, v203\n"
    "v_and_b32 v127, s59, v203\n"
    "v_pk_add_f32 v[164:165], v[164:165], v[124:125]\n"
    "v_pk_add_f32 v[166:167], v[166:167], v[126:127]\n"
    "v_lshlrev_b32 v128, 16, v218\n"
    "v_and_b32 v129, s59, v218\n"
    "v_lshlrev_b32 v130, 16, v219\n"
    "v_and_b32 v131, s59, v219\n"
    "v_pk_add_f32 v[164:165], v[164:165], v[128:129]\n"
    "v_pk_add_f32 v[166:167], v[166:167], v[130:131]\n"
    "v_lshlrev_b32 v132, 16, v234\n"
    "v_and_b32 v133, s59, v234\n"
    "v_lshlrev_b32 v134, 16, v235\n"
    "v_and_b32 v135, s59, v235\n"
    "v_pk_add_f32 v[164:165], v[164:165], v[132:133]\n"
    "v_pk_add_f32 v[166:167], v[166:167], v[134:135]\n"
    "v_cvt_pk_bf16_f32 v164, v164, v165\n"
    "v_cvt_pk_bf16_f32 v165, v166, v167\n"
    "ds_write_b64 v21, v[164:165] offset:10752\n"
    "ds_read_b128 v[40:43], v12 offset:10240\n"
    "s_waitcnt vmcnt(5)\n"
    "v_lshlrev_b32 v136, 16, v204\n"
    "v_and_b32 v137, s59, v204\n"
    "v_lshlrev_b32 v138, 16, v205\n"
    "v_and_b32 v139, s59, v205\n"
    "v_pk_add_f32 v[168:169], v[168:169], v[136:137]\n"
    "v_pk_add_f32 v[170:171], v[170:171], v[138:139]\n"
    "s_waitcnt vmcnt(4)\n"
    "v_lshlrev_b32 v140, 16, v220\n"
    "v_and_b32 v141, s59, v220\n"
    "v_lshlrev_b32 v142, 16, v221\n"
    "v_and_b32 v143, s59, v221\n"
    "v_pk_add_f32 v[168:169], v[168:169], v[140:141]\n"
    "v_pk_add_f32 v[170:171], v[170:171], v[142:143]\n"
    "s_waitcnt vmcnt(3)\n"
    "v_lshlrev_b32 v116, 16, v236\n"
    "v_and_b32 v117, s59, v236\n"
    "v_lshlrev_b32 v118, 16, v237\n"
    "v_and_b32 v119, s59, v237\n"
    "v_pk_add_f32 v[168:169], v[168:169], v[116:117]\n"
    "v_pk_add_f32 v[170:171], v[170:171], v[118:119]\n"
    "v_cvt_pk_bf16_f32 v168, v168, v169\n"
    "v_cvt_pk_bf16_f32 v169, v170, v171\n"
    "ds_write_b64 v21, v[168:169] offset:11264\n"
    "ds_read_b128 v[44:47], v12 offset:11264\n"
    "s_mov_b32 s64, 131072\n"
    "s_waitcnt lgkmcnt(2)\n"
    "buffer_store_dwordx4 v[40:43], v11, s[24:27], s64 offen\n"
    "s_mov_b32 s64, 196608\n"
    "s_waitcnt lgkmcnt(0)\n"
    "buffer_store_dwordx4 v[44:47], v11, s[24:27], s64 offen\n"
    "s_waitcnt vmcnt(4)\n"
    "buffer_store_dword v4, v10, s[72:75], 0 offen sc1\n"
    "s_branch L_end_%=\n"
    "L_retryA_%=:\n"
    "s_mov_b32 s63, 0\n"
    "L_againA_%=:\n"
    "buffer_load_dword v28, v29, s[68:71], 0 offen sc1\n"
    "s_waitcnt vmcnt(0)\n"
    "v_cmp_ne_u32 vcc, v28, v5\n"
    "s_nop 4\n"
    "s_and_b32 s67, vcc_lo, 0xe\n"
    "s_cbranch_scc0 L_okA_%=\n"
    "s_sleep 4\n"
    "s_add_u32 s63, s63, 1\n"
    "s_and_b32 s67, s63, 63\n"
    "s_cbranch_scc1 L_againA_%=\n"
    "buffer_load_dword v28, v2, s[68:71], 0 offen offset:128 sc1\n"
    "s_waitcnt vmcnt(0)\n"
    "buffer_store_dword v28, v8, s[72:75], 0 offen sc1\n"
    "s_waitcnt vmcnt(0)\n"
    "s_cmp_lt_u32 s63, 100000\n"
    "s_cbranch_scc1 L_againA_%=\n"
    "s_branch L_okA_%=\n"
    "L_retryB_%=:\n"
    "s_mov_b32 s63, 0\n"
    "L_againB_%=:\n"
    "buffer_load_dword v28, v31, s[68:71], 0 offen sc1\n"
    "s_waitcnt vmcnt(0)\n"
    "v_cmp_ne_u32 vcc, v28, v5\n"
    "s_nop 4\n"
    "s_and_b32 s67, vcc_lo, 0xe0\n"
    "s_cbranch_scc0 L_okB_%=\n"
    "s_sleep 4\n"
    "s_add_u32 s63, s63, 1\n"
    "s_and_b32 s67, s63, 63\n"
    "s_cbranch_scc1 L_againB_%=\n"
    "buffer_load_dword v28, v2, s[68:71], 0 offen offset:128 sc1\n"
    "s_waitcnt vmcnt(0)\n"
    "buffer_store_dword v28, v9, s[72:75], 0 offen sc1\n"
    "s_waitcnt vmcnt(0)\n"
    "s_cmp_lt_u32 s63, 100000\n"
    "s_cbranch_scc1 L_againB_%=\n"
    "s_branch L_okB_%=\n"
    "L_retryC_%=:\n"
    "s_mov_b32 s63, 0\n"
    "L_againC_%=:\n"
    "buffer_load_dword v28, v30, s[68:71], 0 offen sc1\n"
    "s_waitcnt vmcnt(0)\n"
    "v_cmp_ne_u32 vcc, v28, v5\n"
    "s_nop 4\n"
    "s_and_b32 s67, vcc_lo, 0xe00\n"
    "s_cbranch_scc0 L_okC_%=\n"
    "s_sleep 4\n"
    "s_add_u32 s63, s63, 1\n"
    "s_and_b32 s67, s63, 63\n"
    "s_cbranch_scc1 L_againC_%=\n"
    "buffer_load_dword v28, v2, s[68:71], 0 offen offset:128 sc1\n"
    "s_waitcnt vmcnt(0)\n"
    "buffer_store_dword v28, v3, s[72:75], 0 offen sc1\n"
    "s_waitcnt vmcnt(0)\n"
    "s_cmp_lt_u32 s63, 100000\n"
    "s_cbranch_scc1 L_againC_%=\n"
    "s_branch L_okC_%=\n"
    "L_ta2_%=:\n"
    "s_sleep 1\n"
    "s_sub_i32 s25, s25, 4\n"
    "s_cmp_gt_i32 s25, 4\n"
    "s_cbranch_scc1 L_ta2_%=\n"
    "s_branch L_tb2_%=\n"
    "L_ta3_%=:\n"
    "s_sleep 1\n"
    "s_sub_i32 s25, s25, 4\n"
    "s_cmp_gt_i32 s25, 4\n"
    "s_cbranch_scc1 L_ta3_%=\n"
    "s_branch L_tb3_%=\n"
    "L_ta4_%=:\n"
    "s_sleep 1\n"
    "s_sub_i32 s25, s25, 4\n"
    "s_cmp_gt_i32 s25, 4\n"
    "s_cbranch_scc1 L_ta4_%=\n"
    "s_branch L_tb4_%=\n"
    "L_ta5_%=:\n"
    "s_sleep 1\n"
    "s_sub_i32 s25, s25, 4\n"
    "s_cmp_gt_i32 s25, 4\n"
    "s_cbranch_scc1 L_ta5_%=\n"
    "s_branch L_tb5_%=\n"
    "L_ta6_%=:\n"
    "s_sleep 1\n"
    "s_sub_i32 s25, s25, 4\n"
    "s_cmp_gt_i32 s25, 4\n"
    "s_cbranch_scc1 L_ta6_%=\n"
    "s_branch L_tb6_%=\n"
    "L_ta7_%=:\n"
    "s_sleep 1\n"
    "s_sub_i32 s25, s25, 4\n"
    "s_cmp_gt_i32 s25, 4\n"
    "s_cbranch_scc1 L_ta7_%=\n"
    "s_branch L_tb7_%=\n"
    "L_ta8_%=:\n"
    "s_sleep 1\n"
    "s_sub_i32 s25, s25, 4\n"
    "s_cmp_gt_i32 s25, 4\n"
    "s_cbranch_scc1 L_ta8_%=\n"
    "s_branch L_tb8_%=\n"
    "L_ta9_%=:\n"
    "s_sleep 1\n"
    "s_sub_i32 s25, s25, 4\n"
    "s_cmp_gt_i32 s25, 4\n"
    "s_cbranch_scc1 L_ta9_%=\n"
    "s_branch L_tb9_%=\n"
    "L_ta10_%=:\n"
    "s_sleep 1\n"
    "s_sub_i32 s25, s25, 4\n"
    "s_cmp_gt_i32 s25, 4\n"
    "s_cbranch_scc1 L_ta10_%=\n"
    "s_branch L_tb10_%=\n"
    "L_ta11_%=:\n"
    "s_sleep 1\n"
    "s_sub_i32 s25, s25, 4\n"
    "s_cmp_gt_i32 s25, 4\n"
    "s_cbranch_scc1 L_ta11_%=\n"
    "s_branch L_tb11_%=\n"
    "L_ta12_%=:\n"
    "s_sleep 1\n"
    "s_sub_i32 s25, s25, 4\n"
    "s_cmp_gt_i32 s25, 4\n"
    "s_cbranch_scc1 L_ta12_%=\n"
    "s_branch L_tb12_%=\n"
    "L_ta13_%=:\n"
    "s_sleep 1\n"
    "s_sub_i32 s25, s25, 4\n"
    "s_cmp_gt_i32 s25, 4\n"
    "s_cbranch_scc1 L_ta13_%=\n"
    "s_branch L_tb13_%=\n"
    "L_ta14_%=:\n"
    "s_sleep 1\n"
    "s_sub_i32 s25, s25, 4\n"
    "s_cmp_gt_i32 s25, 4\n"
    "s_cbranch_scc1 L_ta14_%=\n"
    "s_branch L_tb14_%=\n"
    "L_ta15_%=:\n"
    "s_sleep 1\n"
    "s_sub_i32 s25, s25, 4\n"
    "s_cmp_gt_i32 s25, 4\n"
    "s_cbranch_scc1 L_ta15_%=\n"
    "s_branch L_tb15_%=\n"
    "L_slow_%=:\n"
    "s_waitcnt vmcnt(0)\n"
    "s_nop 7\n"
    "v_bfe_u32 v184, v1, 7, 2\n"
    "v_bfe_u32 v185, v1, 9, 2\n"
    "v_lshrrev_b32 v186, 11, v1\n"
    "v_lshlrev_b32 v187, 8, v185\n"
    "v_lshl_add_u32 v187, v186, 6, v187\n"
    "v_xor_b32 v188, 0, v184\n"
    "v_lshl_add_u32 v200, v188, 4, v187\n"
    "v_xor_b32 v188, 2, v184\n"
    "v_lshl_add_u32 v201, v188, 4, v187\n"
    "v_xor_b32 v188, 3, v184\n"
    "v_lshl_add_u32 v202, v188, 4, v187\n"
    "v_xor_b32 v188, 1, v184\n"
    "v_lshl_add_u32 v203, v188, 4, v187\n"
    "v_xor_b32 v188, 0, v184\n"
    "v_lshl_add_u32 v204, v188, 4, v187\n"
    "v_xor_b32 v188, 2, v184\n"
    "v_lshl_add_u32 v205, v188, 4, v187\n"
    "s_lshl_b32 s34, s60, 13\n"
    "s_add_u32 s34, s34, 98304\n"
    "s_lshl_b32 s64, s62, 14\n"
    "s_sub_u32 s64, s34, s64\n"
    "v_add_u32 v11, s64, v11\n"
    "s_sub_u32 s64, s64, 8192\n"
    "v_add_u32 v206, s64, v14\n"
    "v_add_u32 v207, s64, v17\n"
    "v_mov_b32 v20, 0xff800000\n"
    "v_mov_b32 v21, 0xff800000\n"
    "v_mov_b32 v22, 0xff800000\n"
    "v_mov_b32 v23, 0xff800000\n"
    "v_mov_b32 v24, 0xff800000\n"
    "v_mov_b32 v25, 0xff800000\n"
    "v_mov_b32 v26, 0xff800000\n"
    "s_mov_b32 s56, 0\n"
    "L_max_%=:\n"
    "s_add_u32 m0, s34, 0\n"
    "s_add_u32 s58, s56, 0\n"
    "buffer_load_dwordx4 v200, s[44:47], s58 offen lds\n"
    "buffer_load_dwordx4 v201, s[44:47], s58 offen offset:1024 lds\n"
    "buffer_load_dwordx4 v202, s[44:47], s58 offen offset:2048 lds\n"
    "buffer_load_dwordx4 v203, s[44:47], s58 offen offset:3072 lds\n"
    "s_add_u32 m0, s34, 4096\n"
    "s_add_u32 s58, s56, 4096\n"
    "buffer_load_dwordx4 v200, s[44:47], s58 offen lds\n"
    "buffer_load_dwordx4 v201, s[44:47], s58 offen offset:1024 lds\n"
    "buffer_load_dwordx4 v202, s[44:47], s58 offen offset:2048 lds\n"
    "buffer_load_dwordx4 v203, s[44:47], s58 offen offset:3072 lds\n"
    "s_waitcnt vmcnt(0)\n"
    "ds_read_b128 a[240:243], v11 offset:0\n"
    "ds_read_b128 a[244:247], v11 offset:256\n"
    "ds_read_b128 a[248:251], v11 offset:512\n"
    "ds_read_b128 a[252:255], v11 offset:768\n"
    "ds_read_b128 a[224:227], v11 offset:4096\n"
    "ds_read_b128 a[228:231], v11 offset:4352\n"
    "ds_read_b128 a[232:235], v11 offset:4608\n"
    "ds_read_b128 a[236:239], v11 offset:4864\n"
    "s_waitcnt lgkmcnt(7)\n"
    "v_mfma_f32_16x16x32_bf16 v[184:187], a[240:243], v[32:35], 0\n"
    "s_waitcnt lgkmcnt(6)\n"
    "v_mfma_f32_16x16x32_bf16 v[184:187], a[244:247], v[36:39], v[184:187]\n"
    "s_waitcnt lgkmcnt(5)\n"
    "v_mfma_f32_16x16x32_bf16 v[184:187], a[248:251], v[40:43], v[184:187]\n"
    "s_waitcnt lgkmcnt(4)\n"
    "v_mfma_f32_16x16x32_bf16 v[184:187], a[252:255], v[44:47], v[184:187]\n"
    "s_nop 7\n"
    "v_max3_f32 v20, v20, v184, v185\n"
    "v_max3_f32 v20, v20, v186, v187\n"
    "v_mfma_f32_16x16x32_bf16 v[188:191], a[240:243], v[48:51], 0\n"
    "v_mfma_f32_16x16x32_bf16 v[188:191], a[244:247], v[52:55], v[188:191]\n"
    "v_mfma_f32_16x16x32_bf16 v[188:191], a[248:251], v[56:59], v[188:191]\n"
    "v_mfma_f32_16x16x32_bf16 v[188:191], a[252:255], v[60:63], v[188:191]\n"
    "s_nop 7\n"
    "v_max3_f32 v21, v21, v188, v189\n"
    "v_max3_f32 v21, v21, v190, v191\n"
    "v_mfma_f32_16x16x32_bf16 v[192:195], a[240:243], v[64:67], 0\n"
    "v_mfma_f32_16x16x32_bf16 v[192:195], a[244:247], v[68:71], v[192:195]\n"
    "v_mfma_f32_16x16x32_bf16 v[192:195], a[248:251], v[72:75], v[192:195]\n"
    "v_mfma_f32_16x16x32_bf16 v[192:195], a[252:255], v[76:79], v[192:195]\n"
    "s_nop 7\n"
    "v_max3_f32 v22, v22, v192, v193\n"
    "v_max3_f32 v22, v22, v194, v195\n"
    "v_mfma_f32_16x16x32_bf16 v[196:199], a[240:243], v[80:83], 0\n"
    "v_mfma_f32_16x16x32_bf16 v[196:199], a[244:247], v[84:87], v[196:199]\n"
    "v_mfma_f32_16x16x32_bf16 v[196:199], a[248:251], v[88:91], v[196:199]\n"
    "v_mfma_f32_16x16x32_bf16 v[196:199], a[252:255], v[92:95], v[196:199]\n"
    "s_nop 7\n"
    "v_max3_f32 v23, v23, v196, v197\n"
    "v_max3_f32 v23, v23, v198, v199\n"
    "v_mfma_f32_16x16x32_bf16 v[184:187], a[240:243], v[96:99], 0\n"
    "v_mfma_f32_16x16x32_bf16 v[184:187], a[244:247], v[100:103], v[184:187]\n"
    "v_mfma_f32_16x16x32_bf16 v[184:187], a[248:251], v[104:107], v[184:187]\n"
    "v_mfma_f32_16x16x32_bf16 v[184:187], a[252:255], v[108:111], v[184:187]\n"
    "s_nop 7\n"
    "v_max3_f32 v24, v24, v184, v185\n"
    "v_max3_f32 v24, v24, v186, v187\n"
    "v_mfma_f32_16x16x32_bf16 v[188:191], a[240:243], v[112:115], 0\n"
    "v_mfma_f32_16x16x32_bf16 v[188:191], a[244:247], v[116:119], v[188:191]\n"
    "v_mfma_f32_16x16x32_bf16 v[188:191], a[248:251], v[120:123], v[188:191]\n"
    "v_mfma_f32_16x16x32_bf16 v[188:191], a[252:255], v[124:127], v[188:191]\n"
    "s_nop 7\n"
    "v_max3_f32 v25, v25, v188, v189\n"
    "v_max3_f32 v25, v25, v190, v191\n"
    "v_mfma_f32_16x16x32_bf16 v[192:195], a[240:243], v[128:131], 0\n"
    "v_mfma_f32_16x16x32_bf16 v[192:195], a[244:247], v[132:135], v[192:195]\n"
    "v_mfma_f32_16x16x32_bf16 v[192:195], a[248:251], v[136:139], v[192:195]\n"
    "v_mfma_f32_16x16x32_bf16 v[192:195], a[252:255], v[140:143], v[192:195]\n"
    "s_nop 7\n"
    "v_max3_f32 v26, v26, v192, v193\n"
    "v_max3_f32 v26, v26, v194, v195\n"
    "s_waitcnt lgkmcnt(3)\n"
    "v_mfma_f32_16x16x32_bf16 v[196:199], a[224:227], v[32:35], 0\n"
    "s_waitcnt lgkmcnt(2)\n"
    "v_mfma_f32_16x16x32_bf16 v[196:199], a[228:231], v[36:39], v[196:199]\n"
    "s_waitcnt lgkmcnt(1)\n"
    "v_mfma_f32_16x16x32_bf16 v[196:199], a[232:235], v[40:43], v[196:199]\n"
    "s_waitcnt lgkmcnt(0)\n"
    "v_mfma_f32_16x16x32_bf16 v[196:199], a[236:239], v[44:47], v[196:199]\n"
    "s_nop 7\n"
    "v_max3_f32 v20, v20, v196, v197\n"
    "v_max3_f32 v20, v20, v198, v199\n"
    "v_mfma_f32_16x16x32_bf16 v[184:187], a[224:227], v[48:51], 0\n"
    "v_mfma_f32_16x16x32_bf16 v[184:187], a[228:231], v[52:55], v[184:187]\n"
    "v_mfma_f32_16x16x32_bf16 v[184:187], a[232:235], v[56:59], v[184:187]\n"
    "v_mfma_f32_16x16x32_bf16 v[184:187], a[236:239], v[60:63], v[184:187]\n"
    "s_nop 7\n"
    "v_max3_f32 v21, v21, v184, v185\n"
    "v_max3_f32 v21, v21, v186, v187\n"
    "v_mfma_f32_16x16x32_bf16 v[188:191], a[224:227], v[64:67], 0\n"
    "v_mfma_f32_16x16x32_bf16 v[188:191], a[228:231], v[68:71], v[188:191]\n"
    "v_mfma_f32_16x16x32_bf16 v[188:191], a[232:235], v[72:75], v[188:191]\n"
    "v_mfma_f32_16x16x32_bf16 v[188:191], a[236:239], v[76:79], v[188:191]\n"
    "s_nop 7\n"
    "v_max3_f32 v22, v22, v188, v189\n"
    "v_max3_f32 v22, v22, v190, v191\n"
    "v_mfma_f32_16x16x32_bf16 v[192:195], a[224:227], v[80:83], 0\n"
    "v_mfma_f32_16x16x32_bf16 v[192:195], a[228:231], v[84:87], v[192:195]\n"
    "v_mfma_f32_16x16x32_bf16 v[192:195], a[232:235], v[88:91], v[192:195]\n"
    "v_mfma_f32_16x16x32_bf16 v[192:195], a[236:239], v[92:95], v[192:195]\n"
    "s_nop 7\n"
    "v_max3_f32 v23, v23, v192, v193\n"
    "v_max3_f32 v23, v23, v194, v195\n"
    "v_mfma_f32_16x16x32_bf16 v[196:199], a[224:227], v[96:99], 0\n"
    "v_mfma_f32_16x16x32_bf16 v[196:199], a[228:231], v[100:103], v[196:199]\n"
    "v_mfma_f32_16x16x32_bf16 v[196:199], a[232:235], v[104:107], v[196:199]\n"
    "v_mfma_f32_16x16x32_bf16 v[196:199], a[236:239], v[108:111], v[196:199]\n"
    "s_nop 7\n"
    "v_max3_f32 v24, v24, v196, v197\n"
    "v_max3_f32 v24, v24, v198, v199\n"
    "v_mfma_f32_16x16x32_bf16 v[184:187], a[224:227], v[112:115], 0\n"
    "v_mfma_f32_16x16x32_bf16 v[184:187], a[228:231], v[116:119], v[184:187]\n"
    "v_mfma_f32_16x16x32_bf16 v[184:187], a[232:235], v[120:123], v[184:187]\n"
    "v_mfma_f32_16x16x32_bf16 v[184:187], a[236:239], v[124:127], v[184:187]\n"
    "s_nop 7\n"
    "v_max3_f32 v25, v25, v184, v185\n"
    "v_max3_f32 v25, v25, v186, v187\n"
    "v_mfma_f32_16x16x32_bf16 v[188:191], a[224:227], v[128:131], 0\n"
    "v_mfma_f32_16x16x32_bf16 v[188:191], a[228:231], v[132:135], v[188:191]\n"
    "v_mfma_f32_16x16x32_bf16 v[188:191], a[232:235], v[136:139], v[188:191]\n"
    "v_mfma_f32_16x16x32_bf16 v[188:191], a[236:239], v[140:143], v[188:191]\n"
    "s_nop 7\n"
    "v_max3_f32 v26, v26, v188, v189\n"
    "v_max3_f32 v26, v26, v190, v191\n"
    "s_add_u32 s56, s56, 8192\n"
    "s_cmp_lt_u32 s56, 131072\n"
    "s_cbranch_scc1 L_max_%=\n"
    "v_mov_b32 v184, v20\n"
    "s_nop 1\n"
    "v_permlane32_swap_b32 v184, v20\n"
    "v_max_f32 v20, v20, v184\n"
    "v_mov_b32 v184, v20\n"
    "s_nop 1\n"
    "v_permlane16_swap_b32 v184, v20\n"
    "v_max_f32 v20, v20, v184\n"
    "v_mov_b32 v156, v20\n"
    "v_mov_b32 v20, 0\n"
    "v_mov_b32 v184, v21\n"
    "s_nop 1\n"
    "v_permlane32_swap_b32 v184, v21\n"
    "v_max_f32 v21, v21, v184\n"
    "v_mov_b32 v184, v21\n"
    "s_nop 1\n"
    "v_permlane16_swap_b32 v184, v21\n"
    "v_max_f32 v21, v21, v184\n"
    "v_mov_b32 v160, v21\n"
    "v_mov_b32 v21, 0\n"
    "v_mov_b32 v184, v22\n"
    "s_nop 1\n"
    "v_permlane32_swap_b32 v184, v22\n"
    "v_max_f32 v22, v22, v184\n"
    "v_mov_b32 v184, v22\n"
    "s_nop 1\n"
    "v_permlane16_swap_b32 v184, v22\n"
    "v_max_f32 v22, v22, v184\n"
    "v_mov_b32 v164, v22\n"
    "v_mov_b32 v22, 0\n"
    "v_mov_b32 v184, v23\n"
    "s_nop 1\n"
    "v_permlane32_swap_b32 v184, v23\n"
    "v_max_f32 v23, v23, v184\n"
    "v_mov_b32 v184, v23\n"
    "s_nop 1\n"
    "v_permlane16_swap_b32 v184, v23\n"
    "v_max_f32 v23, v23, v184\n"
    "v_mov_b32 v168, v23\n"
    "v_mov_b32 v23, 0\n"
    "v_mov_b32 v184, v24\n"
    "s_nop 1\n"
    "v_permlane32_swap_b32 v184, v24\n"
    "v_max_f32 v24, v24, v184\n"
    "v_mov_b32 v184, v24\n"
    "s_nop 1\n"
    "v_permlane16_swap_b32 v184, v24\n"
    "v_max_f32 v24, v24, v184\n"
    "v_mov_b32 v172, v24\n"
    "v_mov_b32 v24, 0\n"
    "v_mov_b32 v184, v25\n"
    "s_nop 1\n"
    "v_permlane32_swap_b32 v184, v25\n"
    "v_max_f32 v25, v25, v184\n"
    "v_mov_b32 v184, v25\n"
    "s_nop 1\n"
    "v_permlane16_swap_b32 v184, v25\n"
    "v_max_f32 v25, v25, v184\n"
    "v_mov_b32 v176, v25\n"
    "v_mov_b32 v25, 0\n"
    "v_mov_b32 v184, v26\n"
    "s_nop 1\n"
    "v_permlane32_swap_b32 v184, v26\n"
    "v_max_f32 v26, v26, v184\n"
    "v_mov_b32 v184, v26\n"
    "s_nop 1\n"
    "v_permlane16_swap_b32 v184, v26\n"
    "v_max_f32 v26, v26, v184\n"
    "v_mov_b32 v180, v26\n"
    "v_mov_b32 v26, 0\n"
    "v_accvgpr_write_b32 a0, 0\n"
    "v_accvgpr_write_b32 a1, 0\n"
    "v_accvgpr_write_b32 a2, 0\n"
    "v_accvgpr_write_b32 a3, 0\n"
    "v_accvgpr_write_b32 a4, 0\n"
    "v_accvgpr_write_b32 a5, 0\n"
    "v_accvgpr_write_b32 a6, 0\n"
    "v_accvgpr_write_b32 a7, 0\n"
    "v_accvgpr_write_b32 a8, 0\n"
    "v_accvgpr_write_b32 a9, 0\n"
    "v_accvgpr_write_b32 a10, 0\n"
    "v_accvgpr_write_b32 a11, 0\n"
    "v_accvgpr_write_b32 a12, 0\n"
    "v_accvgpr_write_b32 a13, 0\n"
    "v_accvgpr_write_b32 a14, 0\n"
    "v_accvgpr_write_b32 a15, 0\n"
    "v_accvgpr_write_b32 a16, 0\n"
    "v_accvgpr_write_b32 a17, 0\n"
    "v_accvgpr_write_b32 a18, 0\n"
    "v_accvgpr_write_b32 a19, 0\n"
    "v_accvgpr_write_b32 a20, 0\n"
    "v_accvgpr_write_b32 a21, 0\n"
    "v_accvgpr_write_b32 a22, 0\n"
    "v_accvgpr_write_b32 a23, 0\n"
    "v_accvgpr_write_b32 a24, 0\n"
    "v_accvgpr_write_b32 a25, 0\n"
    "v_accvgpr_write_b32 a26, 0\n"
    "v_accvgpr_write_b32 a27, 0\n"
    "v_accvgpr_write_b32 a28, 0\n"
    "v_accvgpr_write_b32 a29, 0\n"
    "v_accvgpr_write_b32 a30, 0\n"
    "v_accvgpr_write_b32 a31, 0\n"
    "v_accvgpr_write_b32 a32, 0\n"
    "v_accvgpr_write_b32 a33, 0\n"
    "v_accvgpr_write_b32 a34, 0\n"
    "v_accvgpr_write_b32 a35, 0\n"
    "v_accvgpr_write_b32 a36, 0\n"
    "v_accvgpr_write_b32 a37, 0\n"
    "v_accvgpr_write_b32 a38, 0\n"
    "v_accvgpr_write_b32 a39, 0\n"
    "v_accvgpr_write_b32 a40, 0\n"
    "v_accvgpr_write_b32 a41, 0\n"
    "v_accvgpr_write_b32 a42, 0\n"
    "v_accvgpr_write_b32 a43, 0\n"
    "v_accvgpr_write_b32 a44, 0\n"
    "v_accvgpr_write_b32 a45, 0\n"
    "v_accvgpr_write_b32 a46, 0\n"
    "v_accvgpr_write_b32 a47, 0\n"
    "v_accvgpr_write_b32 a48, 0\n"
    "v_accvgpr_write_b32 a49, 0\n"
    "v_accvgpr_write_b32 a50, 0\n"
    "v_accvgpr_write_b32 a51, 0\n"
    "v_accvgpr_write_b32 a52, 0\n"
    "v_accvgpr_write_b32 a53, 0\n"
    "v_accvgpr_write_b32 a54, 0\n"
    "v_accvgpr_write_b32 a55, 0\n"
    "v_accvgpr_write_b32 a56, 0\n"
    "v_accvgpr_write_b32 a57, 0\n"
    "v_accvgpr_write_b32 a58, 0\n"
    "v_accvgpr_write_b32 a59, 0\n"
    "v_accvgpr_write_b32 a60, 0\n"
    "v_accvgpr_write_b32 a61, 0\n"
    "v_accvgpr_write_b32 a62, 0\n"
    "v_accvgpr_write_b32 a63, 0\n"
    "v_accvgpr_write_b32 a64, 0\n"
    "v_accvgpr_write_b32 a65, 0\n"
    "v_accvgpr_write_b32 a66, 0\n"
    "v_accvgpr_write_b32 a67, 0\n"
    "v_accvgpr_write_b32 a68, 0\n"
    "v_accvgpr_write_b32 a69, 0\n"
    "v_accvgpr_write_b32 a70, 0\n"
    "v_accvgpr_write_b32 a71, 0\n"
    "v_accvgpr_write_b32 a72, 0\n"
    "v_accvgpr_write_b32 a73, 0\n"
    "v_accvgpr_write_b32 a74, 0\n"
    "v_accvgpr_write_b32 a75, 0\n"
    "v_accvgpr_write_b32 a76, 0\n"
    "v_accvgpr_write_b32 a77, 0\n"
    "v_accvgpr_write_b32 a78, 0\n"
    "v_accvgpr_write_b32 a79, 0\n"
    "v_accvgpr_write_b32 a80, 0\n"
    "v_accvgpr_write_b32 a81, 0\n"
    "v_accvgpr_write_b32 a82, 0\n"
    "v_accvgpr_write_b32 a83, 0\n"
    "v_accvgpr_write_b32 a84, 0\n"
    "v_accvgpr_write_b32 a85, 0\n"
    "v_accvgpr_write_b32 a86, 0\n"
    "v_accvgpr_write_b32 a87, 0\n"
    "v_accvgpr_write_b32 a88, 0\n"
    "v_accvgpr_write_b32 a89, 0\n"
    "v_accvgpr_write_b32 a90, 0\n"
    "v_accvgpr_write_b32 a91, 0\n"
    "v_accvgpr_write_b32 a92, 0\n"
    "v_accvgpr_write_b32 a93, 0\n"
    "v_accvgpr_write_b32 a94, 0\n"
    "v_accvgpr_write_b32 a95, 0\n"
    "v_accvgpr_write_b32 a96, 0\n"
    "v_accvgpr_write_b32 a97, 0\n"
    "v_accvgpr_write_b32 a98, 0\n"
    "v_accvgpr_write_b32 a99, 0\n"
    "v_accvgpr_write_b32 a100, 0\n"
    "v_accvgpr_write_b32 a101, 0\n"
    "v_accvgpr_write_b32 a102, 0\n"
    "v_accvgpr_write_b32 a103, 0\n"
    "v_accvgpr_write_b32 a104, 0\n"
    "v_accvgpr_write_b32 a105, 0\n"
    "v_accvgpr_write_b32 a106, 0\n"
    "v_accvgpr_write_b32 a107, 0\n"
    "v_accvgpr_write_b32 a108, 0\n"
    "v_accvgpr_write_b32 a109, 0\n"
    "v_accvgpr_write_b32 a110, 0\n"
    "v_accvgpr_write_b32 a111, 0\n"
    "v_accvgpr_write_b32 a112, 0\n"
    "v_accvgpr_write_b32 a113, 0\n"
    "v_accvgpr_write_b32 a114, 0\n"
    "v_accvgpr_write_b32 a115, 0\n"
    "v_accvgpr_write_b32 a116, 0\n"
    "v_accvgpr_write_b32 a117, 0\n"
    "v_accvgpr_write_b32 a118, 0\n"
    "v_accvgpr_write_b32 a119, 0\n"
    "v_accvgpr_write_b32 a120, 0\n"
    "v_accvgpr_write_b32 a121, 0\n"
    "v_accvgpr_write_b32 a122, 0\n"
    "v_accvgpr_write_b32 a123, 0\n"
    "v_accvgpr_write_b32 a124, 0\n"
    "v_accvgpr_write_b32 a125, 0\n"
    "v_accvgpr_write_b32 a126, 0\n"
    "v_accvgpr_write_b32 a127, 0\n"
    "v_accvgpr_write_b32 a128, 0\n"
    "v_accvgpr_write_b32 a129, 0\n"
    "v_accvgpr_write_b32 a130, 0\n"
    "v_accvgpr_write_b32 a131, 0\n"
    "v_accvgpr_write_b32 a132, 0\n"
    "v_accvgpr_write_b32 a133, 0\n"
    "v_accvgpr_write_b32 a134, 0\n"
    "v_accvgpr_write_b32 a135, 0\n"
    "v_accvgpr_write_b32 a136, 0\n"
    "v_accvgpr_write_b32 a137, 0\n"
    "v_accvgpr_write_b32 a138, 0\n"
    "v_accvgpr_write_b32 a139, 0\n"
    "v_accvgpr_write_b32 a140, 0\n"
    "v_accvgpr_write_b32 a141, 0\n"
    "v_accvgpr_write_b32 a142, 0\n"
    "v_accvgpr_write_b32 a143, 0\n"
    "v_accvgpr_write_b32 a144, 0\n"
    "v_accvgpr_write_b32 a145, 0\n"
    "v_accvgpr_write_b32 a146, 0\n"
    "v_accvgpr_write_b32 a147, 0\n"
    "v_accvgpr_write_b32 a148, 0\n"
    "v_accvgpr_write_b32 a149, 0\n"
    "v_accvgpr_write_b32 a150, 0\n"
    "v_accvgpr_write_b32 a151, 0\n"
    "v_accvgpr_write_b32 a152, 0\n"
    "v_accvgpr_write_b32 a153, 0\n"
    "v_accvgpr_write_b32 a154, 0\n"
    "v_accvgpr_write_b32 a155, 0\n"
    "v_accvgpr_write_b32 a156, 0\n"
    "v_accvgpr_write_b32 a157, 0\n"
    "v_accvgpr_write_b32 a158, 0\n"
    "v_accvgpr_write_b32 a159, 0\n"
    "v_accvgpr_write_b32 a160, 0\n"
    "v_accvgpr_write_b32 a161, 0\n"
    "v_accvgpr_write_b32 a162, 0\n"
    "v_accvgpr_write_b32 a163, 0\n"
    "v_accvgpr_write_b32 a164, 0\n"
    "v_accvgpr_write_b32 a165, 0\n"
    "v_accvgpr_write_b32 a166, 0\n"
    "v_accvgpr_write_b32 a167, 0\n"
    "v_accvgpr_write_b32 a168, 0\n"
    "v_accvgpr_write_b32 a169, 0\n"
    "v_accvgpr_write_b32 a170, 0\n"
    "v_accvgpr_write_b32 a171, 0\n"
    "v_accvgpr_write_b32 a172, 0\n"
    "v_accvgpr_write_b32 a173, 0\n"
    "v_accvgpr_write_b32 a174, 0\n"
    "v_accvgpr_write_b32 a175, 0\n"
    "v_accvgpr_write_b32 a176, 0\n"
    "v_accvgpr_write_b32 a177, 0\n"
    "v_accvgpr_write_b32 a178, 0\n"
    "v_accvgpr_write_b32 a179, 0\n"
    "v_accvgpr_write_b32 a180, 0\n"
    "v_accvgpr_write_b32 a181, 0\n"
    "v_accvgpr_write_b32 a182, 0\n"
    "v_accvgpr_write_b32 a183, 0\n"
    "v_accvgpr_write_b32 a184, 0\n"
    "v_accvgpr_write_b32 a185, 0\n"
    "v_accvgpr_write_b32 a186, 0\n"
    "v_accvgpr_write_b32 a187, 0\n"
    "v_accvgpr_write_b32 a188, 0\n"
    "v_accvgpr_write_b32 a189, 0\n"
    "v_accvgpr_write_b32 a190, 0\n"
    "v_accvgpr_write_b32 a191, 0\n"
    "v_accvgpr_write_b32 a192, 0\n"
    "v_accvgpr_write_b32 a193, 0\n"
    "v_accvgpr_write_b32 a194, 0\n"
    "v_accvgpr_write_b32 a195, 0\n"
    "v_accvgpr_write_b32 a196, 0\n"
    "v_accvgpr_write_b32 a197, 0\n"
    "v_accvgpr_write_b32 a198, 0\n"
    "v_accvgpr_write_b32 a199, 0\n"
    "v_accvgpr_write_b32 a200, 0\n"
    "v_accvgpr_write_b32 a201, 0\n"
    "v_accvgpr_write_b32 a202, 0\n"
    "v_accvgpr_write_b32 a203, 0\n"
    "v_accvgpr_write_b32 a204, 0\n"
    "v_accvgpr_write_b32 a205, 0\n"
    "v_accvgpr_write_b32 a206, 0\n"
    "v_accvgpr_write_b32 a207, 0\n"
    "v_accvgpr_write_b32 a208, 0\n"
    "v_accvgpr_write_b32 a209, 0\n"
    "v_accvgpr_write_b32 a210, 0\n"
    "v_accvgpr_write_b32 a211, 0\n"
    "v_accvgpr_write_b32 a212, 0\n"
    "v_accvgpr_write_b32 a213, 0\n"
    "v_accvgpr_write_b32 a214, 0\n"
    "v_accvgpr_write_b32 a215, 0\n"
    "v_accvgpr_write_b32 a216, 0\n"
    "v_accvgpr_write_b32 a217, 0\n"
    "v_accvgpr_write_b32 a218, 0\n"
    "v_accvgpr_write_b32 a219, 0\n"
    "v_accvgpr_write_b32 a220, 0\n"
    "v_accvgpr_write_b32 a221, 0\n"
    "v_accvgpr_write_b32 a222, 0\n"
    "v_accvgpr_write_b32 a223, 0\n"
    "s_mov_b32 s56, 0\n"
    "L_redo_%=:\n"
    "s_add_u32 m0, s34, 0\n"
    "s_add_u32 s58, s56, 0\n"
    "buffer_load_dwordx4 v200, s[44:47], s58 offen lds\n"
    "buffer_load_dwordx4 v201, s[44:47], s58 offen offset:1024 lds\n"
    "buffer_load_dwordx4 v202, s[44:47], s58 offen offset:2048 lds\n"
    "buffer_load_dwordx4 v203, s[44:47], s58 offen offset:3072 lds\n"
    "s_add_u32 m0, s34, 4096\n"
    "s_add_u32 s58, s56, 4096\n"
    "buffer_load_dwordx4 v200, s[44:47], s58 offen lds\n"
    "buffer_load_dwordx4 v201, s[44:47], s58 offen offset:1024 lds\n"
    "buffer_load_dwordx4 v202, s[44:47], s58 offen offset:2048 lds\n"
    "buffer_load_dwordx4 v203, s[44:47], s58 offen offset:3072 lds\n"
    "s_waitcnt vmcnt(0)\n"
    "ds_read_b128 a[240:243], v11 offset:0\n"
    "ds_read_b128 a[244:247], v11 offset:256\n"
    "ds_read_b128 a[248:251], v11 offset:512\n"
    "ds_read_b128 a[252:255], v11 offset:768\n"
    "ds_read_b128 a[224:227], v11 offset:4096\n"
    "ds_read_b128 a[228:231], v11 offset:4352\n"
    "ds_read_b128 a[232:235], v11 offset:4608\n"
    "ds_read_b128 a[236:239], v11 offset:4864\n"
    "s_waitcnt lgkmcnt(0)\n"
    "s_add_u32 m0, s34, 0\n"
    "s_add_u32 s58, s56, 0\n"
    "buffer_load_dwordx4 v204, s[48:51], s58 offen lds\n"
    "buffer_load_dwordx4 v205, s[48:51], s58 offen offset:1024 lds\n"
    "buffer_load_dwordx4 v204, s[48:51], s58 offen offset:2048 lds\n"
    "buffer_load_dwordx4 v205, s[48:51], s58 offen offset:3072 lds\n"
    "s_add_u32 m0, s34, 4096\n"
    "s_add_u32 s58, s56, 4096\n"
    "buffer_load_dwordx4 v204, s[48:51], s58 offen lds\n"
    "buffer_load_dwordx4 v205, s[48:51], s58 offen offset:1024 lds\n"
    "buffer_load_dwordx4 v204, s[48:51], s58 offen offset:2048 lds\n"
    "buffer_load_dwordx4 v205, s[48:51], s58 offen offset:3072 lds\n"
    "s_waitcnt vmcnt(0)\n"
    "v_mfma_f32_16x16x32_bf16 v[184:187], a[240:243], v[32:35], 0\n"
    "v_mfma_f32_16x16x32_bf16 v[184:187], a[244:247], v[36:39], v[184:187]\n"
    "v_mfma_f32_16x16x32_bf16 v[184:187], a[248:251], v[40:43], v[184:187]\n"
    "v_mfma_f32_16x16x32_bf16 v[184:187], a[252:255], v[44:47], v[184:187]\n"
    "s_nop 7\n"
    "v_sub_f32 v184, v184, v156\n"
    "v_sub_f32 v185, v185, v156\n"
    "v_sub_f32 v186, v186, v156\n"
    "v_sub_f32 v187, v187, v156\n"
    "v_exp_f32 v184, v184\n"
    "v_exp_f32 v185, v185\n"
    "v_exp_f32 v186, v186\n"
    "v_exp_f32 v187, v187\n"
    "v_cvt_pk_bf16_f32 v228, v184, v185\n"
    "v_cvt_pk_bf16_f32 v229, v186, v187\n"
    "v_add_f32 v20, v20, v184\n"
    "v_add_f32 v20, v20, v185\n"
    "v_add_f32 v20, v20, v186\n"
    "v_add_f32 v20, v20, v187\n"
    "v_mfma_f32_16x16x32_bf16 v[188:191], a[240:243], v[48:51], 0\n"
    "v_mfma_f32_16x16x32_bf16 v[188:191], a[244:247], v[52:55], v[188:191]\n"
    "v_mfma_f32_16x16x32_bf16 v[188:191], a[248:251], v[56:59], v[188:191]\n"
    "v_mfma_f32_16x16x32_bf16 v[188:191], a[252:255], v[60:63], v[188:191]\n"
    "s_nop 7\n"
    "v_sub_f32 v188, v188, v160\n"
    "v_sub_f32 v189, v189, v160\n"
    "v_sub_f32 v190, v190, v160\n"
    "v_sub_f32 v191, v191, v160\n"
    "v_exp_f32 v188, v188\n"
    "v_exp_f32 v189, v189\n"
    "v_exp_f32 v190, v190\n"
    "v_exp_f32 v191, v191\n"
    "v_cvt_pk_bf16_f32 v232, v188, v189\n"
    "v_cvt_pk_bf16_f32 v233, v190, v191\n"
    "v_add_f32 v21, v21, v188\n"
    "v_add_f32 v21, v21, v189\n"
    "v_add_f32 v21, v21, v190\n"
    "v_add_f32 v21, v21, v191\n"
    "v_mfma_f32_16x16x32_bf16 v[192:195], a[240:243], v[64:67], 0\n"
    "v_mfma_f32_16x16x32_bf16 v[192:195], a[244:247], v[68:71], v[192:195]\n"
    "v_mfma_f32_16x16x32_bf16 v[192:195], a[248:251], v[72:75], v[192:195]\n"
    "v_mfma_f32_16x16x32_bf16 v[192:195], a[252:255], v[76:79], v[192:195]\n"
    "s_nop 7\n"
    "v_sub_f32 v192, v192, v164\n"
    "v_sub_f32 v193, v193, v164\n"
    "v_sub_f32 v194, v194, v164\n"
    "v_sub_f32 v195, v195, v164\n"
    "v_exp_f32 v192, v192\n"
    "v_exp_f32 v193, v193\n"
    "v_exp_f32 v194, v194\n"
    "v_exp_f32 v195, v195\n"
    "v_cvt_pk_bf16_f32 v236, v192, v193\n"
    "v_cvt_pk_bf16_f32 v237, v194, v195\n"
    "v_add_f32 v22, v22, v192\n"
    "v_add_f32 v22, v22, v193\n"
    "v_add_f32 v22, v22, v194\n"
    "v_add_f32 v22, v22, v195\n"
    "v_mfma_f32_16x16x32_bf16 v[196:199], a[240:243], v[80:83], 0\n"
    "v_mfma_f32_16x16x32_bf16 v[196:199], a[244:247], v[84:87], v[196:199]\n"
    "v_mfma_f32_16x16x32_bf16 v[196:199], a[248:251], v[88:91], v[196:199]\n"
    "v_mfma_f32_16x16x32_bf16 v[196:199], a[252:255], v[92:95], v[196:199]\n"
    "s_nop 7\n"
    "v_sub_f32 v196, v196, v168\n"
    "v_sub_f32 v197, v197, v168\n"
    "v_sub_f32 v198, v198, v168\n"
    "v_sub_f32 v199, v199, v168\n"
    "v_exp_f32 v196, v196\n"
    "v_exp_f32 v197, v197\n"
    "v_exp_f32 v198, v198\n"
    "v_exp_f32 v199, v199\n"
    "v_cvt_pk_bf16_f32 v240, v196, v197\n"
    "v_cvt_pk_bf16_f32 v241, v198, v199\n"
    "v_add_f32 v23, v23, v196\n"
    "v_add_f32 v23, v23, v197\n"
    "v_add_f32 v23, v23, v198\n"
    "v_add_f32 v23, v23, v199\n"
    "v_mfma_f32_16x16x32_bf16 v[184:187], a[240:243], v[96:99], 0\n"
    "v_mfma_f32_16x16x32_bf16 v[184:187], a[244:247], v[100:103], v[184:187]\n"
    "v_mfma_f32_16x16x32_bf16 v[184:187], a[248:251], v[104:107], v[184:187]\n"
    "v_mfma_f32_16x16x32_bf16 v[184:187], a[252:255], v[108:111], v[184:187]\n"
    "s_nop 7\n"
    "v_sub_f32 v184, v184, v172\n"
    "v_sub_f32 v185, v185, v172\n"
    "v_sub_f32 v186, v186, v172\n"
    "v_sub_f32 v187, v187, v172\n"
    "v_exp_f32 v184, v184\n"
    "v_exp_f32 v185, v185\n"
    "v_exp_f32 v186, v186\n"
    "v_exp_f32 v187, v187\n"
    "v_cvt_pk_bf16_f32 v244, v184, v185\n"
    "v_cvt_pk_bf16_f32 v245, v186, v187\n"
    "v_add_f32 v24, v24, v184\n"
    "v_add_f32 v24, v24, v185\n"
    "v_add_f32 v24, v24, v186\n"
    "v_add_f32 v24, v24, v187\n"
    "v_mfma_f32_16x16x32_bf16 v[188:191], a[240:243], v[112:115], 0\n"
    "v_mfma_f32_16x16x32_bf16 v[188:191], a[244:247], v[116:119], v[188:191]\n"
    "v_mfma_f32_16x16x32_bf16 v[188:191], a[248:251], v[120:123], v[188:191]\n"
    "v_mfma_f32_16x16x32_bf16 v[188:191], a[252:255], v[124:127], v[188:191]\n"
    "s_nop 7\n"
    "v_sub_f32 v188, v188, v176\n"
    "v_sub_f32 v189, v189, v176\n"
    "v_sub_f32 v190, v190, v176\n"
    "v_sub_f32 v191, v191, v176\n"
    "v_exp_f32 v188, v188\n"
    "v_exp_f32 v189, v189\n"
    "v_exp_f32 v190, v190\n"
    "v_exp_f32 v191, v191\n"
    "v_cvt_pk_bf16_f32 v248, v188, v189\n"
    "v_cvt_pk_bf16_f32 v249, v190, v191\n"
    "v_add_f32 v25, v25, v188\n"
    "v_add_f32 v25, v25, v189\n"
    "v_add_f32 v25, v25, v190\n"
    "v_add_f32 v25, v25, v191\n"
    "v_mfma_f32_16x16x32_bf16 v[192:195], a[240:243], v[128:131], 0\n"
    "v_mfma_f32_16x16x32_bf16 v[192:195], a[244:247], v[132:135], v[192:195]\n"
    "v_mfma_f32_16x16x32_bf16 v[192:195], a[248:251], v[136:139], v[192:195]\n"
    "v_mfma_f32_16x16x32_bf16 v[192:195], a[252:255], v[140:143], v[192:195]\n"
    "s_nop 7\n"
    "v_sub_f32 v192, v192, v180\n"
    "v_sub_f32 v193, v193, v180\n"
    "v_sub_f32 v194, v194, v180\n"
    "v_sub_f32 v195, v195, v180\n"
    "v_exp_f32 v192, v192\n"
    "v_exp_f32 v193, v193\n"
    "v_exp_f32 v194, v194\n"
    "v_exp_f32 v195, v195\n"
    "v_cvt_pk_bf16_f32 v252, v192, v193\n"
    "v_cvt_pk_bf16_f32 v253, v194, v195\n"
    "v_add_f32 v26, v26, v192\n"
    "v_add_f32 v26, v26, v193\n"
    "v_add_f32 v26, v26, v194\n"
    "v_add_f32 v26, v26, v195\n"
    "v_mfma_f32_16x16x32_bf16 v[196:199], a[224:227], v[32:35], 0\n"
    "v_mfma_f32_16x16x32_bf16 v[196:199], a[228:231], v[36:39], v[196:199]\n"
    "v_mfma_f32_16x16x32_bf16 v[196:199], a[232:235], v[40:43], v[196:199]\n"
    "v_mfma_f32_16x16x32_bf16 v[196:199], a[236:239], v[44:47], v[196:199]\n"
    "s_nop 7\n"
    "v_sub_f32 v196, v196, v156\n"
    "v_sub_f32 v197, v197, v156\n"
    "v_sub_f32 v198, v198, v156\n"
    "v_sub_f32 v199, v199, v156\n"
    "v_exp_f32 v196, v196\n"
    "v_exp_f32 v197, v197\n"
    "v_exp_f32 v198, v198\n"
    "v_exp_f32 v199, v199\n"
    "v_cvt_pk_bf16_f32 v230, v196, v197\n"
    "v_cvt_pk_bf16_f32 v231, v198, v199\n"
    "v_add_f32 v20, v20, v196\n"
    "v_add_f32 v20, v20, v197\n"
    "v_add_f32 v20, v20, v198\n"
    "v_add_f32 v20, v20, v199\n"
    "v_mfma_f32_16x16x32_bf16 v[184:187], a[224:227], v[48:51], 0\n"
    "v_mfma_f32_16x16x32_bf16 v[184:187], a[228:231], v[52:55], v[184:187]\n"
    "v_mfma_f32_16x16x32_bf16 v[184:187], a[232:235], v[56:59], v[184:187]\n"
    "v_mfma_f32_16x16x32_bf16 v[184:187], a[236:239], v[60:63], v[184:187]\n"
    "s_nop 7\n"
    "v_sub_f32 v184, v184, v160\n"
    "v_sub_f32 v185, v185, v160\n"
    "v_sub_f32 v186, v186, v160\n"
    "v_sub_f32 v187, v187, v160\n"
    "v_exp_f32 v184, v184\n"
    "v_exp_f32 v185, v185\n"
    "v_exp_f32 v186, v186\n"
    "v_exp_f32 v187, v187\n"
    "v_cvt_pk_bf16_f32 v234, v184, v185\n"
    "v_cvt_pk_bf16_f32 v235, v186, v187\n"
    "v_add_f32 v21, v21, v184\n"
    "v_add_f32 v21, v21, v185\n"
    "v_add_f32 v21, v21, v186\n"
    "v_add_f32 v21, v21, v187\n"
    "v_mfma_f32_16x16x32_bf16 v[188:191], a[224:227], v[64:67], 0\n"
    "v_mfma_f32_16x16x32_bf16 v[188:191], a[228:231], v[68:71], v[188:191]\n"
    "v_mfma_f32_16x16x32_bf16 v[188:191], a[232:235], v[72:75], v[188:191]\n"
    "v_mfma_f32_16x16x32_bf16 v[188:191], a[236:239], v[76:79], v[188:191]\n"
    "s_nop 7\n"
    "v_sub_f32 v188, v188, v164\n"
    "v_sub_f32 v189, v189, v164\n"
    "v_sub_f32 v190, v190, v164\n"
    "v_sub_f32 v191, v191, v164\n"
    "v_exp_f32 v188, v188\n"
    "v_exp_f32 v189, v189\n"
    "v_exp_f32 v190, v190\n"
    "v_exp_f32 v191, v191\n"
    "v_cvt_pk_bf16_f32 v238, v188, v189\n"
    "v_cvt_pk_bf16_f32 v239, v190, v191\n"
    "v_add_f32 v22, v22, v188\n"
    "v_add_f32 v22, v22, v189\n"
    "v_add_f32 v22, v22, v190\n"
    "v_add_f32 v22, v22, v191\n"
    "v_mfma_f32_16x16x32_bf16 v[192:195], a[224:227], v[80:83], 0\n"
    "v_mfma_f32_16x16x32_bf16 v[192:195], a[228:231], v[84:87], v[192:195]\n"
    "v_mfma_f32_16x16x32_bf16 v[192:195], a[232:235], v[88:91], v[192:195]\n"
    "v_mfma_f32_16x16x32_bf16 v[192:195], a[236:239], v[92:95], v[192:195]\n"
    "s_nop 7\n"
    "v_sub_f32 v192, v192, v168\n"
    "v_sub_f32 v193, v193, v168\n"
    "v_sub_f32 v194, v194, v168\n"
    "v_sub_f32 v195, v195, v168\n"
    "v_exp_f32 v192, v192\n"
    "v_exp_f32 v193, v193\n"
    "v_exp_f32 v194, v194\n"
    "v_exp_f32 v195, v195\n"
    "v_cvt_pk_bf16_f32 v242, v192, v193\n"
    "v_cvt_pk_bf16_f32 v243, v194, v195\n"
    "v_add_f32 v23, v23, v192\n"
    "v_add_f32 v23, v23, v193\n"
    "v_add_f32 v23, v23, v194\n"
    "v_add_f32 v23, v23, v195\n"
    "v_mfma_f32_16x16x32_bf16 v[196:199], a[224:227], v[96:99], 0\n"
    "v_mfma_f32_16x16x32_bf16 v[196:199], a[228:231], v[100:103], v[196:199]\n"
    "v_mfma_f32_16x16x32_bf16 v[196:199], a[232:235], v[104:107], v[196:199]\n"
    "v_mfma_f32_16x16x32_bf16 v[196:199], a[236:239], v[108:111], v[196:199]\n"
    "s_nop 7\n"
    "v_sub_f32 v196, v196, v172\n"
    "v_sub_f32 v197, v197, v172\n"
    "v_sub_f32 v198, v198, v172\n"
    "v_sub_f32 v199, v199, v172\n"
    "v_exp_f32 v196, v196\n"
    "v_exp_f32 v197, v197\n"
    "v_exp_f32 v198, v198\n"
    "v_exp_f32 v199, v199\n"
    "v_cvt_pk_bf16_f32 v246, v196, v197\n"
    "v_cvt_pk_bf16_f32 v247, v198, v199\n"
    "v_add_f32 v24, v24, v196\n"
    "v_add_f32 v24, v24, v197\n"
    "v_add_f32 v24, v24, v198\n"
    "v_add_f32 v24, v24, v199\n"
    "v_mfma_f32_16x16x32_bf16 v[184:187], a[224:227], v[112:115], 0\n"
    "v_mfma_f32_16x16x32_bf16 v[184:187], a[228:231], v[116:119], v[184:187]\n"
    "v_mfma_f32_16x16x32_bf16 v[184:187], a[232:235], v[120:123], v[184:187]\n"
    "v_mfma_f32_16x16x32_bf16 v[184:187], a[236:239], v[124:127], v[184:187]\n"
    "s_nop 7\n"
    "v_sub_f32 v184, v184, v176\n"
    "v_sub_f32 v185, v185, v176\n"
    "v_sub_f32 v186, v186, v176\n"
    "v_sub_f32 v187, v187, v176\n"
    "v_exp_f32 v184, v184\n"
    "v_exp_f32 v185, v185\n"
    "v_exp_f32 v186, v186\n"
    "v_exp_f32 v187, v187\n"
    "v_cvt_pk_bf16_f32 v250, v184, v185\n"
    "v_cvt_pk_bf16_f32 v251, v186, v187\n"
    "v_add_f32 v25, v25, v184\n"
    "v_add_f32 v25, v25, v185\n"
    "v_add_f32 v25, v25, v186\n"
    "v_add_f32 v25, v25, v187\n"
    "v_mfma_f32_16x16x32_bf16 v[188:191], a[224:227], v[128:131], 0\n"
    "v_mfma_f32_16x16x32_bf16 v[188:191], a[228:231], v[132:135], v[188:191]\n"
    "v_mfma_f32_16x16x32_bf16 v[188:191], a[232:235], v[136:139], v[188:191]\n"
    "v_mfma_f32_16x16x32_bf16 v[188:191], a[236:239], v[140:143], v[188:191]\n"
    "s_nop 7\n"
    "v_sub_f32 v188, v188, v180\n"
    "v_sub_f32 v189, v189, v180\n"
    "v_sub_f32 v190, v190, v180\n"
    "v_sub_f32 v191, v191, v180\n"
    "v_exp_f32 v188, v188\n"
    "v_exp_f32 v189, v189\n"
    "v_exp_f32 v190, v190\n"
    "v_exp_f32 v191, v191\n"
    "v_cvt_pk_bf16_f32 v254, v188, v189\n"
    "v_cvt_pk_bf16_f32 v255, v190, v191\n"
    "v_add_f32 v26, v26, v188\n"
    "v_add_f32 v26, v26, v189\n"
    "v_add_f32 v26, v26, v190\n"
    "v_add_f32 v26, v26, v191\n"
    "ds_read_b64_tr_b16 v[144:145], v206 offset:0\n"
    "ds_read_b64_tr_b16 v[146:147], v206 offset:4096\n"
    "s_waitcnt lgkmcnt(0)\n"
    "v_mfma_f32_16x16x32_bf16 a[0:3], v[144:147], v[228:231], a[0:3]\n"
    "v_mfma_f32_16x16x32_bf16 a[4:7], v[144:147], v[232:235], a[4:7]\n"
    "v_mfma_f32_16x16x32_bf16 a[8:11], v[144:147], v[236:239], a[8:11]\n"
    "v_mfma_f32_16x16x32_bf16 a[12:15], v[144:147], v[240:243], a[12:15]\n"
    "v_mfma_f32_16x16x32_bf16 a[16:19], v[144:147], v[244:247], a[16:19]\n"
    "v_mfma_f32_16x16x32_bf16 a[20:23], v[144:147], v[248:251], a[20:23]\n"
    "v_mfma_f32_16x16x32_bf16 a[24:27], v[144:147], v[252:255], a[24:27]\n"
    "ds_read_b64_tr_b16 v[148:149], v207 offset:0\n"
    "ds_read_b64_tr_b16 v[150:151], v207 offset:4096\n"
    "s_waitcnt lgkmcnt(0)\n"
    "v_mfma_f32_16x16x32_bf16 a[28:31], v[148:151], v[228:231], a[28:31]\n"
    "v_mfma_f32_16x16x32_bf16 a[32:35], v[148:151], v[232:235], a[32:35]\n"
    "v_mfma_f32_16x16x32_bf16 a[36:39], v[148:151], v[236:239], a[36:39]\n"
    "v_mfma_f32_16x16x32_bf16 a[40:43], v[148:151], v[240:243], a[40:43]\n"
    "v_mfma_f32_16x16x32_bf16 a[44:47], v[148:151], v[244:247], a[44:47]\n"
    "v_mfma_f32_16x16x32_bf16 a[48:51], v[148:151], v[248:251], a[48:51]\n"
    "v_mfma_f32_16x16x32_bf16 a[52:55], v[148:151], v[252:255], a[52:55]\n"
    "ds_read_b64_tr_b16 v[152:153], v206 offset:256\n"
    "ds_read_b64_tr_b16 v[154:155], v206 offset:4352\n"
    "s_waitcnt lgkmcnt(0)\n"
    "v_mfma_f32_16x16x32_bf16 a[56:59], v[152:155], v[228:231], a[56:59]\n"
    "v_mfma_f32_16x16x32_bf16 a[60:63], v[152:155], v[232:235], a[60:63]\n"
    "v_mfma_f32_16x16x32_bf16 a[64:67], v[152:155], v[236:239], a[64:67]\n"
    "v_mfma_f32_16x16x32_bf16 a[68:71], v[152:155], v[240:243], a[68:71]\n"
    "v_mfma_f32_16x16x32_bf16 a[72:75], v[152:155], v[244:247], a[72:75]\n"
    "v_mfma_f32_16x16x32_bf16 a[76:79], v[152:155], v[248:251], a[76:79]\n"
    "v_mfma_f32_16x16x32_bf16 a[80:83], v[152:155], v[252:255], a[80:83]\n"
    "ds_read_b64_tr_b16 v[28:29], v207 offset:256\n"
    "ds_read_b64_tr_b16 v[30:31], v207 offset:4352\n"
    "s_waitcnt lgkmcnt(0)\n"
    "v_mfma_f32_16x16x32_bf16 a[84:87], v[28:31], v[228:231], a[84:87]\n"
    "v_mfma_f32_16x16x32_bf16 a[88:91], v[28:31], v[232:235], a[88:91]\n"
    "v_mfma_f32_16x16x32_bf16 a[92:95], v[28:31], v[236:239], a[92:95]\n"
    "v_mfma_f32_16x16x32_bf16 a[96:99], v[28:31], v[240:243], a[96:99]\n"
    "v_mfma_f32_16x16x32_bf16 a[100:103], v[28:31], v[244:247], a[100:103]\n"
    "v_mfma_f32_16x16x32_bf16 a[104:107], v[28:31], v[248:251], a[104:107]\n"
    "v_mfma_f32_16x16x32_bf16 a[108:111], v[28:31], v[252:255], a[108:111]\n"
    "ds_read_b64_tr_b16 v[144:145], v206 offset:512\n"
    "ds_read_b64_tr_b16 v[146:147], v206 offset:4608\n"
    "ds_read_b64_tr_b16 v[148:149], v207 offset:512\n"
    "ds_read_b64_tr_b16 v[150:151], v207 offset:4608\n"
    "ds_read_b64_tr_b16 v[152:153], v206 offset:768\n"
    "ds_read_b64_tr_b16 v[154:155], v206 offset:4864\n"
    "ds_read_b64_tr_b16 v[28:29], v207 offset:768\n"
    "ds_read_b64_tr_b16 v[30:31], v207 offset:4864\n"
    "s_waitcnt lgkmcnt(0)\n"
    "v_mfma_f32_16x16x32_bf16 a[112:115], v[144:147], v[228:231], a[112:115]\n"
    "v_mfma_f32_16x16x32_bf16 a[116:119], v[144:147], v[232:235], a[116:119]\n"
    "v_mfma_f32_16x16x32_bf16 a[120:123], v[144:147], v[236:239], a[120:123]\n"
    "v_mfma_f32_16x16x32_bf16 a[124:127], v[144:147], v[240:243], a[124:127]\n"
    "v_mfma_f32_16x16x32_bf16 a[128:131], v[144:147], v[244:247], a[128:131]\n"
    "v_mfma_f32_16x16x32_bf16 a[132:135], v[144:147], v[248:251], a[132:135]\n"
    "v_mfma_f32_16x16x32_bf16 a[136:139], v[144:147], v[252:255], a[136:139]\n"
    "v_mfma_f32_16x16x32_bf16 a[140:143], v[148:151], v[228:231], a[140:143]\n"
    "v_mfma_f32_16x16x32_bf16 a[144:147], v[148:151], v[232:235], a[144:147]\n"
    "v_mfma_f32_16x16x32_bf16 a[148:151], v[148:151], v[236:239], a[148:151]\n"
    "v_mfma_f32_16x16x32_bf16 a[152:155], v[148:151], v[240:243], a[152:155]\n"
    "v_mfma_f32_16x16x32_bf16 a[156:159], v[148:151], v[244:247], a[156:159]\n"
    "v_mfma_f32_16x16x32_bf16 a[160:163], v[148:151], v[248:251], a[160:163]\n"
    "v_mfma_f32_16x16x32_bf16 a[164:167], v[148:151], v[252:255], a[164:167]\n"
    "v_mfma_f32_16x16x32_bf16 a[168:171], v[152:155], v[228:231], a[168:171]\n"
    "v_mfma_f32_16x16x32_bf16 a[172:175], v[152:155], v[232:235], a[172:175]\n"
    "v_mfma_f32_16x16x32_bf16 a[176:179], v[152:155], v[236:239], a[176:179]\n"
    "v_mfma_f32_16x16x32_bf16 a[180:183], v[152:155], v[240:243], a[180:183]\n"
    "v_mfma_f32_16x16x32_bf16 a[184:187], v[152:155], v[244:247], a[184:187]\n"
    "v_mfma_f32_16x16x32_bf16 a[188:191], v[152:155], v[248:251], a[188:191]\n"
    "v_mfma_f32_16x16x32_bf16 a[192:195], v[152:155], v[252:255], a[192:195]\n"
    "v_mfma_f32_16x16x32_bf16 a[196:199], v[28:31], v[228:231], a[196:199]\n"
    "v_mfma_f32_16x16x32_bf16 a[200:203], v[28:31], v[232:235], a[200:203]\n"
    "v_mfma_f32_16x16x32_bf16 a[204:207], v[28:31], v[236:239], a[204:207]\n"
    "v_mfma_f32_16x16x32_bf16 a[208:211], v[28:31], v[240:243], a[208:211]\n"
    "v_mfma_f32_16x16x32_bf16 a[212:215], v[28:31], v[244:247], a[212:215]\n"
    "v_mfma_f32_16x16x32_bf16 a[216:219], v[28:31], v[248:251], a[216:219]\n"
    "v_mfma_f32_16x16x32_bf16 a[220:223], v[28:31], v[252:255], a[220:223]\n"
    "s_add_u32 s56, s56, 8192\n"
    "s_cmp_ge_u32 s56, 131072\n"
    "s_cbranch_scc1 L_fin_%=\n"
    "s_nop 7\n"
    "s_branch L_redo_%=\n"
    "L_fin_%=:\n"
    "v_mov_b32 v184, v20\n"
    "s_nop 1\n"
    "v_permlane32_swap_b32 v184, v20\n"
    "v_add_f32 v20, v20, v184\n"
    "v_mov_b32 v184, v20\n"
    "s_nop 1\n"
    "v_permlane16_swap_b32 v184, v20\n"
    "v_add_f32 v20, v20, v184\n"
    "v_rcp_f32 v20, v20\n"
    "v_mov_b32 v184, v21\n"
    "s_nop 1\n"
    "v_permlane32_swap_b32 v184, v21\n"
    "v_add_f32 v21, v21, v184\n"
    "v_mov_b32 v184, v21\n"
    "s_nop 1\n"
    "v_permlane16_swap_b32 v184, v21\n"
    "v_add_f32 v21, v21, v184\n"
    "v_rcp_f32 v21, v21\n"
    "v_mov_b32 v184, v22\n"
    "s_nop 1\n"
    "v_permlane32_swap_b32 v184, v22\n"
    "v_add_f32 v22, v22, v184\n"
    "v_mov_b32 v184, v22\n"
    "s_nop 1\n"
    "v_permlane16_swap_b32 v184, v22\n"
    "v_add_f32 v22, v22, v184\n"
    "v_rcp_f32 v22, v22\n"
    "v_mov_b32 v184, v23\n"
    "s_nop 1\n"
    "v_permlane32_swap_b32 v184, v23\n"
    "v_add_f32 v23, v23, v184\n"
    "v_mov_b32 v184, v23\n"
    "s_nop 1\n"
    "v_permlane16_swap_b32 v184, v23\n"
    "v_add_f32 v23, v23, v184\n"
    "v_rcp_f32 v23, v23\n"
    "v_mov_b32 v184, v24\n"
    "s_nop 1\n"
    "v_permlane32_swap_b32 v184, v24\n"
    "v_add_f32 v24, v24, v184\n"
    "v_mov_b32 v184, v24\n"
    "s_nop 1\n"
    "v_permlane16_swap_b32 v184, v24\n"
    "v_add_f32 v24, v24, v184\n"
    "v_rcp_f32 v24, v24\n"
    "v_mov_b32 v184, v25\n"
    "s_nop 1\n"
    "v_permlane32_swap_b32 v184, v25\n"
    "v_add_f32 v25, v25, v184\n"
    "v_mov_b32 v184, v25\n"
    "s_nop 1\n"
    "v_permlane16_swap_b32 v184, v25\n"
    "v_add_f32 v25, v25, v184\n"
    "v_rcp_f32 v25, v25\n"
    "v_mov_b32 v184, v26\n"
    "s_nop 1\n"
    "v_permlane32_swap_b32 v184, v26\n"
    "v_add_f32 v26, v26, v184\n"
    "v_mov_b32 v184, v26\n"
    "s_nop 1\n"
    "v_permlane16_swap_b32 v184, v26\n"
    "v_add_f32 v26, v26, v184\n"
    "v_rcp_f32 v26, v26\n"
    "s_waitcnt vmcnt(0)\n"
    "s_nop 7\n"
    "s_branch L_epi_%=\n"
    "L_end_%=:\n"
    :
    : "s"(__builtin_amdgcn_kernarg_segment_ptr()), "s"(blockIdx.x), "s"(w), "s"((unsigned)(size_t)lds)
    : "memory", "vcc", "scc", "s6", "s7", "s8", "s9", "s10", "s11", "s12", "s13", "s14", "s15", "s16", "s17", "s18", "s19", "s20", "s21", "s22", "s23", "s24", "s25", "s26", "s27", "s28", "s29", "s30", "s31", "s34", "s35", "s36", "s37", "s38", "s39", "s40", "s41", "s42", "s43", "s44", "s45", "s46", "s47", "s48", "s49", "s50", "s51", "s52", "s53", "s54", "s55", "s56", "s57", "s58", "s59", "s60", "s61", "s62", "s63", "s64", "s65", "s66", "s67", "s68", "s69", "s70", "s71", "s72", "s73", "s74", "s75", "v0", "v1", "v2", "v3", "v4", "v5", "v6", "v7", "v8", "v9", "v10", "v11", "v12", "v13", "v14", "v15", "v16", "v17", "v18", "v19", "v20", "v21", "v22", "v23", "v24", "v25", "v26", "v27", "v28", "v29", "v30", "v31", "v32", "v33", "v34", "v35", "v36", "v37", "v38", "v39", "v40", "v41", "v42", "v43", "v44", "v45", "v46", "v47", "v48", "v49", "v50", "v51", "v52", "v53", "v54", "v55", "v56", "v57", "v58", "v59", "v60", "v61", "v62", "v63", "v64", "v65", "v66", "v67", "v68", "v69", "v70", "v71", "v72", "v73", "v74", "v75", "v76", "v77", "v78", "v79", "v80", "v81", "v82", "v83", "v84", "v85", "v86", "v87", "v88", "v89", "v90", "v91", "v92", "v93", "v94", "v95", "v96", "v97", "v98", "v99", "v100", "v101", "v102", "v103", "v104", "v105", "v106", "v107", "v108", "v109", "v110", "v111", "v112", "v113", "v114", "v115", "v116", "v117", "v118", "v119", "v120", "v121", "v122", "v123", "v124", "v125", "v126", "v127", "v128", "v129", "v130", "v131", "v132", "v133", "v134", "v135", "v136", "v137", "v138", "v139", "v140", "v141", "v142", "v143", "v144", "v145", "v146", "v147", "v148", "v149", "v150", "v151", "v152", "v153", "v154", "v155", "v156", "v157", "v158", "v159", "v160", "v161", "v162", "v163", "v164", "v165", "v166", "v167", "v168", "v169", "v170", "v171", "v172", "v173", "v174", "v175", "v176", "v177", "v178", "v179", "v180", "v181", "v182", "v183", "v184", "v185", "v186", "v187", "v188", "v189", "v190", "v191", "v192", "v193", "v194", "v195", "v196", "v197", "v198", "v199", "v200", "v201", "v202", "v203", "v204", "v205", "v206", "v207", "v208", "v209", "v210", "v211", "v212", "v213", "v214", "v215", "v216", "v217", "v218", "v219", "v220", "v221", "v222", "v223", "v224", "v225", "v226", "v227", "v228", "v229", "v230", "v231", "v232", "v233", "v234", "v235", "v236", "v237", "v238", "v239", "v240", "v241", "v242", "v243", "v244", "v245", "v246", "v247", "v248", "v249", "v250", "v251", "v252", "v253", "v254", "v255", "a0", "a1", "a2", "a3", "a4", "a5", "a6", "a7", "a8", "a9", "a10", "a11", "a12", "a13", "a14", "a15", "a16", "a17", "a18", "a19", "a20", "a21", "a22", "a23", "a24", "a25", "a26", "a27", "a28", "a29", "a30", "a31", "a32", "a33", "a34", "a35", "a36", "a37", "a38", "a39", "a40", "a41", "a42", "a43", "a44", "a45", "a46", "a47", "a48", "a49", "a50", "a51", "a52", "a53", "a54", "a55", "a56", "a57", "a58", "a59", "a60", "a61", "a62", "a63", "a64", "a65", "a66", "a67", "a68", "a69", "a70", "a71", "a72", "a73", "a74", "a75", "a76", "a77", "a78", "a79", "a80", "a81", "a82", "a83", "a84", "a85", "a86", "a87", "a88", "a89", "a90", "a91", "a92", "a93", "a94", "a95", "a96", "a97", "a98", "a99", "a100", "a101", "a102", "a103", "a104", "a105", "a106", "a107", "a108", "a109", "a110", "a111", "a112", "a113", "a114", "a115", "a116", "a117", "a118", "a119", "a120", "a121", "a122", "a123", "a124", "a125", "a126", "a127", "a128", "a129", "a130", "a131", "a132", "a133", "a134", "a135", "a136", "a137", "a138", "a139", "a140", "a141", "a142", "a143", "a144", "a145", "a146", "a147", "a148", "a149", "a150", "a151", "a152", "a153", "a154", "a155", "a156", "a157", "a158", "a159", "a160", "a161", "a162", "a163", "a164", "a165", "a166", "a167", "a168", "a169", "a170", "a171", "a172", "a173", "a174", "a175", "a176", "a177", "a178", "a179", "a180", "a181", "a182", "a183", "a184", "a185", "a186", "a187", "a188", "a189", "a190", "a191", "a192", "a193", "a194", "a195", "a196", "a197", "a198", "a199", "a200", "a201", "a202", "a203", "a204", "a205", "a206", "a207", "a208", "a209", "a210", "a211", "a212", "a213", "a214", "a215", "a216", "a217", "a218", "a219", "a220", "a221", "a222", "a223", "a224", "a225", "a226", "a227", "a228", "a229", "a230", "a231", "a232", "a233", "a234", "a235", "a236", "a237", "a238", "a239", "a240", "a241", "a242", "a243", "a244", "a245", "a246", "a247", "a248", "a249", "a250", "a251", "a252", "a253", "a254", "a255");
}

extern "C" void kernel_launch(void* const* d_in, const int* in_sizes, int n_in, void* d_out, int out_size, void* d_ws, size_t ws_size,
                              hipStream_t stream) {
  fused_kernel<<<256, 256, 0, stream>>>((const char*)d_in[0], (const char*)d_in[1], (const char*)d_in[2], (const char*)d_in[3], (const char*)d_in[4],
                                        (const char*)d_in[5], (const char*)d_in[6], (const char*)d_in[7], (const char*)d_in[8], (const char*)d_in[9],
                                        (char*)d_ws, (char*)d_out);
}
```
